```python
import torch

def _softmax(x, axis=-1):
    return torch.softmax(x, dim=axis)

def _gelu(x):
    return torch.nn.functional.gelu(x, approximate='tanh')

def _silu(x):
    return torch.nn.functional.silu(x)

def _sigmoid(x):
    return torch.sigmoid(x)

def _layernorm(x, axis=-1, eps=1e-05):
    m = x.mean(dim=axis, keepdim=True)
    v = x.var(dim=axis, keepdim=True, unbiased=False)
    return (x - m) / torch.sqrt(v + eps)

def _scatter_add(tgt, idx, src, axis=0):
    return tgt.clone().index_add_(axis, idx, src)

def _segment_sum(src, seg, n):
    out = torch.zeros((n,) + src.shape[1:], dtype=src.dtype)
    return out.index_add_(0, seg, src)

def _pad(x, pw):
    flat = tuple((v for pair in reversed(pw) for v in pair))
    return torch.nn.functional.pad(x, flat)
kernel_name = 'concat_heads__spectral_gfnet__memory_aug_recurrent_mem_tr__prompt'

def setup_inputs():

    _w = lambda a: (lambda t: t.to(torch.bfloat16) if t.is_floating_point() else t)(torch.as_tensor(a))

    def _A_setup():
        B, S, D = (1, 2048, 448)
        Df = D // 2 + 1
        return {'x': _w((lambda *s: torch.randn(*s, dtype=torch.bfloat16) * 0.05)(B, S, D)), 'w_r': _w((lambda *s: torch.randn(*s, dtype=torch.bfloat16) * 3.2)(S, Df) * 0.02), 'w_i': _w((lambda *s: torch.randn(*s, dtype=torch.bfloat16) * 3.2)(S, Df) * 0.02)}
    _A_in = _A_setup()

    def _B_setup():
        B, S, Dm = (1, 2048, 448)
        H, Dh = (16, 256)
        M, seg = (8, 32)
        seg = min(seg, S)
        S = S // seg * seg
        return {'x': _w((lambda *s: torch.randn(*s, dtype=torch.bfloat16) * 0.05)(B, S, Dm)), 'mem0': _w((lambda *s: torch.randn(*s, dtype=torch.bfloat16) * 0.4)(B, M, Dm)), 'Wq': _w((lambda *s: torch.randn(*s, dtype=torch.bfloat16) * 6.4)(Dm, H, Dh) * Dm ** (-0.5)), 'Wk': _w((lambda *s: torch.randn(*s, dtype=torch.bfloat16) * 6.4)(Dm, H, Dh) * Dm ** (-0.5)), 'Wv': _w((lambda *s: torch.randn(*s, dtype=torch.bfloat16) * 0.4)(Dm, H, Dh) * Dm ** (-0.5)), 'Wo': _w((lambda *s: torch.randn(*s, dtype=torch.bfloat16) * 0.4)(H, Dh, Dm) * (H * Dh) ** (-0.5)), 'seg_len': seg}
    _B_in = _B_setup()
    return {'A_x': _A_in['x'], 'A_w_r': _A_in['w_r'], 'A_w_i': _A_in['w_i'], 'B_x': _B_in['x'], 'B_mem0': _B_in['mem0'], 'B_Wq': _B_in['Wq'], 'B_Wk': _B_in['Wk'], 'B_Wv': _B_in['Wv'], 'B_Wo': _B_in['Wo'], 'B_seg_len': _B_in['seg_len']}

def reference(A_x, A_w_r, A_w_i, B_x, B_mem0, B_Wq, B_Wk, B_Wv, B_Wo, B_seg_len):

    def _A_ref(x, w_r, w_i):
        B, S, D = x.shape
        X = (lambda x, axes=(-2, -1): torch.fft.rfft2(x.float(), dim=axes))(x, axes=(-2, -1))
        W = w_r + 1j * w_i
        X = X * W
        y = (lambda x, s, axes=(-2, -1): torch.fft.irfft2(x, s=s, dim=axes).to(torch.bfloat16))(X, (S, D), axes=(-2, -1))
        return y

    def _B_ref(x, mem0, Wq, Wk, Wv, Wo, seg_len):
        B, S, Dm = x.shape
        H, Dh = (Wq.shape[1], Wq.shape[2])
        M = mem0.shape[1]
        scale = Dh ** (-0.5)
        n_seg = S // seg_len
        L = M + seg_len
        idx = torch.arange(L)
        mask = (idx[:, None] < M) | (idx[:, None] >= idx[None, :])
        mem = mem0
        outs = []
        for s in range(n_seg):
            xs = x[:, s * seg_len:(s + 1) * seg_len, :].clone()
            h = torch.concatenate([mem, xs], axis=1)
            q = torch.einsum('btd,dhe->bhte', h, Wq)
            k = torch.einsum('btd,dhe->bhte', h, Wk)
            v = torch.einsum('btd,dhe->bhte', h, Wv)
            scores = torch.einsum('bhqe,bhke->bhqk', q, k) * scale
            scores = torch.where(mask[None, None], scores, -1000000000.0)
            p = _softmax(scores, axis=-1)
            o = torch.einsum('bhqk,bhke->bhqe', p, v)
            o = torch.einsum('bhte,hed->btd', o, Wo)
            mem = o[:, :M, :].clone()
            outs.append(o[:, M:, :])
        return torch.concatenate(outs, axis=1)
    a = _A_ref(A_x, A_w_r, A_w_i)
    b = _B_ref(B_x, B_mem0, B_Wq, B_Wk, B_Wv, B_Wo, B_seg_len)
    return torch.concatenate([a, b], axis=-1)
```

```cpp
#include <hip/hip_runtime.h>
#include <hip/hip_bf16.h>
#include <stdint.h>

typedef unsigned v4u __attribute__((ext_vector_type(4)));
typedef unsigned v2u __attribute__((ext_vector_type(2)));
typedef float v4f __attribute__((ext_vector_type(4)));
typedef float v2f __attribute__((ext_vector_type(2)));
typedef short v4s __attribute__((ext_vector_type(4)));
typedef __bf16 v2bf __attribute__((ext_vector_type(2)));
typedef __bf16 v8bf __attribute__((ext_vector_type(8)));
typedef __bf16 v16bf __attribute__((ext_vector_type(16)));
typedef unsigned v8u __attribute__((ext_vector_type(8)));
typedef __amdgpu_buffer_rsrc_t rsrc_t;
#define LAS __attribute__((address_space(3)))
#define DEV __device__ __forceinline__

constexpr int NSEG = 64, NH = 16, DM = 448;
constexpr unsigned SENT = 0xFFFFFFFFu;
constexpr int SC1 = 16;
constexpr int OFF_HO = 0;
constexpr int OFF_CTL = 4 << 20;
constexpr int OFF_AP = OFF_CTL + 65536;
constexpr int OFF_BT = OFF_AP + NH * DM * DM * 2;
constexpr int OFF_G = OFF_BT + NH * DM * DM * 2;
constexpr int OFF_U = OFF_G + NSEG * NH * 28672;
constexpr int OFF_T = OFF_U + NSEG * NH * 28672;
constexpr int OFF_V = OFF_T + NSEG * NH * 7168;
constexpr int OFF_PX = OFF_V + NSEG * NH * 7168;
constexpr int OFF_PM = OFF_PX + NSEG * NH * 2048;
constexpr int OFF_FD = OFF_PM + NSEG * NH * 512;
constexpr int OFF_FI = OFF_FD + 512 * DM * 2;
constexpr int OFF_Z1 = OFF_FI + 512 * DM * 2;
constexpr int OFF_Z2 = OFF_Z1 + (2 << 20);
constexpr int OFF_DBG = OFF_Z2 + (2 << 20);
constexpr int WS_BYTES = OFF_DBG + (1 << 20);
constexpr int HO_MEM = 0, HO_PART = 16384, HO_E = 32768, HO_ST = 49152, HO_PROBE = 53248;
constexpr int FL_W = 0, FL_T = 112, FL_V = 176, FL_P = 208, FL_CAL = 336, FL_TAB = 337, FL_FA = 338, FL_FB = 339, FL_ABORT = 340;
constexpr int LDS_MAIN = 114688 + 2048;
constexpr int LDS_PRO = 57344;
constexpr int SPIN_CAP = 1 << 17;

struct Params {
    const unsigned short* xa; const unsigned short* wr; const unsigned short* wi;
    const unsigned short* xb; const unsigned short* mem0;
    const unsigned short* wq; const unsigned short* wk; const unsigned short* wv; const unsigned short* wo;
    unsigned short* out; char* ws;
};

DEV unsigned pk2(float a, float b) { v2bf r = {(__bf16)a, (__bf16)b}; return __builtin_bit_cast(unsigned, r); }
DEV v4u pk8(v4f a, v4f b) { return (v4u){pk2(a.x, a.y), pk2(a.z, a.w), pk2(b.x, b.y), pk2(b.z, b.w)}; }
DEV v4f mfma32(v4u a, v4u b, v4f c) { return __builtin_amdgcn_mfma_f32_16x16x32_bf16(__builtin_bit_cast(v8bf, a), __builtin_bit_cast(v8bf, b), c, 0, 0, 0); }
DEV v4f mfma16(v2u a, v2u b, v4f c) { return __builtin_amdgcn_mfma_f32_16x16x16bf16_1k(__builtin_bit_cast(v4s, a), __builtin_bit_cast(v4s, b), c, 0, 0, 0); }
DEV v4f smf(v4u a, v4u b0, v4u b1, v4f c, int idx) {
    v8u b = {b0.x, b0.y, b0.z, b0.w, b1.x, b1.y, b1.z, b1.w};
    return __builtin_amdgcn_smfmac_f32_16x16x64_bf16(__builtin_bit_cast(v8bf, a), __builtin_bit_cast(v16bf, b), c, idx, 0, 0);
}
DEV float fmx(float a, float b) { return __builtin_elementwise_maximum(a, b); }
DEV float max_g(float s) {
    auto r = __builtin_amdgcn_permlane16_swap(__float_as_uint(s), __float_as_uint(s), false, false);
    s = fmx(__uint_as_float(r[0]), __uint_as_float(r[1]));
    auto q = __builtin_amdgcn_permlane32_swap(__float_as_uint(s), __float_as_uint(s), false, false);
    return fmx(__uint_as_float(q[0]), __uint_as_float(q[1]));
}
DEV float sum_g(float s) {
    auto r = __builtin_amdgcn_permlane16_swap(__float_as_uint(s), __float_as_uint(s), false, false);
    s = __uint_as_float(r[0]) + __uint_as_float(r[1]);
    auto q = __builtin_amdgcn_permlane32_swap(__float_as_uint(s), __float_as_uint(s), false, false);
    return __uint_as_float(q[0]) + __uint_as_float(q[1]);
}
DEV float ror8(float s) { return __int_as_float(__builtin_amdgcn_update_dpp(0, __float_as_int(s), 0x128, 0xf, 0xf, false)); }
DEV unsigned umax4(v4u v) { return max(max(v.x, v.y), max(v.z, v.w)); }
DEV rsrc_t mkrsrc(const void* p, unsigned bytes) { return __builtin_amdgcn_make_buffer_rsrc((void*)p, 0, bytes, 0x00020000); }
DEV v4u ld16(rsrc_t r, int vo, int so, int aux) { return aux ? __builtin_amdgcn_raw_buffer_load_b128(r, vo, so, SC1) : __builtin_amdgcn_raw_buffer_load_b128(r, vo, so, 0); }
DEV v2u ld8s(rsrc_t r, int vo, int so) { return __builtin_amdgcn_raw_buffer_load_b64(r, vo, so, SC1); }
DEV unsigned ld4s(rsrc_t r, int vo, int so) { return __builtin_amdgcn_raw_buffer_load_b32(r, vo, so, SC1); }
DEV void st16s(rsrc_t r, int vo, int so, v4u v) { __builtin_amdgcn_raw_buffer_store_b128(v, r, vo, so, SC1); }
DEV void st8s(rsrc_t r, int vo, int so, v2u v) { __builtin_amdgcn_raw_buffer_store_b64(v, r, vo, so, SC1); }
DEV void st4s(rsrc_t r, int vo, int so, unsigned v) { __builtin_amdgcn_raw_buffer_store_b32(v, r, vo, so, SC1); }
DEV void dma16(rsrc_t r, int vo, int so, int ldsaddr) {
    unsigned keep;
    asm volatile("s_mov_b32 %0, m0\n\ts_mov_b32 m0, %4\n\ts_nop 4\n\tbuffer_load_dwordx4 %1, %2, %3 offen lds\n\ts_mov_b32 m0, %0"
                 : "=&s"(keep) : "v"(vo), "s"(r), "s"(so), "s"(ldsaddr) : "memory");
}
#define BAR() asm volatile("s_waitcnt lgkmcnt(0)\n\ts_barrier" ::: "memory")
#define VMWAIT(n) asm volatile("s_waitcnt vmcnt(" #n ")" ::: "memory")
#define FENCE() asm volatile("" ::: "memory")
DEV v4u lds16(const char* l, int a) { return *(const v4u*)(l + a); }
DEV v2u lds8(const char* l, int a) { return *(const v2u*)(l + a); }
DEV v2u ldstr(const char* l, int a) { v4s r = __builtin_amdgcn_ds_read_tr16_b64_v4i16((LAS v4s*)(l + a)); return __builtin_bit_cast(v2u, r); }
template <int N> DEV void slp() { if constexpr (N > 0) __builtin_amdgcn_s_sleep(N); }
DEV int flagoff(int i) { return OFF_CTL + i * 128; }
template <int SL> DEV bool wait_flags(rsrc_t rs, int vo, unsigned need, unsigned& seen) {
    for (int sp = 0;; ++sp) {
        FENCE();
        seen = ld4s(rs, vo, 0);
        if (__builtin_amdgcn_ballot_w64(seen < need) == 0) return true;
        slp<SL>();
        if ((sp & 255) == 255) {
            if (sp > SPIN_CAP) st4s(rs, flagoff(FL_ABORT), 0, 1u);
            if (sp > SPIN_CAP || __builtin_amdgcn_readfirstlane(ld4s(rs, flagoff(FL_ABORT), 0))) return false;
        }
    }
}
DEV int img(int p) { return ((p & 7) >> 1) * 4 + (p >> 3) * 2 + (p & 1); }
DEV int pperm(int rho, int tau) { return (rho >> 2) * 8 + tau * 4 + (rho & 3); }

#define STAMP(cond, slot, s)

__global__ void __launch_bounds__(512, 2) k_pro(Params p) {
    extern __shared__ __attribute__((aligned(16))) char lds[];
    const int b = blockIdx.x, tid = threadIdx.x, lane = tid & 63, wave = __builtin_amdgcn_readfirstlane(tid >> 6);
    const int rho = lane & 15, g = lane >> 4;
    const int gemm = (b & 7) * 4 + (b >> 6), sub = (b >> 3) & 7, h = gemm >> 1, which = gemm & 1;
    const int b0 = 224 * (sub & 1), c0 = 112 * (sub >> 1);
    const rsrc_t rs = mkrsrc(p.ws, WS_BYTES);
    if (which == 0) {
        const rsrc_t rq = mkrsrc(p.wq, DM * 4096 * 2);
        for (int k = wave; k < 56; k += 8) dma16(rq, ((c0 + (k >> 3) * 16 + rho) * 4096 + h * 256 + (k & 7) * 32 + g * 8) * 2, 0, k * 1024);
    } else {
        const rsrc_t ro = mkrsrc(p.wo, DM * 4096 * 2);
        for (int k = wave; k < 56; k += 8) { const int ch = k * 64 + lane, e = ch / 14, cc = ch - e * 14; dma16(ro, ((h * 256 + e) * DM + c0 + cc * 8) * 2, 0, k * 1024); }
    }
    v4u a[2][8];
    if (wave < 7) {
        const rsrc_t ra = mkrsrc(which ? p.wv : p.wk, DM * 4096 * 2);
#pragma unroll
        for (int t = 0; t < 2; ++t)
#pragma unroll
            for (int ks = 0; ks < 8; ++ks) a[t][ks] = ld16(ra, ((b0 + 32 * wave + pperm(rho, t)) * 4096 + h * 256 + g * 8) * 2, ks * 64, 0);
    } else {
        const v4u sent = {SENT, SENT, SENT, SENT};
        for (int k = 0; k < 16; ++k) {
            v4u v = sent; const int c = (k & 7) * 64 + lane;
            if (b == 0 && c < 448) { const unsigned* m = (const unsigned*)(p.mem0 + ((c >> 1) & 7) * DM + (c >> 4) * 16 + (c & 1) * 2); v = (v4u){m[0], m[2], m[4], m[6]}; }
            st16s(rs, OFF_HO + b * 16384 + k * 1024 + lane * 16, 0, v);
        }
        if (lane < 16) st16s(rs, OFF_CTL + b * 256 + lane * 16, 0, (v4u){0u, 0u, 0u, 0u});
    }
    VMWAIT(0);
    BAR();
    if (wave >= 7) return;
    const float sc = which ? 1.f : 0.09016844005556021f;
    const int obase = (which ? OFF_BT : OFF_AP) + (h * DM * DM + b0 + 32 * wave + 8 * g) * 2;
    const int tra = (8 * g + (rho >> 2)) * 224 + (rho & 3) * 8;
#pragma unroll 1
    for (int ct = 0; ct < 7; ++ct) {
        v4f c0v = {0, 0, 0, 0}, c1v = {0, 0, 0, 0};
#pragma unroll
        for (int ks = 0; ks < 8; ++ks) {
            v4u bf;
            if (which == 0) bf = lds16(lds, (ct * 8 + ks) * 1024 + lane * 16);
            else { const v2u x = ldstr(lds, tra + ks * 7168 + ct * 32), y = ldstr(lds, tra + ks * 7168 + 896 + ct * 32); bf = (v4u){x.x, x.y, y.x, y.y}; }
            c0v = mfma32(a[0][ks], bf, c0v); c1v = mfma32(a[1][ks], bf, c1v);
        }
        st16s(rs, obase + (c0 + ct * 16 + rho) * DM * 2, 0, pk8(c0v * sc, c1v * sc));
    }
}

struct Ctx { rsrc_t rs; char* lds; int lane, wave, rho, g; };

DEV int slot_base(int s, int cal) { return OFF_HO + s * 65536 + (((cal >> (s >> 5)) & 1) << 13); }
DEV bool poll_piece(rsrc_t rs, int vo, int so, v4u& v) {
    for (int sp = 0;; ++sp) {
        FENCE();
        v = ld16(rs, vo, so, 1);
        if (__builtin_amdgcn_ballot_w64(umax4(v) == SENT) == 0) return true;
        if (sp > SPIN_CAP) { st4s(rs, flagoff(FL_ABORT), 0, 1u); return false; }
    }
}
DEV int get_cal(const Params& p, const Ctx& c, bool measure) {
    if (measure && c.wave == 7) {
        unsigned best[4];
#pragma unroll
        for (int k = 0; k < 4; ++k) {
            best[k] = 0xffffffffu;
            for (int r = 0; r < 6; ++r) {
                const int off = OFF_HO + (k >> 1) * (2 << 20) + (k & 1) * 8192 + HO_PROBE + r * 256;
                unsigned long long t0, t1; unsigned v;
                asm volatile("s_memtime %0\n\ts_waitcnt lgkmcnt(0)\n\tbuffer_load_dword %2, %3, %4, 0 offen sc1\n\ts_waitcnt vmcnt(0)\n\ts_memtime %1\n\ts_waitcnt lgkmcnt(0)"
                             : "=&s"(t0), "=&s"(t1), "=&v"(v) : "v"(off), "s"(c.rs) : "memory");
                best[k] = min(best[k], (unsigned)(t1 - t0));
            }
        }
        const unsigned cal = 4u | (best[1] < best[0] ? 1u : 0u) | (best[3] < best[2] ? 2u : 0u);
        if (c.lane == 0) st4s(c.rs, flagoff(FL_CAL), 0, cal);
    }
    unsigned seen;
    if (!wait_flags<2>(c.rs, flagoff(FL_CAL), 1u, seen)) return -1;
    return __builtin_amdgcn_readfirstlane(seen) & 3;
}

#define DLY_P 16
#define DLY_OM 9
#define DLY_OS 0

DEV void role_p(const Params& p, const Ctx& c, int idx) {
    const int h = idx >> 2, q = idx & 3, lane = c.lane, wave = c.wave, rho = c.rho, g = c.g;
    const rsrc_t rs = c.rs; char* lds = c.lds;
    const int om = q * 4 + wave;
    const bool isT = wave < 4 && om < 14, isX = wave < 4 && om == 15;
    constexpr int L_MEM = 0, L_PART = 8192;
    v4u w[2][14];
    if (isT) {
#pragma unroll
        for (int t = 0; t < 2; ++t)
#pragma unroll
            for (int ks = 0; ks < 14; ++ks) w[t][ks] = ld16(rs, OFF_AP + ((h * DM + (2 * om + t) * 16 + img(rho)) * DM + g * 8) * 2, ks * 64, 0);
    }
    if (wave == 0) { *(v4u*)(lds + L_PART + lane * 16) = (v4u){0u, 0u, 0u, 0u}; *(v4u*)(lds + L_PART + 1024 + lane * 16) = (v4u){0u, 0u, 0u, 0u}; }
    const int cal = get_cal(p, c, idx == 0);
    if (cal < 0) return;
    const int sidx = (rho & 2) ? 0xEEEE : 0x4444;
    const int fa = g * 256 + ((rho >> 2) * 2 + (rho & 1)) * 32 + ((rho >> 1) & 1) * 16;
    const int qa = (2 * om + (rho >> 3)) * 256 + (rho & 7) * 32 + g * 8;
    v2u sel; { const int r0 = rho - 2 * g; sel.x = (r0 == 0 ? 0x3f80u : 0u) | (r0 == 1 ? 0x3f800000u : 0u); sel.y = (r0 == 8 ? 0x3f80u : 0u) | (r0 == 9 ? 0x3f800000u : 0u); }
    const bool dvalid = (g >> 1) == (rho >> 3);
    const int pw = L_PART + (2 * wave + (rho >> 3)) * 256 + (rho & 7) * 32 + (g & 1) * 16;
    const int tw = OFF_T + (h * 14 + om) * 512 + (rho & 7) * 64 + ((rho >> 3) * 8 + (g & 1) * 4 + (g >> 1)) * 4;
    const int gl0 = OFF_G + h * 28672 + pperm(rho, 0) * 64 + g * 16;
    const int gfl = flagoff(FL_W + h * 7 + (lane & 3));
    unsigned known = 0;
    auto load_g = [&](int s) -> bool {
        if (known < (unsigned)s + 1) {
            unsigned seen; if (!wait_flags<1>(rs, gfl, s + 1, seen)) return false;
            known = min(min(__builtin_amdgcn_readlane(seen, 0), __builtin_amdgcn_readlane(seen, 1)), min(__builtin_amdgcn_readlane(seen, 2), __builtin_amdgcn_readlane(seen, 3)));
        }
#pragma unroll
        for (int t = 0; t < 2; ++t)
#pragma unroll
            for (int ks = 0; ks < 14; ++ks) w[t][ks] = ld16(rs, gl0 + t * 256 + ks * 2048, s * (NH * 28672), 1);
        return true;
    };
    bool ok = true;
    if (isX) ok = load_g(0);
#pragma unroll 1
    for (int s = 0; s < NSEG && ok; ++s) {
        const int sb = slot_base(s, cal);
        if (wave < 7) { v4u v; ok = poll_piece(rs, wave * 1024 + lane * 16, sb + HO_MEM, v); *(v4u*)(lds + L_MEM + wave * 1024 + lane * 16) = v; }
        BAR();
        STAMP(idx == 0 && wave == 7, 0, s);
        v2u d = {0u, 0u};
        if (isT || isX) {
            v4f c0 = {0, 0, 0, 0}, c1 = {0, 0, 0, 0};
            v2u mq = lds8(lds, L_MEM + (isT ? qa : 0));
#pragma unroll
            for (int f = 0; f < 7; ++f) {
                const v4u a = lds16(lds, L_MEM + f * 1024 + fa);
                c0 = smf(a, w[0][2 * f], w[0][2 * f + 1], c0, sidx); c1 = smf(a, w[1][2 * f], w[1][2 * f + 1], c1, sidx);
            }
            const v2u tp = {pk2(c0.x + c0.z, c0.y + c0.w), pk2(c1.x + c1.z, c1.y + c1.w)};
            const v4f d2 = mfma16(tp, sel, (v4f){0, 0, 0, 0});
            if (isT) {
                d = (v2u){pk2(d2.x, d2.y), pk2(d2.z, d2.w)};
                const v4f d3 = mfma16(d, mq, (v4f){0, 0, 0, 0});
                if (dvalid) *(v4f*)(lds + pw) = d3;
            } else if (s < NSEG - 1) {
                float m = fmx(fmx(d2.x, d2.y), fmx(d2.z, d2.w));
                m = max_g(fmx(m, ror8(m)));
                const float e0 = __builtin_amdgcn_exp2f(d2.x - m), e1 = __builtin_amdgcn_exp2f(d2.y - m), e2 = __builtin_amdgcn_exp2f(d2.z - m), e3 = __builtin_amdgcn_exp2f(d2.w - m);
                st8s(rs, h * 512 + (rho & 7) * 64 + g * 16 + (rho >> 3) * 8, sb + HO_E, (v2u){pk2(e0, e1), pk2(e2, e3)});
                float l = (e0 + e1) + (e2 + e3);
                l = sum_g(l + ror8(l));
                if (lane < 8) st8s(rs, h * 128 + lane * 8, sb + HO_ST, (v2u){__float_as_uint(m), __float_as_uint(l)});
            }
        }
        BAR();
        if (wave == 7) {
            if (lane < 32 && s < NSEG - 1) {
                v2f x[8];
#pragma unroll
                for (int k = 0; k < 8; ++k) x[k] = *(const v2f*)(lds + L_PART + k * 256 + lane * 8);
                const v2f y = ((x[0] + x[1]) + (x[2] + x[3])) + ((x[4] + x[5]) + (x[6] + x[7]));
                st4s(rs, idx * 128 + lane * 4, sb + HO_PART, pk2(y.x, y.y));
            }
            STAMP(idx == 0, 1, s);
            if (lane == 0 && s > 0) st4s(rs, flagoff(FL_T + idx), 0, s);
        }
        if (isT) { st4s(rs, tw, s * (NH * 7168), d.x); st4s(rs, tw + 8, s * (NH * 7168), d.y); }
        if (isX && s + 1 < NSEG - 1) ok = load_g(s + 1);
        if (s + 1 < NSEG) slp<DLY_P>();
    }
    VMWAIT(0);
    BAR();
    if (wave == 7 && lane == 0 && ok) st4s(rs, flagoff(FL_T + idx), 0, NSEG);
}

DEV void role_o(const Params& p, const Ctx& c, int t) {
    const int lane = c.lane, wave = c.wave, rho = c.rho, g = c.g;
    const rsrc_t rs = c.rs; char* lds = c.lds;
    constexpr int L_MEM = 0, L_OUT = 8192;
    v4u w[2][14];
#pragma unroll
    for (int k = 0; k < 2; ++k)
#pragma unroll
        for (int ks = 0; ks < 14; ++ks) w[k][ks] = ld16(rs, OFF_BT + (((2 * wave + k) * DM + t * 16 + rho) * DM + g * 8) * 2, ks * 64, 0);
    const int cal = get_cal(p, c, false);
    if (cal < 0) return;
    const int sidx = (rho & 2) ? 0xEEEE : 0x4444;
    const int fa = g * 256 + ((rho >> 2) * 2 + (rho & 1)) * 32 + ((rho >> 1) & 1) * 16;
    const int hb = rho >> 3, hd = 2 * wave + hb, i = rho & 7;
    const int pa = HO_PART + hd * 512 + i * 16 + g * 4, sa = HO_ST + hd * 128 + i * 8, ea = HO_E + hd * 512 + i * 64 + g * 16;
    const int ul = OFF_U + (t * 16 + 2 * wave) * 1024 + rho * 64 + g * 16;
    const int ufl = flagoff(FL_W + (2 * wave + (lane & 1)) * 7 + ((14 + (t >> 1)) >> 2));
    const int vw = OFF_V + (t * 8 + wave) * 512 + lane * 8;
    const int p2 = (lane & 4) ? 2 * (lane & 3) + 1 : 2 * (lane & 3);
    const int ra = L_OUT + (lane >> 3) * 64 + p2 * 8;
    unsigned selo = 0x00003f80u, sehi = 0x3f800000u;
    asm volatile("" : "+s"(selo), "+s"(sehi));
    unsigned known = 0; v4u ua, ub;
    auto load_u = [&](int s) -> bool {
        if (known < (unsigned)s + 1) {
            unsigned seen; if (!wait_flags<1>(rs, ufl, s + 1, seen)) return false;
            known = min(__builtin_amdgcn_readlane(seen, 0), __builtin_amdgcn_readlane(seen, 1));
        }
        ua = ld16(rs, ul, s * (NH * 28672), 1); ub = ld16(rs, ul + 1024, s * (NH * 28672), 1);
        return true;
    };
    bool ok = load_u(0);
#pragma unroll 1
    for (int s = 0; ok; ++s) {
        const int sb = slot_base(s, cal);
        if (wave < 7) { v4u v; ok = poll_piece(rs, wave * 1024 + lane * 16, sb + HO_MEM, v); *(v4u*)(lds + L_MEM + wave * 1024 + lane * 16) = v; }
        BAR();
        v4f c0 = {0, 0, 0, 0}, c1 = {0, 0, 0, 0};
#pragma unroll
        for (int f = 0; f < 7; ++f) {
            const v4u a = lds16(lds, L_MEM + f * 1024 + fa);
            c0 = smf(a, w[0][2 * f], w[0][2 * f + 1], c0, sidx); c1 = smf(a, w[1][2 * f], w[1][2 * f + 1], c1, sidx);
        }
        const v2u vp = {pk2(c0.x + c0.z, c0.y + c0.w), pk2(c1.x + c1.z, c1.y + c1.w)};
        if (s == NSEG - 1) { st8s(rs, vw, s * (NH * 7168), vp); break; }
        slp<DLY_OS>();
        unsigned q0, q1, q2, q3; v2u st; v4u e;
        for (int sp = 0;; ++sp) {
            FENCE();
            q0 = ld4s(rs, pa, sb); q1 = ld4s(rs, pa + 128, sb); q2 = ld4s(rs, pa + 256, sb); q3 = ld4s(rs, pa + 384, sb);
            st = ld8s(rs, sa, sb); e = ld16(rs, ea, sb, 1);
            const unsigned m = max(max(max(q0, q1), max(q2, q3)), max(max(st.x, st.y), umax4(e)));
            if (__builtin_amdgcn_ballot_w64(m == SENT) == 0) break;
            if (sp > SPIN_CAP) { st4s(rs, flagoff(FL_ABORT), 0, 1u); ok = false; break; }
        }
        STAMP(t == 0 && wave == 0, 2, s);
        const v4f da = mfma32(ua, e, (v4f){0, 0, 0, 0}), db = mfma32(ub, e, (v4f){0, 0, 0, 0});
        auto dot = [](unsigned x, unsigned sl, float acc) { return __builtin_amdgcn_fdot2_f32_bf16(__builtin_bit_cast(v2bf, x), __builtin_bit_cast(v2bf, sl), acc, false); };
        const float s0 = dot(q3, selo, dot(q2, selo, dot(q1, selo, dot(q0, selo, 0.f))));
        const float s1 = dot(q3, sehi, dot(q2, sehi, dot(q1, sehi, dot(q0, sehi, 0.f))));
        const float mx = __uint_as_float(st.x), lx = __uint_as_float(st.y);
        const float m = max_g(fmx(fmx(s0, s1), mx));
        const float e0 = __builtin_amdgcn_exp2f(s0 - m), e1 = __builtin_amdgcn_exp2f(s1 - m), fx = __builtin_amdgcn_exp2f(mx - m);
        const unsigned pe = pk2(e0, e1);
        const v4f dx = (hb ? db : da) * fx;
        const v4f dd = mfma16(vp, (v2u){hb ? 0u : pe, hb ? pe : 0u}, dx);
        const float rz = __builtin_amdgcn_rcpf(__builtin_fmaf(lx, fx, sum_g(e0 + e1)));
        v4f o = dd * rz;
        { const float x0 = o.x, x1 = o.y, x2 = o.z, x3 = o.w; o = (v4f){x0 + ror8(x0), x1 + ror8(x1), x2 + ror8(x2), x3 + ror8(x3)}; }
        if (hb == 0) *(v4f*)(lds + L_OUT + wave * 512 + i * 64 + g * 16) = o;
        BAR();
        if (wave == 7) {
            v2f x[8];
#pragma unroll
            for (int k = 0; k < 8; ++k) x[k] = *(const v2f*)(lds + ra + k * 512);
            const v2f y = ((x[0] + x[1]) + (x[2] + x[3])) + ((x[4] + x[5]) + (x[6] + x[7]));
            st4s(rs, t * 256 + lane * 4, slot_base(s + 1, cal) + HO_MEM, pk2(y.x, y.y));
            STAMP(t == 0, 3, s);
            if (lane == 0 && s > 0) st4s(rs, flagoff(FL_V + t), 0, s);
        }
        st8s(rs, vw, s * (NH * 7168), vp);
        if (s + 1 < NSEG - 1 && ok) ok = load_u(s + 1);
        slp<DLY_OM>();
    }
    VMWAIT(0);
    BAR();
    if (wave == 7 && lane == 0 && ok) st4s(rs, flagoff(FL_V + t), 0, NSEG);
}

DEV void role_w(const Params& p, const Ctx& c, int wi) {
    const int lane = c.lane, wave = c.wave, rho = c.rho, g = c.g;
    const rsrc_t rs = c.rs; char* lds = c.lds;
    const int h = wi / 7, unit = (wi - h * 7) * 4 + (wave & 3), set = wave >> 2;
    const bool isG = unit < 14;
    const rsrc_t rx = mkrsrc(p.xb, 2048 * DM * 2);
    auto stage = [&](int it) {
        const int sg = 2 * it + set, lb = (set * 2 + (it & 1)) * 28672;
#pragma unroll
        for (int k = 0; k < 7; ++k) { const int pc = (wave & 3) * 7 + k, kt = pc >= 14, ks = pc - kt * 14;
            dma16(rx, ((kt * 16 + (rho ^ (kt * 4))) * DM + ks * 32 + g * 8) * 2, sg * (32 * DM * 2), lb + pc * 1024); }
    };
    stage(0);
    v4u w[2][14];
#pragma unroll
    for (int t = 0; t < 2; ++t) {
        const int row = isG ? unit * 32 + pperm(rho, t) : (unit - 14) * 32 + t * 16 + rho;
#pragma unroll
        for (int ks = 0; ks < 14; ++ks) w[t][ks] = ld16(rs, (isG ? OFF_AP : OFF_BT) + ((h * DM + row) * DM + g * 8) * 2, ks * 64, 0);
    }
    int xa[2];
#pragma unroll
    for (int k = 0; k < 2; ++k) { const int key = isG ? k * 16 + rho : pperm(rho, k), kt = key >> 4; xa[k] = kt * 14336 + (g * 16 + ((key & 15) ^ (kt * 4))) * 16; }
    const int ob = isG ? OFF_G + (h * 14 + unit) * 2048 + rho * 64 + g * 16 : OFF_U + ((unit - 14) * 32 + h) * 1024 + rho * 64 + g * 16;
    const int ostep = isG ? 1024 : 16384;
    VMWAIT(0);
    BAR();
#pragma unroll 1
    for (int it = 0; it < 32; ++it) {
        if (it + 1 < 32) stage(it + 1);
        const char* xb = lds + (set * 2 + (it & 1)) * 28672;
        v4f a00 = {0, 0, 0, 0}, a01 = a00, a10 = a00, a11 = a00;
        if (isG) {
#pragma unroll
            for (int ks = 0; ks < 14; ++ks) { const v4u x0 = lds16(xb, xa[0] + ks * 1024), x1 = lds16(xb, xa[1] + ks * 1024);
                a00 = mfma32(w[0][ks], x0, a00); a01 = mfma32(w[1][ks], x0, a01); a10 = mfma32(w[0][ks], x1, a10); a11 = mfma32(w[1][ks], x1, a11); }
        } else {
#pragma unroll
            for (int ks = 0; ks < 14; ++ks) { const v4u x0 = lds16(xb, xa[0] + ks * 1024), x1 = lds16(xb, xa[1] + ks * 1024);
                a00 = mfma32(x0, w[0][ks], a00); a01 = mfma32(x1, w[0][ks], a01); a10 = mfma32(x0, w[1][ks], a10); a11 = mfma32(x1, w[1][ks], a11); }
        }
        const int so = (2 * it + set) * (NH * 28672);
        st16s(rs, ob, so, pk8(a00, a01)); st16s(rs, ob + ostep, so, pk8(a10, a11));
        const bool eager = it < 2 || it == 31;
        if (eager) VMWAIT(0); else VMWAIT(2);
        BAR();
        if (wave == 0 && lane == 0) st4s(rs, flagoff(FL_W + wi), 0, eager ? 2 * it + 2 : 2 * it);
    }
}

DEV void role_f1(const Params& p, const Ctx& c, int h) {
    const int lane = c.lane, wave = c.wave, rho = c.rho, g = c.g;
    const rsrc_t rs = c.rs;
    const rsrc_t rx = mkrsrc(p.xb, 2048 * DM * 2);
    const int qt = wave & 1, qi = qt * 16 + rho;
    const int gfl = flagoff(FL_W + h * 7 + (lane & 3)), tfl = flagoff(FL_T + h * 4 + (lane & 3));
    const int gl0 = OFF_G + h * 28672 + pperm(rho, 0) * 64 + g * 16;
    const int tl = OFF_T + h * 7168 + ((rho >> 2) * 2 + (rho & 1)) * 64 + g * 16;
    bool wdone = false;
#pragma unroll 1
    for (int s = wave >> 1; s < NSEG; s += 4) {
        v4u x[14];
#pragma unroll
        for (int ks = 0; ks < 14; ++ks) x[ks] = ld16(rx, (qi * DM + g * 8) * 2 + ks * 64, s * (32 * DM * 2), 0);
        unsigned seen;
        if (!wdone) { if (!wait_flags<8>(rs, gfl, s + 1, seen)) return; wdone = __builtin_amdgcn_ballot_w64(seen < NSEG) == 0; }
        v4f a0 = {0, 0, 0, 0}, a1 = a0, am = a0;
#pragma unroll
        for (int hf = 0; hf < 2; ++hf) {
            v4u y[2][7];
#pragma unroll
            for (int k = 0; k < 7; ++k) { y[0][k] = ld16(rs, gl0 + (hf * 7 + k) * 2048, s * (NH * 28672), 1); y[1][k] = ld16(rs, gl0 + 256 + (hf * 7 + k) * 2048, s * (NH * 28672), 1); }
#pragma unroll
            for (int k = 0; k < 7; ++k) { a0 = mfma32(y[0][k], x[hf * 7 + k], a0); a1 = mfma32(y[1][k], x[hf * 7 + k], a1); }
        }
        float v[8] = {a0.x, a0.y, a0.z, a0.w, a1.x, a1.y, a1.z, a1.w};
        float m = -__builtin_inff();
#pragma unroll
        for (int k = 0; k < 8; ++k) { v[k] = (8 * g + k <= qi) ? v[k] : -__builtin_inff(); m = fmx(m, v[k]); }
        if (!wait_flags<2>(rs, tfl, s + 1, seen)) return;
        { v4u y[14];
#pragma unroll
          for (int ks = 0; ks < 14; ++ks) y[ks] = ld16(rs, tl + ks * 512, s * (NH * 7168), 1);
#pragma unroll
          for (int ks = 0; ks < 14; ++ks) am = mfma32(y[ks], x[ks], am); }
        m = max_g(fmx(m, fmx(am.x, am.y)));
        float e0 = __builtin_amdgcn_exp2f(am.x - m), e1 = __builtin_amdgcn_exp2f(am.y - m), z = e0 + e1;
#pragma unroll
        for (int k = 0; k < 8; ++k) { v[k] = __builtin_amdgcn_exp2f(v[k] - m); z += v[k]; }
        const float rz = __builtin_amdgcn_rcpf(sum_g(z));
        st16s(rs, OFF_PX + (h * 2 + qt) * 1024 + lane * 16, s * (NH * 2048), (v4u){pk2(v[0] * rz, v[1] * rz), pk2(v[2] * rz, v[3] * rz), pk2(v[4] * rz, v[5] * rz), pk2(v[6] * rz, v[7] * rz)});
        st4s(rs, OFF_PM + (h * 2 + qt) * 256 + lane * 4, s * (NH * 512), pk2(e0 * rz, e1 * rz));
        VMWAIT(0);
        if (lane == 0) st4s(rs, flagoff(FL_P + h * 8 + wave), 0, s + 1);
    }
}

DEV void role_f2(const Params& p, const Ctx& c, int t) {
    const int lane = c.lane, wave = c.wave, rho = c.rho, g = c.g;
    const rsrc_t rs = c.rs;
    for (int ch = t * 512 + wave * 64 + lane; ch < 2 * 28672; ch += 28 * 512) {
        float v[8];
        if (ch < 28672) { const int row = ch / 56, d0 = (ch - row * 56) * 8, ri = row >> 8, k = row & 255;
#pragma unroll
            for (int j = 0; j < 8; ++j) { const float ph = (float)((k * (d0 + j)) % 448) * (1.f / 448.f); v[j] = k > 224 ? 0.f : ri ? -__builtin_amdgcn_sinf(ph) : __builtin_amdgcn_cosf(ph); }
        } else { const int c2 = ch - 28672, d = c2 >> 6, ri = (c2 >> 5) & 1, k0 = (c2 & 31) * 8;
#pragma unroll
            for (int j = 0; j < 8; ++j) { const int k = k0 + j; const float ph = (float)((k * d) % 448) * (1.f / 448.f); const bool edge = k == 0 || k == 224;
                const float wgt = (edge ? 1.f : 2.f) / (448.f * 2048.f); v[j] = k > 224 ? 0.f : ri ? (edge ? 0.f : -wgt * __builtin_amdgcn_sinf(ph)) : wgt * __builtin_amdgcn_cosf(ph); }
        }
        st16s(rs, OFF_FD + ch * 16, 0, (v4u){pk2(v[0], v[1]), pk2(v[2], v[3]), pk2(v[4], v[5]), pk2(v[6], v[7])});
    }
    VMWAIT(0);
    BAR();
    if (wave == 0 && lane == 0) __hip_atomic_fetch_add((unsigned*)(p.ws + flagoff(FL_TAB)), 1u, __ATOMIC_RELAXED, __HIP_MEMORY_SCOPE_AGENT);
    const int qt = wave & 1;
    const int ufl = flagoff(FL_W + rho * 7 + ((14 + (t >> 1)) >> 2)), pfl = flagoff(lane < 16 ? FL_P + lane * 8 + wave : FL_V + t);
    const int ul = OFF_U + t * 16384 + rho * 64 + g * 16;
    bool wdone = false;
#pragma unroll 1
    for (int s = wave >> 1; s < NSEG; s += 4) {
        unsigned seen;
        if (!wdone) { if (!wait_flags<8>(rs, ufl, s + 1, seen)) return; wdone = __builtin_amdgcn_ballot_w64(seen < NSEG) == 0; }
        v4u u[16];
#pragma unroll
        for (int h = 0; h < 16; ++h) u[h] = ld16(rs, ul + h * 1024, s * (NH * 28672), 1);
        if (!wait_flags<2>(rs, pfl, s + 1, seen)) return;
        v4u px[16]; unsigned pm[16]; v2u vv[8];
#pragma unroll
        for (int h = 0; h < 16; ++h) { px[h] = ld16(rs, OFF_PX + (h * 2 + qt) * 1024 + lane * 16, s * (NH * 2048), 1); pm[h] = ld4s(rs, OFF_PM + (h * 2 + qt) * 256 + lane * 4, s * (NH * 512)); }
#pragma unroll
        for (int k = 0; k < 8; ++k) vv[k] = ld8s(rs, OFF_V + (t * 8 + k) * 512 + lane * 8, s * (NH * 7168));
        v4f a = {0, 0, 0, 0}, b = a;
#pragma unroll
        for (int k = 0; k < 8; ++k) { a = mfma32(u[2 * k], px[2 * k], a); b = mfma32(u[2 * k + 1], px[2 * k + 1], b); }
        v4f m = {0, 0, 0, 0};
#pragma unroll
        for (int k = 0; k < 8; ++k) m = mfma16(vv[k], (v2u){pm[2 * k], pm[2 * k + 1]}, m);
        a = a + b + m;
        *(v2u*)(p.out + (size_t)(s * 32 + qt * 16 + rho) * 896 + DM + t * 16 + g * 4) = (v2u){pk2(a.x, a.y), pk2(a.z, a.w)};
    }
}

DEV bool fft_sync(const Params& p, const Ctx& c, int fl, bool arrive, unsigned need) {
    VMWAIT(0);
    BAR();
    int* okw = (int*)(c.lds + LDS_MAIN - 16);
    if (c.wave == 0) {
        if (arrive && c.lane == 0) __hip_atomic_fetch_add((unsigned*)(p.ws + flagoff(fl)), 1u, __ATOMIC_RELAXED, __HIP_MEMORY_SCOPE_AGENT);
        unsigned seen; const bool ok = wait_flags<8>(c.rs, flagoff(fl), need, seen);
        __builtin_amdgcn_fence(__ATOMIC_ACQUIRE, "agent");
        VMWAIT(0);
        if (c.lane == 0) *okw = ok;
    }
    BAR();
    return *okw != 0;
}
DEV v4u neg8(v4u v) { return v ^ 0x80008000u; }
DEV void twid(int a, const int (&n)[8], int mask, float inv, v4u& C, v4u& S) {
    float cs[8], sn[8];
#pragma unroll
    for (int j = 0; j < 8; ++j) { const float ph = (float)((a * n[j]) & mask) * inv; cs[j] = __builtin_amdgcn_cosf(ph); sn[j] = __builtin_amdgcn_sinf(ph); }
    C = (v4u){pk2(cs[0], cs[1]), pk2(cs[2], cs[3]), pk2(cs[4], cs[5]), pk2(cs[6], cs[7])};
    S = (v4u){pk2(sn[0], sn[1]), pk2(sn[2], sn[3]), pk2(sn[4], sn[5]), pk2(sn[6], sn[7])};
}
DEV void role_fft(const Params& p, const Ctx& c, int job) {
    const int lane = c.lane, wave = c.wave, rho = c.rho, g = c.g;
    const rsrc_t rs = c.rs; char* lds = c.lds;
    const v4f z4 = {0, 0, 0, 0};
    {
        const rsrc_t rx = mkrsrc(p.xa, 2048 * DM * 2);
        for (int pc = wave; pc < 28; pc += 8) { const int nt = pc >= 14, ks = pc - nt * 14; dma16(rx, ((64 * (nt * 16 + rho) + job) * DM + ks * 32 + g * 8) * 2, 0, pc * 1024); }
        if (!fft_sync(p, c, FL_TAB, false, 28)) return;
        v4u C[2], S[2];
        { int n[8];
#pragma unroll
          for (int j = 0; j < 8; ++j) n[j] = 64 * (16 * (j >> 2) + 4 * g + (j & 3)) + job;
          twid(rho, n, 2047, 1.f / 2048.f, C[0], S[0]); twid(16 + rho, n, 2047, 1.f / 2048.f, C[1], S[1]); }
        v4f zr[2][2], zi[2][2];
#pragma unroll
        for (int t = 0; t < 2; ++t) {
            const int fo = OFF_FD + ((wave * 32 + pperm(rho, t)) * DM + g * 8) * 2;
            v4f r0 = z4, r1 = z4, i0 = z4, i1 = z4;
#pragma unroll
            for (int hf = 0; hf < 2; ++hf) {
                v4u fr[7], fi[7];
#pragma unroll
                for (int k = 0; k < 7; ++k) { fr[k] = ld16(rs, fo + (hf * 7 + k) * 64, 0, 0); fi[k] = ld16(rs, fo + (hf * 7 + k) * 64, 256 * DM * 2, 0); }
#pragma unroll
                for (int k = 0; k < 7; ++k) { const v4u x0 = lds16(lds, (hf * 7 + k) * 1024 + lane * 16), x1 = lds16(lds, (14 + hf * 7 + k) * 1024 + lane * 16);
                    r0 = mfma32(x0, fr[k], r0); r1 = mfma32(x1, fr[k], r1); i0 = mfma32(x0, fi[k], i0); i1 = mfma32(x1, fi[k], i1); }
            }
            const v4u yr = pk8(r0, r1), yi = pk8(i0, i1), nr = neg8(yr);
#pragma unroll
            for (int ct = 0; ct < 2; ++ct) { zr[ct][t] = mfma32(yi, S[ct], mfma32(yr, C[ct], z4)); zi[ct][t] = mfma32(nr, S[ct], mfma32(yi, C[ct], z4)); }
        }
#pragma unroll
        for (int ct = 0; ct < 2; ++ct) { const int o = OFF_Z1 + ((ct * 16 + rho) * 64 + job) * 1024 + wave * 64 + g * 16;
            st16s(rs, o, 0, pk8(zr[ct][0], zr[ct][1])); st16s(rs, o + 512, 0, pk8(zi[ct][0], zi[ct][1])); }
    }
    if (!fft_sync(p, c, FL_FA, true, 64)) return;
    if (wave < 4) {
        const int k1 = job >> 1, P = (job & 1) * 4 + wave;
        char* L = lds + wave * 8192;
#pragma unroll
        for (int pc = 0; pc < 8; ++pc) dma16(rs, OFF_Z1 + (k1 * 64 + (pc & 3) * 16 + (lane >> 2)) * 1024 + (pc >> 2) * 512 + P * 64 + (lane & 3) * 16, 0, wave * 8192 + pc * 1024);
        v4u C[4][2], S[4][2];
#pragma unroll
        for (int ks = 0; ks < 2; ++ks) { int n[8];
#pragma unroll
            for (int j = 0; j < 8; ++j) n[j] = 32 * ks + 16 * (j >> 2) + 4 * g + (j & 3);
#pragma unroll
            for (int kt = 0; kt < 4; ++kt) twid(16 * kt + rho, n, 63, 1.f / 64.f, C[kt][ks], S[kt][ks]); }
        VMWAIT(0);
        v2u hr[4], hi[4];
#pragma unroll
        for (int t = 0; t < 2; ++t) {
            const int k = P * 32 + pperm(rho, t);
            v4u xr[2], xi[2];
#pragma unroll
            for (int ks = 0; ks < 2; ++ks) { const int a = (32 * ks + 4 * g + (rho >> 2)) * 64 + (rho & 3) * 16 + t * 8;
                const v2u a0 = ldstr(L, a), a1 = ldstr(L, a + 1024), b0 = ldstr(L, a + 4096), b1 = ldstr(L, a + 5120);
                xr[ks] = (v4u){a0.x, a0.y, a1.x, a1.y}; xi[ks] = (v4u){b0.x, b0.y, b1.x, b1.y}; }
            const v4u n0 = neg8(xr[0]), n1 = neg8(xr[1]);
            v4f vr[4], vi[4];
#pragma unroll
            for (int kt = 0; kt < 4; ++kt) {
                const v4f ar = mfma32(S[kt][1], xi[1], mfma32(S[kt][0], xi[0], mfma32(C[kt][1], xr[1], mfma32(C[kt][0], xr[0], z4))));
                const v4f ai = mfma32(S[kt][1], n1, mfma32(S[kt][0], n0, mfma32(C[kt][1], xi[1], mfma32(C[kt][0], xi[0], z4))));
                float wr[4], wi[4];
#pragma unroll
                for (int r = 0; r < 4; ++r) { const int o = (k1 + 32 * (16 * kt + 4 * g + r)) * 225 + min(k, 224);
                    wr[r] = __uint_as_float((unsigned)p.wr[o] << 16); wi[r] = __uint_as_float((unsigned)p.wi[o] << 16); }
                vr[kt] = (v4f){ar.x * wr[0] - ai.x * wi[0], ar.y * wr[1] - ai.y * wi[1], ar.z * wr[2] - ai.z * wi[2], ar.w * wr[3] - ai.w * wi[3]};
                vi[kt] = (v4f){ar.x * wi[0] + ai.x * wr[0], ar.y * wi[1] + ai.y * wr[1], ar.z * wi[2] + ai.z * wr[2], ar.w * wi[3] + ai.w * wr[3]};
            }
            const v4u pr0 = pk8(vr[0], vr[1]), pr1 = pk8(vr[2], vr[3]), pi0 = pk8(vi[0], vi[1]), pi1 = pk8(vi[2], vi[3]), ni0 = neg8(pi0), ni1 = neg8(pi1);
#pragma unroll
            for (int nt = 0; nt < 4; ++nt) {
                const v4f rr = mfma32(ni1, S[nt][1], mfma32(ni0, S[nt][0], mfma32(pr1, C[nt][1], mfma32(pr0, C[nt][0], z4))));
                const v4f ri = mfma32(pr1, S[nt][1], mfma32(pr0, S[nt][0], mfma32(pi1, C[nt][1], mfma32(pi0, C[nt][0], z4))));
                const v2u qr = {pk2(rr.x, rr.y), pk2(rr.z, rr.w)}, qi = {pk2(ri.x, ri.y), pk2(ri.z, ri.w)};
                if (t == 0) { hr[nt] = qr; hi[nt] = qi; }
                else { const int o = OFF_Z2 + ((nt * 16 + rho) * 32 + k1) * 1024 + P * 64 + g * 16;
                    st16s(rs, o, 0, (v4u){hr[nt].x, hr[nt].y, qr.x, qr.y}); st16s(rs, o + 512, 0, (v4u){hi[nt].x, hi[nt].y, qi.x, qi.y}); }
            }
        }
    }
    if (!fft_sync(p, c, FL_FB, true, 64)) return;
    {
        char* L = lds + wave * 4096;
#pragma unroll
        for (int pc = 0; pc < 4; ++pc) dma16(rs, OFF_Z2 + (job * 32 + (pc & 1) * 16 + (lane >> 2)) * 1024 + (pc >> 1) * 512 + wave * 64 + (lane & 3) * 16, 0, wave * 4096 + pc * 1024);
        v4u C[2], S[2];
        { int n[8];
#pragma unroll
          for (int j = 0; j < 8; ++j) n[j] = 8 * g + j;
          twid(64 * rho + job, n, 2047, 1.f / 2048.f, C[0], S[0]); twid(64 * (16 + rho) + job, n, 2047, 1.f / 2048.f, C[1], S[1]); }
        VMWAIT(0);
        v4f rr[2][2], ri[2][2];
#pragma unroll
        for (int t = 0; t < 2; ++t) {
            const int a = (8 * g + (rho >> 2)) * 64 + (rho & 3) * 16 + t * 8;
            const v2u a0 = ldstr(L, a), a1 = ldstr(L, a + 256), b0 = ldstr(L, a + 2048), b1 = ldstr(L, a + 2304);
            const v4u xr = {a0.x, a0.y, a1.x, a1.y}, xi = {b0.x, b0.y, b1.x, b1.y}, ni = neg8(xi);
#pragma unroll
            for (int nt = 0; nt < 2; ++nt) { rr[nt][t] = mfma32(ni, S[nt], mfma32(xr, C[nt], z4)); ri[nt][t] = mfma32(xr, S[nt], mfma32(xi, C[nt], z4)); }
        }
        char* R = lds + 32768;
#pragma unroll
        for (int nt = 0; nt < 2; ++nt) { *(v4u*)(R + (nt * 16 + wave) * 1024 + lane * 16) = pk8(rr[nt][0], rr[nt][1]); *(v4u*)(R + (nt * 16 + 8 + wave) * 1024 + lane * 16) = pk8(ri[nt][0], ri[nt][1]); }
        BAR();
#pragma unroll 1
        for (int Q = wave; Q < 14; Q += 8) {
            v4f y[2][2] = {{z4, z4}, {z4, z4}};
#pragma unroll
            for (int hf = 0; hf < 2; ++hf) {
                v4u f0[8], f1[8];
#pragma unroll
                for (int k = 0; k < 8; ++k) { f0[k] = ld16(rs, OFF_FI + (Q * 32 + pperm(rho, 0)) * 1024 + g * 16, (hf * 8 + k) * 64, 0); f1[k] = ld16(rs, OFF_FI + (Q * 32 + pperm(rho, 1)) * 1024 + g * 16, (hf * 8 + k) * 64, 0); }
#pragma unroll
                for (int k = 0; k < 8; ++k) { const v4u b0 = lds16(R, (hf * 8 + k) * 1024 + lane * 16), b1 = lds16(R, (16 + hf * 8 + k) * 1024 + lane * 16);
                    y[0][0] = mfma32(f0[k], b0, y[0][0]); y[0][1] = mfma32(f1[k], b0, y[0][1]); y[1][0] = mfma32(f0[k], b1, y[1][0]); y[1][1] = mfma32(f1[k], b1, y[1][1]); }
            }
#pragma unroll
            for (int nt = 0; nt < 2; ++nt) *(v4u*)(p.out + (size_t)(64 * (nt * 16 + rho) + job) * 896 + Q * 32 + g * 8) = pk8(y[nt][0], y[nt][1]);
        }
    }
}

__global__ void __launch_bounds__(512, 2) k_main(Params p) {
    extern __shared__ __attribute__((aligned(16))) char lds[];
    Ctx c; c.rs = mkrsrc(p.ws, WS_BYTES); c.lds = lds; c.lane = threadIdx.x & 63; c.wave = __builtin_amdgcn_readfirstlane(threadIdx.x >> 6); c.rho = c.lane & 15; c.g = c.lane >> 4;
    const int x = blockIdx.x & 7, y = blockIdx.x >> 3;
    if (x < 2) role_p(p, c, x * 32 + y);
    else if (x == 2) { if (y < 28) role_o(p, c, y); }
    else if (x == 3) { if (y < 28) role_f2(p, c, y); }
    else if (x == 4 && y < 16) role_f1(p, c, y);
    else { const int wi = (x - 4) * 32 + y - 16; role_w(p, c, wi); if (wi >= 48) role_fft(p, c, wi - 48); }
}

extern "C" void kernel_launch(void* const* d_in, const int* in_sizes, int n_in, void* d_out, int out_size, void* d_ws, size_t ws_size, hipStream_t stream) {
    Params p;
    p.xa = (const unsigned short*)d_in[0]; p.wr = (const unsigned short*)d_in[1]; p.wi = (const unsigned short*)d_in[2];
    p.xb = (const unsigned short*)d_in[3]; p.mem0 = (const unsigned short*)d_in[4];
    p.wq = (const unsigned short*)d_in[5]; p.wk = (const unsigned short*)d_in[6]; p.wv = (const unsigned short*)d_in[7]; p.wo = (const unsigned short*)d_in[8];
    p.out = (unsigned short*)d_out;
    p.ws = (char*)d_ws + (((size_t)2 << 20) - ((uintptr_t)d_ws & (((size_t)2 << 20) - 1))) % ((size_t)2 << 20);
    (void)hipFuncSetAttribute((const void*)k_main, hipFuncAttributeMaxDynamicSharedMemorySize, LDS_MAIN);
    (void)hipFuncSetAttribute((const void*)k_pro, hipFuncAttributeMaxDynamicSharedMemorySize, LDS_PRO);
    hipLaunchKernelGGL(k_pro, dim3(256), dim3(512), LDS_PRO, stream, p);
    hipLaunchKernelGGL(k_main, dim3(256), dim3(512), LDS_MAIN, stream, p);
}
```

```cpp
#include <hip/hip_runtime.h>
#include <hip/hip_bf16.h>
#include <stdint.h>

typedef unsigned v4u __attribute__((ext_vector_type(4)));
typedef unsigned v2u __attribute__((ext_vector_type(2)));
typedef float v4f __attribute__((ext_vector_type(4)));
typedef float v2f __attribute__((ext_vector_type(2)));
typedef short v4s __attribute__((ext_vector_type(4)));
typedef __bf16 v2bf __attribute__((ext_vector_type(2)));
typedef __bf16 v8bf __attribute__((ext_vector_type(8)));
typedef __bf16 v16bf __attribute__((ext_vector_type(16)));
typedef unsigned v8u __attribute__((ext_vector_type(8)));
typedef __amdgpu_buffer_rsrc_t rsrc_t;
#define LAS __attribute__((address_space(3)))
#define DEV __device__ __forceinline__

constexpr int NSEG = 64, NH = 16, DM = 448;
constexpr unsigned SENT = 0xFFFFFFFFu;
constexpr int OOB = 0x7fffffff;
constexpr int SC1 = 16;
constexpr int OFF_HO = 0;
constexpr int OFF_CTL = 4 << 20;
constexpr int OFF_AP = OFF_CTL + 65536;
constexpr int OFF_BT = OFF_AP + NH * DM * DM * 2;
constexpr int OFF_G = OFF_BT + NH * DM * DM * 2;
constexpr int OFF_U = OFF_G + NSEG * NH * 28672;
constexpr int OFF_T = OFF_U + NSEG * NH * 28672;
constexpr int OFF_V = OFF_T + NSEG * NH * 7168;
constexpr int OFF_PX = OFF_V + NSEG * NH * 7168;
constexpr int OFF_PM = OFF_PX + NSEG * NH * 2048;
constexpr int OFF_FD = OFF_PM + NSEG * NH * 512;
constexpr int OFF_FI = OFF_FD + 512 * DM * 2;
constexpr int OFF_Z1 = OFF_FI + 512 * DM * 2;
constexpr int OFF_Z2 = OFF_Z1 + (2 << 20);
constexpr int OFF_XF = OFF_Z2 + (2 << 20);
constexpr int OFF_DBG = OFF_XF + (2 << 20);
constexpr int WS_BYTES = OFF_DBG + (1 << 20);
constexpr int HO_MEM = 0, HO_PART = 16384, HO_E = 32768;
constexpr int OFF_PROBE = OFF_HO + 49152;
constexpr int FL_W = 0, FL_T = 112, FL_V = 176, FL_P = 208, FL_ROT = 336, FL_TAB = 337, FL_FA = 338, FL_FB = 339, FL_ABORT = 340, FL_CAL = 342, FL_P2 = 344;
constexpr int LDS_MAIN = 114688 + 2048;
constexpr int LDS_PRO = 114688;
constexpr int SPIN_CAP = 1 << 17;

struct Params {
    const unsigned short* xa; const unsigned short* wr; const unsigned short* wi;
    const unsigned short* xb; const unsigned short* mem0;
    const unsigned short* wq; const unsigned short* wk; const unsigned short* wv; const unsigned short* wo;
    unsigned short* out; char* ws;
};

DEV unsigned pk2(float a, float b) { v2bf r = {(__bf16)a, (__bf16)b}; return __builtin_bit_cast(unsigned, r); }
DEV v4u pk8(v4f a, v4f b) { return (v4u){pk2(a.x, a.y), pk2(a.z, a.w), pk2(b.x, b.y), pk2(b.z, b.w)}; }
DEV v4f mfma32(v4u a, v4u b, v4f c) { return __builtin_amdgcn_mfma_f32_16x16x32_bf16(__builtin_bit_cast(v8bf, a), __builtin_bit_cast(v8bf, b), c, 0, 0, 0); }
DEV v4f mfma16(v2u a, v2u b, v4f c) { return __builtin_amdgcn_mfma_f32_16x16x16bf16_1k(__builtin_bit_cast(v4s, a), __builtin_bit_cast(v4s, b), c, 0, 0, 0); }
DEV v4f smf(v4u a, v4u b0, v4u b1, v4f c, int idx) {
    v8u b = {b0.x, b0.y, b0.z, b0.w, b1.x, b1.y, b1.z, b1.w};
    return __builtin_amdgcn_smfmac_f32_16x16x64_bf16(__builtin_bit_cast(v8bf, a), __builtin_bit_cast(v16bf, b), c, idx, 0, 0);
}
DEV float fmx(float a, float b) { return __builtin_elementwise_maximum(a, b); }
DEV float max_g(float s) {
    auto r = __builtin_amdgcn_permlane16_swap(__float_as_uint(s), __float_as_uint(s), false, false);
    s = fmx(__uint_as_float(r[0]), __uint_as_float(r[1]));
    auto q = __builtin_amdgcn_permlane32_swap(__float_as_uint(s), __float_as_uint(s), false, false);
    return fmx(__uint_as_float(q[0]), __uint_as_float(q[1]));
}
DEV float sum_g(float s) {
    auto r = __builtin_amdgcn_permlane16_swap(__float_as_uint(s), __float_as_uint(s), false, false);
    s = __uint_as_float(r[0]) + __uint_as_float(r[1]);
    auto q = __builtin_amdgcn_permlane32_swap(__float_as_uint(s), __float_as_uint(s), false, false);
    return __uint_as_float(q[0]) + __uint_as_float(q[1]);
}
DEV float ror8(float s) { return __int_as_float(__builtin_amdgcn_mov_dpp(__float_as_int(s), 0x128, 0xf, 0xf, true)); }
template <int CTRL> DEV unsigned dppu(unsigned x) { return (unsigned)__builtin_amdgcn_mov_dpp((int)x, CTRL, 0xf, 0xf, true); }
DEV v4u quad4(unsigned x) { return (v4u){x, dppu<0x55>(x), dppu<0xAA>(x), dppu<0xFF>(x)}; }
DEV unsigned umax4(v4u v) { return max(max(v.x, v.y), max(v.z, v.w)); }
DEV rsrc_t mkrsrc(const void* p, unsigned bytes) { return __builtin_amdgcn_make_buffer_rsrc((void*)p, 0, bytes, 0x00020000); }
DEV v4u ld16(rsrc_t r, int vo, int so, int aux) { return aux ? __builtin_amdgcn_raw_buffer_load_b128(r, vo, so, SC1) : __builtin_amdgcn_raw_buffer_load_b128(r, vo, so, 0); }
DEV v2u ld8s(rsrc_t r, int vo, int so) { return __builtin_amdgcn_raw_buffer_load_b64(r, vo, so, SC1); }
DEV unsigned ld4s(rsrc_t r, int vo, int so) { return __builtin_amdgcn_raw_buffer_load_b32(r, vo, so, SC1); }
DEV void st16s(rsrc_t r, int vo, int so, v4u v) {
    __builtin_amdgcn_raw_buffer_store_b128(v, r, vo, so, SC1); asm volatile("s_nop 1" :: "v"(v) : "memory");
}
DEV void st8s(rsrc_t r, int vo, int so, v2u v) { __builtin_amdgcn_raw_buffer_store_b64(v, r, vo, so, SC1); }
DEV void st4s(rsrc_t r, int vo, int so, unsigned v) { __builtin_amdgcn_raw_buffer_store_b32(v, r, vo, so, SC1); }
DEV void dma16(rsrc_t r, int vo, int so, int ldsaddr) {
    unsigned keep;
    asm volatile("s_mov_b32 %0, m0\n\ts_mov_b32 m0, %4\n\ts_nop 4\n\tbuffer_load_dwordx4 %1, %2, %3 offen lds\n\ts_mov_b32 m0, %0"
                 : "=&s"(keep) : "v"(vo), "s"(r), "s"(__builtin_amdgcn_readfirstlane(so)), "s"(__builtin_amdgcn_readfirstlane(ldsaddr)) : "memory");
}
DEV void dma16s(rsrc_t r, int vo, int so, int ldsaddr) {
    unsigned keep;
    asm volatile("s_mov_b32 %0, m0\n\ts_mov_b32 m0, %4\n\ts_nop 4\n\tbuffer_load_dwordx4 %1, %2, %3 offen sc1 lds\n\ts_mov_b32 m0, %0"
                 : "=&s"(keep) : "v"(vo), "s"(r), "s"(__builtin_amdgcn_readfirstlane(so)), "s"(__builtin_amdgcn_readfirstlane(ldsaddr)) : "memory");
}
#define BAR() asm volatile("s_waitcnt lgkmcnt(0)\n\ts_barrier" ::: "memory")
#define BAR0() asm volatile("s_barrier" ::: "memory")
#define VMWAIT(n) asm volatile("s_waitcnt vmcnt(" #n ")" ::: "memory")
#define FENCE() asm volatile("" ::: "memory")
#define SCHED() __builtin_amdgcn_sched_barrier(0)
DEV v4u lds16(const char* l, int a) { return *(const v4u*)(l + a); }
DEV v2u lds8(const char* l, int a) { return *(const v2u*)(l + a); }
DEV v2u ldstr(const char* l, int a) { v4s r = __builtin_amdgcn_ds_read_tr16_b64_v4i16((LAS v4s*)(l + a)); return __builtin_bit_cast(v2u, r); }
template <int N> DEV void slp() { if constexpr (N > 0) __builtin_amdgcn_s_sleep(N); }
DEV int flagoff(int i) { return OFF_CTL + i * 128; }
template <int SL> DEV bool wait_flags(rsrc_t rs, int vo, unsigned need, unsigned& seen) {
    for (int sp = 0;; ++sp) {
        FENCE();
        seen = ld4s(rs, vo, 0);
        if (__builtin_amdgcn_ballot_w64(seen < need) == 0) return true;
        slp<SL>();
        if ((sp & 255) == 255) {
            if (sp > SPIN_CAP) st4s(rs, flagoff(FL_ABORT), 0, 1u);
            if (sp > SPIN_CAP || __builtin_amdgcn_readfirstlane(ld4s(rs, flagoff(FL_ABORT), 0))) return false;
        }
    }
}
DEV int img(int p) { return ((p & 7) >> 1) * 4 + (p >> 3) * 2 + (p & 1); }
DEV int pperm(int rho, int tau) { return (rho >> 2) * 8 + tau * 4 + (rho & 3); }

#define STAMP(cond, slot, s)
#define ACC_DECL
#define ACC_T(k)
#define ACC_OUT(slot)
#define CSTAMP(cond, slot, s)

__global__ void __launch_bounds__(512, 2) k_pro(Params p) {
    extern __shared__ __attribute__((aligned(16))) char lds[];
    const int b = blockIdx.x, tid = threadIdx.x, lane = tid & 63, wave = __builtin_amdgcn_readfirstlane(tid >> 6);
    const int rho = lane & 15, g = lane >> 4;
    const int gemm = (b & 7) * 4 + (b >> 6), sub = (b >> 3) & 7, which = gemm & 1, h = gemm >> 1;
    const int b0 = 224 * (sub & 1), c0 = 112 * (sub >> 1);
    const rsrc_t rs = mkrsrc(p.ws, WS_BYTES);
    STAMP(b == 0 && wave == 0, 4, 8);
    const int lr = lane >> 2, lc = ((lane & 3) ^ ((0 - g) & 3)) * 8, rd = rho * 64 + ((g ^ ((0 - (rho >> 2)) & 3)) * 16);
    if (which == 0) {
        const rsrc_t rq = mkrsrc(p.wq, DM * 4096 * 2);
        for (int k = wave; k < 56; k += 8) dma16(rq, ((c0 + (k >> 3) * 16 + lr) * 4096 + h * 256 + (k & 7) * 32 + lc) * 2, 0, k * 1024);
    } else {
        const rsrc_t ro = mkrsrc(p.wo, DM * 4096 * 2);
        for (int k = wave; k < 56; k += 8) { const int ch = k * 64 + lane, e = ch / 14, cc = ch - e * 14; dma16(ro, ((h * 256 + e) * DM + c0 + cc * 8) * 2, 0, k * 1024); }
    }
    v4u a[2][8];
    char* la = lds + 57344 + wave * 8192;
    if (wave < 7) {
        const rsrc_t ra = mkrsrc(which ? p.wv : p.wk, DM * 4096 * 2);
#pragma unroll
        for (int t = 0; t < 2; ++t) {
            const int src = ((b0 + 32 * wave + pperm(lr, t)) * 4096 + h * 256 + lc) * 2;
#pragma unroll
            for (int ks = 0; ks < 4; ++ks) { dma16(ra, src, ks * 64, 57344 + wave * 8192 + (t * 4 + ks) * 1024); a[t][4 + ks] = ld16(ra, src, 256 + ks * 64, 0); }
        }
        VMWAIT(0);
#pragma unroll
        for (int k = 0; k < 8; ++k) a[k >> 2][k & 3] = lds16(la, k * 1024 + rd);
        asm volatile("s_waitcnt lgkmcnt(0)" : "+v"(a[0][0]), "+v"(a[0][1]), "+v"(a[0][2]), "+v"(a[0][3]), "+v"(a[1][0]), "+v"(a[1][1]), "+v"(a[1][2]), "+v"(a[1][3]) :: "memory");
#pragma unroll
        for (int k = 0; k < 8; ++k) *(v4u*)(la + k * 1024 + lane * 16) = a[k >> 2][4 + (k & 3)];
        FENCE();
#pragma unroll
        for (int k = 0; k < 8; ++k) a[k >> 2][4 + (k & 3)] = lds16(la, k * 1024 + rd);
    } else {
        const rsrc_t rx = mkrsrc(p.xb, 2048 * DM * 2);
        v4u xf[7], m0[7];
#pragma unroll
        for (int k = 0; k < 7; ++k) { const int pc = b * 7 + k, rt = pc / 14, ks = pc - rt * 14; xf[k] = ld16(rx, ((rt * 16 + (rho ^ ((rt & 1) * 4))) * DM + ks * 32 + g * 8) * 2, 0, 0); }
        const v4u sent = {SENT, SENT, SENT, SENT};
        if (b == 0) {
#pragma unroll
            for (int k = 0; k < 7; ++k) { const int c = k * 64 + lane; const unsigned* m = (const unsigned*)(p.mem0 + ((c >> 1) & 7) * DM + (c >> 4) * 16 + (c & 1) * 2); m0[k] = (v4u){m[0], m[2], m[4], m[6]}; }
        } else {
#pragma unroll
            for (int k = 0; k < 7; ++k) m0[k] = sent;
        }
        const unsigned rot = ((unsigned)__builtin_amdgcn_s_getreg(20 | (3 << 11)) - b) & 7u;
        if (lane < 16) st16s(rs, OFF_CTL + b * 256 + lane * 16, 0, (v4u){b * 2 == FL_ROT && lane == 0 ? rot : 0u, 0u, 0u, 0u});
        st4s(rs, b == 3 && lane < 16 ? OFF_PROBE + (lane >> 1 & 1) * (2 << 20) + (lane & 1) * 8192 + (lane >> 2) * 256 : OOB, 0, 0u);
#pragma unroll
        for (int k = 0; k < 16; ++k) st16s(rs, (b & 3) == 3 ? OOB : OFF_HO + b * 16384 + k * 1024 + lane * 16, 0, (k & 7) < 7 ? m0[k & 7] : sent);
#pragma unroll
        for (int k = 0; k < 7; ++k) st16s(rs, OFF_XF + (b * 7 + k) * 1024 + lane * 16, 0, xf[k]);
        VMWAIT(23);
    }
    BAR();
    if (wave >= 7) return;
    STAMP(b == 0 && wave == 0, 4, 9);
    const float sc = which ? 1.f : 0.09016844005556021f;
    const int obase = (which ? OFF_BT : OFF_AP) + ((h * 28 + (c0 >> 4)) * 14 + (b0 >> 5) + wave) * 1024 + lane * 16;
    const int tra = (8 * g + (rho >> 2)) * 224 + (rho & 3) * 8;
    auto tile = [&](const v4u (&bf)[8], int ct) {
        v4f c0v = {0, 0, 0, 0}, c1v = {0, 0, 0, 0};
#pragma unroll
        for (int ks = 0; ks < 8; ++ks) { c0v = mfma32(a[0][ks], bf[ks], c0v); c1v = mfma32(a[1][ks], bf[ks], c1v); }
        st16s(rs, obase, ct * 14336, pk8(c0v * sc, c1v * sc));
    };
    auto fetch = [&](v4u (&bf)[8], int ct) {
        if (which == 0) {
#pragma unroll
            for (int ks = 0; ks < 8; ++ks) bf[ks] = lds16(lds, (ct * 8 + ks) * 1024 + rd);
        } else {
#pragma unroll
            for (int ks = 0; ks < 8; ++ks) { const v2u x = ldstr(lds, tra + ks * 7168 + ct * 32), y = ldstr(lds, tra + ks * 7168 + 896 + ct * 32); bf[ks] = (v4u){x.x, x.y, y.x, y.y}; }
        }
    };
    v4u f0[8], f1[8];
    fetch(f0, 0);
#pragma unroll 1
    for (int ct = 0; ct < 6; ct += 2) { fetch(f1, ct + 1); SCHED(); tile(f0, ct); fetch(f0, ct + 2); SCHED(); tile(f1, ct + 1); }
    tile(f0, 6);
    STAMP(b == 0 && wave == 0, 4, 10);
    STAMP(wave == 0, 8 + (b >> 6), b & 63);
}

struct Ctx { rsrc_t rs; char* lds; int lane, wave, rho, g; };

DEV int slot_base(int s, int cal) { return OFF_HO + s * 65536 + (((cal >> (s >> 5)) & 1) << 13); }
#define POLL_KEY(budget) ((--(budget)) < 0 ? 0xFFFFFFFFu : 0xFFFFFFFEu)
DEV v4u poll_piece(rsrc_t rs, int vo, int so, int& budget) {
    v4u v; unsigned key;
#pragma unroll 1
    do { FENCE(); v = ld16(rs, vo, so, 1); key = POLL_KEY(budget); } while (__builtin_amdgcn_ballot_w64(umax4(v) > key) != 0);
    return v;
}
DEV void give_up(rsrc_t rs) { st4s(rs, flagoff(FL_ABORT), 0, 1u); }
#define PIN2(a, b) asm volatile("" : "+v"(a), "+v"(b))
DEV void role_cal(const Params& p, const Ctx& c) {
    if (c.wave >= 2) return;
    unsigned best[2] = {0xffffffffu, 0xffffffffu};
    for (int r = 0; r < 4; ++r)
#pragma unroll
        for (int k = 0; k < 2; ++k) {
            const int off = OFF_PROBE + c.wave * (2 << 20) + k * 8192 + r * 256;
            unsigned long long t0, t1; unsigned v;
            asm volatile("s_memtime %0\n\ts_waitcnt lgkmcnt(0)\n\tbuffer_load_dword %2, %3, %4, 0 offen sc1\n\ts_waitcnt vmcnt(0)\n\ts_memtime %1\n\ts_waitcnt lgkmcnt(0)"
                         : "=&s"(t0), "=&s"(t1), "=&v"(v) : "v"(off), "s"(c.rs) : "memory");
            best[k] = min(best[k], (unsigned)(t1 - t0));
        }
    if (c.lane == 0) st4s(c.rs, flagoff(FL_CAL + c.wave), 0, best[1] < best[0] ? 3u : 2u);
}
DEV int get_cal(const Ctx& c) {
    unsigned seen;
    if (!wait_flags<2>(c.rs, flagoff(FL_CAL + (c.lane & 1)), 1u, seen)) return -1;
    return (__builtin_amdgcn_readlane(seen, 0) & 1) | ((__builtin_amdgcn_readlane(seen, 1) & 1) << 1);
}
#define WAIT_P() slp<DLY_P>()
#define WAIT_OS() slp<DLY_OS>()
#define WAIT_OM() slp<DLY_OM>()
#define WAIT_V() slp<0>()
#define WAIT_U() slp<0>()
#define WAIT_G() slp<4>()
#define ABL(b) true
#define DLY_P 20
#define DLY_OM 7
#define DLY_OS 2

#define MEM_TIMES_W(lbase)                                                                                           \
    v4u a_[7];                                                                                                       \
    _Pragma("unroll") for (int f = 0; f < 7; ++f) a_[f] = lds16(lds, (lbase) + f * 1024);                            \
    EXTRA_READS;                                                                                                     \
    SCHED();                                                                                                         \
    _Pragma("unroll") for (int f = 0; f < 7; ++f) { c0 = smf(a_[f], w[0][2 * f], w[0][2 * f + 1], c0, sidx); c1 = smf(a_[f], w[1][2 * f], w[1][2 * f + 1], c1, sidx); } \
    const v2u tp = {pk2(c0.x + c0.z, c0.y + c0.w), pk2(c1.x + c1.z, c1.y + c1.w)};

#define VCLOB "v224", "v225", "v226", "v227", "v228", "v229", "v230", "v231", "v232", "v233", "v234", "v235", "v236", "v237", "v238", "v239", \
              "v240", "v241", "v242", "v243", "v244", "v245", "v246", "v247", "v248", "v249", "v250", "v251", "v252", "v253", "v254", "v255"
#define PUBLISH_ASM(ST)                                                                                                                     \
    "s_cmp_lg_u32 %[pub], 7\n\ts_waitcnt lgkmcnt(0)\n\ts_barrier\n\ts_cbranch_scc1 9f\n\t"                                                  \
    "ds_read_b64 v[224:225], %[la]\n\tds_read_b64 v[226:227], %[la] offset:" #ST "*1\n\tds_read_b64 v[228:229], %[la] offset:" #ST "*2\n\t" \
    "ds_read_b64 v[230:231], %[la] offset:" #ST "*3\n\tds_read_b64 v[232:233], %[la] offset:" #ST "*4\n\t"                                  \
    "ds_read_b64 v[234:235], %[la] offset:" #ST "*5\n\tds_read_b64 v[236:237], %[la] offset:" #ST "*6\n\t"                                  \
    "ds_read_b64 v[238:239], %[la] offset:" #ST "*7\n\t"                                                                                    \
    "s_waitcnt lgkmcnt(6)\n\tv_pk_add_f32 v[224:225], v[224:225], v[226:227]\n\t"                                                           \
    "s_waitcnt lgkmcnt(4)\n\tv_pk_add_f32 v[228:229], v[228:229], v[230:231]\n\t"                                                           \
    "s_waitcnt lgkmcnt(2)\n\tv_pk_add_f32 v[232:233], v[232:233], v[234:235]\n\tv_pk_add_f32 v[224:225], v[224:225], v[228:229]\n\t"        \
    "s_waitcnt lgkmcnt(0)\n\tv_pk_add_f32 v[236:237], v[236:237], v[238:239]\n\tv_pk_add_f32 v[224:225], v[224:225], v[232:233]\n\t"        \
    "s_nop 0\n\tv_pk_add_f32 v[224:225], v[224:225], v[236:237]\n\t"                                                                        \
    "s_nop 0\n\tv_cvt_pk_bf16_f32 v224, v224, v225\n\ts_nop 1\n\t"                                                                          \
    "v_mov_b32_dpp v225, v224 quad_perm:[1,1,1,1] row_mask:0xf bank_mask:0xf\n\t"                                                           \
    "v_mov_b32_dpp v226, v224 quad_perm:[2,2,2,2] row_mask:0xf bank_mask:0xf\n\t"                                                           \
    "v_mov_b32_dpp v227, v224 quad_perm:[3,3,3,3] row_mask:0xf bank_mask:0xf\n\t"                                                           \
    "buffer_store_dwordx4 v[224:227], %[vo], %[rs], %[so] offen sc1\n\t"                                                                    \
    "9:\n\ts_barrier\n\ts_nop 0"

DEV void role_p(const Params& p, const Ctx& c, int idx) {
    const int h = idx >> 2, q = idx & 3, lane = c.lane, wave = c.wave, rho = c.rho, g = c.g;
    const rsrc_t rs = c.rs; char* lds = c.lds;
    const int om = q * 4 + wave;
    const bool isT = wave < 4 && om < 14, isX = wave < 4 && om >= 14;
    constexpr int L_MEM = 0, L_PART = 8192, L_DUMP = 10240;
    const int sidx = (rho & 2) ? 0xEEEE : 0x4444;
    const int fa = L_MEM + g * 256 + ((rho >> 2) * 2 + (rho & 1)) * 32 + ((rho >> 1) & 1) * 16;
    v2u sel; { const int r0 = rho - 2 * g; sel.x = (r0 == 0 ? 0x3f80u : 0u) | (r0 == 1 ? 0x3f800000u : 0u); sel.y = (r0 == 8 ? 0x3f80u : 0u) | (r0 == 9 ? 0x3f800000u : 0u); }
    const int piece = q < 3 ? wave : (wave < 2 ? wave : wave - 2);
    const int po = piece * 1024 + lane * 16;
    int budget = SPIN_CAP;
    if (isT) {
        v4u w[2][14];
#pragma unroll
        for (int t = 0; t < 2; ++t)
#pragma unroll
            for (int ks = 0; ks < 14; ++ks) w[t][ks] = ld16(rs, OFF_AP + (h * 28 + 2 * om + t) * 14336 + g * 256 + img(rho) * 16, ks * 1024, 0);
        const int cal = get_cal(c);
        if (cal < 0) return;
        const int qa = L_MEM + (2 * om + (rho >> 3)) * 256 + (rho & 7) * 32 + g * 8;
        const int pw = (g >> 1) == (rho >> 3) ? L_PART + (2 * wave + (rho >> 3)) * 256 + (rho & 7) * 32 + (g & 1) * 16 : L_DUMP + lane * 16;
        const int tw = lane < 32 ? OFF_T + (h * 14 + om) * 512 + ((rho >> 3) * 2 + g) * 128 + (rho & 7) * 16 : OOB;
#pragma unroll 1
        for (int s = 0; s < NSEG && budget >= 0; ++s) {
            const int sb = slot_base(s, cal);
            v4f c0 = {0, 0, 0, 0}, c1 = {0, 0, 0, 0}; PIN2(c0, c1);
            if (s) WAIT_P();
            *(v4u*)(lds + L_MEM + po) = poll_piece(rs, po, sb + HO_MEM, budget);
            BAR();
            v2u mq;
#define EXTRA_READS mq = lds8(lds, qa)
            MEM_TIMES_W(fa)
#undef EXTRA_READS
            const v4f d2 = mfma16(tp, sel, (v4f){0, 0, 0, 0});
            const v2u d = {pk2(d2.x, d2.y), pk2(d2.z, d2.w)};
            *(v4f*)(lds + pw) = mfma16(d, mq, (v4f){0, 0, 0, 0});
            BAR();
            BAR0();
            WAIT_G();
            if (ABL(8)) { auto x = __builtin_amdgcn_permlane32_swap(d.x, d.y, false, false);
              auto y = __builtin_amdgcn_permlane32_swap(x[0], x[0], false, false); auto z = __builtin_amdgcn_permlane32_swap(x[1], x[1], false, false);
              st16s(rs, tw, s * (NH * 7168), (v4u){y[0], z[0], y[1], z[1]}); }
        }
    } else if (isX) {
        v4u w[2][14];
        const int cal = get_cal(c);
        if (cal < 0) return;
        const int gl0 = OFF_G + h * 28672 + (rho >> 3) * 1024 + g * 256 + (pperm(rho, 0) & 15) * 16;
        const int gfl = flagoff(FL_W + h * 7 + (lane & 3));
        const int ew = rho < 8 ? HO_E + h * 512 + g * 128 + rho * 16 : OOB, mw = g == 0 ? L_PART + 1024 + (rho & 7) * 32 + (rho >> 3) * 16 : L_DUMP + lane * 16;
        unsigned known = 0;
        if (om == 15) { BAR0(); BAR0(); BAR0(); }
#pragma unroll 1
        for (int s = om - 14; s < NSEG; s += 2) {
            const int sb = slot_base(s, cal);
            if (s < NSEG - 1 && (s < 2 || ABL(4))) {
                if (s >= 2) WAIT_G();
                if (known < (unsigned)s + 1) {
                    unsigned seen; if (!wait_flags<1>(rs, gfl, s + 1, seen)) break;
                    known = min(min((unsigned)__builtin_amdgcn_readlane(seen, 0), (unsigned)__builtin_amdgcn_readlane(seen, 1)), min((unsigned)__builtin_amdgcn_readlane(seen, 2), (unsigned)__builtin_amdgcn_readlane(seen, 3)));
                }
#pragma unroll
                for (int t = 0; t < 2; ++t)
#pragma unroll
                    for (int ks = 0; ks < 14; ++ks) w[t][ks] = ld16(rs, gl0 + t * 64, s * (NH * 28672) + ks * 2048, 1);
            }
            if (s >= 2) { BAR0(); BAR0(); BAR0(); }
            v4f c0 = {0, 0, 0, 0}, c1 = {0, 0, 0, 0}; PIN2(c0, c1);
            VMWAIT(0);
            BAR();
            if (s < NSEG - 1) {
#define EXTRA_READS
                MEM_TIMES_W(fa)
#undef EXTRA_READS
                const v4f d2 = mfma16(tp, sel, (v4f){0, 0, 0, 0});
                float m = fmx(fmx(d2.x, d2.y), fmx(d2.z, d2.w));
                m = max_g(fmx(m, ror8(m)));
                *(v4f*)(lds + mw) = (v4f){-m, -m, -m, -m};
                const float e0 = __builtin_amdgcn_exp2f(d2.x - m), e1 = __builtin_amdgcn_exp2f(d2.y - m), e2 = __builtin_amdgcn_exp2f(d2.z - m), e3 = __builtin_amdgcn_exp2f(d2.w - m);
                const unsigned u0 = pk2(e0, e1), u1 = pk2(e2, e3);
                st16s(rs, ew, sb, (v4u){u0, u1, dppu<0x128>(u0), dppu<0x128>(u1)});
            }
            BAR();
            BAR0();
        }
        if (om == 14) { BAR0(); BAR0(); BAR0(); }
    } else {
        if (wave == 4) { *(v4u*)(lds + L_PART + lane * 16) = (v4u){0u, 0u, 0u, 0u}; *(v4u*)(lds + L_PART + 1024 + lane * 16) = (v4u){0u, 0u, 0u, 0u}; }
        const int cal = get_cal(c);
        if (cal < 0) return;
        STAMP(idx == 0 && wave == 7, 4, 1);
        const bool stg = q == 3 || wave != 7, two = q == 3 && wave == 5;
        const int pr = L_PART + (lane & 31) * 8, pubo = lane < 32 && (lane & 3) == 0 ? HO_PART + idx * 128 + lane * 4 : OOB;
#pragma unroll 1
        for (int s = 0; s < NSEG && budget >= 0; ++s) {
            const int sb = slot_base(s, cal);
            if (stg) {
                if (s) { WAIT_G(); WAIT_P(); }
                if (two) { v4u v, u; unsigned key;
#pragma unroll 1
                    do { FENCE(); v = ld16(rs, po, sb + HO_MEM, 1); u = ld16(rs, 6144 + lane * 16, sb + HO_MEM, 1); key = POLL_KEY(budget); } while (__builtin_amdgcn_ballot_w64(max(umax4(v), umax4(u)) > key) != 0);
                    *(v4u*)(lds + L_MEM + po) = v; *(v4u*)(lds + L_MEM + 6144 + lane * 16) = u;
                } else *(v4u*)(lds + L_MEM + po) = poll_piece(rs, po, sb + HO_MEM, budget);
            }
            BAR();
            STAMP(idx == 0 && wave == 7, 0, s); STAMP(idx == 3 && wave == 6, 12, s);
            asm volatile(PUBLISH_ASM(256) :: [pub] "s"(wave), [la] "v"(pr), [vo] "v"(pubo), [rs] "s"(rs), [so] "s"(sb) : "memory", "scc", VCLOB);
            if (wave == 7) {
                STAMP(idx == 0, 1, s); STAMP(idx == 3, 7, s);
                if (lane == 0) st4s(rs, flagoff(FL_T + idx), 0, s);
            }
        }
    }
    if (budget < 0) give_up(rs);
    VMWAIT(0);
    BAR();
    if (wave == 7 && lane == 0) st4s(rs, flagoff(FL_T + idx), 0, NSEG);
}

DEV void role_o(const Params& p, const Ctx& c, int t) {
    const int lane = c.lane, wave = c.wave, rho = c.rho, g = c.g;
    const rsrc_t rs = c.rs; char* lds = c.lds;
    constexpr int L_MEM = 0, L_OUT = 8192, L_DUMP = 12288, L_V = 13312, L_U = 20480;
    v4u w[2][14];
#pragma unroll
    for (int k = 0; k < 2; ++k)
#pragma unroll
        for (int ks = 0; ks < 14; ++ks) w[k][ks] = ld16(rs, OFF_BT + ((2 * wave + k) * 28 + t) * 14336 + lane * 16, ks * 1024, 0);
    const int cal = get_cal(c);
    if (cal < 0) return;
    const int sidx = (rho & 2) ? 0xEEEE : 0x4444;
    const int fa = L_MEM + g * 256 + ((rho >> 2) * 2 + (rho & 1)) * 32 + ((rho >> 1) & 1) * 16;
    const int hb = rho >> 3, hd = 2 * wave + hb, i = rho & 7;
    const int pa = HO_PART + hd * 512 + g * 128 + i * 16, ea = HO_E + hd * 512 + g * 128 + i * 16;
    const int ul = OFF_U + t * 16384 + (wave & 3) * 4096 + lane * 16;
    const int ufl = flagoff(FL_W + ((wave & 3) * 4 + (lane & 3)) * 7 + ((14 + (t >> 1)) >> 2));
    const int mo = (lane & 3) == 0 ? HO_MEM + t * 256 + lane * 4 : OOB;
    const int p2 = (lane & 4) ? 2 * (lane & 3) + 1 : 2 * (lane & 3);
    const int ra = L_OUT + (lane >> 3) * 64 + p2 * 8;
    const int ow = hb == 0 ? L_OUT + wave * 512 + i * 64 + g * 16 : L_DUMP + lane * 16;
    const int po = wave * 1024 + lane * 16, po2 = min(wave + 4, 6) * 1024 + lane * 16;
    const v4u ones = {0x3f803f80u, 0x3f803f80u, 0x3f803f80u, 0x3f803f80u};
    const v2u ones2 = {0x3f803f80u, 0x3f803f80u};
    const unsigned mlo = hb ? 0u : 0xffffffffu, mhi = ~mlo;
    const unsigned long long hbm = 0xff00ff00ff00ff00ull;
    v4u psel; { const int kq = (rho >> 2) * 2 + (rho & 1); const unsigned one = (kq & 1) ? 0x3f800000u : 0x3f80u;
        psel = (v4u){(kq >> 1) == 0 ? one : 0u, (kq >> 1) == 1 ? one : 0u, (kq >> 1) == 2 ? one : 0u, (kq >> 1) == 3 ? one : 0u}; }
    unsigned known = 0; v4u ua, ub;
    int budget = SPIN_CAP;
    auto fetch_u = [&](int s, bool paced) {
        if (known < (unsigned)s + 1) {
            unsigned seen; if (!wait_flags<1>(rs, ufl, s + 1, seen)) budget = -1;
            known = s + 1;
            if (__builtin_amdgcn_ballot_w64(seen < (unsigned)s + 8) == 0) known = s + 8;
        }
#pragma unroll 1
        for (int k = 0; k < 4; ++k) { dma16s(rs, ul, s * (NH * 28672) + k * 1024, L_U + (s & 1) * 16384 + (wave & 3) * 4096 + k * 1024); if (paced) WAIT_U(); }
    };
    ACC_DECL;
    auto mem_times_b = [&](int sb, int s) -> v2u {
        v4f c0 = {0, 0, 0, 0}, c1 = {0, 0, 0, 0};
        if (wave < 4 && (s == 0 || ABL(16))) { v4u v, u; unsigned key;
#pragma unroll 1
            do { FENCE(); v = ld16(rs, po, sb + HO_MEM, 1); u = ld16(rs, po2, sb + HO_MEM, 1); key = POLL_KEY(budget); } while (__builtin_amdgcn_ballot_w64(max(umax4(v), umax4(u)) > key) != 0);
            *(v4u*)(lds + L_MEM + po) = v; *(v4u*)(lds + L_MEM + po2) = u;
        }
        ACC_T(1);
        BAR();
        ACC_T(2);
#define EXTRA_READS ua = lds16(lds, L_U + (s & 1) * 16384 + wave * 2048 + lane * 16); ub = lds16(lds, L_U + (s & 1) * 16384 + wave * 2048 + 1024 + lane * 16)
        MEM_TIMES_W(fa)
#undef EXTRA_READS
        *(v2u*)(lds + L_V + wave * 512 + lane * 8) = tp;
        return tp;
    };
    auto store_v = [&](int s) { st16s(rs, OFF_V + t * 4096 + (wave & 3) * 1024 + lane * 16, s * (NH * 7168), lds16(lds, L_V + (wave & 3) * 1024 + lane * 16)); };
    if (wave >= 4) { fetch_u(0, false); fetch_u(1, false); VMWAIT(0); }
#pragma unroll 1
    for (int s = 0; s < NSEG - 1 && budget >= 0; ++s) {
        const int sb = slot_base(s, cal), sn = slot_base(s + 1, cal);
        ACC_T(0);
        const v2u vp = mem_times_b(sb, s);
        ACC_T(3);
        WAIT_OS();
        asm volatile(
            "1:\n\t"
            "buffer_load_dwordx4 v[224:227], %[pa], %[rs], %[sb] offen sc1\n\t"
            "buffer_load_dwordx4 v[228:231], %[ea], %[rs], %[sb] offen sc1\n\t"
            "s_add_i32 %[bud], %[bud], -1\n\ts_cmp_lt_i32 %[bud], 0\n\ts_cselect_b32 vcc_hi, -1, -2\n\tv_mov_b32 v253, vcc_hi\n\t"
            "s_waitcnt vmcnt(1)\n\t"
            "v_mfma_f32_16x16x32_bf16 v[232:235], %[psel], v[224:227], 0\n\t"
            "v_max3_u32 v250, v224, v225, v226\n\t"
            "s_waitcnt vmcnt(0)\n\t"
            "v_mfma_f32_16x16x32_bf16 v[244:247], %[ones], v[228:231], 0\n\t"
            "v_max3_u32 v251, v228, v229, v230\n\t"
            "v_mfma_f32_16x16x32_bf16 v[236:239], %[ua], v[228:231], 0\n\t"
            "v_max3_u32 v250, v250, v227, v231\n\t"
            "v_mfma_f32_16x16x32_bf16 v[240:243], %[ub], v[228:231], 0\n\t"
            "v_max_u32 v250, v250, v251\n\t"
            "v_cmp_gt_u32 vcc, v250, v253\n\t"
            "s_cbranch_vccnz 1b\n\t"
            "v_exp_f32 v250, v232\n\tv_exp_f32 v251, v233\n\t"
            "v_max_f32 v252, v232, v233\n\t"
            "v_cvt_pk_bf16_f32 v250, v250, v251\n\t"
            "v_cmp_lt_f32 vcc, 0x42800000, v252\n\t"
            "v_and_b32 v248, v250, %[mlo]\n\tv_and_b32 v249, v250, %[mhi]\n\t"
            "v_cndmask_b32 v236, v236, v240, %[hbm]\n\tv_cndmask_b32 v237, v237, v241, %[hbm]\n\t"
            "v_mfma_f32_16x16x16_bf16 v[244:247], %[ones2], v[248:249], v[244:247]\n\t"
            "v_cndmask_b32 v238, v238, v242, %[hbm]\n\tv_cndmask_b32 v239, v239, v243, %[hbm]\n\t"
            "s_nop 1\n\t"
            "v_mfma_f32_16x16x16_bf16 v[236:239], %[vp], v[248:249], v[236:239]\n\t"
            "s_cbranch_vccz 2f\n\t"
            "s_nop 7\n\t"
            "v_max_f32 v252, 0, v252\n\tv_mov_b32 v253, v252\n\ts_nop 1\n\tv_permlane16_swap_b32 v252, v253\n\ts_nop 1\n\t"
            "v_max_f32 v252, v252, v253\n\tv_mov_b32 v253, v252\n\ts_nop 1\n\tv_permlane32_swap_b32 v252, v253\n\ts_nop 1\n\t"
            "v_max_f32 v252, v252, v253\n\t"
            "v_mfma_f32_16x16x32_bf16 v[244:247], %[ones], v[228:231], 0\n\t"
            "v_mfma_f32_16x16x32_bf16 v[236:239], %[ua], v[228:231], 0\n\t"
            "v_sub_f32 v250, v232, v252\n\tv_sub_f32 v251, v233, v252\n\t"
            "v_exp_f32 v250, v250\n\tv_exp_f32 v251, v251\n\tv_exp_f32 v253, -v252\n\ts_nop 0\n\t"
            "v_cvt_pk_bf16_f32 v250, v250, v251\n\t"
            "v_and_b32 v248, v250, %[mlo]\n\tv_and_b32 v249, v250, %[mhi]\n\ts_nop 7\n\t"
            "v_cndmask_b32 v236, v236, v240, %[hbm]\n\tv_cndmask_b32 v237, v237, v241, %[hbm]\n\tv_cndmask_b32 v238, v238, v242, %[hbm]\n\tv_cndmask_b32 v239, v239, v243, %[hbm]\n\t"
            "v_mul_f32 v236, v236, v253\n\tv_mul_f32 v237, v237, v253\n\tv_mul_f32 v238, v238, v253\n\tv_mul_f32 v239, v239, v253\n\t"
            "v_mul_f32 v244, v244, v253\n\tv_mul_f32 v245, v245, v253\n\tv_mul_f32 v246, v246, v253\n\tv_mul_f32 v247, v247, v253\n\ts_nop 1\n\t"
            "v_mfma_f32_16x16x16_bf16 v[244:247], %[ones2], v[248:249], v[244:247]\n\t"
            "v_mfma_f32_16x16x16_bf16 v[236:239], %[vp], v[248:249], v[236:239]\n\t"
            "s_nop 7\n\t"
            "2:\n\t"
            "s_nop 1\n\t"
            "v_rcp_f32 v252, v244\n\t"
            "s_nop 3\n\t"
            "v_mul_f32 v236, v236, v252\n\tv_mul_f32 v237, v237, v252\n\tv_mul_f32 v238, v238, v252\n\tv_mul_f32 v239, v239, v252\n\t"
            "v_add_f32_dpp v240, v236, v236 row_ror:8 row_mask:0xf bank_mask:0xf\n\tv_add_f32_dpp v241, v237, v237 row_ror:8 row_mask:0xf bank_mask:0xf\n\t"
            "v_add_f32_dpp v242, v238, v238 row_ror:8 row_mask:0xf bank_mask:0xf\n\tv_add_f32_dpp v243, v239, v239 row_ror:8 row_mask:0xf bank_mask:0xf\n\t"
            "ds_write_b128 %[ow], v[240:243]\n\t"
            PUBLISH_ASM(512)
            : [bud] "+s"(budget)
            : [pa] "v"(pa), [ea] "v"(ea), [rs] "s"(rs), [sb] "s"(sb), [psel] "v"(psel), [ones] "v"(ones), [ones2] "v"(ones2), [ua] "v"(ua), [ub] "v"(ub), [vp] "v"(vp),
              [mlo] "v"(mlo), [mhi] "v"(mhi), [hbm] "s"(hbm), [ow] "v"(ow),
              [pub] "s"(wave), [la] "v"(ra), [vo] "v"(mo), [so] "s"(sn)
            : "memory", "vcc", "scc", VCLOB);
        if (wave >= 4) {
            STAMP(t == 0 && wave == 7, 3, s);
            if (wave == 7 && lane == 0) st4s(rs, flagoff(FL_V + t), 0, s);
            WAIT_V();
            if (ABL(2)) store_v(s);
            if (s + 2 < NSEG - 1 && ABL(1)) fetch_u(s + 2, true);
        } else WAIT_OM();
        ACC_T(7);
    }
    ACC_OUT(t * 8 + wave);
    mem_times_b(slot_base(NSEG - 1, cal), NSEG - 1);
    if (budget < 0) give_up(rs);
    BAR();
    if (wave >= 4) store_v(NSEG - 1);
    VMWAIT(0);
    BAR0();
    if (wave == 7 && lane == 0) st4s(rs, flagoff(FL_V + t), 0, NSEG);
}

DEV void role_w(const Params& p, const Ctx& c, int wi, int y) {
    const int lane = c.lane, wave = c.wave, rho = c.rho, g = c.g;
    const rsrc_t rs = c.rs; char* lds = c.lds;
    const int h = wi / 7, unit = (wi - h * 7) * 4 + (wave & 3), set = wave >> 2;
    const bool isG = unit < 14;
    auto stage = [&](int it) {
        const int sg = 2 * it + set, lb = (set * 2 + (it & 1)) * 28672;
#pragma unroll
        for (int k = 0; k < 7; ++k) { const int pc = (wave & 3) * 7 + k; dma16(rs, OFF_XF + lane * 16, sg * 28672 + pc * 1024, lb + pc * 1024); }
    };
    stage(0);
    v4u w[2][14];
#pragma unroll
    for (int t = 0; t < 2; ++t) {
        const int row = isG ? unit * 32 + pperm(rho, t) : (unit - 14) * 32 + t * 16 + rho;
#pragma unroll
        for (int ks = 0; ks < 14; ++ks) w[t][ks] = ld16(rs, (isG ? OFF_AP : OFF_BT) + (h * 28 + (row >> 4)) * 14336 + g * 256 + (row & 15) * 16, ks * 1024, 0);
    }
    int xa[2];
#pragma unroll
    for (int k = 0; k < 2; ++k) { const int key = isG ? k * 16 + rho : pperm(rho, k), kt = key >> 4; xa[k] = kt * 14336 + (g * 16 + ((key & 15) ^ (kt * 4))) * 16; }
    const int ob = (isG ? OFF_G + (h * 14 + unit) * 2048 : OFF_U + ((unit - 14) * 32 + h) * 1024) + lane * 16;
    const int ostep = isG ? 1024 : 16384;
    VMWAIT(0);
    BAR();
    STAMP(wi == 0 && wave == 1, 4, 2);
    v4u r0 = {0u, 0u, 0u, 0u}, r1 = r0;
    constexpr int sstep = 2 * NH * 28672;
#pragma unroll 1
    for (int it = 0; it < 32; ++it) {
        const int so = (2 * it + set) * (NH * 28672);
        if (it + 1 < 32) stage(it + 1);
        if (it >= 3) { st16s(rs, ob, so - sstep, r0); st16s(rs, ob + ostep, so - sstep, r1); }
        dma16(rs, it + 4 < 32 && wave < 4 ? OFF_XF + lane * 16 : OOB, (2 * (it + 4) + (wave & 1)) * 28672 + ((y + (wave & 2) * 8) % 28) * 1024, 114688);
        const char* xb = lds + (set * 2 + (it & 1)) * 28672;
        v4f a00 = {0, 0, 0, 0}, a01 = a00, a10 = a00, a11 = a00;
        if (isG) {
#pragma unroll
            for (int ks = 0; ks < 14; ++ks) { const v4u x0 = lds16(xb, xa[0] + ks * 1024), x1 = lds16(xb, xa[1] + ks * 1024);
                a00 = mfma32(w[0][ks], x0, a00); a01 = mfma32(w[1][ks], x0, a01); a10 = mfma32(w[0][ks], x1, a10); a11 = mfma32(w[1][ks], x1, a11); }
        } else {
#pragma unroll
            for (int ks = 0; ks < 14; ++ks) { const v4u x0 = lds16(xb, xa[0] + ks * 1024), x1 = lds16(xb, xa[1] + ks * 1024);
                a00 = mfma32(x0, w[0][ks], a00); a01 = mfma32(x1, w[0][ks], a01); a10 = mfma32(x0, w[1][ks], a10); a11 = mfma32(x1, w[1][ks], a11); }
        }
        r0 = pk8(a00, a01); r1 = pk8(a10, a11);
        const bool eager = it < 2 || it == 31;
        if (eager) { st16s(rs, ob, so, r0); st16s(rs, ob + ostep, so, r1); }
        if (eager || it == 2) VMWAIT(0); else VMWAIT(3);
        BAR();
        if (wave == 0 && lane == 0 && it != 2) st4s(rs, flagoff(FL_W + wi), 0, eager ? 2 * it + 2 : max(2 * it - 2, 4));
        STAMP(wi == 0 && wave == 1, 5, it); CSTAMP(wi == 0 && wave == 1, 6, it);
    }
}

DEV int f_first(int set, int wave) { return set ? 33 + 2 * wave : wave; }
DEV int f_next(int s, int wave) { return s >= 32 ? s + 16 : s >= 24 ? 32 + 2 * wave : s + 8; }
DEV void role_f1(const Params& p, const Ctx& c, int h, int set) {
    const int lane = c.lane, wave = c.wave, rho = c.rho, g = c.g;
    const rsrc_t rs = c.rs;
    const int gfl = flagoff(FL_W + h * 7 + (lane & 3)), tfl = flagoff(FL_T + h * 4 + (lane & 3));
    const int gl0 = OFF_G + h * 28672 + (rho >> 3) * 1024 + g * 256 + (pperm(rho, 0) & 15) * 16;
    const int tl = OFF_T + h * 7168 + g * 128 + ((rho >> 2) * 2 + (rho & 1)) * 16;
    const float ninf = -__builtin_inff();
    bool wdone = false;
#pragma unroll 1
    for (int s = f_first(set, wave); s < NSEG; s = f_next(s, wave)) {
        STAMP(h == 0, 16, s);
        unsigned seen;
        if (!wdone) { if (!wait_flags<8>(rs, gfl, s + 1, seen)) return; wdone = __builtin_amdgcn_ballot_w64(seen < NSEG) == 0; }
        v4u x[2][14];
        v4f a[2][2] = {{{0, 0, 0, 0}, {0, 0, 0, 0}}, {{0, 0, 0, 0}, {0, 0, 0, 0}}};
#pragma unroll
        for (int hf = 0; hf < 2; ++hf) {
            v4u y[2][7];
#pragma unroll
            for (int k = 0; k < 7; ++k) {
#pragma unroll
                for (int qt = 0; qt < 2; ++qt) x[qt][hf * 7 + k] = ld16(rs, OFF_XF + qt * 14336 + g * 256 + (rho ^ (qt * 4)) * 16, s * 28672 + (hf * 7 + k) * 1024, 0);
                y[0][k] = ld16(rs, gl0, s * (NH * 28672) + (hf * 7 + k) * 2048, 1); y[1][k] = ld16(rs, gl0 + 64, s * (NH * 28672) + (hf * 7 + k) * 2048, 1);
            }
            SCHED();
#pragma unroll
            for (int k = 0; k < 7; ++k)
#pragma unroll
                for (int qt = 0; qt < 2; ++qt) { a[qt][0] = mfma32(y[0][k], x[qt][hf * 7 + k], a[qt][0]); a[qt][1] = mfma32(y[1][k], x[qt][hf * 7 + k], a[qt][1]); }
        }
        float v[2][8], m[2];
#pragma unroll
        for (int qt = 0; qt < 2; ++qt) {
            const float r[8] = {a[qt][0].x, a[qt][0].y, a[qt][0].z, a[qt][0].w, a[qt][1].x, a[qt][1].y, a[qt][1].z, a[qt][1].w};
            m[qt] = ninf;
#pragma unroll
            for (int k = 0; k < 8; ++k) { v[qt][k] = (8 * g + k <= qt * 16 + rho) ? r[k] : ninf; m[qt] = fmx(m[qt], v[qt][k]); }
        }
        asm volatile("" : "+v"(m[0]), "+v"(m[1]));
        STAMP(h == 0, 17, s);
        if (!wait_flags<2>(rs, tfl, s + 1, seen)) return;
        STAMP(h == 0, 18, s);
        v4f am[2] = {{0, 0, 0, 0}, {0, 0, 0, 0}};
        { v4u y[14];
#pragma unroll
          for (int ks = 0; ks < 14; ++ks) y[ks] = ld16(rs, tl, s * (NH * 7168) + ks * 512, 1);
          SCHED();
#pragma unroll
          for (int ks = 0; ks < 14; ++ks) { am[0] = mfma32(y[ks], x[0][ks], am[0]); am[1] = mfma32(y[ks], x[1][ks], am[1]); } }
        unsigned pm[2];
#pragma unroll
        for (int qt = 0; qt < 2; ++qt) {
            const float mm = max_g(fmx(m[qt], fmx(am[qt].x, am[qt].y)));
            const float e0 = __builtin_amdgcn_exp2f(am[qt].x - mm), e1 = __builtin_amdgcn_exp2f(am[qt].y - mm);
            float z = e0 + e1;
#pragma unroll
            for (int k = 0; k < 8; ++k) { v[qt][k] = __builtin_amdgcn_exp2f(v[qt][k] - mm); z += v[qt][k]; }
            const float rz = __builtin_amdgcn_rcpf(sum_g(z));
            st16s(rs, OFF_PX + (h * 2 + qt) * 1024 + lane * 16, s * (NH * 2048),
                  (v4u){pk2(v[qt][0] * rz, v[qt][1] * rz), pk2(v[qt][2] * rz, v[qt][3] * rz), pk2(v[qt][4] * rz, v[qt][5] * rz), pk2(v[qt][6] * rz, v[qt][7] * rz)});
            pm[qt] = pk2(e0 * rz, e1 * rz);
        }
        st16s(rs, (lane & 1) == 0 ? OFF_PM + h * 512 + lane * 8 : OOB, s * (NH * 512), (v4u){pm[0], pm[1], dppu<0xF5>(pm[0]), dppu<0xF5>(pm[1])});
        VMWAIT(0);
        if (lane == 0) st4s(rs, flagoff((set ? FL_P2 : FL_P) + h * 8 + wave), 0, s + 1);
        STAMP(h == 0, 19, s);
    }
}

DEV void make_tables(const Params& p, const Ctx& c, int t) {
    const int lane = c.lane, wave = c.wave, rho = c.rho, g = c.g;
    const rsrc_t rs = c.rs;
    for (int ch = t * 512 + wave * 64 + lane; ch < 2 * 28672; ch += 28 * 512) {
        float v[8];
        if (ch < 28672) { const int fr = ch >> 6, tl = fr / 14, d0 = (fr - tl * 14) * 32 + g * 8, ri = tl >> 4, k = (tl & 15) * 16 + rho;
#pragma unroll
            for (int j = 0; j < 8; ++j) { const float ph = (float)((k * (d0 + j)) % 448) * (1.f / 448.f); v[j] = k > 224 ? 0.f : ri ? -__builtin_amdgcn_sinf(ph) : __builtin_amdgcn_cosf(ph); }
        } else { const int fr = (ch - 28672) >> 6, d = (fr >> 4) * 16 + rho, ri = (fr >> 3) & 1, k0 = (fr & 7) * 32 + g * 8;
#pragma unroll
            for (int j = 0; j < 8; ++j) { const int k = k0 + j; const float ph = (float)((k * d) % 448) * (1.f / 448.f); const bool edge = k == 0 || k == 224;
                const float wgt = (edge ? 1.f : 2.f) / (448.f * 2048.f); v[j] = k > 224 ? 0.f : ri ? (edge ? 0.f : -wgt * __builtin_amdgcn_sinf(ph)) : wgt * __builtin_amdgcn_cosf(ph); }
        }
        st16s(rs, OFF_FD + ch * 16, 0, (v4u){pk2(v[0], v[1]), pk2(v[2], v[3]), pk2(v[4], v[5]), pk2(v[6], v[7])});
    }
    VMWAIT(0);
    BAR();
    if (wave == 0 && lane == 0) __hip_atomic_fetch_add((unsigned*)(p.ws + flagoff(FL_TAB)), 1u, __ATOMIC_RELAXED, __HIP_MEMORY_SCOPE_AGENT);
}
DEV void role_f2(const Params& p, const Ctx& c, int t, int set) {
    const int lane = c.lane, wave = c.wave, rho = c.rho, g = c.g;
    const rsrc_t rs = c.rs;
    const int ufl = flagoff(FL_W + rho * 7 + ((14 + (t >> 1)) >> 2)), pfl = flagoff(lane < 16 ? (set ? FL_P2 : FL_P) + lane * 8 + wave : FL_V + t);
    const int ul = OFF_U + t * 16384 + lane * 16;
    bool wdone = false;
#pragma unroll 1
    for (int s = f_first(set, wave); s < NSEG; s = f_next(s, wave)) {
        unsigned seen;
        if (!wdone) { if (!wait_flags<8>(rs, ufl, s + 1, seen)) return; wdone = __builtin_amdgcn_ballot_w64(seen < NSEG) == 0; }
        STAMP(t == 0, 20, s);
        v4u u[16];
#pragma unroll
        for (int h = 0; h < 16; ++h) u[h] = ld16(rs, ul, s * (NH * 28672) + h * 1024, 1);
        if (!wait_flags<2>(rs, pfl, s + 1, seen)) return;
        STAMP(t == 0, 21, s);
        v4f a[2] = {{0, 0, 0, 0}, {0, 0, 0, 0}}, m[2] = {{0, 0, 0, 0}, {0, 0, 0, 0}};
        v4u px[16]; v2u pm[16], vv[8];
#pragma unroll
        for (int h = 0; h < 16; ++h) px[h] = ld16(rs, OFF_PX + lane * 16, s * (NH * 2048) + h * 2048, 1);
#pragma unroll
        for (int h = 0; h < 16; ++h) { pm[h] = ld8s(rs, OFF_PM + lane * 8, s * (NH * 512) + h * 512); if (h & 1) vv[h >> 1] = ld8s(rs, OFF_V + t * 4096 + lane * 8, s * (NH * 7168) + (h >> 1) * 512); }
        SCHED();
#pragma unroll
        for (int h = 0; h < 16; ++h) { a[0] = mfma32(u[h], px[h], a[0]); SCHED(); px[h] = ld16(rs, OFF_PX + 1024 + lane * 16, s * (NH * 2048) + h * 2048, 1); SCHED(); }
#pragma unroll
        for (int k = 0; k < 8; ++k) { m[0] = mfma16(vv[k], (v2u){pm[2 * k].x, pm[2 * k + 1].x}, m[0]); m[1] = mfma16(vv[k], (v2u){pm[2 * k].y, pm[2 * k + 1].y}, m[1]); }
        SCHED();
#pragma unroll
        for (int h = 0; h < 16; ++h) a[1] = mfma32(u[h], px[h], a[1]);
#pragma unroll
        for (int qt = 0; qt < 2; ++qt) { const v4f o = a[qt] + m[qt];
            *(v2u*)(p.out + (size_t)(s * 32 + qt * 16 + rho) * 896 + DM + t * 16 + g * 4) = (v2u){pk2(o.x, o.y), pk2(o.z, o.w)}; }
        STAMP(t == 0, 22, s);
    }
}

DEV bool fft_sync(const Params& p, const Ctx& c, int fl, bool arrive, unsigned need) {
    VMWAIT(0);
    BAR();
    int* okw = (int*)(c.lds + LDS_MAIN - 16);
    if (c.wave == 0) {
        if (arrive && c.lane == 0) __hip_atomic_fetch_add((unsigned*)(p.ws + flagoff(fl)), 1u, __ATOMIC_RELAXED, __HIP_MEMORY_SCOPE_AGENT);
        unsigned seen; const bool ok = wait_flags<8>(c.rs, flagoff(fl), need, seen);
        __builtin_amdgcn_fence(__ATOMIC_ACQUIRE, "agent");
        VMWAIT(0);
        if (c.lane == 0) *okw = ok;
    }
    BAR();
    return *okw != 0;
}
DEV v4u neg8(v4u v) { return v ^ 0x80008000u; }
DEV void twid(int a, const int (&n)[8], int mask, float inv, v4u& C, v4u& S) {
    float cs[8], sn[8];
#pragma unroll
    for (int j = 0; j < 8; ++j) { const float ph = (float)((a * n[j]) & mask) * inv; cs[j] = __builtin_amdgcn_cosf(ph); sn[j] = __builtin_amdgcn_sinf(ph); }
    C = (v4u){pk2(cs[0], cs[1]), pk2(cs[2], cs[3]), pk2(cs[4], cs[5]), pk2(cs[6], cs[7])};
    S = (v4u){pk2(sn[0], sn[1]), pk2(sn[2], sn[3]), pk2(sn[4], sn[5]), pk2(sn[6], sn[7])};
}
DEV void role_fft(const Params& p, const Ctx& c, int job) {
    const int lane = c.lane, wave = c.wave, rho = c.rho, g = c.g;
    const rsrc_t rs = c.rs; char* lds = c.lds;
    const v4f z4 = {0, 0, 0, 0};
    {
        const rsrc_t rx = mkrsrc(p.xa, 2048 * DM * 2);
        for (int pc = wave; pc < 28; pc += 8) { const int nt = pc >= 14, ks = pc - nt * 14; dma16(rx, ((64 * (nt * 16 + rho) + job) * DM + ks * 32 + g * 8) * 2, 0, pc * 1024); }
        if (!fft_sync(p, c, FL_TAB, false, 28)) return;
        v4u C[2], S[2];
        { int n[8];
#pragma unroll
          for (int j = 0; j < 8; ++j) n[j] = 64 * (16 * (j >> 2) + 4 * g + (j & 3)) + job;
          twid(rho, n, 2047, 1.f / 2048.f, C[0], S[0]); twid(16 + rho, n, 2047, 1.f / 2048.f, C[1], S[1]); }
        v4f zr[2][2], zi[2][2];
#pragma unroll
        for (int t = 0; t < 2; ++t) {
            const int fo = OFF_FD + (wave * 2 + (rho >> 3)) * 14336 + g * 256 + (pperm(rho, t) & 15) * 16;
            v4f r0 = z4, r1 = z4, i0 = z4, i1 = z4;
#pragma unroll
            for (int hf = 0; hf < 2; ++hf) {
                v4u fr[7], fi[7];
#pragma unroll
                for (int k = 0; k < 7; ++k) { fr[k] = ld16(rs, fo, (hf * 7 + k) * 1024, 0); fi[k] = ld16(rs, fo, 256 * DM * 2 + (hf * 7 + k) * 1024, 0); }
                SCHED();
#pragma unroll
                for (int k = 0; k < 7; ++k) { const v4u x0 = lds16(lds, (hf * 7 + k) * 1024 + lane * 16), x1 = lds16(lds, (14 + hf * 7 + k) * 1024 + lane * 16);
                    r0 = mfma32(x0, fr[k], r0); r1 = mfma32(x1, fr[k], r1); i0 = mfma32(x0, fi[k], i0); i1 = mfma32(x1, fi[k], i1); }
            }
            const v4u yr = pk8(r0, r1), yi = pk8(i0, i1), nr = neg8(yr);
#pragma unroll
            for (int ct = 0; ct < 2; ++ct) { zr[ct][t] = mfma32(yi, S[ct], mfma32(yr, C[ct], z4)); zi[ct][t] = mfma32(nr, S[ct], mfma32(yi, C[ct], z4)); }
        }
#pragma unroll
        for (int ct = 0; ct < 2; ++ct) { const int o = OFF_Z1 + ((ct * 16 + rho) * 64 + job) * 1024 + wave * 64 + g * 16;
            st16s(rs, o, 0, pk8(zr[ct][0], zr[ct][1])); st16s(rs, o + 512, 0, pk8(zi[ct][0], zi[ct][1])); }
    }
    STAMP(job == 0 && wave == 1, 4, 3);
    if (!fft_sync(p, c, FL_FA, true, 64)) return;
    STAMP(job == 0 && wave == 1, 4, 4);
    if (wave < 4) {
        const int k1 = job >> 1, P = (job & 1) * 4 + wave;
        char* L = lds + wave * 8192;
#pragma unroll
        for (int pc = 0; pc < 8; ++pc) dma16(rs, OFF_Z1 + (k1 * 64 + (pc & 3) * 16 + (lane >> 2)) * 1024 + (pc >> 2) * 512 + P * 64 + (lane & 3) * 16, 0, wave * 8192 + pc * 1024);
        v4u C[4][2], S[4][2];
#pragma unroll
        for (int ks = 0; ks < 2; ++ks) { int n[8];
#pragma unroll
            for (int j = 0; j < 8; ++j) n[j] = 32 * ks + 16 * (j >> 2) + 4 * g + (j & 3);
#pragma unroll
            for (int kt = 0; kt < 4; ++kt) twid(16 * kt + rho, n, 63, 1.f / 64.f, C[kt][ks], S[kt][ks]); }
        VMWAIT(0);
        v2u hr[4], hi[4];
#pragma unroll
        for (int t = 0; t < 2; ++t) {
            const int k = P * 32 + pperm(rho, t);
            v4u xr[2], xi[2];
#pragma unroll
            for (int ks = 0; ks < 2; ++ks) { const int a = (32 * ks + 4 * g + (rho >> 2)) * 64 + (rho & 3) * 16 + t * 8;
                const v2u a0 = ldstr(L, a), a1 = ldstr(L, a + 1024), b0 = ldstr(L, a + 4096), b1 = ldstr(L, a + 5120);
                xr[ks] = (v4u){a0.x, a0.y, a1.x, a1.y}; xi[ks] = (v4u){b0.x, b0.y, b1.x, b1.y}; }
            const v4u n0 = neg8(xr[0]), n1 = neg8(xr[1]);
            unsigned short fr[16], fi[16];
#pragma unroll
            for (int j = 0; j < 16; ++j) { const int o = (k1 + 32 * (16 * (j >> 2) + 4 * g + (j & 3))) * 225 + min(k, 224); fr[j] = p.wr[o]; fi[j] = p.wi[o]; }
            SCHED();
            v4f vr[4], vi[4];
#pragma unroll
            for (int kt = 0; kt < 4; ++kt) {
                const v4f ar = mfma32(S[kt][1], xi[1], mfma32(S[kt][0], xi[0], mfma32(C[kt][1], xr[1], mfma32(C[kt][0], xr[0], z4))));
                const v4f ai = mfma32(S[kt][1], n1, mfma32(S[kt][0], n0, mfma32(C[kt][1], xi[1], mfma32(C[kt][0], xi[0], z4))));
                float wr[4], wi[4];
#pragma unroll
                for (int r = 0; r < 4; ++r) { wr[r] = __uint_as_float((unsigned)fr[kt * 4 + r] << 16); wi[r] = __uint_as_float((unsigned)fi[kt * 4 + r] << 16); }
                vr[kt] = (v4f){ar.x * wr[0] - ai.x * wi[0], ar.y * wr[1] - ai.y * wi[1], ar.z * wr[2] - ai.z * wi[2], ar.w * wr[3] - ai.w * wi[3]};
                vi[kt] = (v4f){ar.x * wi[0] + ai.x * wr[0], ar.y * wi[1] + ai.y * wr[1], ar.z * wi[2] + ai.z * wr[2], ar.w * wi[3] + ai.w * wr[3]};
            }
            const v4u pr0 = pk8(vr[0], vr[1]), pr1 = pk8(vr[2], vr[3]), pi0 = pk8(vi[0], vi[1]), pi1 = pk8(vi[2], vi[3]), ni0 = neg8(pi0), ni1 = neg8(pi1);
#pragma unroll
            for (int nt = 0; nt < 4; ++nt) {
                const v4f rr = mfma32(ni1, S[nt][1], mfma32(ni0, S[nt][0], mfma32(pr1, C[nt][1], mfma32(pr0, C[nt][0], z4))));
                const v4f ri = mfma32(pr1, S[nt][1], mfma32(pr0, S[nt][0], mfma32(pi1, C[nt][1], mfma32(pi0, C[nt][0], z4))));
                const v2u qr = {pk2(rr.x, rr.y), pk2(rr.z, rr.w)}, qi = {pk2(ri.x, ri.y), pk2(ri.z, ri.w)};
                if (t == 0) { hr[nt] = qr; hi[nt] = qi; }
                else { const int o = OFF_Z2 + ((nt * 16 + rho) * 32 + k1) * 1024 + P * 64 + g * 16;
                    st16s(rs, o, 0, (v4u){hr[nt].x, hr[nt].y, qr.x, qr.y}); st16s(rs, o + 512, 0, (v4u){hi[nt].x, hi[nt].y, qi.x, qi.y}); }
            }
        }
    }
    STAMP(job == 0 && wave == 1, 4, 5);
    if (!fft_sync(p, c, FL_FB, true, 64)) return;
    STAMP(job == 0 && wave == 1, 4, 6);
    {
        char* L = lds + wave * 4096;
#pragma unroll
        for (int pc = 0; pc < 4; ++pc) dma16(rs, OFF_Z2 + (job * 32 + (pc & 1) * 16 + (lane >> 2)) * 1024 + (pc >> 1) * 512 + wave * 64 + (lane & 3) * 16, 0, wave * 4096 + pc * 1024);
        v4u C[2], S[2];
        { int n[8];
#pragma unroll
          for (int j = 0; j < 8; ++j) n[j] = 8 * g + j;
          twid(64 * rho + job, n, 2047, 1.f / 2048.f, C[0], S[0]); twid(64 * (16 + rho) + job, n, 2047, 1.f / 2048.f, C[1], S[1]); }
        VMWAIT(0);
        v4f rr[2][2], ri[2][2];
#pragma unroll
        for (int t = 0; t < 2; ++t) {
            const int a = (8 * g + (rho >> 2)) * 64 + (rho & 3) * 16 + t * 8;
            const v2u a0 = ldstr(L, a), a1 = ldstr(L, a + 256), b0 = ldstr(L, a + 2048), b1 = ldstr(L, a + 2304);
            const v4u xr = {a0.x, a0.y, a1.x, a1.y}, xi = {b0.x, b0.y, b1.x, b1.y}, ni = neg8(xi);
#pragma unroll
            for (int nt = 0; nt < 2; ++nt) { rr[nt][t] = mfma32(ni, S[nt], mfma32(xr, C[nt], z4)); ri[nt][t] = mfma32(xr, S[nt], mfma32(xi, C[nt], z4)); }
        }
        char* R = lds + 32768;
#pragma unroll
        for (int nt = 0; nt < 2; ++nt) { *(v4u*)(R + (nt * 16 + wave) * 1024 + lane * 16) = pk8(rr[nt][0], rr[nt][1]); *(v4u*)(R + (nt * 16 + 8 + wave) * 1024 + lane * 16) = pk8(ri[nt][0], ri[nt][1]); }
        BAR();
#pragma unroll 1
        for (int Q = wave; Q < 14; Q += 8) {
            v4f y[2][2] = {{z4, z4}, {z4, z4}};
#pragma unroll
            for (int hf = 0; hf < 2; ++hf) {
                v4u f0[8], f1[8];
#pragma unroll
                for (int k = 0; k < 8; ++k) { const int fo = OFF_FI + (Q * 2 + (rho >> 3)) * 16384 + g * 256 + (pperm(rho, 0) & 15) * 16; f0[k] = ld16(rs, fo, (hf * 8 + k) * 1024, 0); f1[k] = ld16(rs, fo + 64, (hf * 8 + k) * 1024, 0); }
                SCHED();
#pragma unroll
                for (int k = 0; k < 8; ++k) { const v4u b0 = lds16(R, (hf * 8 + k) * 1024 + lane * 16), b1 = lds16(R, (16 + hf * 8 + k) * 1024 + lane * 16);
                    y[0][0] = mfma32(f0[k], b0, y[0][0]); y[0][1] = mfma32(f1[k], b0, y[0][1]); y[1][0] = mfma32(f0[k], b1, y[1][0]); y[1][1] = mfma32(f1[k], b1, y[1][1]); }
            }
#pragma unroll
            for (int nt = 0; nt < 2; ++nt) *(v4u*)(p.out + (size_t)(64 * (nt * 16 + rho) + job) * 896 + Q * 32 + g * 8) = pk8(y[nt][0], y[nt][1]);
        }
    }
}

__global__ void __launch_bounds__(512, 2) k_main(Params p) {
    extern __shared__ __attribute__((aligned(16))) char lds[];
    Ctx c; c.rs = mkrsrc(p.ws, WS_BYTES); c.lds = lds; c.lane = threadIdx.x & 63; c.wave = __builtin_amdgcn_readfirstlane(threadIdx.x >> 6); c.rho = c.lane & 15; c.g = c.lane >> 4;
    { const int lane = c.lane; (void)lane; STAMP(blockIdx.x == 0 && c.wave == 7, 4, 0); }
    const int rot = *(const int*)(p.ws + flagoff(FL_ROT));
    const int x = (blockIdx.x + rot) & 7, y = blockIdx.x >> 3;
    if (x < 2) role_p(p, c, x * 32 + y);
    else if (x == 2) { if (y < 28) role_o(p, c, y); }
    else if (x == 3 && y == 28) role_cal(p, c);
    else {
        const int wi = (x - 4) * 32 + y - 16;
        if (x == 3) { if (y < 28) make_tables(p, c, y); } else if (wi >= 0) role_w(p, c, wi, y);
        if (wi >= 48) role_fft(p, c, wi - 48);
        else if (x == 3 ? y < 28 : wi >= 16 && wi < 44) role_f2(p, c, x == 3 ? y : wi - 16, x != 3);
        else if (x == 4) role_f1(p, c, y & 15, wi >= 0);
    }
    { const int lane = c.lane; (void)lane; STAMP(c.wave == 7, 8 + (blockIdx.x >> 6), blockIdx.x & 63); }
}

extern "C" void kernel_launch(void* const* d_in, const int* in_sizes, int n_in, void* d_out, int out_size, void* d_ws, size_t ws_size, hipStream_t stream) {
    Params p;
    p.xa = (const unsigned short*)d_in[0]; p.wr = (const unsigned short*)d_in[1]; p.wi = (const unsigned short*)d_in[2];
    p.xb = (const unsigned short*)d_in[3]; p.mem0 = (const unsigned short*)d_in[4];
    p.wq = (const unsigned short*)d_in[5]; p.wk = (const unsigned short*)d_in[6]; p.wv = (const unsigned short*)d_in[7]; p.wo = (const unsigned short*)d_in[8];
    p.out = (unsigned short*)d_out;
    p.ws = (char*)d_ws + (((size_t)2 << 20) - ((uintptr_t)d_ws & (((size_t)2 << 20) - 1))) % ((size_t)2 << 20);
    (void)hipFuncSetAttribute((const void*)k_main, hipFuncAttributeMaxDynamicSharedMemorySize, LDS_MAIN);
    (void)hipFuncSetAttribute((const void*)k_pro, hipFuncAttributeMaxDynamicSharedMemorySize, LDS_PRO);
    hipLaunchKernelGGL(k_pro, dim3(256), dim3(512), LDS_PRO, stream, p);
    hipLaunchKernelGGL(k_main, dim3(256), dim3(512), LDS_MAIN, stream, p);
}
```

```cpp
#include <hip/hip_runtime.h>
#include <hip/hip_bf16.h>
#include <stdint.h>

typedef unsigned v4u __attribute__((ext_vector_type(4)));
typedef unsigned v2u __attribute__((ext_vector_type(2)));
typedef float v4f __attribute__((ext_vector_type(4)));
typedef float v2f __attribute__((ext_vector_type(2)));
typedef short v4s __attribute__((ext_vector_type(4)));
typedef __bf16 v2bf __attribute__((ext_vector_type(2)));
typedef __bf16 v8bf __attribute__((ext_vector_type(8)));
typedef __bf16 v16bf __attribute__((ext_vector_type(16)));
typedef unsigned v8u __attribute__((ext_vector_type(8)));
typedef __amdgpu_buffer_rsrc_t rsrc_t;
#define LAS __attribute__((address_space(3)))
#define DEV __device__ __forceinline__

constexpr int NSEG = 64, NH = 16, DM = 448;
constexpr unsigned SENT = 0xFFFFFFFFu;
constexpr int OOB = 0x7fffffff;
constexpr int SC1 = 16;
constexpr int OFF_HO = 0;
constexpr int OFF_CTL = 4 << 20;
constexpr int OFF_AP = OFF_CTL + 65536;
constexpr int OFF_BT = OFF_AP + NH * DM * DM * 2;
constexpr int OFF_G = OFF_BT + NH * DM * DM * 2;
constexpr int OFF_U = OFF_G + NSEG * NH * 28672;
constexpr int OFF_T = OFF_U + NSEG * NH * 28672;
constexpr int OFF_V = OFF_T + NSEG * NH * 7168;
constexpr int OFF_PX = OFF_V + NSEG * NH * 7168;
constexpr int OFF_PM = OFF_PX + NSEG * NH * 2048;
constexpr int OFF_FD = OFF_PM + NSEG * NH * 512;
constexpr int OFF_FI = OFF_FD + 512 * DM * 2;
constexpr int OFF_Z1 = OFF_FI + 512 * DM * 2;
constexpr int OFF_Z2 = OFF_Z1 + (2 << 20);
constexpr int OFF_XF = OFF_Z2 + (2 << 20);
constexpr int OFF_DBG = OFF_XF + (2 << 20);
constexpr int WS_BYTES = OFF_DBG + (1 << 20);
constexpr int HO_MEM = 512, HO_PART = 16384, HO_E = 32768;
constexpr int OFF_PROBE = OFF_HO + 49152;
constexpr int FL_W = 0, FL_T = 112, FL_V = 176, FL_P = 208, FL_ROT = 336, FL_TAB = 337, FL_FA = 338, FL_FB = 339, FL_ABORT = 340, FL_CAL = 341, FL_P2 = 344;
constexpr int LDS_MAIN = 114688 + 2048;
constexpr int LDS_PRO = 114688;
constexpr int SPIN_CAP = 1 << 17;

struct Params {
    const unsigned short* xa; const unsigned short* wr; const unsigned short* wi;
    const unsigned short* xb; const unsigned short* mem0;
    const unsigned short* wq; const unsigned short* wk; const unsigned short* wv; const unsigned short* wo;
    unsigned short* out; char* ws;
};

DEV unsigned pk2(float a, float b) { v2bf r = {(__bf16)a, (__bf16)b}; return __builtin_bit_cast(unsigned, r); }
DEV v4u pk8(v4f a, v4f b) { return (v4u){pk2(a.x, a.y), pk2(a.z, a.w), pk2(b.x, b.y), pk2(b.z, b.w)}; }
DEV v4f mfma32(v4u a, v4u b, v4f c) { return __builtin_amdgcn_mfma_f32_16x16x32_bf16(__builtin_bit_cast(v8bf, a), __builtin_bit_cast(v8bf, b), c, 0, 0, 0); }
DEV v4f mfma16(v2u a, v2u b, v4f c) { return __builtin_amdgcn_mfma_f32_16x16x16bf16_1k(__builtin_bit_cast(v4s, a), __builtin_bit_cast(v4s, b), c, 0, 0, 0); }
DEV v4f smf(v4u a, v4u b0, v4u b1, v4f c, int idx) {
    v8u b = {b0.x, b0.y, b0.z, b0.w, b1.x, b1.y, b1.z, b1.w};
    return __builtin_amdgcn_smfmac_f32_16x16x64_bf16(__builtin_bit_cast(v8bf, a), __builtin_bit_cast(v16bf, b), c, idx, 0, 0);
}
DEV float fmx(float a, float b) { return __builtin_elementwise_maximum(a, b); }
DEV float max_g(float s) {
    auto r = __builtin_amdgcn_permlane16_swap(__float_as_uint(s), __float_as_uint(s), false, false);
    s = fmx(__uint_as_float(r[0]), __uint_as_float(r[1]));
    auto q = __builtin_amdgcn_permlane32_swap(__float_as_uint(s), __float_as_uint(s), false, false);
    return fmx(__uint_as_float(q[0]), __uint_as_float(q[1]));
}
DEV float sum_g(float s) {
    auto r = __builtin_amdgcn_permlane16_swap(__float_as_uint(s), __float_as_uint(s), false, false);
    s = __uint_as_float(r[0]) + __uint_as_float(r[1]);
    auto q = __builtin_amdgcn_permlane32_swap(__float_as_uint(s), __float_as_uint(s), false, false);
    return __uint_as_float(q[0]) + __uint_as_float(q[1]);
}
DEV float ror8(float s) { return __int_as_float(__builtin_amdgcn_mov_dpp(__float_as_int(s), 0x128, 0xf, 0xf, true)); }
template <int CTRL> DEV unsigned dppu(unsigned x) { return (unsigned)__builtin_amdgcn_mov_dpp((int)x, CTRL, 0xf, 0xf, true); }
DEV v4u quad4(unsigned x) { return (v4u){x, dppu<0x55>(x), dppu<0xAA>(x), dppu<0xFF>(x)}; }
DEV unsigned umax4(v4u v) { return max(max(v.x, v.y), max(v.z, v.w)); }
DEV rsrc_t mkrsrc(const void* p, unsigned bytes) { return __builtin_amdgcn_make_buffer_rsrc((void*)p, 0, bytes, 0x00020000); }
DEV v4u ld16(rsrc_t r, int vo, int so, int aux) { return aux ? __builtin_amdgcn_raw_buffer_load_b128(r, vo, so, SC1) : __builtin_amdgcn_raw_buffer_load_b128(r, vo, so, 0); }
DEV v2u ld8s(rsrc_t r, int vo, int so) { return __builtin_amdgcn_raw_buffer_load_b64(r, vo, so, SC1); }
DEV unsigned ld4s(rsrc_t r, int vo, int so) { return __builtin_amdgcn_raw_buffer_load_b32(r, vo, so, SC1); }
DEV void st16s(rsrc_t r, int vo, int so, v4u v) {
    __builtin_amdgcn_raw_buffer_store_b128(v, r, vo, so, SC1); asm volatile("s_nop 1" :: "v"(v) : "memory");
}
DEV void st8s(rsrc_t r, int vo, int so, v2u v) { __builtin_amdgcn_raw_buffer_store_b64(v, r, vo, so, SC1); }
DEV void st4s(rsrc_t r, int vo, int so, unsigned v) { __builtin_amdgcn_raw_buffer_store_b32(v, r, vo, so, SC1); }
DEV void dma16(rsrc_t r, int vo, int so, int ldsaddr) {
    unsigned keep;
    asm volatile("s_mov_b32 %0, m0\n\ts_mov_b32 m0, %4\n\ts_nop 4\n\tbuffer_load_dwordx4 %1, %2, %3 offen lds\n\ts_mov_b32 m0, %0"
                 : "=&s"(keep) : "v"(vo), "s"(r), "s"(__builtin_amdgcn_readfirstlane(so)), "s"(__builtin_amdgcn_readfirstlane(ldsaddr)) : "memory");
}
DEV void dma16s(rsrc_t r, int vo, int so, int ldsaddr) {
    unsigned keep;
    asm volatile("s_mov_b32 %0, m0\n\ts_mov_b32 m0, %4\n\ts_nop 4\n\tbuffer_load_dwordx4 %1, %2, %3 offen sc1 lds\n\ts_mov_b32 m0, %0"
                 : "=&s"(keep) : "v"(vo), "s"(r), "s"(__builtin_amdgcn_readfirstlane(so)), "s"(__builtin_amdgcn_readfirstlane(ldsaddr)) : "memory");
}
#define BAR() asm volatile("s_waitcnt lgkmcnt(0)\n\ts_barrier" ::: "memory")
#define BAR0() asm volatile("s_barrier" ::: "memory")
#define VMWAIT(n) asm volatile("s_waitcnt vmcnt(" #n ")" ::: "memory")
#define FENCE() asm volatile("" ::: "memory")
#define SCHED() __builtin_amdgcn_sched_barrier(0)
DEV v4u lds16(const char* l, int a) { return *(const v4u*)(l + a); }
DEV v2u lds8(const char* l, int a) { return *(const v2u*)(l + a); }
DEV v2u ldstr(const char* l, int a) { v4s r = __builtin_amdgcn_ds_read_tr16_b64_v4i16((LAS v4s*)(l + a)); return __builtin_bit_cast(v2u, r); }
template <int N> DEV void slp() { if constexpr (N > 0) __builtin_amdgcn_s_sleep(N); }
DEV int flagoff(int i) { return OFF_CTL + i * 128; }
template <int SL> DEV bool wait_flags(rsrc_t rs, int vo, unsigned need, unsigned& seen) {
    for (int sp = 0;; ++sp) {
        FENCE();
        seen = ld4s(rs, vo, 0);
        if (__builtin_amdgcn_ballot_w64(seen < need) == 0) return true;
        slp<SL>();
        if ((sp & 255) == 255) {
            if (sp > SPIN_CAP) st4s(rs, flagoff(FL_ABORT), 0, 1u);
            if (sp > SPIN_CAP || __builtin_amdgcn_readfirstlane(ld4s(rs, flagoff(FL_ABORT), 0))) return false;
        }
    }
}
DEV int img(int p) { return ((p & 7) >> 1) * 4 + (p >> 3) * 2 + (p & 1); }
DEV int pperm(int rho, int tau) { return (rho >> 2) * 8 + tau * 4 + (rho & 3); }

#define STAMP(cond, slot, s)
#define LSTAMP(cond, slot, s)
#define ACC_DECL
#define ACC_T(k)
#define ACC_OUT(slot)
#define CSTAMP(cond, slot, s)

extern "C" __global__ void __launch_bounds__(512, 2) k_pro(Params p) {
    extern __shared__ __attribute__((aligned(16))) char lds[];
    const int b = blockIdx.x, tid = threadIdx.x, lane = tid & 63, wave = __builtin_amdgcn_readfirstlane(tid >> 6);
    const int rho = lane & 15, g = lane >> 4;
    const int gemm = (b & 7) * 4 + (b >> 6), sub = (b >> 3) & 7, which = gemm & 1, h = gemm >> 1;
    const int b0 = 224 * (sub & 1), c0 = 112 * (sub >> 1);
    const rsrc_t rs = mkrsrc(p.ws, WS_BYTES);
    LSTAMP(b == 0 && wave == 0, 4, 8); STAMP(wave == 0 && b < 64, 29, b);
    const int lr = lane >> 2, lc = ((lane & 3) ^ ((0 - g) & 3)) * 8, rd = rho * 64 + ((g ^ ((0 - (rho >> 2)) & 3)) * 16);
    if (which == 0) {
        const rsrc_t rq = mkrsrc(p.wq, DM * 4096 * 2);
        for (int k = wave; k < 56; k += 8) dma16(rq, ((c0 + (k >> 3) * 16 + lr) * 4096 + h * 256 + (k & 7) * 32 + lc) * 2, 0, k * 1024);
    } else {
        const rsrc_t ro = mkrsrc(p.wo, DM * 4096 * 2);
        for (int k = wave; k < 56; k += 8) { const int ch = k * 64 + lane, e = ch / 14, cc = ch - e * 14; dma16(ro, ((h * 256 + e) * DM + c0 + cc * 8) * 2, 0, k * 1024); }
    }
    v4u a[2][8];
    char* la = lds + 57344 + wave * 8192;
    if (wave < 7) {
        const rsrc_t ra = mkrsrc(which ? p.wv : p.wk, DM * 4096 * 2);
#pragma unroll
        for (int t = 0; t < 2; ++t) {
            const int src = ((b0 + 32 * wave + pperm(lr, t)) * 4096 + h * 256 + lc) * 2;
#pragma unroll
            for (int ks = 0; ks < 4; ++ks) { dma16(ra, src, ks * 64, 57344 + wave * 8192 + (t * 4 + ks) * 1024); a[t][4 + ks] = ld16(ra, src, 256 + ks * 64, 0); }
        }
        VMWAIT(0);
#pragma unroll
        for (int k = 0; k < 8; ++k) a[k >> 2][k & 3] = lds16(la, k * 1024 + rd);
        asm volatile("s_waitcnt lgkmcnt(0)" : "+v"(a[0][0]), "+v"(a[0][1]), "+v"(a[0][2]), "+v"(a[0][3]), "+v"(a[1][0]), "+v"(a[1][1]), "+v"(a[1][2]), "+v"(a[1][3]) :: "memory");
#pragma unroll
        for (int k = 0; k < 8; ++k) *(v4u*)(la + k * 1024 + lane * 16) = a[k >> 2][4 + (k & 3)];
        FENCE();
#pragma unroll
        for (int k = 0; k < 8; ++k) a[k >> 2][4 + (k & 3)] = lds16(la, k * 1024 + rd);
    } else {
        const rsrc_t rx = mkrsrc(p.xb, 2048 * DM * 2);
        v4u xf[7], m0[7];
#pragma unroll
        for (int k = 0; k < 7; ++k) { const int pc = b * 7 + k, rt = pc / 14, ks = pc - rt * 14; xf[k] = ld16(rx, ((rt * 16 + (rho ^ ((rt & 1) * 4))) * DM + ks * 32 + g * 8) * 2, 0, 0); }
        const v4u sent = {SENT, SENT, SENT, SENT};
        if (b == 0) {
#pragma unroll
            for (int k = 0; k < 7; ++k) { const int c = k * 64 + lane; const unsigned* m = (const unsigned*)(p.mem0 + ((c >> 1) & 7) * DM + (c >> 4) * 16 + (c & 1) * 2); m0[k] = (v4u){m[0], m[2], m[4], m[6]}; }
        } else {
#pragma unroll
            for (int k = 0; k < 7; ++k) m0[k] = sent;
        }
        const unsigned rot = ((unsigned)__builtin_amdgcn_s_getreg(20 | (3 << 11)) - b) & 7u;
        if (lane < 16) st16s(rs, OFF_CTL + b * 256 + lane * 16, 0, (v4u){b * 2 == FL_ROT && lane == 0 ? rot : 0u, 0u, 0u, 0u});
#pragma unroll
        for (int k = 0; k < 16; ++k) {
            const int o = ((k & 7) * 1024 + lane * 16 + HO_MEM) & 8191;
            st16s(rs, (b & 3) == 3 && (b & 127) != 3 ? OOB : OFF_HO + b * 16384 + (k >> 3) * 8192 + o, 0, (k & 7) < 7 ? m0[k & 7] : sent);
        }
#pragma unroll
        for (int k = 0; k < 7; ++k) st16s(rs, OFF_XF + (b * 7 + k) * 1024 + lane * 16, 0, xf[k]);
        VMWAIT(24);
    }
    BAR();
    if (wave >= 7) return;
    STAMP(b == 0 && wave == 0, 4, 9); STAMP(wave == 0 && b < 64, 30, b);
    const float sc = which ? 1.f : 0.09016844005556021f;
    const int obase = (which ? OFF_BT : OFF_AP) + ((h * 28 + (c0 >> 4)) * 14 + (b0 >> 5) + wave) * 1024 + lane * 16;
    const int tra = (8 * g + (rho >> 2)) * 224 + (rho & 3) * 8;
    auto tile = [&](const v4u (&bf)[8], int ct) {
        v4f c0v = {0, 0, 0, 0}, c1v = {0, 0, 0, 0};
#pragma unroll
        for (int ks = 0; ks < 8; ++ks) { c0v = mfma32(a[0][ks], bf[ks], c0v); c1v = mfma32(a[1][ks], bf[ks], c1v); }
        st16s(rs, obase, ct * 14336, pk8(c0v * sc, c1v * sc));
    };
    auto fetch = [&](v4u (&bf)[8], int ct) {
        if (which == 0) {
#pragma unroll
            for (int ks = 0; ks < 8; ++ks) bf[ks] = lds16(lds, (ct * 8 + ks) * 1024 + rd);
        } else {
#pragma unroll
            for (int ks = 0; ks < 8; ++ks) { const v2u x = ldstr(lds, tra + ks * 7168 + ct * 32), y = ldstr(lds, tra + ks * 7168 + 896 + ct * 32); bf[ks] = (v4u){x.x, x.y, y.x, y.y}; }
        }
    };
    v4u f0[8], f1[8];
    fetch(f0, 0);
#pragma unroll 1
    for (int ct = 0; ct < 6; ct += 2) { fetch(f1, ct + 1); SCHED(); tile(f0, ct); fetch(f0, ct + 2); SCHED(); tile(f1, ct + 1); }
    tile(f0, 6);
    STAMP(b == 0 && wave == 0, 4, 10);
    STAMP(wave == 0 && b >= 192, 31, b & 63);
}

struct Ctx { rsrc_t rs; char* lds; int lane, wave, rho, g; };

DEV int slot_base(int s, int cal) { return OFF_HO + s * 65536 + (((cal >> (s >> 5)) & 1) << 13); }
#define STAGE_HEAD "s_add_i32 %[bud], %[bud], -1\n\ts_cmp_lt_i32 %[bud], 0\n\ts_cselect_b32 vcc_hi, -1, -2\n\tv_mov_b32 v233, vcc_hi\n\t"
#define STAGE_TAIL "v_cmp_gt_u32 vcc, v232, v233\n\ts_cbranch_vccnz 1b\n\ts_waitcnt lgkmcnt(0)\n\ts_barrier"
#define STAGE_CLOB "memory", "vcc", "scc", "v224", "v225", "v226", "v227", "v228", "v229", "v230", "v231", "v232", "v233"
DEV void stage1(rsrc_t rs, int vo, int so, int& budget) {
    asm volatile("1:\n\tbuffer_load_dwordx4 v[224:227], %[vo], %[rs], %[so] offen sc1\n\t" STAGE_HEAD
                 "s_waitcnt vmcnt(0)\n\tds_write_b128 %[vo], v[224:227]\n\tv_max3_u32 v232, v224, v225, v226\n\tv_max_u32 v232, v232, v227\n\t" STAGE_TAIL
                 : [bud] "+s"(budget) : [vo] "v"(vo), [rs] "s"(rs), [so] "s"(so) : STAGE_CLOB);
}
DEV void stage2(rsrc_t rs, int vo, int vo2, int so, int& budget) {
    asm volatile("1:\n\tbuffer_load_dwordx4 v[224:227], %[vo], %[rs], %[so] offen sc1\n\tbuffer_load_dwordx4 v[228:231], %[vo2], %[rs], %[so] offen sc1\n\t" STAGE_HEAD
                 "s_waitcnt vmcnt(1)\n\tds_write_b128 %[vo], v[224:227]\n\tv_max3_u32 v232, v224, v225, v226\n\t"
                 "s_waitcnt vmcnt(0)\n\tds_write_b128 %[vo2], v[228:231]\n\tv_max3_u32 v232, v232, v227, v228\n\tv_max3_u32 v232, v232, v229, v230\n\tv_max_u32 v232, v232, v231\n\t" STAGE_TAIL
                 : [bud] "+s"(budget) : [vo] "v"(vo), [vo2] "v"(vo2), [rs] "s"(rs), [so] "s"(so) : STAGE_CLOB);
}
DEV void give_up(rsrc_t rs) { st4s(rs, flagoff(FL_ABORT), 0, 1u); }
#define PIN2(a, b) asm volatile("" : "+v"(a), "+v"(b))
constexpr int CAL_STEP = 8;
#define TOUCH_LEAD 3
DEV void role_cal(const Params& p, const Ctx& c) {
    if (c.wave >= 2) return;
    unsigned best[2] = {0xffffffffu, 0xffffffffu};
    for (int r = 0; r < 20; ++r) {
#pragma unroll
        for (int k = 0; k < 2; ++k) {
            const int off = OFF_PROBE + c.wave * (2 << 20) + k * 8192 + r * 256;
            unsigned long long t0, t1; unsigned v;
            asm volatile("s_memtime %0\n\ts_waitcnt lgkmcnt(0)\n\tbuffer_load_dword %2, %3, %4, 0 offen sc1\n\ts_waitcnt vmcnt(0)\n\ts_memtime %1\n\ts_waitcnt lgkmcnt(0)"
                         : "=&s"(t0), "=&s"(t1), "=&v"(v) : "v"(off), "s"(c.rs) : "memory");
            best[k] = min(best[k], (unsigned)(t1 - t0));
        }
        if (r == 5 && c.wave == 0) { if (c.lane == 0) st4s(c.rs, flagoff(FL_CAL), 0, best[1] < best[0] ? 3u : 2u); VMWAIT(0); }
    }
    if (c.lane == 0) st4s(c.rs, flagoff(FL_CAL + 1 + c.wave), 0, best[1] < best[0] ? 3u : 2u);
    { const int k = c.lane & 3, o = (k == 0 ? 0 : k == 1 ? 7936 : 49152 + (k & 1) * 4096 + 2048) + (best[1] < best[0] ? 8192 : 0);
      for (int s = c.wave ? 32 : 12; s < 32 * (c.wave + 1); s += 4) {
          unsigned seen; if (!wait_flags<16>(c.rs, flagoff(FL_V), s - TOUCH_LEAD, seen)) return;
          unsigned v = ld4s(c.rs, OFF_HO + s * 65536 + o, 0); asm volatile("" :: "v"(v));
      } }
}
DEV int get_cal(const Ctx& c, int late, int& budget) {
    unsigned seen;
    if (!wait_flags<2>(c.rs, flagoff(FL_CAL + late + (late & c.lane)), 1u, seen)) budget = -1;
    return (__builtin_amdgcn_readlane(seen, 0) & 1) | ((__builtin_amdgcn_readlane(seen, 1) & 1) << 1);
}
DEV unsigned now() { return (unsigned)__builtin_amdgcn_s_memtime(); }
DEV void wait_until(unsigned t) {
    int rem = (int)(t - now());
    while (rem > 260) { __builtin_amdgcn_s_sleep(1); rem = (int)(t - now()); }
    rem -= 60;
    asm volatile("1:\n\ts_add_i32 %0, %0, -40\n\ts_cmp_gt_i32 %0, 0\n\ts_cbranch_scc1 1b" : "+s"(rem) :: "scc");
}
#define T_PM 2050
#define T_OS 2200
#define T_OM 400
#define T_OU 750
#define T_ST 0
#define T_PG 200
#define ABL(b) true
#define S_AT 6

#define MEM_TIMES_W(lbase)                                                                                           \
    v4u a_[7];                                                                                                       \
    _Pragma("unroll") for (int f = 0; f < 7; ++f) a_[f] = lds16(lds, (lbase) + f * 1024);                            \
    EXTRA_READS;                                                                                                     \
    SCHED();                                                                                                         \
    _Pragma("unroll") for (int f = 0; f < 7; ++f) { c0 = smf(a_[f], w[0][2 * f], w[0][2 * f + 1], c0, sidx); c1 = smf(a_[f], w[1][2 * f], w[1][2 * f + 1], c1, sidx); AFTER_STEP(f); } \
    const v2u tp = {pk2(c0.x + c0.z, c0.y + c0.w), pk2(c1.x + c1.z, c1.y + c1.w)};

#define VCLOB "v224", "v225", "v226", "v227", "v228", "v229", "v230", "v231", VCLOB_HI
#define VCLOB_HI "v232", "v233", "v234", "v235", "v236", "v237", "v238", "v239", \
              "v240", "v241", "v242", "v243", "v244", "v245", "v246", "v247", "v248", "v249", "v250", "v251", "v252", "v253", "v254", "v255"
#define PUBLISH_ASM(ST)                                                                                                                     \
    "s_cmp_lg_u32 %[pub], 7\n\ts_waitcnt lgkmcnt(0)\n\ts_barrier\n\ts_cbranch_scc1 9f\n\t"                                                  \
    "ds_read_b64 v[224:225], %[la]\n\tds_read_b64 v[226:227], %[la] offset:" #ST "*1\n\tds_read_b64 v[228:229], %[la] offset:" #ST "*2\n\t" \
    "ds_read_b64 v[230:231], %[la] offset:" #ST "*3\n\tds_read_b64 v[232:233], %[la] offset:" #ST "*4\n\t"                                  \
    "ds_read_b64 v[234:235], %[la] offset:" #ST "*5\n\tds_read_b64 v[236:237], %[la] offset:" #ST "*6\n\t"                                  \
    "ds_read_b64 v[238:239], %[la] offset:" #ST "*7\n\t"                                                                                    \
    "s_waitcnt lgkmcnt(6)\n\tv_pk_add_f32 v[224:225], v[224:225], v[226:227]\n\t"                                                           \
    "s_waitcnt lgkmcnt(4)\n\tv_pk_add_f32 v[228:229], v[228:229], v[230:231]\n\t"                                                           \
    "s_waitcnt lgkmcnt(2)\n\tv_pk_add_f32 v[232:233], v[232:233], v[234:235]\n\tv_pk_add_f32 v[224:225], v[224:225], v[228:229]\n\t"        \
    "s_waitcnt lgkmcnt(0)\n\tv_pk_add_f32 v[236:237], v[236:237], v[238:239]\n\tv_pk_add_f32 v[224:225], v[224:225], v[232:233]\n\t"        \
    "s_nop 0\n\tv_pk_add_f32 v[224:225], v[224:225], v[236:237]\n\t"                                                                        \
    "s_nop 0\n\tv_cvt_pk_bf16_f32 v224, v224, v225\n\ts_nop 1\n\t"                                                                          \
    "v_mov_b32_dpp v225, v224 quad_perm:[1,1,1,1] row_mask:0xf bank_mask:0xf\n\t"                                                           \
    "v_mov_b32_dpp v226, v224 quad_perm:[2,2,2,2] row_mask:0xf bank_mask:0xf\n\t"                                                           \
    "v_mov_b32_dpp v227, v224 quad_perm:[3,3,3,3] row_mask:0xf bank_mask:0xf\n\t"                                                           \
    "buffer_store_dwordx4 v[224:227], %[vo], %[rs], %[so] offen sc1\n\t"                                                                    \
    "9:\n\ts_barrier\n\ts_nop 0"

DEV void role_p(const Params& p, const Ctx& c, int idx) {
    const int h = idx >> 2, q = idx & 3, lane = c.lane, wave = c.wave, rho = c.rho, g = c.g;
    const rsrc_t rs = c.rs; char* lds = c.lds;
    const int om = q * 4 + wave;
    const bool isT = wave < 4 && om < 14, isX = wave < 4 && om >= 14;
    constexpr int L_MEM = 0, L_PART = 8192, L_DUMP = 10240;
    const int sidx = (rho & 2) ? 0xEEEE : 0x4444;
    const int fa = L_MEM + g * 256 + ((rho >> 2) * 2 + (rho & 1)) * 32 + ((rho >> 1) & 1) * 16;
    v2u sel; { const int r0 = rho - 2 * g; sel.x = (r0 == 0 ? 0x3f80u : 0u) | (r0 == 1 ? 0x3f800000u : 0u); sel.y = (r0 == 8 ? 0x3f80u : 0u) | (r0 == 9 ? 0x3f800000u : 0u); }
    const int piece = q < 3 ? wave : (wave < 2 ? wave : wave - 2);
    const int po = piece * 1024 + lane * 16;
    int budget = SPIN_CAP;
    if (isT) {
        v4u w[2][14];
#pragma unroll
        for (int t = 0; t < 2; ++t)
#pragma unroll
            for (int ks = 0; ks < 14; ++ks) w[t][ks] = ld16(rs, OFF_AP + (h * 28 + 2 * om + t) * 14336 + g * 256 + img(rho) * 16, ks * 1024, 0);
        int cal = get_cal(c, 0, budget);
        unsigned tref = now() - 65536;
        const int qa = L_MEM + (2 * om + (rho >> 3)) * 256 + (rho & 7) * 32 + g * 8;
        const int pw = (g >> 1) == (rho >> 3) ? L_PART + (2 * wave + (rho >> 3)) * 256 + (rho & 7) * 32 + (g & 1) * 16 : L_DUMP + lane * 16;
        const int tw = lane < 32 ? OFF_T + (h * 14 + om) * 512 + ((rho >> 3) * 2 + g) * 128 + (rho & 7) * 16 : OOB;
#pragma unroll 1
        for (int s = 0; s < NSEG && budget >= 0; ++s) {
            if (s == CAL_STEP) cal = get_cal(c, 1, budget);
            const int sb = slot_base(s, cal);
            v4f c0 = {0, 0, 0, 0}, c1 = {0, 0, 0, 0}; PIN2(c0, c1);
            wait_until(tref + T_PM);
            stage1(rs, po, sb + HO_MEM, budget);
            v2u mq;
#define EXTRA_READS mq = lds8(lds, qa)
#define AFTER_STEP(f)
            MEM_TIMES_W(fa)
#undef EXTRA_READS
            const v4f d2 = mfma16(tp, sel, (v4f){0, 0, 0, 0});
            const v2u d = {pk2(d2.x, d2.y), pk2(d2.z, d2.w)};
            *(v4f*)(lds + pw) = mfma16(d, mq, (v4f){0, 0, 0, 0});
            BAR();
            BAR0();
            tref = now();
            wait_until(tref + T_PG);
            if (ABL(8)) { auto x = __builtin_amdgcn_permlane32_swap(d.x, d.y, false, false);
              auto y = __builtin_amdgcn_permlane32_swap(x[0], x[0], false, false); auto z = __builtin_amdgcn_permlane32_swap(x[1], x[1], false, false);
              st16s(rs, tw, s * (NH * 7168), (v4u){y[0], z[0], y[1], z[1]}); }
        }
    } else if (isX) {
        v4u w[2][14];
        int cal = get_cal(c, 0, budget);
        const int gl0 = OFF_G + h * 28672 + (rho >> 3) * 1024 + g * 256 + (pperm(rho, 0) & 15) * 16;
        const int gfl = flagoff(FL_W + h * 7 + (lane & 3));
        const int ew = rho < 8 ? HO_E + h * 512 + g * 128 + rho * 16 : OOB, mw = g == 0 ? L_PART + 1024 + (rho & 7) * 32 + (rho >> 3) * 16 : L_DUMP + lane * 16;
        unsigned known = 0;
        if (om == 15) { BAR0(); BAR0(); BAR0(); }
#pragma unroll 1
        for (int s = om - 14; s < NSEG; s += 2) {
            if ((s | 1) == CAL_STEP + 1) cal = get_cal(c, 1, budget);
            const int sb = slot_base(s, cal);
            if (s < NSEG - 1 && (s < 2 || ABL(4))) {
                if (s >= 2) wait_until(now() + T_PG);
                if (known < (unsigned)s + 1) {
                    unsigned seen; if (!wait_flags<1>(rs, gfl, s + 1, seen)) break;
                    known = min(min((unsigned)__builtin_amdgcn_readlane(seen, 0), (unsigned)__builtin_amdgcn_readlane(seen, 1)), min((unsigned)__builtin_amdgcn_readlane(seen, 2), (unsigned)__builtin_amdgcn_readlane(seen, 3)));
                }
#pragma unroll
                for (int t = 0; t < 2; ++t)
#pragma unroll
                    for (int ks = 0; ks < 14; ++ks) w[t][ks] = ld16(rs, gl0 + t * 64, s * (NH * 28672) + ks * 2048, 1);
            }
            if (s >= 2) { BAR0(); BAR0(); BAR0(); }
            v4f c0 = {0, 0, 0, 0}, c1 = {0, 0, 0, 0}; PIN2(c0, c1);
            VMWAIT(0);
            STAMP(idx == 3 && s == 0, 4, 12);
            BAR();
            if (s < NSEG - 1) {
#define EXTRA_READS
                MEM_TIMES_W(fa)
#undef EXTRA_READS
                const v4f d2 = mfma16(tp, sel, (v4f){0, 0, 0, 0});
                float m = fmx(fmx(d2.x, d2.y), fmx(d2.z, d2.w));
                m = max_g(fmx(m, ror8(m)));
                *(v4f*)(lds + mw) = (v4f){-m, -m, -m, -m};
                SCHED();
                BAR();
                const float e0 = __builtin_amdgcn_exp2f(d2.x - m), e1 = __builtin_amdgcn_exp2f(d2.y - m), e2 = __builtin_amdgcn_exp2f(d2.z - m), e3 = __builtin_amdgcn_exp2f(d2.w - m);
                const unsigned u0 = pk2(e0, e1), u1 = pk2(e2, e3);
                st16s(rs, ew, sb, (v4u){u0, u1, dppu<0x128>(u0), dppu<0x128>(u1)});
            } else BAR0();
            BAR0();
        }
        if (om == 14) { BAR0(); BAR0(); BAR0(); }
    } else {
        if (wave == 4) { *(v4u*)(lds + L_PART + lane * 16) = (v4u){0u, 0u, 0u, 0u}; *(v4u*)(lds + L_PART + 1024 + lane * 16) = (v4u){0u, 0u, 0u, 0u}; }
        int cal = get_cal(c, 0, budget);
        unsigned tref = now() - 65536;
        STAMP(idx == 0 && wave == 7, 4, 1);
        const bool stg = q == 3 || wave != 7, two = q == 3 && wave == 5;
        const int pr = L_PART + (lane & 31) * 8, pubo = lane < 32 && (lane & 3) == 0 ? HO_PART + idx * 128 + lane * 4 : OOB;
#pragma unroll 1
        for (int s = 0; s < NSEG && budget >= 0; ++s) {
            if (s == CAL_STEP) cal = get_cal(c, 1, budget);
            const int sb = slot_base(s, cal);
            if (stg) {
                wait_until(tref + T_PM);
                if (two) stage2(rs, po, 6144 + lane * 16, sb + HO_MEM, budget); else stage1(rs, po, sb + HO_MEM, budget);
            } else BAR0();
            STAMP(idx == 0 && wave == 7, 0, s); STAMP(idx == 3 && wave == 6, 12, s);
            asm volatile(PUBLISH_ASM(256) :: [pub] "s"(wave), [la] "v"(pr), [vo] "v"(pubo), [rs] "s"(rs), [so] "s"(sb) : "memory", "scc", VCLOB);
            tref = now();
            if (wave == 7) {
                STAMP(idx == 0, 1, s);
                LSTAMP(idx == 0 && (s == 1 || s == 32 || s == 62), 14, s);
                if (lane == 0) st4s(rs, flagoff(FL_T + idx), 0, s);
            }
        }
    }
    if (budget < 0) give_up(rs);
    VMWAIT(0);
    BAR();
    if (wave == 7 && lane == 0) st4s(rs, flagoff(FL_T + idx), 0, NSEG);
}

DEV void role_o(const Params& p, const Ctx& c, int t) {
    const int lane = c.lane, wave = c.wave, rho = c.rho, g = c.g;
    const rsrc_t rs = c.rs; char* lds = c.lds;
    constexpr int L_MEM = 0, L_OUT = 8192, L_DUMP = 12288, L_V = 13312;
    v4u w[2][14];
#pragma unroll
    for (int k = 0; k < 2; ++k)
#pragma unroll
        for (int ks = 0; ks < 14; ++ks) w[k][ks] = ld16(rs, OFF_BT + ((2 * wave + k) * 28 + t) * 14336 + lane * 16, ks * 1024, 0);
    int budget = SPIN_CAP;
    int cal = get_cal(c, 0, budget), caln = cal;
    const int sidx = (rho & 2) ? 0xEEEE : 0x4444;
    const int fa = L_MEM + g * 256 + ((rho >> 2) * 2 + (rho & 1)) * 32 + ((rho >> 1) & 1) * 16;
    const int hb = rho >> 3, hd = 2 * wave + hb, i = rho & 7;
    const int pa = HO_PART + hd * 512 + g * 128 + i * 16, ea = HO_E + hd * 512 + g * 128 + i * 16;
    const int ul = OFF_U + (t * 16 + 2 * wave) * 1024 + lane * 16;
    const int ufl = flagoff(FL_W + rho * 7 + ((14 + (t >> 1)) >> 2));
    const int mo = (lane & 3) == 0 ? HO_MEM + t * 256 + lane * 4 : OOB;
    const int p2 = (lane & 4) ? 2 * (lane & 3) + 1 : 2 * (lane & 3);
    const int ra = L_OUT + (lane >> 3) * 64 + p2 * 8;
    const int ow = hb == 0 ? L_OUT + wave * 512 + i * 64 + g * 16 : L_DUMP + lane * 16;
    const int po = wave * 1024 + lane * 16, po2 = min(wave + 4, 6) * 1024 + lane * 16;
    const v4u ones = {0x3f803f80u, 0x3f803f80u, 0x3f803f80u, 0x3f803f80u};
    const v2u ones2 = {0x3f803f80u, 0x3f803f80u};
    const unsigned mlo = hb ? 0u : 0xffffffffu, mhi = ~mlo;
    const unsigned long long hbm = 0xff00ff00ff00ff00ull;
    v4u psel; { const int kq = (rho >> 2) * 2 + (rho & 1); const unsigned one = (kq & 1) ? 0x3f800000u : 0x3f80u;
        psel = (v4u){(kq >> 1) == 0 ? one : 0u, (kq >> 1) == 1 ? one : 0u, (kq >> 1) == 2 ? one : 0u, (kq >> 1) == 3 ? one : 0u}; }
    asm volatile("v_mov_b32 v215, 0" ::: "v215");
    auto check_u = [&](int s) {
        unsigned long long lack; unsigned seen;
        asm volatile("v_cmp_ge_u32 %0, %1, v215" : "=s"(lack) : "s"(s) : "memory");
        if (lack != 0 && !wait_flags<1>(rs, ufl, s + 1, seen)) budget = -1;
    };
    auto load_u = [&](int s) {
        asm volatile("s_nop 4\n\tbuffer_load_dwordx4 v[216:219], %0, %1, %2 offen sc1\n\tbuffer_load_dwordx4 v[220:223], %0, %1, %2 offen offset:1024 sc1"
                     :: "v"(ul), "s"(rs), "s"(s * (NH * 28672)) : "memory", "v216", "v217", "v218", "v219", "v220", "v221", "v222", "v223");
    };
    auto store_v = [&](int s) { st16s(rs, OFF_V + t * 4096 + (wave & 3) * 1024 + lane * 16, s * (NH * 7168), lds16(lds, L_V + (s & 1) * 4096 + (wave & 3) * 1024 + lane * 16)); };
    ACC_DECL;
    unsigned tref = now() - 65536;
    auto mem_times_b = [&](int sb, int s, bool last) -> v2u {
        v4f c0 = {0, 0, 0, 0}, c1 = {0, 0, 0, 0};
        if (wave < 4) stage2(rs, po, po2, sb + HO_MEM, budget);
        else BAR0();
        ACC_T(2);
#undef AFTER_STEP
#define AFTER_STEP(f) if (f == S_AT && !last) { SCHED(); ACC_T(4); if (T_OS) wait_until(tref + T_OS); ACC_T(5); asm volatile("buffer_load_dwordx4 v[224:227], %0, %2, %3 offen sc1\n\tbuffer_load_dwordx4 v[228:231], %1, %2, %3 offen sc1" \
                          :: "v"(pa), "v"(ea), "s"(rs), "s"(sb) : "memory", VCLOB); SCHED(); }
#define EXTRA_READS if (wave < 4 && !last && ABL(1)) load_u(s)
        MEM_TIMES_W(fa)
#undef EXTRA_READS
        *(v2u*)(lds + L_V + (s & 1) * 4096 + wave * 512 + lane * 8) = tp;
        return tp;
    };
    STAMP(t == 0 && wave == 0, 4, 13);
    check_u(1);
    STAMP(t == 0 && wave == 0, 4, 14);
    if (wave >= 4) load_u(0);
#pragma unroll 1
    for (int s = 0; s < NSEG - 1 && budget >= 0; ++s) {
        const int sb = slot_base(s, s < CAL_STEP ? cal : caln), sn = slot_base(s + 1, s + 1 < CAL_STEP ? cal : caln);
        ACC_T(0);
        const v2u vp = mem_times_b(sb, s, false);
        ACC_T(3);
        asm volatile(
            "s_branch 3f\n\t"
            "1:\n\t"
            "buffer_load_dwordx4 v[224:227], %[pa], %[rs], %[sb] offen sc1\n\t"
            "buffer_load_dwordx4 v[228:231], %[ea], %[rs], %[sb] offen sc1\n\t"
            "3:\n\t"
            "s_add_i32 %[bud], %[bud], -1\n\ts_cmp_lt_i32 %[bud], 0\n\ts_cselect_b32 vcc_hi, -1, -2\n\tv_mov_b32 v253, vcc_hi\n\t"
            "s_waitcnt vmcnt(1)\n\t"
            "v_mfma_f32_16x16x32_bf16 v[232:235], %[psel], v[224:227], 0\n\t"
            "v_max3_u32 v250, v224, v225, v226\n\t"
            "s_waitcnt vmcnt(0)\n\t"
            "v_mfma_f32_16x16x32_bf16 v[244:247], %[ones], v[228:231], 0\n\t"
            "v_max3_u32 v251, v228, v229, v230\n\t"
            "v_mfma_f32_16x16x32_bf16 v[236:239], v[216:219], v[228:231], 0\n\t"
            "v_max3_u32 v250, v250, v227, v231\n\t"
            "v_mfma_f32_16x16x32_bf16 v[240:243], v[220:223], v[228:231], 0\n\t"
            "v_max_u32 v250, v250, v251\n\t"
            "v_cmp_gt_u32 vcc, v250, v253\n\t"
            "s_cbranch_vccnz 1b\n\t"
            "v_exp_f32 v250, v232\n\tv_exp_f32 v251, v233\n\t"
            "v_max_f32 v252, v232, v233\n\t"
            "v_cvt_pk_bf16_f32 v250, v250, v251\n\t"
            "v_cmp_lt_f32 vcc, 0x42800000, v252\n\t"
            "v_and_b32 v248, v250, %[mlo]\n\tv_and_b32 v249, v250, %[mhi]\n\t"
            "v_cndmask_b32 v236, v236, v240, %[hbm]\n\tv_cndmask_b32 v237, v237, v241, %[hbm]\n\t"
            "v_mfma_f32_16x16x16_bf16 v[244:247], %[ones2], v[248:249], v[244:247]\n\t"
            "v_cndmask_b32 v238, v238, v242, %[hbm]\n\tv_cndmask_b32 v239, v239, v243, %[hbm]\n\t"
            "s_nop 1\n\t"
            "v_mfma_f32_16x16x16_bf16 v[236:239], %[vp], v[248:249], v[236:239]\n\t"
            "s_cbranch_vccz 2f\n\t"
            "s_nop 7\n\t"
            "v_max_f32 v252, 0, v252\n\tv_mov_b32 v253, v252\n\ts_nop 1\n\tv_permlane16_swap_b32 v252, v253\n\ts_nop 1\n\t"
            "v_max_f32 v252, v252, v253\n\tv_mov_b32 v253, v252\n\ts_nop 1\n\tv_permlane32_swap_b32 v252, v253\n\ts_nop 1\n\t"
            "v_max_f32 v252, v252, v253\n\t"
            "v_mfma_f32_16x16x32_bf16 v[244:247], %[ones], v[228:231], 0\n\t"
            "v_mfma_f32_16x16x32_bf16 v[236:239], v[216:219], v[228:231], 0\n\t"
            "v_sub_f32 v250, v232, v252\n\tv_sub_f32 v251, v233, v252\n\t"
            "v_exp_f32 v250, v250\n\tv_exp_f32 v251, v251\n\tv_exp_f32 v253, -v252\n\ts_nop 0\n\t"
            "v_cvt_pk_bf16_f32 v250, v250, v251\n\t"
            "v_and_b32 v248, v250, %[mlo]\n\tv_and_b32 v249, v250, %[mhi]\n\ts_nop 7\n\t"
            "v_cndmask_b32 v236, v236, v240, %[hbm]\n\tv_cndmask_b32 v237, v237, v241, %[hbm]\n\tv_cndmask_b32 v238, v238, v242, %[hbm]\n\tv_cndmask_b32 v239, v239, v243, %[hbm]\n\t"
            "v_mul_f32 v236, v236, v253\n\tv_mul_f32 v237, v237, v253\n\tv_mul_f32 v238, v238, v253\n\tv_mul_f32 v239, v239, v253\n\t"
            "v_mul_f32 v244, v244, v253\n\tv_mul_f32 v245, v245, v253\n\tv_mul_f32 v246, v246, v253\n\tv_mul_f32 v247, v247, v253\n\ts_nop 1\n\t"
            "v_mfma_f32_16x16x16_bf16 v[244:247], %[ones2], v[248:249], v[244:247]\n\t"
            "v_mfma_f32_16x16x16_bf16 v[236:239], %[vp], v[248:249], v[236:239]\n\t"
            "s_nop 7\n\t"
            "2:\n\t"
            "s_nop 1\n\t"
            "v_rcp_f32 v252, v244\n\t"
            "s_nop 3\n\t"
            "v_mul_f32 v236, v236, v252\n\tv_mul_f32 v237, v237, v252\n\tv_mul_f32 v238, v238, v252\n\tv_mul_f32 v239, v239, v252\n\t"
            "v_add_f32_dpp v240, v236, v236 row_ror:8 row_mask:0xf bank_mask:0xf\n\tv_add_f32_dpp v241, v237, v237 row_ror:8 row_mask:0xf bank_mask:0xf\n\t"
            "v_add_f32_dpp v242, v238, v238 row_ror:8 row_mask:0xf bank_mask:0xf\n\tv_add_f32_dpp v243, v239, v239 row_ror:8 row_mask:0xf bank_mask:0xf\n\t"
            "ds_write_b128 %[ow], v[240:243]\n\t"
            PUBLISH_ASM(512)
            : [bud] "+s"(budget)
            : [pa] "v"(pa), [ea] "v"(ea), [rs] "s"(rs), [sb] "s"(sb), [psel] "v"(psel), [ones] "v"(ones), [ones2] "v"(ones2), [vp] "v"(vp),
              [mlo] "v"(mlo), [mhi] "v"(mhi), [hbm] "s"(hbm), [ow] "v"(ow),
              [pub] "s"(wave), [la] "v"(ra), [vo] "v"(mo), [so] "s"(sn)
            : "memory", "vcc", "scc", VCLOB);
        tref = now();
        STAMP(t == 0 && wave == 7, 3, s);
        if (wave == 7 && lane == 0) st4s(rs, flagoff(FL_V + t), 0, s);
        if (s + 2 == CAL_STEP) caln = get_cal(c, 1, budget);
        if (wave == 4) check_u(min(s + 2, NSEG - 2));
        ACC_T(6);
        if (wave < 4) wait_until(tref + T_OM);
        else {
            wait_until(tref + T_OU + (wave - 4) * T_ST);
            if (s + 1 < NSEG - 1 && ABL(1)) load_u(s + 1);
            if (wave == 4) asm volatile("buffer_load_dword v215, %0, %1, 0 offen sc1" :: "v"(ufl), "s"(rs) : "memory", "v215");
            if (ABL(2)) store_v(s);
        }
        ACC_T(7);
    }
    VMWAIT(0);
    ACC_OUT(t * 8 + wave);
    mem_times_b(slot_base(NSEG - 1, caln), NSEG - 1, true);
    if (budget < 0) give_up(rs);
    BAR();
    if (wave >= 4) store_v(NSEG - 1);
    VMWAIT(0);
    BAR0();
    if (wave == 7 && lane == 0) st4s(rs, flagoff(FL_V + t), 0, NSEG);
}

DEV void role_w(const Params& p, const Ctx& c, int wi) {
    const int lane = c.lane, wave = c.wave, rho = c.rho, g = c.g;
    const rsrc_t rs = c.rs; char* lds = c.lds;
    const int h = wi / 7, unit = (wi - h * 7) * 4 + (wave & 3), set = wave >> 2;
    const bool isG = unit < 14;
    auto stage = [&](int it) {
        const int sg = 2 * it + set, lb = (set * 2 + (it & 1)) * 28672;
#pragma unroll
        for (int k = 0; k < 7; ++k) { const int pc = (wave & 3) * 7 + k; dma16(rs, OFF_XF + lane * 16, sg * 28672 + pc * 1024, lb + pc * 1024); }
    };
    v4u w[2][14];
    auto load_w = [&]() {
#pragma unroll
        for (int t = 0; t < 2; ++t) {
            const int row = isG ? unit * 32 + pperm(rho, t) : (unit - 14) * 32 + t * 16 + rho;
#pragma unroll
            for (int ks = 0; ks < 14; ++ks) w[t][ks] = ld16(rs, (isG ? OFF_AP : OFF_BT) + (h * 28 + (row >> 4)) * 14336 + g * 256 + (row & 15) * 16, ks * 1024, 0);
        }
    };
    int xa[2];
#pragma unroll
    for (int k = 0; k < 2; ++k) { const int key = isG ? k * 16 + rho : pperm(rho, k), kt = key >> 4; xa[k] = kt * 14336 + (g * 16 + ((key & 15) ^ (kt * 4))) * 16; }
    const int ob = (isG ? OFF_G + (h * 14 + unit) * 2048 : OFF_U + ((unit - 14) * 32 + h) * 1024) + lane * 16;
    const int ostep = isG ? 1024 : 16384;
    auto segment = [&](int buf, int sg) {
        const char* xb = lds + buf * 28672;
        v4f a00 = {0, 0, 0, 0}, a01 = a00, a10 = a00, a11 = a00;
        if (isG) {
#pragma unroll
            for (int ks = 0; ks < 14; ++ks) { const v4u x0 = lds16(xb, xa[0] + ks * 1024), x1 = lds16(xb, xa[1] + ks * 1024);
                a00 = mfma32(w[0][ks], x0, a00); a01 = mfma32(w[1][ks], x0, a01); a10 = mfma32(w[0][ks], x1, a10); a11 = mfma32(w[1][ks], x1, a11); }
        } else {
#pragma unroll
            for (int ks = 0; ks < 14; ++ks) { const v4u x0 = lds16(xb, xa[0] + ks * 1024), x1 = lds16(xb, xa[1] + ks * 1024);
                a00 = mfma32(x0, w[0][ks], a00); a01 = mfma32(x1, w[0][ks], a01); a10 = mfma32(x0, w[1][ks], a10); a11 = mfma32(x1, w[1][ks], a11); }
        }
        st16s(rs, ob, sg * (NH * 28672), pk8(a00, a01)); st16s(rs, ob + ostep, sg * (NH * 28672), pk8(a10, a11));
    };
    if (set == 0) load_w();
    if (wave == 7) *(int*)(lds + LDS_MAIN - 32) = 0;
    stage(0);
    VMWAIT(0);
    BAR();
    STAMP(wi == 20 && wave == 1, 4, 2);
#pragma unroll 1
    for (int it = -1; it < 32; ++it) {
        if (it == -1 && set == 1) load_w();
        if (it >= 0 && it + 1 < 32) stage(it + 1);
        if (it > 0 || set == 0) segment(it > 0 ? set * 2 + (it & 1) : 2 * (it + 1), it > 0 ? 2 * it + set : it + 1);
        if (it < 0) continue;
        if (it == 0 && set == 0) {
            VMWAIT(0);
            if (lane == 0 && __hip_atomic_fetch_add((int*)(lds + LDS_MAIN - 32), 1, __ATOMIC_RELAXED, __HIP_MEMORY_SCOPE_WORKGROUP) == 3) st4s(rs, flagoff(FL_W + wi), 0, 2);
            STAMP(wi == 20 && wave == 1, 4, 15);
        }
        const bool eager = it < 4 || it == 31;
        if (eager) VMWAIT(0); else VMWAIT(2);
        BAR();
        if (wave == 0 && lane == 0) st4s(rs, flagoff(FL_W + wi), 0, eager ? 2 * it + 2 : 2 * it);
        if (it == 12 && wave == 0 && wi >= 48) {
            unsigned seen; wait_flags<8>(rs, flagoff(FL_TAB), 28, seen);
            __builtin_amdgcn_fence(__ATOMIC_ACQUIRE, "agent");
            VMWAIT(0);
        }
        STAMP(wi == 20 && wave == 1, 5, it); CSTAMP(wi == 20 && wave == 1, 6, it);
    }
}

DEV int f_first(int set, int wave) { return set ? 33 + 2 * wave : wave; }
DEV int f_next(int s, int wave) { return s >= 32 ? s + 16 : s >= 24 ? 32 + 2 * wave : s + 8; }
DEV void role_f1(const Params& p, const Ctx& c, int h, int set) {
    const int lane = c.lane, wave = c.wave, rho = c.rho, g = c.g;
    const rsrc_t rs = c.rs;
    const int gfl = flagoff(FL_W + h * 7 + (lane & 3)), tfl = flagoff(FL_T + h * 4 + (lane & 3));
    const int gl0 = OFF_G + h * 28672 + (rho >> 3) * 1024 + g * 256 + (pperm(rho, 0) & 15) * 16;
    const int tl = OFF_T + h * 7168 + g * 128 + ((rho >> 2) * 2 + (rho & 1)) * 16;
    const float ninf = -__builtin_inff();
    bool wdone = false;
#pragma unroll 1
    for (int s = f_first(set, wave); s < NSEG; s = f_next(s, wave)) {
        STAMP(h == 0, 16, s);
        unsigned seen;
        if (!wdone) { if (!wait_flags<8>(rs, gfl, s + 1, seen)) return; wdone = __builtin_amdgcn_ballot_w64(seen < NSEG) == 0; }
        v4u x[2][14];
        v4f a[2][2] = {{{0, 0, 0, 0}, {0, 0, 0, 0}}, {{0, 0, 0, 0}, {0, 0, 0, 0}}};
#pragma unroll
        for (int hf = 0; hf < 2; ++hf) {
            v4u y[2][7];
#pragma unroll
            for (int k = 0; k < 7; ++k) {
#pragma unroll
                for (int qt = 0; qt < 2; ++qt) x[qt][hf * 7 + k] = ld16(rs, OFF_XF + qt * 14336 + g * 256 + (rho ^ (qt * 4)) * 16, s * 28672 + (hf * 7 + k) * 1024, 0);
                y[0][k] = ld16(rs, gl0, s * (NH * 28672) + (hf * 7 + k) * 2048, 1); y[1][k] = ld16(rs, gl0 + 64, s * (NH * 28672) + (hf * 7 + k) * 2048, 1);
            }
            SCHED();
#pragma unroll
            for (int k = 0; k < 7; ++k)
#pragma unroll
                for (int qt = 0; qt < 2; ++qt) { a[qt][0] = mfma32(y[0][k], x[qt][hf * 7 + k], a[qt][0]); a[qt][1] = mfma32(y[1][k], x[qt][hf * 7 + k], a[qt][1]); }
        }
        float v[2][8], m[2];
#pragma unroll
        for (int qt = 0; qt < 2; ++qt) {
            const float r[8] = {a[qt][0].x, a[qt][0].y, a[qt][0].z, a[qt][0].w, a[qt][1].x, a[qt][1].y, a[qt][1].z, a[qt][1].w};
            m[qt] = ninf;
#pragma unroll
            for (int k = 0; k < 8; ++k) { v[qt][k] = (8 * g + k <= qt * 16 + rho) ? r[k] : ninf; m[qt] = fmx(m[qt], v[qt][k]); }
        }
        asm volatile("" : "+v"(m[0]), "+v"(m[1]));
        STAMP(h == 0, 17, s);
        if (!wait_flags<2>(rs, tfl, s + 1, seen)) return;
        STAMP(h == 0, 18, s);
        v4f am[2] = {{0, 0, 0, 0}, {0, 0, 0, 0}};
        { v4u y[14];
#pragma unroll
          for (int ks = 0; ks < 14; ++ks) y[ks] = ld16(rs, tl, s * (NH * 7168) + ks * 512, 1);
          SCHED();
#pragma unroll
          for (int ks = 0; ks < 14; ++ks) { am[0] = mfma32(y[ks], x[0][ks], am[0]); am[1] = mfma32(y[ks], x[1][ks], am[1]); } }
        unsigned pm[2];
#pragma unroll
        for (int qt = 0; qt < 2; ++qt) {
            const float mm = max_g(fmx(m[qt], fmx(am[qt].x, am[qt].y)));
            const float e0 = __builtin_amdgcn_exp2f(am[qt].x - mm), e1 = __builtin_amdgcn_exp2f(am[qt].y - mm);
            float z = e0 + e1;
#pragma unroll
            for (int k = 0; k < 8; ++k) { v[qt][k] = __builtin_amdgcn_exp2f(v[qt][k] - mm); z += v[qt][k]; }
            const float rz = __builtin_amdgcn_rcpf(sum_g(z));
            st16s(rs, OFF_PX + (h * 2 + qt) * 1024 + lane * 16, s * (NH * 2048),
                  (v4u){pk2(v[qt][0] * rz, v[qt][1] * rz), pk2(v[qt][2] * rz, v[qt][3] * rz), pk2(v[qt][4] * rz, v[qt][5] * rz), pk2(v[qt][6] * rz, v[qt][7] * rz)});
            pm[qt] = pk2(e0 * rz, e1 * rz);
        }
        st16s(rs, (lane & 1) == 0 ? OFF_PM + h * 512 + lane * 8 : OOB, s * (NH * 512), (v4u){pm[0], pm[1], dppu<0xF5>(pm[0]), dppu<0xF5>(pm[1])});
        VMWAIT(0);
        if (lane == 0) st4s(rs, flagoff((set ? FL_P2 : FL_P) + h * 8 + wave), 0, s + 1);
        STAMP(h == 0, 19, s);
    }
}

DEV void make_tables(const Params& p, const Ctx& c, int t) {
    const int lane = c.lane, wave = c.wave, rho = c.rho, g = c.g;
    const rsrc_t rs = c.rs;
    for (int ch = t * 512 + wave * 64 + lane; ch < 2 * 28672; ch += 28 * 512) {
        float v[8];
        if (ch < 28672) { const int fr = ch >> 6, tl = fr / 14, d0 = (fr - tl * 14) * 32 + g * 8, ri = tl >> 4, k = (tl & 15) * 16 + rho;
#pragma unroll
            for (int j = 0; j < 8; ++j) { const float ph = (float)((k * (d0 + j)) % 448) * (1.f / 448.f); v[j] = k > 224 ? 0.f : ri ? -__builtin_amdgcn_sinf(ph) : __builtin_amdgcn_cosf(ph); }
        } else { const int fr = (ch - 28672) >> 6, d = (fr >> 4) * 16 + rho, ri = (fr >> 3) & 1, k0 = (fr & 7) * 32 + g * 8;
#pragma unroll
            for (int j = 0; j < 8; ++j) { const int k = k0 + j; const float ph = (float)((k * d) % 448) * (1.f / 448.f); const bool edge = k == 0 || k == 224;
                const float wgt = (edge ? 1.f : 2.f) / (448.f * 2048.f); v[j] = k > 224 ? 0.f : ri ? (edge ? 0.f : -wgt * __builtin_amdgcn_sinf(ph)) : wgt * __builtin_amdgcn_cosf(ph); }
        }
        st16s(rs, OFF_FD + ch * 16, 0, (v4u){pk2(v[0], v[1]), pk2(v[2], v[3]), pk2(v[4], v[5]), pk2(v[6], v[7])});
    }
    VMWAIT(0);
    BAR();
    if (wave == 0 && lane == 0) __hip_atomic_fetch_add((unsigned*)(p.ws + flagoff(FL_TAB)), 1u, __ATOMIC_RELAXED, __HIP_MEMORY_SCOPE_AGENT);
}
DEV void role_f2(const Params& p, const Ctx& c, int t, int set) {
    const int lane = c.lane, wave = c.wave, rho = c.rho, g = c.g;
    const rsrc_t rs = c.rs;
    const int ufl = flagoff(FL_W + rho * 7 + ((14 + (t >> 1)) >> 2)), pfl = flagoff(lane < 16 ? (set ? FL_P2 : FL_P) + lane * 8 + wave : FL_V + t);
    const int ul = OFF_U + t * 16384 + lane * 16;
    bool wdone = false;
#pragma unroll 1
    for (int s = f_first(set, wave); s < NSEG; s = f_next(s, wave)) {
        unsigned seen;
        if (!wdone) { if (!wait_flags<8>(rs, ufl, s + 1, seen)) return; wdone = __builtin_amdgcn_ballot_w64(seen < NSEG) == 0; }
        STAMP(t == 0, 20, s);
        v4u u[16];
#pragma unroll
        for (int h = 0; h < 16; ++h) u[h] = ld16(rs, ul, s * (NH * 28672) + h * 1024, 1);
        if (!wait_flags<2>(rs, pfl, s + 1, seen)) return;
        STAMP(t == 0, 21, s);
        v4f a[2] = {{0, 0, 0, 0}, {0, 0, 0, 0}}, m[2] = {{0, 0, 0, 0}, {0, 0, 0, 0}};
        v4u px[16]; v2u pm[16], vv[8];
#pragma unroll
        for (int h = 0; h < 16; ++h) px[h] = ld16(rs, OFF_PX + lane * 16, s * (NH * 2048) + h * 2048, 1);
#pragma unroll
        for (int h = 0; h < 16; ++h) { pm[h] = ld8s(rs, OFF_PM + lane * 8, s * (NH * 512) + h * 512); if (h & 1) vv[h >> 1] = ld8s(rs, OFF_V + t * 4096 + lane * 8, s * (NH * 7168) + (h >> 1) * 512); }
        SCHED();
#pragma unroll
        for (int h = 0; h < 16; ++h) { a[0] = mfma32(u[h], px[h], a[0]); SCHED(); px[h] = ld16(rs, OFF_PX + 1024 + lane * 16, s * (NH * 2048) + h * 2048, 1); SCHED(); }
#pragma unroll
        for (int k = 0; k < 8; ++k) { m[0] = mfma16(vv[k], (v2u){pm[2 * k].x, pm[2 * k + 1].x}, m[0]); m[1] = mfma16(vv[k], (v2u){pm[2 * k].y, pm[2 * k + 1].y}, m[1]); }
        SCHED();
#pragma unroll
        for (int h = 0; h < 16; ++h) a[1] = mfma32(u[h], px[h], a[1]);
#pragma unroll
        for (int qt = 0; qt < 2; ++qt) { const v4f o = a[qt] + m[qt];
            *(v2u*)(p.out + (size_t)(s * 32 + qt * 16 + rho) * 896 + DM + t * 16 + g * 4) = (v2u){pk2(o.x, o.y), pk2(o.z, o.w)}; }
        STAMP(t == 0, 22, s);
    }
}

DEV bool fft_sync(const Params& p, const Ctx& c, int fl) {
    VMWAIT(0);
    BAR();
    int* okw = (int*)(c.lds + LDS_MAIN - 16);
    if (c.wave == 0) {
        if (c.lane == 0) __hip_atomic_fetch_add((unsigned*)(p.ws + flagoff(fl)), 1u, __ATOMIC_RELAXED, __HIP_MEMORY_SCOPE_AGENT);
        unsigned seen; const bool ok = wait_flags<2>(c.rs, flagoff(fl), 64, seen);
        if (c.lane == 0) *okw = ok;
    }
    BAR();
    return *okw != 0;
}
DEV v4u neg8(v4u v) { return v ^ 0x80008000u; }
DEV void twid(int a, const int (&n)[8], int mask, float inv, v4u& C, v4u& S) {
    float cs[8], sn[8];
#pragma unroll
    for (int j = 0; j < 8; ++j) { const float ph = (float)((a * n[j]) & mask) * inv; cs[j] = __builtin_amdgcn_cosf(ph); sn[j] = __builtin_amdgcn_sinf(ph); }
    C = (v4u){pk2(cs[0], cs[1]), pk2(cs[2], cs[3]), pk2(cs[4], cs[5]), pk2(cs[6], cs[7])};
    S = (v4u){pk2(sn[0], sn[1]), pk2(sn[2], sn[3]), pk2(sn[4], sn[5]), pk2(sn[6], sn[7])};
}
DEV void role_fft(const Params& p, const Ctx& c, int job) {
    const int lane = c.lane, wave = c.wave, rho = c.rho, g = c.g;
    const rsrc_t rs = c.rs; char* lds = c.lds;
    const v4f z4 = {0, 0, 0, 0};
    STAMP(wave == 1, 23, job);
    {
        const rsrc_t rx = mkrsrc(p.xa, 2048 * DM * 2);
        for (int pc = wave; pc < 28; pc += 8) { const int nt = pc >= 14, ks = pc - nt * 14; dma16(rx, ((64 * (nt * 16 + rho) + job) * DM + ks * 32 + g * 8) * 2, 0, pc * 1024); }
        STAMP(wave == 1, 24, job);
        v4u C[2], S[2];
        { int n[8];
#pragma unroll
          for (int j = 0; j < 8; ++j) n[j] = 64 * (16 * (j >> 2) + 4 * g + (j & 3)) + job;
          twid(rho, n, 2047, 1.f / 2048.f, C[0], S[0]); twid(16 + rho, n, 2047, 1.f / 2048.f, C[1], S[1]); }
        VMWAIT(0);
        BAR();
        v4f zr[2][2], zi[2][2];
#pragma unroll
        for (int t = 0; t < 2; ++t) {
            const int fo = OFF_FD + (wave * 2 + (rho >> 3)) * 14336 + g * 256 + (pperm(rho, t) & 15) * 16;
            v4f r0 = z4, r1 = z4, i0 = z4, i1 = z4;
#pragma unroll
            for (int hf = 0; hf < 2; ++hf) {
                v4u fr[7], fi[7];
#pragma unroll
                for (int k = 0; k < 7; ++k) { fr[k] = ld16(rs, fo, (hf * 7 + k) * 1024, 0); fi[k] = ld16(rs, fo, 256 * DM * 2 + (hf * 7 + k) * 1024, 0); }
                SCHED();
#pragma unroll
                for (int k = 0; k < 7; ++k) { const v4u x0 = lds16(lds, (hf * 7 + k) * 1024 + lane * 16), x1 = lds16(lds, (14 + hf * 7 + k) * 1024 + lane * 16);
                    r0 = mfma32(x0, fr[k], r0); r1 = mfma32(x1, fr[k], r1); i0 = mfma32(x0, fi[k], i0); i1 = mfma32(x1, fi[k], i1); }
            }
            const v4u yr = pk8(r0, r1), yi = pk8(i0, i1), nr = neg8(yr);
#pragma unroll
            for (int ct = 0; ct < 2; ++ct) { zr[ct][t] = mfma32(yi, S[ct], mfma32(yr, C[ct], z4)); zi[ct][t] = mfma32(nr, S[ct], mfma32(yi, C[ct], z4)); }
        }
        char* O = lds + 32768;
#pragma unroll
        for (int ct = 0; ct < 2; ++ct) { const int o = (ct * 16 + rho) * 1040 + wave * 64 + g * 16;
            *(v4u*)(O + o) = pk8(zr[ct][0], zr[ct][1]); *(v4u*)(O + o + 512) = pk8(zi[ct][0], zi[ct][1]); }
        BAR();
#pragma unroll
        for (int k = 0; k < 4; ++k) st16s(rs, OFF_Z1 + job * 1024 + lane * 16, (wave * 4 + k) * 65536, lds16(O, (wave * 4 + k) * 1040 + lane * 16));
    }
    STAMP(job == 0 && wave == 1, 4, 3); STAMP(wave == 1, 25, job);
    if (!fft_sync(p, c, FL_FA)) return;
    STAMP(job == 0 && wave == 1, 4, 4); STAMP(wave == 1, 26, job);
    if (wave < 4) {
        const int k1 = job >> 1, P = (job & 1) * 4 + wave;
        char* L = lds + wave * 8192;
#pragma unroll
        for (int pc = 0; pc < 8; ++pc) dma16s(rs, OFF_Z1 + (k1 * 64 + (pc & 3) * 16 + (lane >> 2)) * 1024 + (pc >> 2) * 512 + P * 64 + (lane & 3) * 16, 0, wave * 8192 + pc * 1024);
        v4u C[4][2], S[4][2];
#pragma unroll
        for (int ks = 0; ks < 2; ++ks) { int n[8];
#pragma unroll
            for (int j = 0; j < 8; ++j) n[j] = 32 * ks + 16 * (j >> 2) + 4 * g + (j & 3);
#pragma unroll
            for (int kt = 0; kt < 4; ++kt) twid(16 * kt + rho, n, 63, 1.f / 64.f, C[kt][ks], S[kt][ks]); }
        VMWAIT(0);
        v2u hr[4], hi[4];
#pragma unroll
        for (int t = 0; t < 2; ++t) {
            const int k = P * 32 + pperm(rho, t);
            v4u xr[2], xi[2];
#pragma unroll
            for (int ks = 0; ks < 2; ++ks) { const int a = (32 * ks + 4 * g + (rho >> 2)) * 64 + (rho & 3) * 16 + t * 8;
                const v2u a0 = ldstr(L, a), a1 = ldstr(L, a + 1024), b0 = ldstr(L, a + 4096), b1 = ldstr(L, a + 5120);
                xr[ks] = (v4u){a0.x, a0.y, a1.x, a1.y}; xi[ks] = (v4u){b0.x, b0.y, b1.x, b1.y}; }
            const v4u n0 = neg8(xr[0]), n1 = neg8(xr[1]);
            unsigned short fr[16], fi[16];
#pragma unroll
            for (int j = 0; j < 16; ++j) { const int o = (k1 + 32 * (16 * (j >> 2) + 4 * g + (j & 3))) * 225 + min(k, 224); fr[j] = p.wr[o]; fi[j] = p.wi[o]; }
            SCHED();
            v4f vr[4], vi[4];
#pragma unroll
            for (int kt = 0; kt < 4; ++kt) {
                const v4f ar = mfma32(S[kt][1], xi[1], mfma32(S[kt][0], xi[0], mfma32(C[kt][1], xr[1], mfma32(C[kt][0], xr[0], z4))));
                const v4f ai = mfma32(S[kt][1], n1, mfma32(S[kt][0], n0, mfma32(C[kt][1], xi[1], mfma32(C[kt][0], xi[0], z4))));
                float wr[4], wi[4];
#pragma unroll
                for (int r = 0; r < 4; ++r) { wr[r] = __uint_as_float((unsigned)fr[kt * 4 + r] << 16); wi[r] = __uint_as_float((unsigned)fi[kt * 4 + r] << 16); }
                vr[kt] = (v4f){ar.x * wr[0] - ai.x * wi[0], ar.y * wr[1] - ai.y * wi[1], ar.z * wr[2] - ai.z * wi[2], ar.w * wr[3] - ai.w * wi[3]};
                vi[kt] = (v4f){ar.x * wi[0] + ai.x * wr[0], ar.y * wi[1] + ai.y * wr[1], ar.z * wi[2] + ai.z * wr[2], ar.w * wi[3] + ai.w * wr[3]};
            }
            const v4u pr0 = pk8(vr[0], vr[1]), pr1 = pk8(vr[2], vr[3]), pi0 = pk8(vi[0], vi[1]), pi1 = pk8(vi[2], vi[3]), ni0 = neg8(pi0), ni1 = neg8(pi1);
#pragma unroll
            for (int nt = 0; nt < 4; ++nt) {
                const v4f rr = mfma32(ni1, S[nt][1], mfma32(ni0, S[nt][0], mfma32(pr1, C[nt][1], mfma32(pr0, C[nt][0], z4))));
                const v4f ri = mfma32(pr1, S[nt][1], mfma32(pr0, S[nt][0], mfma32(pi1, C[nt][1], mfma32(pi0, C[nt][0], z4))));
                const v2u qr = {pk2(rr.x, rr.y), pk2(rr.z, rr.w)}, qi = {pk2(ri.x, ri.y), pk2(ri.z, ri.w)};
                if (t == 0) { hr[nt] = qr; hi[nt] = qi; }
                else { char* o = lds + 32768 + (nt * 16 + rho) * 528 + wave * 64 + g * 16;
                    *(v4u*)o = (v4u){hr[nt].x, hr[nt].y, qr.x, qr.y}; *(v4u*)(o + 256) = (v4u){hi[nt].x, hi[nt].y, qi.x, qi.y}; }
            }
        }
    }
    BAR();
#pragma unroll
    for (int k = 0; k < 4; ++k) { const int n2 = wave * 8 + k * 2 + (lane >> 5);
        st16s(rs, OFF_Z2 + (n2 * 32 + (job >> 1)) * 1024 + (g & 1) * 512 + (job & 1) * 256 + rho * 16, 0, lds16(lds, 32768 + n2 * 528 + (lane & 31) * 16)); }
    STAMP(job == 0 && wave == 1, 4, 5); STAMP(wave == 1, 27, job);
    if (!fft_sync(p, c, FL_FB)) return;
    STAMP(job == 0 && wave == 1, 4, 6); STAMP(wave == 1, 28, job);
    {
        char* L = lds + wave * 4096;
#pragma unroll
        for (int pc = 0; pc < 4; ++pc) dma16s(rs, OFF_Z2 + (job * 32 + (pc & 1) * 16 + (lane >> 2)) * 1024 + (pc >> 1) * 512 + wave * 64 + (lane & 3) * 16, 0, wave * 4096 + pc * 1024);
        v4u C[2], S[2];
        { int n[8];
#pragma unroll
          for (int j = 0; j < 8; ++j) n[j] = 8 * g + j;
          twid(64 * rho + job, n, 2047, 1.f / 2048.f, C[0], S[0]); twid(64 * (16 + rho) + job, n, 2047, 1.f / 2048.f, C[1], S[1]); }
        VMWAIT(0);
        v4f rr[2][2], ri[2][2];
#pragma unroll
        for (int t = 0; t < 2; ++t) {
            const int a = (8 * g + (rho >> 2)) * 64 + (rho & 3) * 16 + t * 8;
            const v2u a0 = ldstr(L, a), a1 = ldstr(L, a + 256), b0 = ldstr(L, a + 2048), b1 = ldstr(L, a + 2304);
            const v4u xr = {a0.x, a0.y, a1.x, a1.y}, xi = {b0.x, b0.y, b1.x, b1.y}, ni = neg8(xi);
#pragma unroll
            for (int nt = 0; nt < 2; ++nt) { rr[nt][t] = mfma32(ni, S[nt], mfma32(xr, C[nt], z4)); ri[nt][t] = mfma32(xr, S[nt], mfma32(xi, C[nt], z4)); }
        }
        char* R = lds + 32768;
#pragma unroll
        for (int nt = 0; nt < 2; ++nt) { *(v4u*)(R + (nt * 16 + wave) * 1024 + lane * 16) = pk8(rr[nt][0], rr[nt][1]); *(v4u*)(R + (nt * 16 + 8 + wave) * 1024 + lane * 16) = pk8(ri[nt][0], ri[nt][1]); }
        BAR();
#pragma unroll 1
        for (int Q = wave; Q < 14; Q += 8) {
            v4f y[2][2] = {{z4, z4}, {z4, z4}};
#pragma unroll
            for (int hf = 0; hf < 2; ++hf) {
                v4u f0[8], f1[8];
#pragma unroll
                for (int k = 0; k < 8; ++k) { const int fo = OFF_FI + (Q * 2 + (rho >> 3)) * 16384 + g * 256 + (pperm(rho, 0) & 15) * 16; f0[k] = ld16(rs, fo, (hf * 8 + k) * 1024, 0); f1[k] = ld16(rs, fo + 64, (hf * 8 + k) * 1024, 0); }
                SCHED();
#pragma unroll
                for (int k = 0; k < 8; ++k) { const v4u b0 = lds16(R, (hf * 8 + k) * 1024 + lane * 16), b1 = lds16(R, (16 + hf * 8 + k) * 1024 + lane * 16);
                    y[0][0] = mfma32(f0[k], b0, y[0][0]); y[0][1] = mfma32(f1[k], b0, y[0][1]); y[1][0] = mfma32(f0[k], b1, y[1][0]); y[1][1] = mfma32(f1[k], b1, y[1][1]); }
            }
#pragma unroll
            for (int nt = 0; nt < 2; ++nt) *(v4u*)(p.out + (size_t)(64 * (nt * 16 + rho) + job) * 896 + Q * 32 + g * 8) = pk8(y[nt][0], y[nt][1]);
        }
    }
}

extern "C" __global__ void __launch_bounds__(512, 2) k_main(Params p) {
    extern __shared__ __attribute__((aligned(16))) char lds[];
    Ctx c; c.rs = mkrsrc(p.ws, WS_BYTES); c.lds = lds; c.lane = threadIdx.x & 63; c.wave = __builtin_amdgcn_readfirstlane(threadIdx.x >> 6); c.rho = c.lane & 15; c.g = c.lane >> 4;
    { const int lane = c.lane; (void)lane; STAMP(blockIdx.x == 0 && c.wave == 7, 4, 0); }
    const int rot = *(const int*)(p.ws + flagoff(FL_ROT));
    const int x = (blockIdx.x + rot) & 7, y = blockIdx.x >> 3;
    if (x < 2) role_p(p, c, x * 32 + y);
    else if (x == 2) { if (y < 28) role_o(p, c, y); }
    else if (x == 3 && y == 28) role_cal(p, c);
    else {
        const int wi = (x - 4) * 32 + y - 16;
        if (x == 3) { if (y < 28) make_tables(p, c, y); } else if (wi >= 0) role_w(p, c, wi);
        if (wi >= 48) role_fft(p, c, wi - 48);
        else if (x == 3 ? y < 28 : wi >= 16 && wi < 44) role_f2(p, c, x == 3 ? y : wi - 16, x != 3);
        else if (x == 4) role_f1(p, c, y & 15, wi >= 0);
    }
    { const int lane = c.lane; (void)lane; LSTAMP(c.wave == 7, 8 + (blockIdx.x >> 6), blockIdx.x & 63); }
}

extern "C" void kernel_launch(void* const* d_in, const int* in_sizes, int n_in, void* d_out, int out_size, void* d_ws, size_t ws_size, hipStream_t stream) {
    Params p;
    p.xa = (const unsigned short*)d_in[0]; p.wr = (const unsigned short*)d_in[1]; p.wi = (const unsigned short*)d_in[2];
    p.xb = (const unsigned short*)d_in[3]; p.mem0 = (const unsigned short*)d_in[4];
    p.wq = (const unsigned short*)d_in[5]; p.wk = (const unsigned short*)d_in[6]; p.wv = (const unsigned short*)d_in[7]; p.wo = (const unsigned short*)d_in[8];
    p.out = (unsigned short*)d_out;
    p.ws = (char*)d_ws + (((size_t)2 << 20) - ((uintptr_t)d_ws & (((size_t)2 << 20) - 1))) % ((size_t)2 << 20);
    (void)hipFuncSetAttribute((const void*)k_main, hipFuncAttributeMaxDynamicSharedMemorySize, LDS_MAIN);
    (void)hipFuncSetAttribute((const void*)k_pro, hipFuncAttributeMaxDynamicSharedMemorySize, LDS_PRO);
    hipLaunchKernelGGL(k_pro, dim3(256), dim3(512), LDS_PRO, stream, p);
    hipLaunchKernelGGL(k_main, dim3(256), dim3(512), LDS_MAIN, stream, p);
}
```

```cpp
#define STAMPS
#include <stdio.h>
#include <hip/hip_runtime.h>
#include <hip/hip_bf16.h>
#include <stdint.h>

typedef unsigned v4u __attribute__((ext_vector_type(4)));
typedef unsigned v2u __attribute__((ext_vector_type(2)));
typedef float v4f __attribute__((ext_vector_type(4)));
typedef float v2f __attribute__((ext_vector_type(2)));
typedef short v4s __attribute__((ext_vector_type(4)));
typedef __bf16 v2bf __attribute__((ext_vector_type(2)));
typedef __bf16 v8bf __attribute__((ext_vector_type(8)));
typedef __bf16 v16bf __attribute__((ext_vector_type(16)));
typedef unsigned v8u __attribute__((ext_vector_type(8)));
typedef __amdgpu_buffer_rsrc_t rsrc_t;
#define LAS __attribute__((address_space(3)))
#define DEV __device__ __forceinline__

constexpr int NSEG = 64, NH = 16, DM = 448;
constexpr unsigned SENT = 0xFFFFFFFFu;
constexpr int OOB = 0x7fffffff;
constexpr int SC1 = 16;
constexpr int OFF_HO = 0;
constexpr int OFF_CTL = 4 << 20;
constexpr int OFF_AP = OFF_CTL + 65536;
constexpr int OFF_BT = OFF_AP + NH * DM * DM * 2;
constexpr int OFF_G = OFF_BT + NH * DM * DM * 2;
constexpr int OFF_U = OFF_G + NSEG * NH * 28672;
constexpr int OFF_T = OFF_U + NSEG * NH * 28672;
constexpr int OFF_V = OFF_T + NSEG * NH * 7168;
constexpr int OFF_PX = OFF_V + NSEG * NH * 7168;
constexpr int OFF_PM = OFF_PX + NSEG * NH * 2048;
constexpr int OFF_FD = OFF_PM + NSEG * NH * 512;
constexpr int OFF_FI = OFF_FD + 512 * DM * 2;
constexpr int OFF_Z1 = OFF_FI + 512 * DM * 2;
constexpr int OFF_Z2 = OFF_Z1 + (2 << 20);
constexpr int OFF_XF = OFF_Z2 + (2 << 20);
constexpr int OFF_DBG = OFF_XF + (2 << 20);
constexpr int WS_BYTES = OFF_DBG + (1 << 20);
constexpr int HO_MEM = 512, HO_PART = 16384, HO_E = 32768;
constexpr int OFF_PROBE = OFF_HO + 49152;
constexpr int FL_W = 0, FL_T = 112, FL_V = 176, FL_P = 208, FL_ROT = 336, FL_TAB = 337, FL_FA = 338, FL_FB = 339, FL_ABORT = 340, FL_CAL = 341, FL_P2 = 344;
constexpr int LDS_MAIN = 114688 + 2048;
constexpr int LDS_PRO = 114688;
constexpr int SPIN_CAP = 1 << 17;

struct Params {
    const unsigned short* xa; const unsigned short* wr; const unsigned short* wi;
    const unsigned short* xb; const unsigned short* mem0;
    const unsigned short* wq; const unsigned short* wk; const unsigned short* wv; const unsigned short* wo;
    unsigned short* out; char* ws;
};

DEV unsigned pk2(float a, float b) { v2bf r = {(__bf16)a, (__bf16)b}; return __builtin_bit_cast(unsigned, r); }
DEV v4u pk8(v4f a, v4f b) { return (v4u){pk2(a.x, a.y), pk2(a.z, a.w), pk2(b.x, b.y), pk2(b.z, b.w)}; }
DEV v4f mfma32(v4u a, v4u b, v4f c) { return __builtin_amdgcn_mfma_f32_16x16x32_bf16(__builtin_bit_cast(v8bf, a), __builtin_bit_cast(v8bf, b), c, 0, 0, 0); }
DEV v4f mfma16(v2u a, v2u b, v4f c) { return __builtin_amdgcn_mfma_f32_16x16x16bf16_1k(__builtin_bit_cast(v4s, a), __builtin_bit_cast(v4s, b), c, 0, 0, 0); }
DEV v4f smf(v4u a, v4u b0, v4u b1, v4f c, int idx) {
    v8u b = {b0.x, b0.y, b0.z, b0.w, b1.x, b1.y, b1.z, b1.w};
    return __builtin_amdgcn_smfmac_f32_16x16x64_bf16(__builtin_bit_cast(v8bf, a), __builtin_bit_cast(v16bf, b), c, idx, 0, 0);
}
DEV float fmx(float a, float b) { return __builtin_elementwise_maximum(a, b); }
DEV float max_g(float s) {
    auto r = __builtin_amdgcn_permlane16_swap(__float_as_uint(s), __float_as_uint(s), false, false);
    s = fmx(__uint_as_float(r[0]), __uint_as_float(r[1]));
    auto q = __builtin_amdgcn_permlane32_swap(__float_as_uint(s), __float_as_uint(s), false, false);
    return fmx(__uint_as_float(q[0]), __uint_as_float(q[1]));
}
DEV float sum_g(float s) {
    auto r = __builtin_amdgcn_permlane16_swap(__float_as_uint(s), __float_as_uint(s), false, false);
    s = __uint_as_float(r[0]) + __uint_as_float(r[1]);
    auto q = __builtin_amdgcn_permlane32_swap(__float_as_uint(s), __float_as_uint(s), false, false);
    return __uint_as_float(q[0]) + __uint_as_float(q[1]);
}
DEV float ror8(float s) { return __int_as_float(__builtin_amdgcn_mov_dpp(__float_as_int(s), 0x128, 0xf, 0xf, true)); }
template <int CTRL> DEV unsigned dppu(unsigned x) { return (unsigned)__builtin_amdgcn_mov_dpp((int)x, CTRL, 0xf, 0xf, true); }
DEV v4u quad4(unsigned x) { return (v4u){x, dppu<0x55>(x), dppu<0xAA>(x), dppu<0xFF>(x)}; }
DEV unsigned umax4(v4u v) { return max(max(v.x, v.y), max(v.z, v.w)); }
DEV rsrc_t mkrsrc(const void* p, unsigned bytes) { return __builtin_amdgcn_make_buffer_rsrc((void*)p, 0, bytes, 0x00020000); }
DEV v4u ld16(rsrc_t r, int vo, int so, int aux) { return aux ? __builtin_amdgcn_raw_buffer_load_b128(r, vo, so, SC1) : __builtin_amdgcn_raw_buffer_load_b128(r, vo, so, 0); }
DEV v2u ld8s(rsrc_t r, int vo, int so) { return __builtin_amdgcn_raw_buffer_load_b64(r, vo, so, SC1); }
DEV unsigned ld4s(rsrc_t r, int vo, int so) { return __builtin_amdgcn_raw_buffer_load_b32(r, vo, so, SC1); }
DEV void st16s(rsrc_t r, int vo, int so, v4u v) {
    __builtin_amdgcn_raw_buffer_store_b128(v, r, vo, so, SC1); asm volatile("s_nop 1" :: "v"(v) : "memory");
}
DEV void st8s(rsrc_t r, int vo, int so, v2u v) { __builtin_amdgcn_raw_buffer_store_b64(v, r, vo, so, SC1); }
DEV void st4s(rsrc_t r, int vo, int so, unsigned v) { __builtin_amdgcn_raw_buffer_store_b32(v, r, vo, so, SC1); }
DEV void dma16(rsrc_t r, int vo, int so, int ldsaddr) {
    unsigned keep;
    asm volatile("s_mov_b32 %0, m0\n\ts_mov_b32 m0, %4\n\ts_nop 4\n\tbuffer_load_dwordx4 %1, %2, %3 offen lds\n\ts_mov_b32 m0, %0"
                 : "=&s"(keep) : "v"(vo), "s"(r), "s"(__builtin_amdgcn_readfirstlane(so)), "s"(__builtin_amdgcn_readfirstlane(ldsaddr)) : "memory");
}
DEV void dma16s(rsrc_t r, int vo, int so, int ldsaddr) {
    unsigned keep;
    asm volatile("s_mov_b32 %0, m0\n\ts_mov_b32 m0, %4\n\ts_nop 4\n\tbuffer_load_dwordx4 %1, %2, %3 offen sc1 lds\n\ts_mov_b32 m0, %0"
                 : "=&s"(keep) : "v"(vo), "s"(r), "s"(__builtin_amdgcn_readfirstlane(so)), "s"(__builtin_amdgcn_readfirstlane(ldsaddr)) : "memory");
}
#define BAR() asm volatile("s_waitcnt lgkmcnt(0)\n\ts_barrier" ::: "memory")
#define BAR0() asm volatile("s_barrier" ::: "memory")
#define VMWAIT(n) asm volatile("s_waitcnt vmcnt(" #n ")" ::: "memory")
#define FENCE() asm volatile("" ::: "memory")
#define SCHED() __builtin_amdgcn_sched_barrier(0)
DEV v4u lds16(const char* l, int a) { return *(const v4u*)(l + a); }
DEV v2u lds8(const char* l, int a) { return *(const v2u*)(l + a); }
DEV v2u ldstr(const char* l, int a) { v4s r = __builtin_amdgcn_ds_read_tr16_b64_v4i16((LAS v4s*)(l + a)); return __builtin_bit_cast(v2u, r); }
template <int N> DEV void slp() { if constexpr (N > 0) __builtin_amdgcn_s_sleep(N); }
DEV int flagoff(int i) { return OFF_CTL + i * 128; }
template <int SL> DEV bool wait_flags(rsrc_t rs, int vo, unsigned need, unsigned& seen) {
    for (int sp = 0;; ++sp) {
        FENCE();
        seen = ld4s(rs, vo, 0);
        if (__builtin_amdgcn_ballot_w64(seen < need) == 0) return true;
        slp<SL>();
        if ((sp & 255) == 255) {
            if (sp > SPIN_CAP) st4s(rs, flagoff(FL_ABORT), 0, 1u);
            if (sp > SPIN_CAP || __builtin_amdgcn_readfirstlane(ld4s(rs, flagoff(FL_ABORT), 0))) return false;
        }
    }
}
DEV int img(int p) { return ((p & 7) >> 1) * 4 + (p >> 3) * 2 + (p & 1); }
DEV int pperm(int rho, int tau) { return (rho >> 2) * 8 + tau * 4 + (rho & 3); }

#define STAMP(cond, slot, s) do { if ((cond) && lane == 0) { unsigned long long t_ = __builtin_amdgcn_s_memrealtime(); __hip_atomic_store((unsigned long long*)(p.ws + OFF_DBG + ((slot) * 64 + (s)) * 8), t_, __ATOMIC_RELAXED, __HIP_MEMORY_SCOPE_AGENT); } } while (0)
#define LSTAMP(cond, slot, s) do { if ((cond) && lane == 0) { unsigned long long t_ = __builtin_amdgcn_s_memrealtime(); __hip_atomic_store((unsigned long long*)(p.ws + OFF_DBG + ((slot) * 64 + (s)) * 8), t_, __ATOMIC_RELAXED, __HIP_MEMORY_SCOPE_AGENT); } } while (0)
#define ACC_DECL
#define ACC_T(k)
#define ACC_OUT(slot)
#define CSTAMP(cond, slot, s) do { if ((cond) && lane == 0) { unsigned long long t_ = __builtin_amdgcn_s_memtime(); __hip_atomic_store((unsigned long long*)(p.ws + OFF_DBG + ((slot) * 64 + (s)) * 8), t_, __ATOMIC_RELAXED, __HIP_MEMORY_SCOPE_AGENT); } } while (0)

extern "C" __global__ void __launch_bounds__(512, 2) k_pro(Params p) {
    extern __shared__ __attribute__((aligned(16))) char lds[];
    const int b = blockIdx.x, tid = threadIdx.x, lane = tid & 63, wave = __builtin_amdgcn_readfirstlane(tid >> 6);
    const int rho = lane & 15, g = lane >> 4;
    const int gemm = (b & 7) * 4 + (b >> 6), sub = (b >> 3) & 7, which = gemm & 1, h = gemm >> 1;
    const int b0 = 224 * (sub & 1), c0 = 112 * (sub >> 1);
    const rsrc_t rs = mkrsrc(p.ws, WS_BYTES);
    LSTAMP(b == 0 && wave == 0, 4, 8); STAMP(wave == 0 && b < 64, 29, b);
    const int lr = lane >> 2, lc = ((lane & 3) ^ ((0 - g) & 3)) * 8, rd = rho * 64 + ((g ^ ((0 - (rho >> 2)) & 3)) * 16);
    if (which == 0) {
        const rsrc_t rq = mkrsrc(p.wq, DM * 4096 * 2);
        for (int k = wave; k < 56; k += 8) dma16(rq, ((c0 + (k >> 3) * 16 + lr) * 4096 + h * 256 + (k & 7) * 32 + lc) * 2, 0, k * 1024);
    } else {
        const rsrc_t ro = mkrsrc(p.wo, DM * 4096 * 2);
        for (int k = wave; k < 56; k += 8) { const int ch = k * 64 + lane, e = ch / 14, cc = ch - e * 14; dma16(ro, ((h * 256 + e) * DM + c0 + cc * 8) * 2, 0, k * 1024); }
    }
    v4u a[2][8];
    char* la = lds + 57344 + wave * 8192;
    if (wave < 7) {
        const rsrc_t ra = mkrsrc(which ? p.wv : p.wk, DM * 4096 * 2);
#pragma unroll
        for (int t = 0; t < 2; ++t) {
            const int src = ((b0 + 32 * wave + pperm(lr, t)) * 4096 + h * 256 + lc) * 2;
#pragma unroll
            for (int ks = 0; ks < 4; ++ks) { dma16(ra, src, ks * 64, 57344 + wave * 8192 + (t * 4 + ks) * 1024); a[t][4 + ks] = ld16(ra, src, 256 + ks * 64, 0); }
        }
        VMWAIT(0);
#pragma unroll
        for (int k = 0; k < 8; ++k) a[k >> 2][k & 3] = lds16(la, k * 1024 + rd);
        asm volatile("s_waitcnt lgkmcnt(0)" : "+v"(a[0][0]), "+v"(a[0][1]), "+v"(a[0][2]), "+v"(a[0][3]), "+v"(a[1][0]), "+v"(a[1][1]), "+v"(a[1][2]), "+v"(a[1][3]) :: "memory");
#pragma unroll
        for (int k = 0; k < 8; ++k) *(v4u*)(la + k * 1024 + lane * 16) = a[k >> 2][4 + (k & 3)];
        FENCE();
#pragma unroll
        for (int k = 0; k < 8; ++k) a[k >> 2][4 + (k & 3)] = lds16(la, k * 1024 + rd);
    } else {
        const rsrc_t rx = mkrsrc(p.xb, 2048 * DM * 2);
        v4u xf[7], m0[7];
#pragma unroll
        for (int k = 0; k < 7; ++k) { const int pc = b * 7 + k, rt = pc / 14, ks = pc - rt * 14; xf[k] = ld16(rx, ((rt * 16 + (rho ^ ((rt & 1) * 4))) * DM + ks * 32 + g * 8) * 2, 0, 0); }
        const v4u sent = {SENT, SENT, SENT, SENT};
        if (b == 0) {
#pragma unroll
            for (int k = 0; k < 7; ++k) { const int c = k * 64 + lane; const unsigned* m = (const unsigned*)(p.mem0 + ((c >> 1) & 7) * DM + (c >> 4) * 16 + (c & 1) * 2); m0[k] = (v4u){m[0], m[2], m[4], m[6]}; }
        } else {
#pragma unroll
            for (int k = 0; k < 7; ++k) m0[k] = sent;
        }
        const unsigned rot = ((unsigned)__builtin_amdgcn_s_getreg(20 | (3 << 11)) - b) & 7u;
        if (lane < 16) st16s(rs, OFF_CTL + b * 256 + lane * 16, 0, (v4u){b * 2 == FL_ROT && lane == 0 ? rot : 0u, 0u, 0u, 0u});
#pragma unroll
        for (int k = 0; k < 16; ++k) {
            const int o = ((k & 7) * 1024 + lane * 16 + HO_MEM) & 8191;
            st16s(rs, (b & 3) == 3 && (b & 127) != 3 ? OOB : OFF_HO + b * 16384 + (k >> 3) * 8192 + o, 0, (k & 7) < 7 ? m0[k & 7] : sent);
        }
#pragma unroll
        for (int k = 0; k < 7; ++k) st16s(rs, OFF_XF + (b * 7 + k) * 1024 + lane * 16, 0, xf[k]);
        VMWAIT(24);
    }
    BAR();
    if (wave >= 7) return;
    STAMP(b == 0 && wave == 0, 4, 9); STAMP(wave == 0 && b < 64, 30, b);
    const float sc = which ? 1.f : 0.09016844005556021f;
    const int obase = (which ? OFF_BT : OFF_AP) + ((h * 28 + (c0 >> 4)) * 14 + (b0 >> 5) + wave) * 1024 + lane * 16;
    const int tra = (8 * g + (rho >> 2)) * 224 + (rho & 3) * 8;
    auto tile = [&](const v4u (&bf)[8], int ct) {
        v4f c0v = {0, 0, 0, 0}, c1v = {0, 0, 0, 0};
#pragma unroll
        for (int ks = 0; ks < 8; ++ks) { c0v = mfma32(a[0][ks], bf[ks], c0v); c1v = mfma32(a[1][ks], bf[ks], c1v); }
        st16s(rs, obase, ct * 14336, pk8(c0v * sc, c1v * sc));
    };
    auto fetch = [&](v4u (&bf)[8], int ct) {
        if (which == 0) {
#pragma unroll
            for (int ks = 0; ks < 8; ++ks) bf[ks] = lds16(lds, (ct * 8 + ks) * 1024 + rd);
        } else {
#pragma unroll
            for (int ks = 0; ks < 8; ++ks) { const v2u x = ldstr(lds, tra + ks * 7168 + ct * 32), y = ldstr(lds, tra + ks * 7168 + 896 + ct * 32); bf[ks] = (v4u){x.x, x.y, y.x, y.y}; }
        }
    };
    v4u f0[8], f1[8];
    fetch(f0, 0);
#pragma unroll 1
    for (int ct = 0; ct < 6; ct += 2) { fetch(f1, ct + 1); SCHED(); tile(f0, ct); fetch(f0, ct + 2); SCHED(); tile(f1, ct + 1); }
    tile(f0, 6);
    STAMP(b == 0 && wave == 0, 4, 10);
    STAMP(wave == 0 && b >= 192, 31, b & 63);
}

struct Ctx { rsrc_t rs; char* lds; int lane, wave, rho, g; };

DEV int slot_base(int s, int cal) { return OFF_HO + s * 65536 + (((cal >> (s >> 5)) & 1) << 13); }
#define STAGE_HEAD "s_add_i32 %[bud], %[bud], -1\n\ts_cmp_lt_i32 %[bud], 0\n\ts_cselect_b32 vcc_hi, -1, -2\n\tv_mov_b32 v233, vcc_hi\n\t"
#define STAGE_TAIL "v_cmp_gt_u32 vcc, v232, v233\n\ts_cbranch_vccnz 1b\n\ts_waitcnt lgkmcnt(0)\n\ts_barrier"
#define STAGE_CLOB "memory", "vcc", "scc", "v224", "v225", "v226", "v227", "v228", "v229", "v230", "v231", "v232", "v233"
DEV void stage1(rsrc_t rs, int vo, int so, int& budget) {
    asm volatile("1:\n\tbuffer_load_dwordx4 v[224:227], %[vo], %[rs], %[so] offen sc1\n\t" STAGE_HEAD
                 "s_waitcnt vmcnt(0)\n\tds_write_b128 %[vo], v[224:227]\n\tv_max3_u32 v232, v224, v225, v226\n\tv_max_u32 v232, v232, v227\n\t" STAGE_TAIL
                 : [bud] "+s"(budget) : [vo] "v"(vo), [rs] "s"(rs), [so] "s"(so) : STAGE_CLOB);
}
DEV void stage2(rsrc_t rs, int vo, int vo2, int so, int& budget) {
    asm volatile("1:\n\tbuffer_load_dwordx4 v[224:227], %[vo], %[rs], %[so] offen sc1\n\tbuffer_load_dwordx4 v[228:231], %[vo2], %[rs], %[so] offen sc1\n\t" STAGE_HEAD
                 "s_waitcnt vmcnt(1)\n\tds_write_b128 %[vo], v[224:227]\n\tv_max3_u32 v232, v224, v225, v226\n\t"
                 "s_waitcnt vmcnt(0)\n\tds_write_b128 %[vo2], v[228:231]\n\tv_max3_u32 v232, v232, v227, v228\n\tv_max3_u32 v232, v232, v229, v230\n\tv_max_u32 v232, v232, v231\n\t" STAGE_TAIL
                 : [bud] "+s"(budget) : [vo] "v"(vo), [vo2] "v"(vo2), [rs] "s"(rs), [so] "s"(so) : STAGE_CLOB);
}
DEV void give_up(rsrc_t rs) { st4s(rs, flagoff(FL_ABORT), 0, 1u); }
#define PIN2(a, b) asm volatile("" : "+v"(a), "+v"(b))
constexpr int CAL_STEP = 8;
#define TOUCH_LEAD 3
DEV void role_cal(const Params& p, const Ctx& c) {
    if (c.wave >= 2) return;
    unsigned best[2] = {0xffffffffu, 0xffffffffu};
    for (int r = 0; r < 20; ++r) {
#pragma unroll
        for (int k = 0; k < 2; ++k) {
            const int off = OFF_PROBE + c.wave * (2 << 20) + k * 8192 + r * 256;
            unsigned long long t0, t1; unsigned v;
            asm volatile("s_memtime %0\n\ts_waitcnt lgkmcnt(0)\n\tbuffer_load_dword %2, %3, %4, 0 offen sc1\n\ts_waitcnt vmcnt(0)\n\ts_memtime %1\n\ts_waitcnt lgkmcnt(0)"
                         : "=&s"(t0), "=&s"(t1), "=&v"(v) : "v"(off), "s"(c.rs) : "memory");
            best[k] = min(best[k], (unsigned)(t1 - t0));
        }
        if (r == 5 && c.wave == 0) { if (c.lane == 0) st4s(c.rs, flagoff(FL_CAL), 0, best[1] < best[0] ? 3u : 2u); VMWAIT(0); }
        if (r == 5 && c.wave == 0 && c.lane == 0) for (int k = 0; k < 2; ++k) *(unsigned long long*)(p.ws + OFF_DBG + (13 * 64 + 4 + k) * 8) = best[k];
    }
    if (c.lane == 0) st4s(c.rs, flagoff(FL_CAL + 1 + c.wave), 0, best[1] < best[0] ? 3u : 2u);
    { const int k = c.lane & 3, o = (k == 0 ? 0 : k == 1 ? 7936 : 49152 + (k & 1) * 4096 + 2048) + (best[1] < best[0] ? 8192 : 0);
      for (int s = c.wave ? 32 : 12; s < 32 * (c.wave + 1); s += 4) {
          unsigned seen; if (!wait_flags<16>(c.rs, flagoff(FL_V), s - TOUCH_LEAD, seen)) return;
          unsigned v = ld4s(c.rs, OFF_HO + s * 65536 + o, 0); asm volatile("" :: "v"(v));
      } }
    { unsigned seen; wait_flags<16>(c.rs, flagoff(FL_V), 64, seen); for (int i = 0; i < 200; ++i) __builtin_amdgcn_s_sleep(100);
      auto S = [&](int sl, int k) { return (long long)__hip_atomic_load((unsigned long long*)(p.ws + OFF_DBG + (sl * 64 + k) * 8), __ATOMIC_RELAXED, __HIP_MEMORY_SCOPE_AGENT); };
      if (c.lane == 0) { printf("wave %d best %u %u ws %p\n", c.wave, best[0], best[1], p.ws);
        if (c.wave == 0) { long long t0 = S(4, 8); printf("kpro loaded %lld end %lld main %lld Wready %lld Wflag %lld G0 %lld Ow %lld barA0 %lld barA1 %lld barA32 %lld barA62 %lld\n", (S(4,9)-t0)*10, (S(4,10)-t0)*10, (S(4,0)-t0)*10, (S(4,2)-t0)*10, (S(4,15)-t0)*10, (S(4,12)-t0)*10, (S(4,13)-t0)*10, (S(0,0)-t0)*10, (S(0,1)-t0)*10, (S(0,32)-t0)*10, (S(0,62)-t0)*10);
          for (int k = 1; k < 63; ++k) printf("%lld ", (S(0, k + 1) - S(0, k)) * 10); printf("\n"); } } }
    if (c.lane == 0) for (int k = 0; k < 2; ++k) *(unsigned long long*)(p.ws + OFF_DBG + (13 * 64 + c.wave * 2 + k) * 8) = best[k];
    { const int lane = c.lane; STAMP(c.wave == 0, 4, 11); }
}
DEV int get_cal(const Ctx& c, int late, int& budget) {
    unsigned seen;
    if (!wait_flags<2>(c.rs, flagoff(FL_CAL + late + (late & c.lane)), 1u, seen)) budget = -1;
    return (__builtin_amdgcn_readlane(seen, 0) & 1) | ((__builtin_amdgcn_readlane(seen, 1) & 1) << 1);
}
DEV unsigned now() { return (unsigned)__builtin_amdgcn_s_memtime(); }
DEV void wait_until(unsigned t) {
    int rem = (int)(t - now());
    while (rem > 260) { __builtin_amdgcn_s_sleep(1); rem = (int)(t - now()); }
    rem -= 60;
    asm volatile("1:\n\ts_add_i32 %0, %0, -40\n\ts_cmp_gt_i32 %0, 0\n\ts_cbranch_scc1 1b" : "+s"(rem) :: "scc");
}
#define T_PM 2050
#define T_OS 2200
#define T_OM 400
#define T_OU 750
#define T_ST 0
#define T_PG 200
#define ABL(b) true
#define S_AT 6

#define MEM_TIMES_W(lbase)                                                                                           \
    v4u a_[7];                                                                                                       \
    _Pragma("unroll") for (int f = 0; f < 7; ++f) a_[f] = lds16(lds, (lbase) + f * 1024);                            \
    EXTRA_READS;                                                                                                     \
    SCHED();                                                                                                         \
    _Pragma("unroll") for (int f = 0; f < 7; ++f) { c0 = smf(a_[f], w[0][2 * f], w[0][2 * f + 1], c0, sidx); c1 = smf(a_[f], w[1][2 * f], w[1][2 * f + 1], c1, sidx); AFTER_STEP(f); } \
    const v2u tp = {pk2(c0.x + c0.z, c0.y + c0.w), pk2(c1.x + c1.z, c1.y + c1.w)};

#define VCLOB "v224", "v225", "v226", "v227", "v228", "v229", "v230", "v231", VCLOB_HI
#define VCLOB_HI "v232", "v233", "v234", "v235", "v236", "v237", "v238", "v239", \
              "v240", "v241", "v242", "v243", "v244", "v245", "v246", "v247", "v248", "v249", "v250", "v251", "v252", "v253", "v254", "v255"
#define PUBLISH_ASM(ST)                                                                                                                     \
    "s_cmp_lg_u32 %[pub], 7\n\ts_waitcnt lgkmcnt(0)\n\ts_barrier\n\ts_cbranch_scc1 9f\n\t"                                                  \
    "ds_read_b64 v[224:225], %[la]\n\tds_read_b64 v[226:227], %[la] offset:" #ST "*1\n\tds_read_b64 v[228:229], %[la] offset:" #ST "*2\n\t" \
    "ds_read_b64 v[230:231], %[la] offset:" #ST "*3\n\tds_read_b64 v[232:233], %[la] offset:" #ST "*4\n\t"                                  \
    "ds_read_b64 v[234:235], %[la] offset:" #ST "*5\n\tds_read_b64 v[236:237], %[la] offset:" #ST "*6\n\t"                                  \
    "ds_read_b64 v[238:239], %[la] offset:" #ST "*7\n\t"                                                                                    \
    "s_waitcnt lgkmcnt(6)\n\tv_pk_add_f32 v[224:225], v[224:225], v[226:227]\n\t"                                                           \
    "s_waitcnt lgkmcnt(4)\n\tv_pk_add_f32 v[228:229], v[228:229], v[230:231]\n\t"                                                           \
    "s_waitcnt lgkmcnt(2)\n\tv_pk_add_f32 v[232:233], v[232:233], v[234:235]\n\tv_pk_add_f32 v[224:225], v[224:225], v[228:229]\n\t"        \
    "s_waitcnt lgkmcnt(0)\n\tv_pk_add_f32 v[236:237], v[236:237], v[238:239]\n\tv_pk_add_f32 v[224:225], v[224:225], v[232:233]\n\t"        \
    "s_nop 0\n\tv_pk_add_f32 v[224:225], v[224:225], v[236:237]\n\t"                                                                        \
    "s_nop 0\n\tv_cvt_pk_bf16_f32 v224, v224, v225\n\ts_nop 1\n\t"                                                                          \
    "v_mov_b32_dpp v225, v224 quad_perm:[1,1,1,1] row_mask:0xf bank_mask:0xf\n\t"                                                           \
    "v_mov_b32_dpp v226, v224 quad_perm:[2,2,2,2] row_mask:0xf bank_mask:0xf\n\t"                                                           \
    "v_mov_b32_dpp v227, v224 quad_perm:[3,3,3,3] row_mask:0xf bank_mask:0xf\n\t"                                                           \
    "buffer_store_dwordx4 v[224:227], %[vo], %[rs], %[so] offen sc1\n\t"                                                                    \
    "9:\n\ts_barrier\n\ts_nop 0"

DEV void role_p(const Params& p, const Ctx& c, int idx) {
    const int h = idx >> 2, q = idx & 3, lane = c.lane, wave = c.wave, rho = c.rho, g = c.g;
    const rsrc_t rs = c.rs; char* lds = c.lds;
    const int om = q * 4 + wave;
    const bool isT = wave < 4 && om < 14, isX = wave < 4 && om >= 14;
    constexpr int L_MEM = 0, L_PART = 8192, L_DUMP = 10240;
    const int sidx = (rho & 2) ? 0xEEEE : 0x4444;
    const int fa = L_MEM + g * 256 + ((rho >> 2) * 2 + (rho & 1)) * 32 + ((rho >> 1) & 1) * 16;
    v2u sel; { const int r0 = rho - 2 * g; sel.x = (r0 == 0 ? 0x3f80u : 0u) | (r0 == 1 ? 0x3f800000u : 0u); sel.y = (r0 == 8 ? 0x3f80u : 0u) | (r0 == 9 ? 0x3f800000u : 0u); }
    const int piece = q < 3 ? wave : (wave < 2 ? wave : wave - 2);
    const int po = piece * 1024 + lane * 16;
    int budget = SPIN_CAP;
    if (isT) {
        v4u w[2][14];
#pragma unroll
        for (int t = 0; t < 2; ++t)
#pragma unroll
            for (int ks = 0; ks < 14; ++ks) w[t][ks] = ld16(rs, OFF_AP + (h * 28 + 2 * om + t) * 14336 + g * 256 + img(rho) * 16, ks * 1024, 0);
        int cal = get_cal(c, 0, budget);
        unsigned tref = now() - 65536;
        const int qa = L_MEM + (2 * om + (rho >> 3)) * 256 + (rho & 7) * 32 + g * 8;
        const int pw = (g >> 1) == (rho >> 3) ? L_PART + (2 * wave + (rho >> 3)) * 256 + (rho & 7) * 32 + (g & 1) * 16 : L_DUMP + lane * 16;
        const int tw = lane < 32 ? OFF_T + (h * 14 + om) * 512 + ((rho >> 3) * 2 + g) * 128 + (rho & 7) * 16 : OOB;
#pragma unroll 1
        for (int s = 0; s < NSEG && budget >= 0; ++s) {
            if (s == CAL_STEP) cal = get_cal(c, 1, budget);
            const int sb = slot_base(s, cal);
            v4f c0 = {0, 0, 0, 0}, c1 = {0, 0, 0, 0}; PIN2(c0, c1);
            wait_until(tref + T_PM);
            stage1(rs, po, sb + HO_MEM, budget);
            v2u mq;
#define EXTRA_READS mq = lds8(lds, qa)
#define AFTER_STEP(f)
            MEM_TIMES_W(fa)
#undef EXTRA_READS
            const v4f d2 = mfma16(tp, sel, (v4f){0, 0, 0, 0});
            const v2u d = {pk2(d2.x, d2.y), pk2(d2.z, d2.w)};
            *(v4f*)(lds + pw) = mfma16(d, mq, (v4f){0, 0, 0, 0});
            BAR();
            BAR0();
            tref = now();
            wait_until(tref + T_PG);
            if (ABL(8)) { auto x = __builtin_amdgcn_permlane32_swap(d.x, d.y, false, false);
              auto y = __builtin_amdgcn_permlane32_swap(x[0], x[0], false, false); auto z = __builtin_amdgcn_permlane32_swap(x[1], x[1], false, false);
              st16s(rs, tw, s * (NH * 7168), (v4u){y[0], z[0], y[1], z[1]}); }
        }
    } else if (isX) {
        v4u w[2][14];
        int cal = get_cal(c, 0, budget);
        const int gl0 = OFF_G + h * 28672 + (rho >> 3) * 1024 + g * 256 + (pperm(rho, 0) & 15) * 16;
        const int gfl = flagoff(FL_W + h * 7 + (lane & 3));
        const int ew = rho < 8 ? HO_E + h * 512 + g * 128 + rho * 16 : OOB, mw = g == 0 ? L_PART + 1024 + (rho & 7) * 32 + (rho >> 3) * 16 : L_DUMP + lane * 16;
        unsigned known = 0;
        if (om == 15) { BAR0(); BAR0(); BAR0(); }
#pragma unroll 1
        for (int s = om - 14; s < NSEG; s += 2) {
            if ((s | 1) == CAL_STEP + 1) cal = get_cal(c, 1, budget);
            const int sb = slot_base(s, cal);
            if (s < NSEG - 1 && (s < 2 || ABL(4))) {
                if (s >= 2) wait_until(now() + T_PG);
                if (known < (unsigned)s + 1) {
                    unsigned seen; if (!wait_flags<1>(rs, gfl, s + 1, seen)) break;
                    known = min(min((unsigned)__builtin_amdgcn_readlane(seen, 0), (unsigned)__builtin_amdgcn_readlane(seen, 1)), min((unsigned)__builtin_amdgcn_readlane(seen, 2), (unsigned)__builtin_amdgcn_readlane(seen, 3)));
                }
#pragma unroll
                for (int t = 0; t < 2; ++t)
#pragma unroll
                    for (int ks = 0; ks < 14; ++ks) w[t][ks] = ld16(rs, gl0 + t * 64, s * (NH * 28672) + ks * 2048, 1);
            }
            if (s >= 2) { BAR0(); BAR0(); BAR0(); }
            v4f c0 = {0, 0, 0, 0}, c1 = {0, 0, 0, 0}; PIN2(c0, c1);
            VMWAIT(0);
            STAMP(idx == 3 && s == 0, 4, 12);
            BAR();
            if (s < NSEG - 1) {
#define EXTRA_READS
                MEM_TIMES_W(fa)
#undef EXTRA_READS
                const v4f d2 = mfma16(tp, sel, (v4f){0, 0, 0, 0});
                float m = fmx(fmx(d2.x, d2.y), fmx(d2.z, d2.w));
                m = max_g(fmx(m, ror8(m)));
                *(v4f*)(lds + mw) = (v4f){-m, -m, -m, -m};
                SCHED();
                BAR();
                const float e0 = __builtin_amdgcn_exp2f(d2.x - m), e1 = __builtin_amdgcn_exp2f(d2.y - m), e2 = __builtin_amdgcn_exp2f(d2.z - m), e3 = __builtin_amdgcn_exp2f(d2.w - m);
                const unsigned u0 = pk2(e0, e1), u1 = pk2(e2, e3);
                st16s(rs, ew, sb, (v4u){u0, u1, dppu<0x128>(u0), dppu<0x128>(u1)});
            } else BAR0();
            BAR0();
        }
        if (om == 14) { BAR0(); BAR0(); BAR0(); }
    } else {
        if (wave == 4) { *(v4u*)(lds + L_PART + lane * 16) = (v4u){0u, 0u, 0u, 0u}; *(v4u*)(lds + L_PART + 1024 + lane * 16) = (v4u){0u, 0u, 0u, 0u}; }
        int cal = get_cal(c, 0, budget);
        unsigned tref = now() - 65536;
        STAMP(idx == 0 && wave == 7, 4, 1);
        const bool stg = q == 3 || wave != 7, two = q == 3 && wave == 5;
        const int pr = L_PART + (lane & 31) * 8, pubo = lane < 32 && (lane & 3) == 0 ? HO_PART + idx * 128 + lane * 4 : OOB;
#pragma unroll 1
        for (int s = 0; s < NSEG && budget >= 0; ++s) {
            if (s == CAL_STEP) cal = get_cal(c, 1, budget);
            const int sb = slot_base(s, cal);
            if (stg) {
                wait_until(tref + T_PM);
                if (two) stage2(rs, po, 6144 + lane * 16, sb + HO_MEM, budget); else stage1(rs, po, sb + HO_MEM, budget);
            } else BAR0();
            STAMP(idx == 0 && wave == 7, 0, s); STAMP(idx == 3 && wave == 6, 12, s);
            asm volatile(PUBLISH_ASM(256) :: [pub] "s"(wave), [la] "v"(pr), [vo] "v"(pubo), [rs] "s"(rs), [so] "s"(sb) : "memory", "scc", VCLOB);
            tref = now();
            if (wave == 7) {
                STAMP(idx == 0, 1, s);
                LSTAMP(idx == 0 && (s == 1 || s == 32 || s == 62), 14, s);
                if (lane == 0) st4s(rs, flagoff(FL_T + idx), 0, s);
            }
        }
    }
    if (lane == 0) *(int*)(p.ws + OFF_DBG + 16384 + (idx * 8 + wave) * 4) = SPIN_CAP - budget;
    if (budget < 0) give_up(rs);
    VMWAIT(0);
    BAR();
    if (wave == 7 && lane == 0) st4s(rs, flagoff(FL_T + idx), 0, NSEG);
}

DEV void role_o(const Params& p, const Ctx& c, int t) {
    const int lane = c.lane, wave = c.wave, rho = c.rho, g = c.g;
    const rsrc_t rs = c.rs; char* lds = c.lds;
    constexpr int L_MEM = 0, L_OUT = 8192, L_DUMP = 12288, L_V = 13312;
    v4u w[2][14];
#pragma unroll
    for (int k = 0; k < 2; ++k)
#pragma unroll
        for (int ks = 0; ks < 14; ++ks) w[k][ks] = ld16(rs, OFF_BT + ((2 * wave + k) * 28 + t) * 14336 + lane * 16, ks * 1024, 0);
    int budget = SPIN_CAP;
    int cal = get_cal(c, 0, budget), caln = cal;
    const int sidx = (rho & 2) ? 0xEEEE : 0x4444;
    const int fa = L_MEM + g * 256 + ((rho >> 2) * 2 + (rho & 1)) * 32 + ((rho >> 1) & 1) * 16;
    const int hb = rho >> 3, hd = 2 * wave + hb, i = rho & 7;
    const int pa = HO_PART + hd * 512 + g * 128 + i * 16, ea = HO_E + hd * 512 + g * 128 + i * 16;
    const int ul = OFF_U + (t * 16 + 2 * wave) * 1024 + lane * 16;
    const int ufl = flagoff(FL_W + rho * 7 + ((14 + (t >> 1)) >> 2));
    const int mo = (lane & 3) == 0 ? HO_MEM + t * 256 + lane * 4 : OOB;
    const int p2 = (lane & 4) ? 2 * (lane & 3) + 1 : 2 * (lane & 3);
    const int ra = L_OUT + (lane >> 3) * 64 + p2 * 8;
    const int ow = hb == 0 ? L_OUT + wave * 512 + i * 64 + g * 16 : L_DUMP + lane * 16;
    const int po = wave * 1024 + lane * 16, po2 = min(wave + 4, 6) * 1024 + lane * 16;
    const v4u ones = {0x3f803f80u, 0x3f803f80u, 0x3f803f80u, 0x3f803f80u};
    const v2u ones2 = {0x3f803f80u, 0x3f803f80u};
    const unsigned mlo = hb ? 0u : 0xffffffffu, mhi = ~mlo;
    const unsigned long long hbm = 0xff00ff00ff00ff00ull;
    v4u psel; { const int kq = (rho >> 2) * 2 + (rho & 1); const unsigned one = (kq & 1) ? 0x3f800000u : 0x3f80u;
        psel = (v4u){(kq >> 1) == 0 ? one : 0u, (kq >> 1) == 1 ? one : 0u, (kq >> 1) == 2 ? one : 0u, (kq >> 1) == 3 ? one : 0u}; }
    asm volatile("v_mov_b32 v215, 0" ::: "v215");
    auto check_u = [&](int s) {
        unsigned long long lack; unsigned seen;
        asm volatile("v_cmp_ge_u32 %0, %1, v215" : "=s"(lack) : "s"(s) : "memory");
        if (lack != 0 && !wait_flags<1>(rs, ufl, s + 1, seen)) budget = -1;
    };
    auto load_u = [&](int s) {
        asm volatile("s_nop 4\n\tbuffer_load_dwordx4 v[216:219], %0, %1, %2 offen sc1\n\tbuffer_load_dwordx4 v[220:223], %0, %1, %2 offen offset:1024 sc1"
                     :: "v"(ul), "s"(rs), "s"(s * (NH * 28672)) : "memory", "v216", "v217", "v218", "v219", "v220", "v221", "v222", "v223");
    };
    auto store_v = [&](int s) { st16s(rs, OFF_V + t * 4096 + (wave & 3) * 1024 + lane * 16, s * (NH * 7168), lds16(lds, L_V + (s & 1) * 4096 + (wave & 3) * 1024 + lane * 16)); };
    ACC_DECL;
    unsigned tref = now() - 65536;
    auto mem_times_b = [&](int sb, int s, bool last) -> v2u {
        v4f c0 = {0, 0, 0, 0}, c1 = {0, 0, 0, 0};
        if (wave < 4) stage2(rs, po, po2, sb + HO_MEM, budget);
        else BAR0();
        ACC_T(2);
#undef AFTER_STEP
#define AFTER_STEP(f) if (f == S_AT && !last) { SCHED(); ACC_T(4); if (T_OS) wait_until(tref + T_OS); ACC_T(5); asm volatile("buffer_load_dwordx4 v[224:227], %0, %2, %3 offen sc1\n\tbuffer_load_dwordx4 v[228:231], %1, %2, %3 offen sc1" \
                          :: "v"(pa), "v"(ea), "s"(rs), "s"(sb) : "memory", VCLOB); SCHED(); }
#define EXTRA_READS if (wave < 4 && !last && ABL(1)) load_u(s)
        MEM_TIMES_W(fa)
#undef EXTRA_READS
        *(v2u*)(lds + L_V + (s & 1) * 4096 + wave * 512 + lane * 8) = tp;
        return tp;
    };
    STAMP(t == 0 && wave == 0, 4, 13);
    check_u(1);
    STAMP(t == 0 && wave == 0, 4, 14);
    if (wave >= 4) load_u(0);
#pragma unroll 1
    for (int s = 0; s < NSEG - 1 && budget >= 0; ++s) {
        const int sb = slot_base(s, s < CAL_STEP ? cal : caln), sn = slot_base(s + 1, s + 1 < CAL_STEP ? cal : caln);
        ACC_T(0);
        const v2u vp = mem_times_b(sb, s, false);
        ACC_T(3);
        asm volatile(
            "s_branch 3f\n\t"
            "1:\n\t"
            "buffer_load_dwordx4 v[224:227], %[pa], %[rs], %[sb] offen sc1\n\t"
            "buffer_load_dwordx4 v[228:231], %[ea], %[rs], %[sb] offen sc1\n\t"
            "3:\n\t"
            "s_add_i32 %[bud], %[bud], -1\n\ts_cmp_lt_i32 %[bud], 0\n\ts_cselect_b32 vcc_hi, -1, -2\n\tv_mov_b32 v253, vcc_hi\n\t"
            "s_waitcnt vmcnt(1)\n\t"
            "v_mfma_f32_16x16x32_bf16 v[232:235], %[psel], v[224:227], 0\n\t"
            "v_max3_u32 v250, v224, v225, v226\n\t"
            "s_waitcnt vmcnt(0)\n\t"
            "v_mfma_f32_16x16x32_bf16 v[244:247], %[ones], v[228:231], 0\n\t"
            "v_max3_u32 v251, v228, v229, v230\n\t"
            "v_mfma_f32_16x16x32_bf16 v[236:239], v[216:219], v[228:231], 0\n\t"
            "v_max3_u32 v250, v250, v227, v231\n\t"
            "v_mfma_f32_16x16x32_bf16 v[240:243], v[220:223], v[228:231], 0\n\t"
            "v_max_u32 v250, v250, v251\n\t"
            "v_cmp_gt_u32 vcc, v250, v253\n\t"
            "s_cbranch_vccnz 1b\n\t"
            "v_exp_f32 v250, v232\n\tv_exp_f32 v251, v233\n\t"
            "v_max_f32 v252, v232, v233\n\t"
            "v_cvt_pk_bf16_f32 v250, v250, v251\n\t"
            "v_cmp_lt_f32 vcc, 0x42800000, v252\n\t"
            "v_and_b32 v248, v250, %[mlo]\n\tv_and_b32 v249, v250, %[mhi]\n\t"
            "v_cndmask_b32 v236, v236, v240, %[hbm]\n\tv_cndmask_b32 v237, v237, v241, %[hbm]\n\t"
            "v_mfma_f32_16x16x16_bf16 v[244:247], %[ones2], v[248:249], v[244:247]\n\t"
            "v_cndmask_b32 v238, v238, v242, %[hbm]\n\tv_cndmask_b32 v239, v239, v243, %[hbm]\n\t"
            "s_nop 1\n\t"
            "v_mfma_f32_16x16x16_bf16 v[236:239], %[vp], v[248:249], v[236:239]\n\t"
            "s_cbranch_vccz 2f\n\t"
            "s_nop 7\n\t"
            "v_max_f32 v252, 0, v252\n\tv_mov_b32 v253, v252\n\ts_nop 1\n\tv_permlane16_swap_b32 v252, v253\n\ts_nop 1\n\t"
            "v_max_f32 v252, v252, v253\n\tv_mov_b32 v253, v252\n\ts_nop 1\n\tv_permlane32_swap_b32 v252, v253\n\ts_nop 1\n\t"
            "v_max_f32 v252, v252, v253\n\t"
            "v_mfma_f32_16x16x32_bf16 v[244:247], %[ones], v[228:231], 0\n\t"
            "v_mfma_f32_16x16x32_bf16 v[236:239], v[216:219], v[228:231], 0\n\t"
            "v_sub_f32 v250, v232, v252\n\tv_sub_f32 v251, v233, v252\n\t"
            "v_exp_f32 v250, v250\n\tv_exp_f32 v251, v251\n\tv_exp_f32 v253, -v252\n\ts_nop 0\n\t"
            "v_cvt_pk_bf16_f32 v250, v250, v251\n\t"
            "v_and_b32 v248, v250, %[mlo]\n\tv_and_b32 v249, v250, %[mhi]\n\ts_nop 7\n\t"
            "v_cndmask_b32 v236, v236, v240, %[hbm]\n\tv_cndmask_b32 v237, v237, v241, %[hbm]\n\tv_cndmask_b32 v238, v238, v242, %[hbm]\n\tv_cndmask_b32 v239, v239, v243, %[hbm]\n\t"
            "v_mul_f32 v236, v236, v253\n\tv_mul_f32 v237, v237, v253\n\tv_mul_f32 v238, v238, v253\n\tv_mul_f32 v239, v239, v253\n\t"
            "v_mul_f32 v244, v244, v253\n\tv_mul_f32 v245, v245, v253\n\tv_mul_f32 v246, v246, v253\n\tv_mul_f32 v247, v247, v253\n\ts_nop 1\n\t"
            "v_mfma_f32_16x16x16_bf16 v[244:247], %[ones2], v[248:249], v[244:247]\n\t"
            "v_mfma_f32_16x16x16_bf16 v[236:239], %[vp], v[248:249], v[236:239]\n\t"
            "s_nop 7\n\t"
            "2:\n\t"
            "s_nop 1\n\t"
            "v_rcp_f32 v252, v244\n\t"
            "s_nop 3\n\t"
            "v_mul_f32 v236, v236, v252\n\tv_mul_f32 v237, v237, v252\n\tv_mul_f32 v238, v238, v252\n\tv_mul_f32 v239, v239, v252\n\t"
            "v_add_f32_dpp v240, v236, v236 row_ror:8 row_mask:0xf bank_mask:0xf\n\tv_add_f32_dpp v241, v237, v237 row_ror:8 row_mask:0xf bank_mask:0xf\n\t"
            "v_add_f32_dpp v242, v238, v238 row_ror:8 row_mask:0xf bank_mask:0xf\n\tv_add_f32_dpp v243, v239, v239 row_ror:8 row_mask:0xf bank_mask:0xf\n\t"
            "ds_write_b128 %[ow], v[240:243]\n\t"
            PUBLISH_ASM(512)
            : [bud] "+s"(budget)
            : [pa] "v"(pa), [ea] "v"(ea), [rs] "s"(rs), [sb] "s"(sb), [psel] "v"(psel), [ones] "v"(ones), [ones2] "v"(ones2), [vp] "v"(vp),
              [mlo] "v"(mlo), [mhi] "v"(mhi), [hbm] "s"(hbm), [ow] "v"(ow),
              [pub] "s"(wave), [la] "v"(ra), [vo] "v"(mo), [so] "s"(sn)
            : "memory", "vcc", "scc", VCLOB);
        tref = now();
        STAMP(t == 0 && wave == 7, 3, s);
        if (wave == 7 && lane == 0) st4s(rs, flagoff(FL_V + t), 0, s);
        if (s + 2 == CAL_STEP) caln = get_cal(c, 1, budget);
        if (wave == 4) check_u(min(s + 2, NSEG - 2));
        ACC_T(6);
        if (wave < 4) wait_until(tref + T_OM);
        else {
            wait_until(tref + T_OU + (wave - 4) * T_ST);
            if (s + 1 < NSEG - 1 && ABL(1)) load_u(s + 1);
            if (wave == 4) asm volatile("buffer_load_dword v215, %0, %1, 0 offen sc1" :: "v"(ufl), "s"(rs) : "memory", "v215");
            if (ABL(2)) store_v(s);
        }
        ACC_T(7);
    }
    VMWAIT(0);
    ACC_OUT(t * 8 + wave);
    mem_times_b(slot_base(NSEG - 1, caln), NSEG - 1, true);
    if (lane == 0) *(int*)(p.ws + OFF_DBG + 16384 + ((64 + t) * 8 + wave) * 4) = SPIN_CAP - budget;
    if (budget < 0) give_up(rs);
    BAR();
    if (wave >= 4) store_v(NSEG - 1);
    VMWAIT(0);
    BAR0();
    if (wave == 7 && lane == 0) st4s(rs, flagoff(FL_V + t), 0, NSEG);
}

DEV void role_w(const Params& p, const Ctx& c, int wi) {
    const int lane = c.lane, wave = c.wave, rho = c.rho, g = c.g;
    const rsrc_t rs = c.rs; char* lds = c.lds;
    const int h = wi / 7, unit = (wi - h * 7) * 4 + (wave & 3), set = wave >> 2;
    const bool isG = unit < 14;
    auto stage = [&](int it) {
        const int sg = 2 * it + set, lb = (set * 2 + (it & 1)) * 28672;
#pragma unroll
        for (int k = 0; k < 7; ++k) { const int pc = (wave & 3) * 7 + k; dma16(rs, OFF_XF + lane * 16, sg * 28672 + pc * 1024, lb + pc * 1024); }
    };
    v4u w[2][14];
    auto load_w = [&]() {
#pragma unroll
        for (int t = 0; t < 2; ++t) {
            const int row = isG ? unit * 32 + pperm(rho, t) : (unit - 14) * 32 + t * 16 + rho;
#pragma unroll
            for (int ks = 0; ks < 14; ++ks) w[t][ks] = ld16(rs, (isG ? OFF_AP : OFF_BT) + (h * 28 + (row >> 4)) * 14336 + g * 256 + (row & 15) * 16, ks * 1024, 0);
        }
    };
    int xa[2];
#pragma unroll
    for (int k = 0; k < 2; ++k) { const int key = isG ? k * 16 + rho : pperm(rho, k), kt = key >> 4; xa[k] = kt * 14336 + (g * 16 + ((key & 15) ^ (kt * 4))) * 16; }
    const int ob = (isG ? OFF_G + (h * 14 + unit) * 2048 : OFF_U + ((unit - 14) * 32 + h) * 1024) + lane * 16;
    const int ostep = isG ? 1024 : 16384;
    auto segment = [&](int buf, int sg) {
        const char* xb = lds + buf * 28672;
        v4f a00 = {0, 0, 0, 0}, a01 = a00, a10 = a00, a11 = a00;
        if (isG) {
#pragma unroll
            for (int ks = 0; ks < 14; ++ks) { const v4u x0 = lds16(xb, xa[0] + ks * 1024), x1 = lds16(xb, xa[1] + ks * 1024);
                a00 = mfma32(w[0][ks], x0, a00); a01 = mfma32(w[1][ks], x0, a01); a10 = mfma32(w[0][ks], x1, a10); a11 = mfma32(w[1][ks], x1, a11); }
        } else {
#pragma unroll
            for (int ks = 0; ks < 14; ++ks) { const v4u x0 = lds16(xb, xa[0] + ks * 1024), x1 = lds16(xb, xa[1] + ks * 1024);
                a00 = mfma32(x0, w[0][ks], a00); a01 = mfma32(x1, w[0][ks], a01); a10 = mfma32(x0, w[1][ks], a10); a11 = mfma32(x1, w[1][ks], a11); }
        }
        st16s(rs, ob, sg * (NH * 28672), pk8(a00, a01)); st16s(rs, ob + ostep, sg * (NH * 28672), pk8(a10, a11));
    };
    if (set == 0) load_w();
    if (wave == 7) *(int*)(lds + LDS_MAIN - 32) = 0;
    stage(0);
    VMWAIT(0);
    BAR();
    STAMP(wi == 20 && wave == 1, 4, 2);
#pragma unroll 1
    for (int it = -1; it < 32; ++it) {
        if (it == -1 && set == 1) load_w();
        if (it >= 0 && it + 1 < 32) stage(it + 1);
        if (it > 0 || set == 0) segment(it > 0 ? set * 2 + (it & 1) : 2 * (it + 1), it > 0 ? 2 * it + set : it + 1);
        if (it < 0) continue;
        if (it == 0 && set == 0) {
            VMWAIT(0);
            if (lane == 0 && __hip_atomic_fetch_add((int*)(lds + LDS_MAIN - 32), 1, __ATOMIC_RELAXED, __HIP_MEMORY_SCOPE_WORKGROUP) == 3) st4s(rs, flagoff(FL_W + wi), 0, 2);
            STAMP(wi == 20 && wave == 1, 4, 15);
        }
        const bool eager = it < 4 || it == 31;
        if (eager) VMWAIT(0); else VMWAIT(2);
        BAR();
        if (wave == 0 && lane == 0) st4s(rs, flagoff(FL_W + wi), 0, eager ? 2 * it + 2 : 2 * it);
        if (it == 12 && wave == 0 && wi >= 48) {
            unsigned seen; wait_flags<8>(rs, flagoff(FL_TAB), 28, seen);
            __builtin_amdgcn_fence(__ATOMIC_ACQUIRE, "agent");
            VMWAIT(0);
        }
        STAMP(wi == 20 && wave == 1, 5, it); CSTAMP(wi == 20 && wave == 1, 6, it);
    }
}

DEV int f_first(int set, int wave) { return set ? 33 + 2 * wave : wave; }
DEV int f_next(int s, int wave) { return s >= 32 ? s + 16 : s >= 24 ? 32 + 2 * wave : s + 8; }
DEV void role_f1(const Params& p, const Ctx& c, int h, int set) {
    const int lane = c.lane, wave = c.wave, rho = c.rho, g = c.g;
    const rsrc_t rs = c.rs;
    const int gfl = flagoff(FL_W + h * 7 + (lane & 3)), tfl = flagoff(FL_T + h * 4 + (lane & 3));
    const int gl0 = OFF_G + h * 28672 + (rho >> 3) * 1024 + g * 256 + (pperm(rho, 0) & 15) * 16;
    const int tl = OFF_T + h * 7168 + g * 128 + ((rho >> 2) * 2 + (rho & 1)) * 16;
    const float ninf = -__builtin_inff();
    bool wdone = false;
#pragma unroll 1
    for (int s = f_first(set, wave); s < NSEG; s = f_next(s, wave)) {
        STAMP(h == 0, 16, s);
        unsigned seen;
        if (!wdone) { if (!wait_flags<8>(rs, gfl, s + 1, seen)) return; wdone = __builtin_amdgcn_ballot_w64(seen < NSEG) == 0; }
        v4u x[2][14];
        v4f a[2][2] = {{{0, 0, 0, 0}, {0, 0, 0, 0}}, {{0, 0, 0, 0}, {0, 0, 0, 0}}};
#pragma unroll
        for (int hf = 0; hf < 2; ++hf) {
            v4u y[2][7];
#pragma unroll
            for (int k = 0; k < 7; ++k) {
#pragma unroll
                for (int qt = 0; qt < 2; ++qt) x[qt][hf * 7 + k] = ld16(rs, OFF_XF + qt * 14336 + g * 256 + (rho ^ (qt * 4)) * 16, s * 28672 + (hf * 7 + k) * 1024, 0);
                y[0][k] = ld16(rs, gl0, s * (NH * 28672) + (hf * 7 + k) * 2048, 1); y[1][k] = ld16(rs, gl0 + 64, s * (NH * 28672) + (hf * 7 + k) * 2048, 1);
            }
            SCHED();
#pragma unroll
            for (int k = 0; k < 7; ++k)
#pragma unroll
                for (int qt = 0; qt < 2; ++qt) { a[qt][0] = mfma32(y[0][k], x[qt][hf * 7 + k], a[qt][0]); a[qt][1] = mfma32(y[1][k], x[qt][hf * 7 + k], a[qt][1]); }
        }
        float v[2][8], m[2];
#pragma unroll
        for (int qt = 0; qt < 2; ++qt) {
            const float r[8] = {a[qt][0].x, a[qt][0].y, a[qt][0].z, a[qt][0].w, a[qt][1].x, a[qt][1].y, a[qt][1].z, a[qt][1].w};
            m[qt] = ninf;
#pragma unroll
            for (int k = 0; k < 8; ++k) { v[qt][k] = (8 * g + k <= qt * 16 + rho) ? r[k] : ninf; m[qt] = fmx(m[qt], v[qt][k]); }
        }
        asm volatile("" : "+v"(m[0]), "+v"(m[1]));
        STAMP(h == 0, 17, s);
        if (!wait_flags<2>(rs, tfl, s + 1, seen)) return;
        STAMP(h == 0, 18, s);
        v4f am[2] = {{0, 0, 0, 0}, {0, 0, 0, 0}};
        { v4u y[14];
#pragma unroll
          for (int ks = 0; ks < 14; ++ks) y[ks] = ld16(rs, tl, s * (NH * 7168) + ks * 512, 1);
          SCHED();
#pragma unroll
          for (int ks = 0; ks < 14; ++ks) { am[0] = mfma32(y[ks], x[0][ks], am[0]); am[1] = mfma32(y[ks], x[1][ks], am[1]); } }
        unsigned pm[2];
#pragma unroll
        for (int qt = 0; qt < 2; ++qt) {
            const float mm = max_g(fmx(m[qt], fmx(am[qt].x, am[qt].y)));
            const float e0 = __builtin_amdgcn_exp2f(am[qt].x - mm), e1 = __builtin_amdgcn_exp2f(am[qt].y - mm);
            float z = e0 + e1;
#pragma unroll
            for (int k = 0; k < 8; ++k) { v[qt][k] = __builtin_amdgcn_exp2f(v[qt][k] - mm); z += v[qt][k]; }
            const float rz = __builtin_amdgcn_rcpf(sum_g(z));
            st16s(rs, OFF_PX + (h * 2 + qt) * 1024 + lane * 16, s * (NH * 2048),
                  (v4u){pk2(v[qt][0] * rz, v[qt][1] * rz), pk2(v[qt][2] * rz, v[qt][3] * rz), pk2(v[qt][4] * rz, v[qt][5] * rz), pk2(v[qt][6] * rz, v[qt][7] * rz)});
            pm[qt] = pk2(e0 * rz, e1 * rz);
        }
        st16s(rs, (lane & 1) == 0 ? OFF_PM + h * 512 + lane * 8 : OOB, s * (NH * 512), (v4u){pm[0], pm[1], dppu<0xF5>(pm[0]), dppu<0xF5>(pm[1])});
        VMWAIT(0);
        if (lane == 0) st4s(rs, flagoff((set ? FL_P2 : FL_P) + h * 8 + wave), 0, s + 1);
        STAMP(h == 0, 19, s);
    }
}

DEV void make_tables(const Params& p, const Ctx& c, int t) {
    const int lane = c.lane, wave = c.wave, rho = c.rho, g = c.g;
    const rsrc_t rs = c.rs;
    for (int ch = t * 512 + wave * 64 + lane; ch < 2 * 28672; ch += 28 * 512) {
        float v[8];
        if (ch < 28672) { const int fr = ch >> 6, tl = fr / 14, d0 = (fr - tl * 14) * 32 + g * 8, ri = tl >> 4, k = (tl & 15) * 16 + rho;
#pragma unroll
            for (int j = 0; j < 8; ++j) { const float ph = (float)((k * (d0 + j)) % 448) * (1.f / 448.f); v[j] = k > 224 ? 0.f : ri ? -__builtin_amdgcn_sinf(ph) : __builtin_amdgcn_cosf(ph); }
        } else { const int fr = (ch - 28672) >> 6, d = (fr >> 4) * 16 + rho, ri = (fr >> 3) & 1, k0 = (fr & 7) * 32 + g * 8;
#pragma unroll
            for (int j = 0; j < 8; ++j) { const int k = k0 + j; const float ph = (float)((k * d) % 448) * (1.f / 448.f); const bool edge = k == 0 || k == 224;
                const float wgt = (edge ? 1.f : 2.f) / (448.f * 2048.f); v[j] = k > 224 ? 0.f : ri ? (edge ? 0.f : -wgt * __builtin_amdgcn_sinf(ph)) : wgt * __builtin_amdgcn_cosf(ph); }
        }
        st16s(rs, OFF_FD + ch * 16, 0, (v4u){pk2(v[0], v[1]), pk2(v[2], v[3]), pk2(v[4], v[5]), pk2(v[6], v[7])});
    }
    VMWAIT(0);
    BAR();
    if (wave == 0 && lane == 0) __hip_atomic_fetch_add((unsigned*)(p.ws + flagoff(FL_TAB)), 1u, __ATOMIC_RELAXED, __HIP_MEMORY_SCOPE_AGENT);
}
DEV void role_f2(const Params& p, const Ctx& c, int t, int set) {
    const int lane = c.lane, wave = c.wave, rho = c.rho, g = c.g;
    const rsrc_t rs = c.rs;
    const int ufl = flagoff(FL_W + rho * 7 + ((14 + (t >> 1)) >> 2)), pfl = flagoff(lane < 16 ? (set ? FL_P2 : FL_P) + lane * 8 + wave : FL_V + t);
    const int ul = OFF_U + t * 16384 + lane * 16;
    bool wdone = false;
#pragma unroll 1
    for (int s = f_first(set, wave); s < NSEG; s = f_next(s, wave)) {
        unsigned seen;
        if (!wdone) { if (!wait_flags<8>(rs, ufl, s + 1, seen)) return; wdone = __builtin_amdgcn_ballot_w64(seen < NSEG) == 0; }
        STAMP(t == 0, 20, s);
        v4u u[16];
#pragma unroll
        for (int h = 0; h < 16; ++h) u[h] = ld16(rs, ul, s * (NH * 28672) + h * 1024, 1);
        if (!wait_flags<2>(rs, pfl, s + 1, seen)) return;
        STAMP(t == 0, 21, s);
        v4f a[2] = {{0, 0, 0, 0}, {0, 0, 0, 0}}, m[2] = {{0, 0, 0, 0}, {0, 0, 0, 0}};
        v4u px[16]; v2u pm[16], vv[8];
#pragma unroll
        for (int h = 0; h < 16; ++h) px[h] = ld16(rs, OFF_PX + lane * 16, s * (NH * 2048) + h * 2048, 1);
#pragma unroll
        for (int h = 0; h < 16; ++h) { pm[h] = ld8s(rs, OFF_PM + lane * 8, s * (NH * 512) + h * 512); if (h & 1) vv[h >> 1] = ld8s(rs, OFF_V + t * 4096 + lane * 8, s * (NH * 7168) + (h >> 1) * 512); }
        SCHED();
#pragma unroll
        for (int h = 0; h < 16; ++h) { a[0] = mfma32(u[h], px[h], a[0]); SCHED(); px[h] = ld16(rs, OFF_PX + 1024 + lane * 16, s * (NH * 2048) + h * 2048, 1); SCHED(); }
#pragma unroll
        for (int k = 0; k < 8; ++k) { m[0] = mfma16(vv[k], (v2u){pm[2 * k].x, pm[2 * k + 1].x}, m[0]); m[1] = mfma16(vv[k], (v2u){pm[2 * k].y, pm[2 * k + 1].y}, m[1]); }
        SCHED();
#pragma unroll
        for (int h = 0; h < 16; ++h) a[1] = mfma32(u[h], px[h], a[1]);
#pragma unroll
        for (int qt = 0; qt < 2; ++qt) { const v4f o = a[qt] + m[qt];
            *(v2u*)(p.out + (size_t)(s * 32 + qt * 16 + rho) * 896 + DM + t * 16 + g * 4) = (v2u){pk2(o.x, o.y), pk2(o.z, o.w)}; }
        STAMP(t == 0, 22, s);
    }
}

DEV bool fft_sync(const Params& p, const Ctx& c, int fl) {
    VMWAIT(0);
    BAR();
    int* okw = (int*)(c.lds + LDS_MAIN - 16);
    if (c.wave == 0) {
        if (c.lane == 0) __hip_atomic_fetch_add((unsigned*)(p.ws + flagoff(fl)), 1u, __ATOMIC_RELAXED, __HIP_MEMORY_SCOPE_AGENT);
        unsigned seen; const bool ok = wait_flags<2>(c.rs, flagoff(fl), 64, seen);
        if (c.lane == 0) *okw = ok;
    }
    BAR();
    return *okw != 0;
}
DEV v4u neg8(v4u v) { return v ^ 0x80008000u; }
DEV void twid(int a, const int (&n)[8], int mask, float inv, v4u& C, v4u& S) {
    float cs[8], sn[8];
#pragma unroll
    for (int j = 0; j < 8; ++j) { const float ph = (float)((a * n[j]) & mask) * inv; cs[j] = __builtin_amdgcn_cosf(ph); sn[j] = __builtin_amdgcn_sinf(ph); }
    C = (v4u){pk2(cs[0], cs[1]), pk2(cs[2], cs[3]), pk2(cs[4], cs[5]), pk2(cs[6], cs[7])};
    S = (v4u){pk2(sn[0], sn[1]), pk2(sn[2], sn[3]), pk2(sn[4], sn[5]), pk2(sn[6], sn[7])};
}
DEV void role_fft(const Params& p, const Ctx& c, int job) {
    const int lane = c.lane, wave = c.wave, rho = c.rho, g = c.g;
    const rsrc_t rs = c.rs; char* lds = c.lds;
    const v4f z4 = {0, 0, 0, 0};
    STAMP(wave == 1, 23, job);
    {
        const rsrc_t rx = mkrsrc(p.xa, 2048 * DM * 2);
        for (int pc = wave; pc < 28; pc += 8) { const int nt = pc >= 14, ks = pc - nt * 14; dma16(rx, ((64 * (nt * 16 + rho) + job) * DM + ks * 32 + g * 8) * 2, 0, pc * 1024); }
        STAMP(wave == 1, 24, job);
        v4u C[2], S[2];
        { int n[8];
#pragma unroll
          for (int j = 0; j < 8; ++j) n[j] = 64 * (16 * (j >> 2) + 4 * g + (j & 3)) + job;
          twid(rho, n, 2047, 1.f / 2048.f, C[0], S[0]); twid(16 + rho, n, 2047, 1.f / 2048.f, C[1], S[1]); }
        VMWAIT(0);
        BAR();
        v4f zr[2][2], zi[2][2];
#pragma unroll
        for (int t = 0; t < 2; ++t) {
            const int fo = OFF_FD + (wave * 2 + (rho >> 3)) * 14336 + g * 256 + (pperm(rho, t) & 15) * 16;
            v4f r0 = z4, r1 = z4, i0 = z4, i1 = z4;
#pragma unroll
            for (int hf = 0; hf < 2; ++hf) {
                v4u fr[7], fi[7];
#pragma unroll
                for (int k = 0; k < 7; ++k) { fr[k] = ld16(rs, fo, (hf * 7 + k) * 1024, 0); fi[k] = ld16(rs, fo, 256 * DM * 2 + (hf * 7 + k) * 1024, 0); }
                SCHED();
#pragma unroll
                for (int k = 0; k < 7; ++k) { const v4u x0 = lds16(lds, (hf * 7 + k) * 1024 + lane * 16), x1 = lds16(lds, (14 + hf * 7 + k) * 1024 + lane * 16);
                    r0 = mfma32(x0, fr[k], r0); r1 = mfma32(x1, fr[k], r1); i0 = mfma32(x0, fi[k], i0); i1 = mfma32(x1, fi[k], i1); }
            }
            const v4u yr = pk8(r0, r1), yi = pk8(i0, i1), nr = neg8(yr);
#pragma unroll
            for (int ct = 0; ct < 2; ++ct) { zr[ct][t] = mfma32(yi, S[ct], mfma32(yr, C[ct], z4)); zi[ct][t] = mfma32(nr, S[ct], mfma32(yi, C[ct], z4)); }
        }
        char* O = lds + 32768;
#pragma unroll
        for (int ct = 0; ct < 2; ++ct) { const int o = (ct * 16 + rho) * 1040 + wave * 64 + g * 16;
            *(v4u*)(O + o) = pk8(zr[ct][0], zr[ct][1]); *(v4u*)(O + o + 512) = pk8(zi[ct][0], zi[ct][1]); }
        BAR();
#pragma unroll
        for (int k = 0; k < 4; ++k) st16s(rs, OFF_Z1 + job * 1024 + lane * 16, (wave * 4 + k) * 65536, lds16(O, (wave * 4 + k) * 1040 + lane * 16));
    }
    STAMP(job == 0 && wave == 1, 4, 3); STAMP(wave == 1, 25, job);
    if (!fft_sync(p, c, FL_FA)) return;
    STAMP(job == 0 && wave == 1, 4, 4); STAMP(wave == 1, 26, job);
    if (wave < 4) {
        const int k1 = job >> 1, P = (job & 1) * 4 + wave;
        char* L = lds + wave * 8192;
#pragma unroll
        for (int pc = 0; pc < 8; ++pc) dma16s(rs, OFF_Z1 + (k1 * 64 + (pc & 3) * 16 + (lane >> 2)) * 1024 + (pc >> 2) * 512 + P * 64 + (lane & 3) * 16, 0, wave * 8192 + pc * 1024);
        v4u C[4][2], S[4][2];
#pragma unroll
        for (int ks = 0; ks < 2; ++ks) { int n[8];
#pragma unroll
            for (int j = 0; j < 8; ++j) n[j] = 32 * ks + 16 * (j >> 2) + 4 * g + (j & 3);
#pragma unroll
            for (int kt = 0; kt < 4; ++kt) twid(16 * kt + rho, n, 63, 1.f / 64.f, C[kt][ks], S[kt][ks]); }
        VMWAIT(0);
        v2u hr[4], hi[4];
#pragma unroll
        for (int t = 0; t < 2; ++t) {
            const int k = P * 32 + pperm(rho, t);
            v4u xr[2], xi[2];
#pragma unroll
            for (int ks = 0; ks < 2; ++ks) { const int a = (32 * ks + 4 * g + (rho >> 2)) * 64 + (rho & 3) * 16 + t * 8;
                const v2u a0 = ldstr(L, a), a1 = ldstr(L, a + 1024), b0 = ldstr(L, a + 4096), b1 = ldstr(L, a + 5120);
                xr[ks] = (v4u){a0.x, a0.y, a1.x, a1.y}; xi[ks] = (v4u){b0.x, b0.y, b1.x, b1.y}; }
            const v4u n0 = neg8(xr[0]), n1 = neg8(xr[1]);
            unsigned short fr[16], fi[16];
#pragma unroll
            for (int j = 0; j < 16; ++j) { const int o = (k1 + 32 * (16 * (j >> 2) + 4 * g + (j & 3))) * 225 + min(k, 224); fr[j] = p.wr[o]; fi[j] = p.wi[o]; }
            SCHED();
            v4f vr[4], vi[4];
#pragma unroll
            for (int kt = 0; kt < 4; ++kt) {
                const v4f ar = mfma32(S[kt][1], xi[1], mfma32(S[kt][0], xi[0], mfma32(C[kt][1], xr[1], mfma32(C[kt][0], xr[0], z4))));
                const v4f ai = mfma32(S[kt][1], n1, mfma32(S[kt][0], n0, mfma32(C[kt][1], xi[1], mfma32(C[kt][0], xi[0], z4))));
                float wr[4], wi[4];
#pragma unroll
                for (int r = 0; r < 4; ++r) { wr[r] = __uint_as_float((unsigned)fr[kt * 4 + r] << 16); wi[r] = __uint_as_float((unsigned)fi[kt * 4 + r] << 16); }
                vr[kt] = (v4f){ar.x * wr[0] - ai.x * wi[0], ar.y * wr[1] - ai.y * wi[1], ar.z * wr[2] - ai.z * wi[2], ar.w * wr[3] - ai.w * wi[3]};
                vi[kt] = (v4f){ar.x * wi[0] + ai.x * wr[0], ar.y * wi[1] + ai.y * wr[1], ar.z * wi[2] + ai.z * wr[2], ar.w * wi[3] + ai.w * wr[3]};
            }
            const v4u pr0 = pk8(vr[0], vr[1]), pr1 = pk8(vr[2], vr[3]), pi0 = pk8(vi[0], vi[1]), pi1 = pk8(vi[2], vi[3]), ni0 = neg8(pi0), ni1 = neg8(pi1);
#pragma unroll
            for (int nt = 0; nt < 4; ++nt) {
                const v4f rr = mfma32(ni1, S[nt][1], mfma32(ni0, S[nt][0], mfma32(pr1, C[nt][1], mfma32(pr0, C[nt][0], z4))));
                const v4f ri = mfma32(pr1, S[nt][1], mfma32(pr0, S[nt][0], mfma32(pi1, C[nt][1], mfma32(pi0, C[nt][0], z4))));
                const v2u qr = {pk2(rr.x, rr.y), pk2(rr.z, rr.w)}, qi = {pk2(ri.x, ri.y), pk2(ri.z, ri.w)};
                if (t == 0) { hr[nt] = qr; hi[nt] = qi; }
                else { char* o = lds + 32768 + (nt * 16 + rho) * 528 + wave * 64 + g * 16;
                    *(v4u*)o = (v4u){hr[nt].x, hr[nt].y, qr.x, qr.y}; *(v4u*)(o + 256) = (v4u){hi[nt].x, hi[nt].y, qi.x, qi.y}; }
            }
        }
    }
    BAR();
#pragma unroll
    for (int k = 0; k < 4; ++k) { const int n2 = wave * 8 + k * 2 + (lane >> 5);
        st16s(rs, OFF_Z2 + (n2 * 32 + (job >> 1)) * 1024 + (g & 1) * 512 + (job & 1) * 256 + rho * 16, 0, lds16(lds, 32768 + n2 * 528 + (lane & 31) * 16)); }
    STAMP(job == 0 && wave == 1, 4, 5); STAMP(wave == 1, 27, job);
    if (!fft_sync(p, c, FL_FB)) return;
    STAMP(job == 0 && wave == 1, 4, 6); STAMP(wave == 1, 28, job);
    {
        char* L = lds + wave * 4096;
#pragma unroll
        for (int pc = 0; pc < 4; ++pc) dma16s(rs, OFF_Z2 + (job * 32 + (pc & 1) * 16 + (lane >> 2)) * 1024 + (pc >> 1) * 512 + wave * 64 + (lane & 3) * 16, 0, wave * 4096 + pc * 1024);
        v4u C[2], S[2];
        { int n[8];
#pragma unroll
          for (int j = 0; j < 8; ++j) n[j] = 8 * g + j;
          twid(64 * rho + job, n, 2047, 1.f / 2048.f, C[0], S[0]); twid(64 * (16 + rho) + job, n, 2047, 1.f / 2048.f, C[1], S[1]); }
        VMWAIT(0);
        v4f rr[2][2], ri[2][2];
#pragma unroll
        for (int t = 0; t < 2; ++t) {
            const int a = (8 * g + (rho >> 2)) * 64 + (rho & 3) * 16 + t * 8;
            const v2u a0 = ldstr(L, a), a1 = ldstr(L, a + 256), b0 = ldstr(L, a + 2048), b1 = ldstr(L, a + 2304);
            const v4u xr = {a0.x, a0.y, a1.x, a1.y}, xi = {b0.x, b0.y, b1.x, b1.y}, ni = neg8(xi);
#pragma unroll
            for (int nt = 0; nt < 2; ++nt) { rr[nt][t] = mfma32(ni, S[nt], mfma32(xr, C[nt], z4)); ri[nt][t] = mfma32(xr, S[nt], mfma32(xi, C[nt], z4)); }
        }
        char* R = lds + 32768;
#pragma unroll
        for (int nt = 0; nt < 2; ++nt) { *(v4u*)(R + (nt * 16 + wave) * 1024 + lane * 16) = pk8(rr[nt][0], rr[nt][1]); *(v4u*)(R + (nt * 16 + 8 + wave) * 1024 + lane * 16) = pk8(ri[nt][0], ri[nt][1]); }
        BAR();
#pragma unroll 1
        for (int Q = wave; Q < 14; Q += 8) {
            v4f y[2][2] = {{z4, z4}, {z4, z4}};
#pragma unroll
            for (int hf = 0; hf < 2; ++hf) {
                v4u f0[8], f1[8];
#pragma unroll
                for (int k = 0; k < 8; ++k) { const int fo = OFF_FI + (Q * 2 + (rho >> 3)) * 16384 + g * 256 + (pperm(rho, 0) & 15) * 16; f0[k] = ld16(rs, fo, (hf * 8 + k) * 1024, 0); f1[k] = ld16(rs, fo + 64, (hf * 8 + k) * 1024, 0); }
                SCHED();
#pragma unroll
                for (int k = 0; k < 8; ++k) { const v4u b0 = lds16(R, (hf * 8 + k) * 1024 + lane * 16), b1 = lds16(R, (16 + hf * 8 + k) * 1024 + lane * 16);
                    y[0][0] = mfma32(f0[k], b0, y[0][0]); y[0][1] = mfma32(f1[k], b0, y[0][1]); y[1][0] = mfma32(f0[k], b1, y[1][0]); y[1][1] = mfma32(f1[k], b1, y[1][1]); }
            }
#pragma unroll
            for (int nt = 0; nt < 2; ++nt) *(v4u*)(p.out + (size_t)(64 * (nt * 16 + rho) + job) * 896 + Q * 32 + g * 8) = pk8(y[nt][0], y[nt][1]);
        }
    }
}

extern "C" __global__ void __launch_bounds__(512, 2) k_main(Params p) {
    extern __shared__ __attribute__((aligned(16))) char lds[];
    Ctx c; c.rs = mkrsrc(p.ws, WS_BYTES); c.lds = lds; c.lane = threadIdx.x & 63; c.wave = __builtin_amdgcn_readfirstlane(threadIdx.x >> 6); c.rho = c.lane & 15; c.g = c.lane >> 4;
    { const int lane = c.lane; (void)lane; STAMP(blockIdx.x == 0 && c.wave == 7, 4, 0); }
    if (threadIdx.x == 0 && blockIdx.x < 16) *(unsigned long long*)(p.ws + OFF_DBG + (13 * 64 + 8 + blockIdx.x) * 8) = __builtin_amdgcn_s_getreg(20 | (3 << 11)) & 0xf;
    const int rot = *(const int*)(p.ws + flagoff(FL_ROT));
    const int x = (blockIdx.x + rot) & 7, y = blockIdx.x >> 3;
    if (x < 2) role_p(p, c, x * 32 + y);
    else if (x == 2) { if (y < 28) role_o(p, c, y); }
    else if (x == 3 && y == 28) role_cal(p, c);
    else {
        const int wi = (x - 4) * 32 + y - 16;
        if (x == 3) { if (y < 28) make_tables(p, c, y); } else if (wi >= 0) role_w(p, c, wi);
        if (wi >= 48) role_fft(p, c, wi - 48);
        else if (x == 3 ? y < 28 : wi >= 16 && wi < 44) role_f2(p, c, x == 3 ? y : wi - 16, x != 3);
        else if (x == 4) role_f1(p, c, y & 15, wi >= 0);
    }
    { const int lane = c.lane; (void)lane; LSTAMP(c.wave == 7, 8 + (blockIdx.x >> 6), blockIdx.x & 63); }
}

extern "C" void kernel_launch(void* const* d_in, const int* in_sizes, int n_in, void* d_out, int out_size, void* d_ws, size_t ws_size, hipStream_t stream) {
    Params p;
    p.xa = (const unsigned short*)d_in[0]; p.wr = (const unsigned short*)d_in[1]; p.wi = (const unsigned short*)d_in[2];
    p.xb = (const unsigned short*)d_in[3]; p.mem0 = (const unsigned short*)d_in[4];
    p.wq = (const unsigned short*)d_in[5]; p.wk = (const unsigned short*)d_in[6]; p.wv = (const unsigned short*)d_in[7]; p.wo = (const unsigned short*)d_in[8];
    p.out = (unsigned short*)d_out;
    p.ws = (char*)d_ws + (((size_t)2 << 20) - ((uintptr_t)d_ws & (((size_t)2 << 20) - 1))) % ((size_t)2 << 20);
    (void)hipFuncSetAttribute((const void*)k_main, hipFuncAttributeMaxDynamicSharedMemorySize, LDS_MAIN);
    (void)hipFuncSetAttribute((const void*)k_pro, hipFuncAttributeMaxDynamicSharedMemorySize, LDS_PRO);
    hipLaunchKernelGGL(k_pro, dim3(256), dim3(512), LDS_PRO, stream, p);
    hipLaunchKernelGGL(k_main, dim3(256), dim3(512), LDS_MAIN, stream, p);
}
```

```cpp
#define LITE
#include <hip/hip_runtime.h>
#include <hip/hip_bf16.h>
#include <stdint.h>

typedef unsigned v4u __attribute__((ext_vector_type(4)));
typedef unsigned v2u __attribute__((ext_vector_type(2)));
typedef float v4f __attribute__((ext_vector_type(4)));
typedef float v2f __attribute__((ext_vector_type(2)));
typedef short v4s __attribute__((ext_vector_type(4)));
typedef __bf16 v2bf __attribute__((ext_vector_type(2)));
typedef __bf16 v8bf __attribute__((ext_vector_type(8)));
typedef __bf16 v16bf __attribute__((ext_vector_type(16)));
typedef unsigned v8u __attribute__((ext_vector_type(8)));
typedef __amdgpu_buffer_rsrc_t rsrc_t;
#define LAS __attribute__((address_space(3)))
#define DEV __device__ __forceinline__

constexpr int NSEG = 64, NH = 16, DM = 448;
constexpr unsigned SENT = 0xFFFFFFFFu;
constexpr int OOB = 0x7fffffff;
constexpr int SC1 = 16;
constexpr int OFF_HO = 0;
constexpr int OFF_CTL = 4 << 20;
constexpr int OFF_AP = OFF_CTL + 65536;
constexpr int OFF_BT = OFF_AP + NH * DM * DM * 2;
constexpr int OFF_G = OFF_BT + NH * DM * DM * 2;
constexpr int OFF_U = OFF_G + NSEG * NH * 28672;
constexpr int OFF_T = OFF_U + NSEG * NH * 28672;
constexpr int OFF_V = OFF_T + NSEG * NH * 7168;
constexpr int OFF_PX = OFF_V + NSEG * NH * 7168;
constexpr int OFF_PM = OFF_PX + NSEG * NH * 2048;
constexpr int OFF_FD = OFF_PM + NSEG * NH * 512;
constexpr int OFF_FI = OFF_FD + 512 * DM * 2;
constexpr int OFF_Z1 = OFF_FI + 512 * DM * 2;
constexpr int OFF_Z2 = OFF_Z1 + (2 << 20);
constexpr int OFF_XF = OFF_Z2 + (2 << 20);
constexpr int OFF_DBG = OFF_XF + (2 << 20);
constexpr int WS_BYTES = OFF_DBG + (1 << 20);
constexpr int HO_MEM = 512, HO_PART = 16384, HO_E = 32768;
constexpr int OFF_PROBE = OFF_HO + 49152;
constexpr int FL_W = 0, FL_T = 112, FL_V = 176, FL_P = 208, FL_ROT = 336, FL_TAB = 337, FL_FA = 338, FL_FB = 339, FL_ABORT = 340, FL_CAL = 341, FL_P2 = 344;
constexpr int LDS_MAIN = 114688 + 2048;
constexpr int LDS_PRO = 114688;
constexpr int SPIN_CAP = 1 << 17;

struct Params {
    const unsigned short* xa; const unsigned short* wr; const unsigned short* wi;
    const unsigned short* xb; const unsigned short* mem0;
    const unsigned short* wq; const unsigned short* wk; const unsigned short* wv; const unsigned short* wo;
    unsigned short* out; char* ws;
};

DEV unsigned pk2(float a, float b) { v2bf r = {(__bf16)a, (__bf16)b}; return __builtin_bit_cast(unsigned, r); }
DEV v4u pk8(v4f a, v4f b) { return (v4u){pk2(a.x, a.y), pk2(a.z, a.w), pk2(b.x, b.y), pk2(b.z, b.w)}; }
DEV v4f mfma32(v4u a, v4u b, v4f c) { return __builtin_amdgcn_mfma_f32_16x16x32_bf16(__builtin_bit_cast(v8bf, a), __builtin_bit_cast(v8bf, b), c, 0, 0, 0); }
DEV v4f mfma16(v2u a, v2u b, v4f c) { return __builtin_amdgcn_mfma_f32_16x16x16bf16_1k(__builtin_bit_cast(v4s, a), __builtin_bit_cast(v4s, b), c, 0, 0, 0); }
DEV v4f smf(v4u a, v4u b0, v4u b1, v4f c, int idx) {
    v8u b = {b0.x, b0.y, b0.z, b0.w, b1.x, b1.y, b1.z, b1.w};
    return __builtin_amdgcn_smfmac_f32_16x16x64_bf16(__builtin_bit_cast(v8bf, a), __builtin_bit_cast(v16bf, b), c, idx, 0, 0);
}
DEV float fmx(float a, float b) { return __builtin_elementwise_maximum(a, b); }
DEV float max_g(float s) {
    auto r = __builtin_amdgcn_permlane16_swap(__float_as_uint(s), __float_as_uint(s), false, false);
    s = fmx(__uint_as_float(r[0]), __uint_as_float(r[1]));
    auto q = __builtin_amdgcn_permlane32_swap(__float_as_uint(s), __float_as_uint(s), false, false);
    return fmx(__uint_as_float(q[0]), __uint_as_float(q[1]));
}
DEV float sum_g(float s) {
    auto r = __builtin_amdgcn_permlane16_swap(__float_as_uint(s), __float_as_uint(s), false, false);
    s = __uint_as_float(r[0]) + __uint_as_float(r[1]);
    auto q = __builtin_amdgcn_permlane32_swap(__float_as_uint(s), __float_as_uint(s), false, false);
    return __uint_as_float(q[0]) + __uint_as_float(q[1]);
}
DEV float ror8(float s) { return __int_as_float(__builtin_amdgcn_mov_dpp(__float_as_int(s), 0x128, 0xf, 0xf, true)); }
template <int CTRL> DEV unsigned dppu(unsigned x) { return (unsigned)__builtin_amdgcn_mov_dpp((int)x, CTRL, 0xf, 0xf, true); }
DEV v4u quad4(unsigned x) { return (v4u){x, dppu<0x55>(x), dppu<0xAA>(x), dppu<0xFF>(x)}; }
DEV unsigned umax4(v4u v) { return max(max(v.x, v.y), max(v.z, v.w)); }
DEV rsrc_t mkrsrc(const void* p, unsigned bytes) { return __builtin_amdgcn_make_buffer_rsrc((void*)p, 0, bytes, 0x00020000); }
DEV v4u ld16(rsrc_t r, int vo, int so, int aux) { return aux ? __builtin_amdgcn_raw_buffer_load_b128(r, vo, so, SC1) : __builtin_amdgcn_raw_buffer_load_b128(r, vo, so, 0); }
DEV v2u ld8s(rsrc_t r, int vo, int so) { return __builtin_amdgcn_raw_buffer_load_b64(r, vo, so, SC1); }
DEV unsigned ld4s(rsrc_t r, int vo, int so) { return __builtin_amdgcn_raw_buffer_load_b32(r, vo, so, SC1); }
DEV void st16s(rsrc_t r, int vo, int so, v4u v) {
    __builtin_amdgcn_raw_buffer_store_b128(v, r, vo, so, SC1); asm volatile("s_nop 1" :: "v"(v) : "memory");
}
DEV void st8s(rsrc_t r, int vo, int so, v2u v) { __builtin_amdgcn_raw_buffer_store_b64(v, r, vo, so, SC1); }
DEV void st4s(rsrc_t r, int vo, int so, unsigned v) { __builtin_amdgcn_raw_buffer_store_b32(v, r, vo, so, SC1); }
DEV void dma16(rsrc_t r, int vo, int so, int ldsaddr) {
    unsigned keep;
    asm volatile("s_mov_b32 %0, m0\n\ts_mov_b32 m0, %4\n\ts_nop 4\n\tbuffer_load_dwordx4 %1, %2, %3 offen lds\n\ts_mov_b32 m0, %0"
                 : "=&s"(keep) : "v"(vo), "s"(r), "s"(__builtin_amdgcn_readfirstlane(so)), "s"(__builtin_amdgcn_readfirstlane(ldsaddr)) : "memory");
}
DEV void dma16s(rsrc_t r, int vo, int so, int ldsaddr) {
    unsigned keep;
    asm volatile("s_mov_b32 %0, m0\n\ts_mov_b32 m0, %4\n\ts_nop 4\n\tbuffer_load_dwordx4 %1, %2, %3 offen sc1 lds\n\ts_mov_b32 m0, %0"
                 : "=&s"(keep) : "v"(vo), "s"(r), "s"(__builtin_amdgcn_readfirstlane(so)), "s"(__builtin_amdgcn_readfirstlane(ldsaddr)) : "memory");
}
#define BAR() asm volatile("s_waitcnt lgkmcnt(0)\n\ts_barrier" ::: "memory")
#define BAR0() asm volatile("s_barrier" ::: "memory")
#define VMWAIT(n) asm volatile("s_waitcnt vmcnt(" #n ")" ::: "memory")
#define FENCE() asm volatile("" ::: "memory")
#define SCHED() __builtin_amdgcn_sched_barrier(0)
DEV v4u lds16(const char* l, int a) { return *(const v4u*)(l + a); }
DEV v2u lds8(const char* l, int a) { return *(const v2u*)(l + a); }
DEV v2u ldstr(const char* l, int a) { v4s r = __builtin_amdgcn_ds_read_tr16_b64_v4i16((LAS v4s*)(l + a)); return __builtin_bit_cast(v2u, r); }
template <int N> DEV void slp() { if constexpr (N > 0) __builtin_amdgcn_s_sleep(N); }
DEV int flagoff(int i) { return OFF_CTL + i * 128; }
template <int SL> DEV bool wait_flags(rsrc_t rs, int vo, unsigned need, unsigned& seen) {
    for (int sp = 0;; ++sp) {
        FENCE();
        seen = ld4s(rs, vo, 0);
        if (__builtin_amdgcn_ballot_w64(seen < need) == 0) return true;
        slp<SL>();
        if ((sp & 255) == 255) {
            if (sp > SPIN_CAP) st4s(rs, flagoff(FL_ABORT), 0, 1u);
            if (sp > SPIN_CAP || __builtin_amdgcn_readfirstlane(ld4s(rs, flagoff(FL_ABORT), 0))) return false;
        }
    }
}
DEV int img(int p) { return ((p & 7) >> 1) * 4 + (p >> 3) * 2 + (p & 1); }
DEV int pperm(int rho, int tau) { return (rho >> 2) * 8 + tau * 4 + (rho & 3); }

#define STAMP(cond, slot, s)
#define LSTAMP(cond, slot, s) do { if ((cond) && lane == 0) { unsigned long long t_ = __builtin_amdgcn_s_memrealtime(); __hip_atomic_store((unsigned long long*)(p.ws + OFF_DBG + ((slot) * 64 + (s)) * 8), t_, __ATOMIC_RELAXED, __HIP_MEMORY_SCOPE_AGENT); } } while (0)
#define ACC_DECL
#define ACC_T(k)
#define ACC_OUT(slot)
#define CSTAMP(cond, slot, s)

extern "C" __global__ void __launch_bounds__(512, 2) k_pro(Params p) {
    extern __shared__ __attribute__((aligned(16))) char lds[];
    const int b = blockIdx.x, tid = threadIdx.x, lane = tid & 63, wave = __builtin_amdgcn_readfirstlane(tid >> 6);
    const int rho = lane & 15, g = lane >> 4;
    const int gemm = (b & 7) * 4 + (b >> 6), sub = (b >> 3) & 7, which = gemm & 1, h = gemm >> 1;
    const int b0 = 224 * (sub & 1), c0 = 112 * (sub >> 1);
    const rsrc_t rs = mkrsrc(p.ws, WS_BYTES);
    LSTAMP(b == 0 && wave == 0, 4, 8); STAMP(wave == 0 && b < 64, 29, b);
    const int lr = lane >> 2, lc = ((lane & 3) ^ ((0 - g) & 3)) * 8, rd = rho * 64 + ((g ^ ((0 - (rho >> 2)) & 3)) * 16);
    if (which == 0) {
        const rsrc_t rq = mkrsrc(p.wq, DM * 4096 * 2);
        for (int k = wave; k < 56; k += 8) dma16(rq, ((c0 + (k >> 3) * 16 + lr) * 4096 + h * 256 + (k & 7) * 32 + lc) * 2, 0, k * 1024);
    } else {
        const rsrc_t ro = mkrsrc(p.wo, DM * 4096 * 2);
        for (int k = wave; k < 56; k += 8) { const int ch = k * 64 + lane, e = ch / 14, cc = ch - e * 14; dma16(ro, ((h * 256 + e) * DM + c0 + cc * 8) * 2, 0, k * 1024); }
    }
    v4u a[2][8];
    char* la = lds + 57344 + wave * 8192;
    if (wave < 7) {
        const rsrc_t ra = mkrsrc(which ? p.wv : p.wk, DM * 4096 * 2);
#pragma unroll
        for (int t = 0; t < 2; ++t) {
            const int src = ((b0 + 32 * wave + pperm(lr, t)) * 4096 + h * 256 + lc) * 2;
#pragma unroll
            for (int ks = 0; ks < 4; ++ks) { dma16(ra, src, ks * 64, 57344 + wave * 8192 + (t * 4 + ks) * 1024); a[t][4 + ks] = ld16(ra, src, 256 + ks * 64, 0); }
        }
        VMWAIT(0);
#pragma unroll
        for (int k = 0; k < 8; ++k) a[k >> 2][k & 3] = lds16(la, k * 1024 + rd);
        asm volatile("s_waitcnt lgkmcnt(0)" : "+v"(a[0][0]), "+v"(a[0][1]), "+v"(a[0][2]), "+v"(a[0][3]), "+v"(a[1][0]), "+v"(a[1][1]), "+v"(a[1][2]), "+v"(a[1][3]) :: "memory");
#pragma unroll
        for (int k = 0; k < 8; ++k) *(v4u*)(la + k * 1024 + lane * 16) = a[k >> 2][4 + (k & 3)];
        FENCE();
#pragma unroll
        for (int k = 0; k < 8; ++k) a[k >> 2][4 + (k & 3)] = lds16(la, k * 1024 + rd);
    } else {
        const rsrc_t rx = mkrsrc(p.xb, 2048 * DM * 2);
        v4u xf[7], m0[7];
#pragma unroll
        for (int k = 0; k < 7; ++k) { const int pc = b * 7 + k, rt = pc / 14, ks = pc - rt * 14; xf[k] = ld16(rx, ((rt * 16 + (rho ^ ((rt & 1) * 4))) * DM + ks * 32 + g * 8) * 2, 0, 0); }
        const v4u sent = {SENT, SENT, SENT, SENT};
        if (b == 0) {
#pragma unroll
            for (int k = 0; k < 7; ++k) { const int c = k * 64 + lane; const unsigned* m = (const unsigned*)(p.mem0 + ((c >> 1) & 7) * DM + (c >> 4) * 16 + (c & 1) * 2); m0[k] = (v4u){m[0], m[2], m[4], m[6]}; }
        } else {
#pragma unroll
            for (int k = 0; k < 7; ++k) m0[k] = sent;
        }
        const unsigned rot = ((unsigned)__builtin_amdgcn_s_getreg(20 | (3 << 11)) - b) & 7u;
        if (lane < 16) st16s(rs, OFF_CTL + b * 256 + lane * 16, 0, (v4u){b * 2 == FL_ROT && lane == 0 ? rot : 0u, 0u, 0u, 0u});
#pragma unroll
        for (int k = 0; k < 16; ++k) {
            const int o = ((k & 7) * 1024 + lane * 16 + HO_MEM) & 8191;
            st16s(rs, (b & 3) == 3 && (b & 127) != 3 ? OOB : OFF_HO + b * 16384 + (k >> 3) * 8192 + o, 0, (k & 7) < 7 ? m0[k & 7] : sent);
        }
#pragma unroll
        for (int k = 0; k < 7; ++k) st16s(rs, OFF_XF + (b * 7 + k) * 1024 + lane * 16, 0, xf[k]);
        VMWAIT(24);
    }
    BAR();
    if (wave >= 7) return;
    STAMP(b == 0 && wave == 0, 4, 9); STAMP(wave == 0 && b < 64, 30, b);
    const float sc = which ? 1.f : 0.09016844005556021f;
    const int obase = (which ? OFF_BT : OFF_AP) + ((h * 28 + (c0 >> 4)) * 14 + (b0 >> 5) + wave) * 1024 + lane * 16;
    const int tra = (8 * g + (rho >> 2)) * 224 + (rho & 3) * 8;
    auto tile = [&](const v4u (&bf)[8], int ct) {
        v4f c0v = {0, 0, 0, 0}, c1v = {0, 0, 0, 0};
#pragma unroll
        for (int ks = 0; ks < 8; ++ks) { c0v = mfma32(a[0][ks], bf[ks], c0v); c1v = mfma32(a[1][ks], bf[ks], c1v); }
        st16s(rs, obase, ct * 14336, pk8(c0v * sc, c1v * sc));
    };
    auto fetch = [&](v4u (&bf)[8], int ct) {
        if (which == 0) {
#pragma unroll
            for (int ks = 0; ks < 8; ++ks) bf[ks] = lds16(lds, (ct * 8 + ks) * 1024 + rd);
        } else {
#pragma unroll
            for (int ks = 0; ks < 8; ++ks) { const v2u x = ldstr(lds, tra + ks * 7168 + ct * 32), y = ldstr(lds, tra + ks * 7168 + 896 + ct * 32); bf[ks] = (v4u){x.x, x.y, y.x, y.y}; }
        }
    };
    v4u f0[8], f1[8];
    fetch(f0, 0);
#pragma unroll 1
    for (int ct = 0; ct < 6; ct += 2) { fetch(f1, ct + 1); SCHED(); tile(f0, ct); fetch(f0, ct + 2); SCHED(); tile(f1, ct + 1); }
    tile(f0, 6);
    STAMP(b == 0 && wave == 0, 4, 10);
    STAMP(wave == 0 && b >= 192, 31, b & 63);
}

struct Ctx { rsrc_t rs; char* lds; int lane, wave, rho, g; };

DEV int slot_base(int s, int cal) { return OFF_HO + s * 65536 + (((cal >> (s >> 5)) & 1) << 13); }
#define STAGE_HEAD "s_add_i32 %[bud], %[bud], -1\n\ts_cmp_lt_i32 %[bud], 0\n\ts_cselect_b32 vcc_hi, -1, -2\n\tv_mov_b32 v233, vcc_hi\n\t"
#define STAGE_TAIL "v_cmp_gt_u32 vcc, v232, v233\n\ts_cbranch_vccnz 1b\n\ts_waitcnt lgkmcnt(0)\n\ts_barrier"
#define STAGE_CLOB "memory", "vcc", "scc", "v224", "v225", "v226", "v227", "v228", "v229", "v230", "v231", "v232", "v233"
DEV void stage1(rsrc_t rs, int vo, int so, int& budget) {
    asm volatile("1:\n\tbuffer_load_dwordx4 v[224:227], %[vo], %[rs], %[so] offen sc1\n\t" STAGE_HEAD
                 "s_waitcnt vmcnt(0)\n\tds_write_b128 %[vo], v[224:227]\n\tv_max3_u32 v232, v224, v225, v226\n\tv_max_u32 v232, v232, v227\n\t" STAGE_TAIL
                 : [bud] "+s"(budget) : [vo] "v"(vo), [rs] "s"(rs), [so] "s"(so) : STAGE_CLOB);
}
DEV void stage2(rsrc_t rs, int vo, int vo2, int so, int& budget) {
    asm volatile("1:\n\tbuffer_load_dwordx4 v[224:227], %[vo], %[rs], %[so] offen sc1\n\tbuffer_load_dwordx4 v[228:231], %[vo2], %[rs], %[so] offen sc1\n\t" STAGE_HEAD
                 "s_waitcnt vmcnt(1)\n\tds_write_b128 %[vo], v[224:227]\n\tv_max3_u32 v232, v224, v225, v226\n\t"
                 "s_waitcnt vmcnt(0)\n\tds_write_b128 %[vo2], v[228:231]\n\tv_max3_u32 v232, v232, v227, v228\n\tv_max3_u32 v232, v232, v229, v230\n\tv_max_u32 v232, v232, v231\n\t" STAGE_TAIL
                 : [bud] "+s"(budget) : [vo] "v"(vo), [vo2] "v"(vo2), [rs] "s"(rs), [so] "s"(so) : STAGE_CLOB);
}
DEV void give_up(rsrc_t rs) { st4s(rs, flagoff(FL_ABORT), 0, 1u); }
#define PIN2(a, b) asm volatile("" : "+v"(a), "+v"(b))
constexpr int CAL_STEP = 8;
#define TOUCH_LEAD 3
DEV void role_cal(const Params& p, const Ctx& c) {
    if (c.wave >= 2) return;
    unsigned best[2] = {0xffffffffu, 0xffffffffu};
    for (int r = 0; r < 20; ++r) {
#pragma unroll
        for (int k = 0; k < 2; ++k) {
            const int off = OFF_PROBE + c.wave * (2 << 20) + k * 8192 + r * 256;
            unsigned long long t0, t1; unsigned v;
            asm volatile("s_memtime %0\n\ts_waitcnt lgkmcnt(0)\n\tbuffer_load_dword %2, %3, %4, 0 offen sc1\n\ts_waitcnt vmcnt(0)\n\ts_memtime %1\n\ts_waitcnt lgkmcnt(0)"
                         : "=&s"(t0), "=&s"(t1), "=&v"(v) : "v"(off), "s"(c.rs) : "memory");
            best[k] = min(best[k], (unsigned)(t1 - t0));
        }
        if (r == 5 && c.wave == 0) { if (c.lane == 0) st4s(c.rs, flagoff(FL_CAL), 0, best[1] < best[0] ? 3u : 2u); VMWAIT(0); }
    }
    if (c.lane == 0) st4s(c.rs, flagoff(FL_CAL + 1 + c.wave), 0, best[1] < best[0] ? 3u : 2u);
    { const int k = c.lane & 3, o = (k == 0 ? 0 : k == 1 ? 7936 : 49152 + (k & 1) * 4096 + 2048) + (best[1] < best[0] ? 8192 : 0);
      for (int s = c.wave ? 32 : 12; s < 32 * (c.wave + 1); s += 4) {
          unsigned seen; if (!wait_flags<16>(c.rs, flagoff(FL_V), s - TOUCH_LEAD, seen)) return;
          unsigned v = ld4s(c.rs, OFF_HO + s * 65536 + o, 0); asm volatile("" :: "v"(v));
      } }
    if (c.wave == 0) { unsigned seen; wait_flags<16>(c.rs, flagoff(FL_V), 64, seen);
      auto S = [&](int sl, int k) { return (long long)__hip_atomic_load((unsigned long long*)(p.ws + OFF_DBG + (sl * 64 + k) * 8), __ATOMIC_RELAXED, __HIP_MEMORY_SCOPE_AGENT); };
      long long I = 1000; I = I < 0 ? 0 : I > 20000 ? 20000 : I;
      const long long until = (long long)__builtin_amdgcn_s_memrealtime() + 4 * I;
      while ((long long)__builtin_amdgcn_s_memrealtime() < until) __builtin_amdgcn_s_sleep(1); }
}
DEV int get_cal(const Ctx& c, int late, int& budget) {
    unsigned seen;
    if (!wait_flags<2>(c.rs, flagoff(FL_CAL + late + (late & c.lane)), 1u, seen)) budget = -1;
    return (__builtin_amdgcn_readlane(seen, 0) & 1) | ((__builtin_amdgcn_readlane(seen, 1) & 1) << 1);
}
DEV unsigned now() { return (unsigned)__builtin_amdgcn_s_memtime(); }
DEV void wait_until(unsigned t) {
    int rem = (int)(t - now());
    while (rem > 260) { __builtin_amdgcn_s_sleep(1); rem = (int)(t - now()); }
    rem -= 60;
    asm volatile("1:\n\ts_add_i32 %0, %0, -40\n\ts_cmp_gt_i32 %0, 0\n\ts_cbranch_scc1 1b" : "+s"(rem) :: "scc");
}
#define T_PM 2050
#define T_OS 2200
#define T_OM 400
#define T_OU 750
#define T_ST 0
#define T_PG 200
#define ABL(b) true
#define S_AT 6

#define MEM_TIMES_W(lbase)                                                                                           \
    v4u a_[7];                                                                                                       \
    _Pragma("unroll") for (int f = 0; f < 7; ++f) a_[f] = lds16(lds, (lbase) + f * 1024);                            \
    EXTRA_READS;                                                                                                     \
    SCHED();                                                                                                         \
    _Pragma("unroll") for (int f = 0; f < 7; ++f) { c0 = smf(a_[f], w[0][2 * f], w[0][2 * f + 1], c0, sidx); c1 = smf(a_[f], w[1][2 * f], w[1][2 * f + 1], c1, sidx); AFTER_STEP(f); } \
    const v2u tp = {pk2(c0.x + c0.z, c0.y + c0.w), pk2(c1.x + c1.z, c1.y + c1.w)};

#define VCLOB "v224", "v225", "v226", "v227", "v228", "v229", "v230", "v231", VCLOB_HI
#define VCLOB_HI "v232", "v233", "v234", "v235", "v236", "v237", "v238", "v239", \
              "v240", "v241", "v242", "v243", "v244", "v245", "v246", "v247", "v248", "v249", "v250", "v251", "v252", "v253", "v254", "v255"
#define PUBLISH_ASM(ST)                                                                                                                     \
    "s_cmp_lg_u32 %[pub], 7\n\ts_waitcnt lgkmcnt(0)\n\ts_barrier\n\ts_cbranch_scc1 9f\n\t"                                                  \
    "ds_read_b64 v[224:225], %[la]\n\tds_read_b64 v[226:227], %[la] offset:" #ST "*1\n\tds_read_b64 v[228:229], %[la] offset:" #ST "*2\n\t" \
    "ds_read_b64 v[230:231], %[la] offset:" #ST "*3\n\tds_read_b64 v[232:233], %[la] offset:" #ST "*4\n\t"                                  \
    "ds_read_b64 v[234:235], %[la] offset:" #ST "*5\n\tds_read_b64 v[236:237], %[la] offset:" #ST "*6\n\t"                                  \
    "ds_read_b64 v[238:239], %[la] offset:" #ST "*7\n\t"                                                                                    \
    "s_waitcnt lgkmcnt(6)\n\tv_pk_add_f32 v[224:225], v[224:225], v[226:227]\n\t"                                                           \
    "s_waitcnt lgkmcnt(4)\n\tv_pk_add_f32 v[228:229], v[228:229], v[230:231]\n\t"                                                           \
    "s_waitcnt lgkmcnt(2)\n\tv_pk_add_f32 v[232:233], v[232:233], v[234:235]\n\tv_pk_add_f32 v[224:225], v[224:225], v[228:229]\n\t"        \
    "s_waitcnt lgkmcnt(0)\n\tv_pk_add_f32 v[236:237], v[236:237], v[238:239]\n\tv_pk_add_f32 v[224:225], v[224:225], v[232:233]\n\t"        \
    "s_nop 0\n\tv_pk_add_f32 v[224:225], v[224:225], v[236:237]\n\t"                                                                        \
    "s_nop 0\n\tv_cvt_pk_bf16_f32 v224, v224, v225\n\ts_nop 1\n\t"                                                                          \
    "v_mov_b32_dpp v225, v224 quad_perm:[1,1,1,1] row_mask:0xf bank_mask:0xf\n\t"                                                           \
    "v_mov_b32_dpp v226, v224 quad_perm:[2,2,2,2] row_mask:0xf bank_mask:0xf\n\t"                                                           \
    "v_mov_b32_dpp v227, v224 quad_perm:[3,3,3,3] row_mask:0xf bank_mask:0xf\n\t"                                                           \
    "buffer_store_dwordx4 v[224:227], %[vo], %[rs], %[so] offen sc1\n\t"                                                                    \
    "9:\n\ts_barrier\n\ts_nop 0"

DEV void role_p(const Params& p, const Ctx& c, int idx) {
    const int h = idx >> 2, q = idx & 3, lane = c.lane, wave = c.wave, rho = c.rho, g = c.g;
    const rsrc_t rs = c.rs; char* lds = c.lds;
    const int om = q * 4 + wave;
    const bool isT = wave < 4 && om < 14, isX = wave < 4 && om >= 14;
    constexpr int L_MEM = 0, L_PART = 8192, L_DUMP = 10240;
    const int sidx = (rho & 2) ? 0xEEEE : 0x4444;
    const int fa = L_MEM + g * 256 + ((rho >> 2) * 2 + (rho & 1)) * 32 + ((rho >> 1) & 1) * 16;
    v2u sel; { const int r0 = rho - 2 * g; sel.x = (r0 == 0 ? 0x3f80u : 0u) | (r0 == 1 ? 0x3f800000u : 0u); sel.y = (r0 == 8 ? 0x3f80u : 0u) | (r0 == 9 ? 0x3f800000u : 0u); }
    const int piece = q < 3 ? wave : (wave < 2 ? wave : wave - 2);
    const int po = piece * 1024 + lane * 16;
    int budget = SPIN_CAP;
    if (isT) {
        v4u w[2][14];
#pragma unroll
        for (int t = 0; t < 2; ++t)
#pragma unroll
            for (int ks = 0; ks < 14; ++ks) w[t][ks] = ld16(rs, OFF_AP + (h * 28 + 2 * om + t) * 14336 + g * 256 + img(rho) * 16, ks * 1024, 0);
        int cal = get_cal(c, 0, budget);
        unsigned tref = now() - 65536;
        const int qa = L_MEM + (2 * om + (rho >> 3)) * 256 + (rho & 7) * 32 + g * 8;
        const int pw = (g >> 1) == (rho >> 3) ? L_PART + (2 * wave + (rho >> 3)) * 256 + (rho & 7) * 32 + (g & 1) * 16 : L_DUMP + lane * 16;
        const int tw = lane < 32 ? OFF_T + (h * 14 + om) * 512 + ((rho >> 3) * 2 + g) * 128 + (rho & 7) * 16 : OOB;
#pragma unroll 1
        for (int s = 0; s < NSEG && budget >= 0; ++s) {
            if (s == CAL_STEP) cal = get_cal(c, 1, budget);
            const int sb = slot_base(s, cal);
            v4f c0 = {0, 0, 0, 0}, c1 = {0, 0, 0, 0}; PIN2(c0, c1);
            wait_until(tref + T_PM);
            stage1(rs, po, sb + HO_MEM, budget);
            v2u mq;
#define EXTRA_READS mq = lds8(lds, qa)
#define AFTER_STEP(f)
            MEM_TIMES_W(fa)
#undef EXTRA_READS
            const v4f d2 = mfma16(tp, sel, (v4f){0, 0, 0, 0});
            const v2u d = {pk2(d2.x, d2.y), pk2(d2.z, d2.w)};
            *(v4f*)(lds + pw) = mfma16(d, mq, (v4f){0, 0, 0, 0});
            BAR();
            BAR0();
            tref = now();
            wait_until(tref + T_PG);
            if (ABL(8)) { auto x = __builtin_amdgcn_permlane32_swap(d.x, d.y, false, false);
              auto y = __builtin_amdgcn_permlane32_swap(x[0], x[0], false, false); auto z = __builtin_amdgcn_permlane32_swap(x[1], x[1], false, false);
              st16s(rs, tw, s * (NH * 7168), (v4u){y[0], z[0], y[1], z[1]}); }
        }
    } else if (isX) {
        v4u w[2][14];
        int cal = get_cal(c, 0, budget);
        const int gl0 = OFF_G + h * 28672 + (rho >> 3) * 1024 + g * 256 + (pperm(rho, 0) & 15) * 16;
        const int gfl = flagoff(FL_W + h * 7 + (lane & 3));
        const int ew = rho < 8 ? HO_E + h * 512 + g * 128 + rho * 16 : OOB, mw = g == 0 ? L_PART + 1024 + (rho & 7) * 32 + (rho >> 3) * 16 : L_DUMP + lane * 16;
        unsigned known = 0;
        if (om == 15) { BAR0(); BAR0(); BAR0(); }
#pragma unroll 1
        for (int s = om - 14; s < NSEG; s += 2) {
            if ((s | 1) == CAL_STEP + 1) cal = get_cal(c, 1, budget);
            const int sb = slot_base(s, cal);
            if (s < NSEG - 1 && (s < 2 || ABL(4))) {
                if (s >= 2) wait_until(now() + T_PG);
                if (known < (unsigned)s + 1) {
                    unsigned seen; if (!wait_flags<1>(rs, gfl, s + 1, seen)) break;
                    known = min(min((unsigned)__builtin_amdgcn_readlane(seen, 0), (unsigned)__builtin_amdgcn_readlane(seen, 1)), min((unsigned)__builtin_amdgcn_readlane(seen, 2), (unsigned)__builtin_amdgcn_readlane(seen, 3)));
                }
#pragma unroll
                for (int t = 0; t < 2; ++t)
#pragma unroll
                    for (int ks = 0; ks < 14; ++ks) w[t][ks] = ld16(rs, gl0 + t * 64, s * (NH * 28672) + ks * 2048, 1);
            }
            if (s >= 2) { BAR0(); BAR0(); BAR0(); }
            v4f c0 = {0, 0, 0, 0}, c1 = {0, 0, 0, 0}; PIN2(c0, c1);
            VMWAIT(0);
            STAMP(idx == 3 && s == 0, 4, 12);
            BAR();
            if (s < NSEG - 1) {
#define EXTRA_READS
                MEM_TIMES_W(fa)
#undef EXTRA_READS
                const v4f d2 = mfma16(tp, sel, (v4f){0, 0, 0, 0});
                float m = fmx(fmx(d2.x, d2.y), fmx(d2.z, d2.w));
                m = max_g(fmx(m, ror8(m)));
                *(v4f*)(lds + mw) = (v4f){-m, -m, -m, -m};
                SCHED();
                BAR();
                const float e0 = __builtin_amdgcn_exp2f(d2.x - m), e1 = __builtin_amdgcn_exp2f(d2.y - m), e2 = __builtin_amdgcn_exp2f(d2.z - m), e3 = __builtin_amdgcn_exp2f(d2.w - m);
                const unsigned u0 = pk2(e0, e1), u1 = pk2(e2, e3);
                st16s(rs, ew, sb, (v4u){u0, u1, dppu<0x128>(u0), dppu<0x128>(u1)});
            } else BAR0();
            BAR0();
        }
        if (om == 14) { BAR0(); BAR0(); BAR0(); }
    } else {
        if (wave == 4) { *(v4u*)(lds + L_PART + lane * 16) = (v4u){0u, 0u, 0u, 0u}; *(v4u*)(lds + L_PART + 1024 + lane * 16) = (v4u){0u, 0u, 0u, 0u}; }
        int cal = get_cal(c, 0, budget);
        unsigned tref = now() - 65536;
        STAMP(idx == 0 && wave == 7, 4, 1);
        const bool stg = q == 3 || wave != 7, two = q == 3 && wave == 5;
        const int pr = L_PART + (lane & 31) * 8, pubo = lane < 32 && (lane & 3) == 0 ? HO_PART + idx * 128 + lane * 4 : OOB;
#pragma unroll 1
        for (int s = 0; s < NSEG && budget >= 0; ++s) {
            if (s == CAL_STEP) cal = get_cal(c, 1, budget);
            const int sb = slot_base(s, cal);
            if (stg) {
                wait_until(tref + T_PM);
                if (two) stage2(rs, po, 6144 + lane * 16, sb + HO_MEM, budget); else stage1(rs, po, sb + HO_MEM, budget);
            } else BAR0();
            STAMP(idx == 0 && wave == 7, 0, s); STAMP(idx == 3 && wave == 6, 12, s);
            asm volatile(PUBLISH_ASM(256) :: [pub] "s"(wave), [la] "v"(pr), [vo] "v"(pubo), [rs] "s"(rs), [so] "s"(sb) : "memory", "scc", VCLOB);
            tref = now();
            if (wave == 7) {
                STAMP(idx == 0, 1, s);
                LSTAMP(idx == 0 && (s == 1 || s == 32 || s == 62), 14, s);
                if (lane == 0) st4s(rs, flagoff(FL_T + idx), 0, s);
            }
        }
    }
    if (budget < 0) give_up(rs);
    VMWAIT(0);
    BAR();
    if (wave == 7 && lane == 0) st4s(rs, flagoff(FL_T + idx), 0, NSEG);
}

DEV void role_o(const Params& p, const Ctx& c, int t) {
    const int lane = c.lane, wave = c.wave, rho = c.rho, g = c.g;
    const rsrc_t rs = c.rs; char* lds = c.lds;
    constexpr int L_MEM = 0, L_OUT = 8192, L_DUMP = 12288, L_V = 13312;
    v4u w[2][14];
#pragma unroll
    for (int k = 0; k < 2; ++k)
#pragma unroll
        for (int ks = 0; ks < 14; ++ks) w[k][ks] = ld16(rs, OFF_BT + ((2 * wave + k) * 28 + t) * 14336 + lane * 16, ks * 1024, 0);
    int budget = SPIN_CAP;
    int cal = get_cal(c, 0, budget), caln = cal;
    const int sidx = (rho & 2) ? 0xEEEE : 0x4444;
    const int fa = L_MEM + g * 256 + ((rho >> 2) * 2 + (rho & 1)) * 32 + ((rho >> 1) & 1) * 16;
    const int hb = rho >> 3, hd = 2 * wave + hb, i = rho & 7;
    const int pa = HO_PART + hd * 512 + g * 128 + i * 16, ea = HO_E + hd * 512 + g * 128 + i * 16;
    const int ul = OFF_U + (t * 16 + 2 * wave) * 1024 + lane * 16;
    const int ufl = flagoff(FL_W + rho * 7 + ((14 + (t >> 1)) >> 2));
    const int mo = (lane & 3) == 0 ? HO_MEM + t * 256 + lane * 4 : OOB;
    const int p2 = (lane & 4) ? 2 * (lane & 3) + 1 : 2 * (lane & 3);
    const int ra = L_OUT + (lane >> 3) * 64 + p2 * 8;
    const int ow = hb == 0 ? L_OUT + wave * 512 + i * 64 + g * 16 : L_DUMP + lane * 16;
    const int po = wave * 1024 + lane * 16, po2 = min(wave + 4, 6) * 1024 + lane * 16;
    const v4u ones = {0x3f803f80u, 0x3f803f80u, 0x3f803f80u, 0x3f803f80u};
    const v2u ones2 = {0x3f803f80u, 0x3f803f80u};
    const unsigned mlo = hb ? 0u : 0xffffffffu, mhi = ~mlo;
    const unsigned long long hbm = 0xff00ff00ff00ff00ull;
    v4u psel; { const int kq = (rho >> 2) * 2 + (rho & 1); const unsigned one = (kq & 1) ? 0x3f800000u : 0x3f80u;
        psel = (v4u){(kq >> 1) == 0 ? one : 0u, (kq >> 1) == 1 ? one : 0u, (kq >> 1) == 2 ? one : 0u, (kq >> 1) == 3 ? one : 0u}; }
    asm volatile("v_mov_b32 v215, 0" ::: "v215");
    auto check_u = [&](int s) {
        unsigned long long lack; unsigned seen;
        asm volatile("v_cmp_ge_u32 %0, %1, v215" : "=s"(lack) : "s"(s) : "memory");
        if (lack != 0 && !wait_flags<1>(rs, ufl, s + 1, seen)) budget = -1;
    };
    auto load_u = [&](int s) {
        asm volatile("s_nop 4\n\tbuffer_load_dwordx4 v[216:219], %0, %1, %2 offen sc1\n\tbuffer_load_dwordx4 v[220:223], %0, %1, %2 offen offset:1024 sc1"
                     :: "v"(ul), "s"(rs), "s"(s * (NH * 28672)) : "memory", "v216", "v217", "v218", "v219", "v220", "v221", "v222", "v223");
    };
    auto store_v = [&](int s) { st16s(rs, OFF_V + t * 4096 + (wave & 3) * 1024 + lane * 16, s * (NH * 7168), lds16(lds, L_V + (s & 1) * 4096 + (wave & 3) * 1024 + lane * 16)); };
    ACC_DECL;
    unsigned tref = now() - 65536;
    auto mem_times_b = [&](int sb, int s, bool last) -> v2u {
        v4f c0 = {0, 0, 0, 0}, c1 = {0, 0, 0, 0};
        if (wave < 4) stage2(rs, po, po2, sb + HO_MEM, budget);
        else BAR0();
        ACC_T(2);
#undef AFTER_STEP
#define AFTER_STEP(f) if (f == S_AT && !last) { SCHED(); ACC_T(4); if (T_OS) wait_until(tref + T_OS); ACC_T(5); asm volatile("buffer_load_dwordx4 v[224:227], %0, %2, %3 offen sc1\n\tbuffer_load_dwordx4 v[228:231], %1, %2, %3 offen sc1" \
                          :: "v"(pa), "v"(ea), "s"(rs), "s"(sb) : "memory", VCLOB); SCHED(); }
#define EXTRA_READS if (wave < 4 && !last && ABL(1)) load_u(s)
        MEM_TIMES_W(fa)
#undef EXTRA_READS
        *(v2u*)(lds + L_V + (s & 1) * 4096 + wave * 512 + lane * 8) = tp;
        return tp;
    };
    STAMP(t == 0 && wave == 0, 4, 13);
    check_u(1);
    STAMP(t == 0 && wave == 0, 4, 14);
    if (wave >= 4) load_u(0);
#pragma unroll 1
    for (int s = 0; s < NSEG - 1 && budget >= 0; ++s) {
        const int sb = slot_base(s, s < CAL_STEP ? cal : caln), sn = slot_base(s + 1, s + 1 < CAL_STEP ? cal : caln);
        ACC_T(0);
        const v2u vp = mem_times_b(sb, s, false);
        ACC_T(3);
        asm volatile(
            "s_branch 3f\n\t"
            "1:\n\t"
            "buffer_load_dwordx4 v[224:227], %[pa], %[rs], %[sb] offen sc1\n\t"
            "buffer_load_dwordx4 v[228:231], %[ea], %[rs], %[sb] offen sc1\n\t"
            "3:\n\t"
            "s_add_i32 %[bud], %[bud], -1\n\ts_cmp_lt_i32 %[bud], 0\n\ts_cselect_b32 vcc_hi, -1, -2\n\tv_mov_b32 v253, vcc_hi\n\t"
            "s_waitcnt vmcnt(1)\n\t"
            "v_mfma_f32_16x16x32_bf16 v[232:235], %[psel], v[224:227], 0\n\t"
            "v_max3_u32 v250, v224, v225, v226\n\t"
            "s_waitcnt vmcnt(0)\n\t"
            "v_mfma_f32_16x16x32_bf16 v[244:247], %[ones], v[228:231], 0\n\t"
            "v_max3_u32 v251, v228, v229, v230\n\t"
            "v_mfma_f32_16x16x32_bf16 v[236:239], v[216:219], v[228:231], 0\n\t"
            "v_max3_u32 v250, v250, v227, v231\n\t"
            "v_mfma_f32_16x16x32_bf16 v[240:243], v[220:223], v[228:231], 0\n\t"
            "v_max_u32 v250, v250, v251\n\t"
            "v_cmp_gt_u32 vcc, v250, v253\n\t"
            "s_cbranch_vccnz 1b\n\t"
            "v_exp_f32 v250, v232\n\tv_exp_f32 v251, v233\n\t"
            "v_max_f32 v252, v232, v233\n\t"
            "v_cvt_pk_bf16_f32 v250, v250, v251\n\t"
            "v_cmp_lt_f32 vcc, 0x42800000, v252\n\t"
            "v_and_b32 v248, v250, %[mlo]\n\tv_and_b32 v249, v250, %[mhi]\n\t"
            "v_cndmask_b32 v236, v236, v240, %[hbm]\n\tv_cndmask_b32 v237, v237, v241, %[hbm]\n\t"
            "v_mfma_f32_16x16x16_bf16 v[244:247], %[ones2], v[248:249], v[244:247]\n\t"
            "v_cndmask_b32 v238, v238, v242, %[hbm]\n\tv_cndmask_b32 v239, v239, v243, %[hbm]\n\t"
            "s_nop 1\n\t"
            "v_mfma_f32_16x16x16_bf16 v[236:239], %[vp], v[248:249], v[236:239]\n\t"
            "s_cbranch_vccz 2f\n\t"
            "s_nop 7\n\t"
            "v_max_f32 v252, 0, v252\n\tv_mov_b32 v253, v252\n\ts_nop 1\n\tv_permlane16_swap_b32 v252, v253\n\ts_nop 1\n\t"
            "v_max_f32 v252, v252, v253\n\tv_mov_b32 v253, v252\n\ts_nop 1\n\tv_permlane32_swap_b32 v252, v253\n\ts_nop 1\n\t"
            "v_max_f32 v252, v252, v253\n\t"
            "v_mfma_f32_16x16x32_bf16 v[244:247], %[ones], v[228:231], 0\n\t"
            "v_mfma_f32_16x16x32_bf16 v[236:239], v[216:219], v[228:231], 0\n\t"
            "v_sub_f32 v250, v232, v252\n\tv_sub_f32 v251, v233, v252\n\t"
            "v_exp_f32 v250, v250\n\tv_exp_f32 v251, v251\n\tv_exp_f32 v253, -v252\n\ts_nop 0\n\t"
            "v_cvt_pk_bf16_f32 v250, v250, v251\n\t"
            "v_and_b32 v248, v250, %[mlo]\n\tv_and_b32 v249, v250, %[mhi]\n\ts_nop 7\n\t"
            "v_cndmask_b32 v236, v236, v240, %[hbm]\n\tv_cndmask_b32 v237, v237, v241, %[hbm]\n\tv_cndmask_b32 v238, v238, v242, %[hbm]\n\tv_cndmask_b32 v239, v239, v243, %[hbm]\n\t"
            "v_mul_f32 v236, v236, v253\n\tv_mul_f32 v237, v237, v253\n\tv_mul_f32 v238, v238, v253\n\tv_mul_f32 v239, v239, v253\n\t"
            "v_mul_f32 v244, v244, v253\n\tv_mul_f32 v245, v245, v253\n\tv_mul_f32 v246, v246, v253\n\tv_mul_f32 v247, v247, v253\n\ts_nop 1\n\t"
            "v_mfma_f32_16x16x16_bf16 v[244:247], %[ones2], v[248:249], v[244:247]\n\t"
            "v_mfma_f32_16x16x16_bf16 v[236:239], %[vp], v[248:249], v[236:239]\n\t"
            "s_nop 7\n\t"
            "2:\n\t"
            "s_nop 1\n\t"
            "v_rcp_f32 v252, v244\n\t"
            "s_nop 3\n\t"
            "v_mul_f32 v236, v236, v252\n\tv_mul_f32 v237, v237, v252\n\tv_mul_f32 v238, v238, v252\n\tv_mul_f32 v239, v239, v252\n\t"
            "v_add_f32_dpp v240, v236, v236 row_ror:8 row_mask:0xf bank_mask:0xf\n\tv_add_f32_dpp v241, v237, v237 row_ror:8 row_mask:0xf bank_mask:0xf\n\t"
            "v_add_f32_dpp v242, v238, v238 row_ror:8 row_mask:0xf bank_mask:0xf\n\tv_add_f32_dpp v243, v239, v239 row_ror:8 row_mask:0xf bank_mask:0xf\n\t"
            "ds_write_b128 %[ow], v[240:243]\n\t"
            PUBLISH_ASM(512)
            : [bud] "+s"(budget)
            : [pa] "v"(pa), [ea] "v"(ea), [rs] "s"(rs), [sb] "s"(sb), [psel] "v"(psel), [ones] "v"(ones), [ones2] "v"(ones2), [vp] "v"(vp),
              [mlo] "v"(mlo), [mhi] "v"(mhi), [hbm] "s"(hbm), [ow] "v"(ow),
              [pub] "s"(wave), [la] "v"(ra), [vo] "v"(mo), [so] "s"(sn)
            : "memory", "vcc", "scc", VCLOB);
        tref = now();
        STAMP(t == 0 && wave == 7, 3, s);
        if (wave == 7 && lane == 0) st4s(rs, flagoff(FL_V + t), 0, s);
        if (s + 2 == CAL_STEP) caln = get_cal(c, 1, budget);
        if (wave == 4) check_u(min(s + 2, NSEG - 2));
        ACC_T(6);
        if (wave < 4) wait_until(tref + T_OM);
        else {
            wait_until(tref + T_OU + (wave - 4) * T_ST);
            if (s + 1 < NSEG - 1 && ABL(1)) load_u(s + 1);
            if (wave == 4) asm volatile("buffer_load_dword v215, %0, %1, 0 offen sc1" :: "v"(ufl), "s"(rs) : "memory", "v215");
            if (ABL(2)) store_v(s);
        }
        ACC_T(7);
    }
    VMWAIT(0);
    ACC_OUT(t * 8 + wave);
    mem_times_b(slot_base(NSEG - 1, caln), NSEG - 1, true);
    if (budget < 0) give_up(rs);
    BAR();
    if (wave >= 4) store_v(NSEG - 1);
    VMWAIT(0);
    BAR0();
    if (wave == 7 && lane == 0) st4s(rs, flagoff(FL_V + t), 0, NSEG);
}

DEV void role_w(const Params& p, const Ctx& c, int wi) {
    const int lane = c.lane, wave = c.wave, rho = c.rho, g = c.g;
    const rsrc_t rs = c.rs; char* lds = c.lds;
    const int h = wi / 7, unit = (wi - h * 7) * 4 + (wave & 3), set = wave >> 2;
    const bool isG = unit < 14;
    auto stage = [&](int it) {
        const int sg = 2 * it + set, lb = (set * 2 + (it & 1)) * 28672;
#pragma unroll
        for (int k = 0; k < 7; ++k) { const int pc = (wave & 3) * 7 + k; dma16(rs, OFF_XF + lane * 16, sg * 28672 + pc * 1024, lb + pc * 1024); }
    };
    v4u w[2][14];
    auto load_w = [&]() {
#pragma unroll
        for (int t = 0; t < 2; ++t) {
            const int row = isG ? unit * 32 + pperm(rho, t) : (unit - 14) * 32 + t * 16 + rho;
#pragma unroll
            for (int ks = 0; ks < 14; ++ks) w[t][ks] = ld16(rs, (isG ? OFF_AP : OFF_BT) + (h * 28 + (row >> 4)) * 14336 + g * 256 + (row & 15) * 16, ks * 1024, 0);
        }
    };
    int xa[2];
#pragma unroll
    for (int k = 0; k < 2; ++k) { const int key = isG ? k * 16 + rho : pperm(rho, k), kt = key >> 4; xa[k] = kt * 14336 + (g * 16 + ((key & 15) ^ (kt * 4))) * 16; }
    const int ob = (isG ? OFF_G + (h * 14 + unit) * 2048 : OFF_U + ((unit - 14) * 32 + h) * 1024) + lane * 16;
    const int ostep = isG ? 1024 : 16384;
    auto segment = [&](int buf, int sg) {
        const char* xb = lds + buf * 28672;
        v4f a00 = {0, 0, 0, 0}, a01 = a00, a10 = a00, a11 = a00;
        if (isG) {
#pragma unroll
            for (int ks = 0; ks < 14; ++ks) { const v4u x0 = lds16(xb, xa[0] + ks * 1024), x1 = lds16(xb, xa[1] + ks * 1024);
                a00 = mfma32(w[0][ks], x0, a00); a01 = mfma32(w[1][ks], x0, a01); a10 = mfma32(w[0][ks], x1, a10); a11 = mfma32(w[1][ks], x1, a11); }
        } else {
#pragma unroll
            for (int ks = 0; ks < 14; ++ks) { const v4u x0 = lds16(xb, xa[0] + ks * 1024), x1 = lds16(xb, xa[1] + ks * 1024);
                a00 = mfma32(x0, w[0][ks], a00); a01 = mfma32(x1, w[0][ks], a01); a10 = mfma32(x0, w[1][ks], a10); a11 = mfma32(x1, w[1][ks], a11); }
        }
        st16s(rs, ob, sg * (NH * 28672), pk8(a00, a01)); st16s(rs, ob + ostep, sg * (NH * 28672), pk8(a10, a11));
    };
    if (set == 0) load_w();
    if (wave == 7) *(int*)(lds + LDS_MAIN - 32) = 0;
    stage(0);
    VMWAIT(0);
    BAR();
    STAMP(wi == 20 && wave == 1, 4, 2);
#pragma unroll 1
    for (int it = -1; it < 32; ++it) {
        if (it == -1 && set == 1) load_w();
        if (it >= 0 && it + 1 < 32) stage(it + 1);
        if (it > 0 || set == 0) segment(it > 0 ? set * 2 + (it & 1) : 2 * (it + 1), it > 0 ? 2 * it + set : it + 1);
        if (it < 0) continue;
        if (it == 0 && set == 0) {
            VMWAIT(0);
            if (lane == 0 && __hip_atomic_fetch_add((int*)(lds + LDS_MAIN - 32), 1, __ATOMIC_RELAXED, __HIP_MEMORY_SCOPE_WORKGROUP) == 3) st4s(rs, flagoff(FL_W + wi), 0, 2);
            STAMP(wi == 20 && wave == 1, 4, 15);
        }
        const bool eager = it < 4 || it == 31;
        if (eager) VMWAIT(0); else VMWAIT(2);
        BAR();
        if (wave == 0 && lane == 0) st4s(rs, flagoff(FL_W + wi), 0, eager ? 2 * it + 2 : 2 * it);
        if (it == 12 && wave == 0 && wi >= 48) {
            unsigned seen; wait_flags<8>(rs, flagoff(FL_TAB), 28, seen);
            __builtin_amdgcn_fence(__ATOMIC_ACQUIRE, "agent");
            VMWAIT(0);
        }
        STAMP(wi == 20 && wave == 1, 5, it); CSTAMP(wi == 20 && wave == 1, 6, it);
    }
}

DEV int f_first(int set, int wave) { return set ? 33 + 2 * wave : wave; }
DEV int f_next(int s, int wave) { return s >= 32 ? s + 16 : s >= 24 ? 32 + 2 * wave : s + 8; }
DEV void role_f1(const Params& p, const Ctx& c, int h, int set) {
    const int lane = c.lane, wave = c.wave, rho = c.rho, g = c.g;
    const rsrc_t rs = c.rs;
    const int gfl = flagoff(FL_W + h * 7 + (lane & 3)), tfl = flagoff(FL_T + h * 4 + (lane & 3));
    const int gl0 = OFF_G + h * 28672 + (rho >> 3) * 1024 + g * 256 + (pperm(rho, 0) & 15) * 16;
    const int tl = OFF_T + h * 7168 + g * 128 + ((rho >> 2) * 2 + (rho & 1)) * 16;
    const float ninf = -__builtin_inff();
    bool wdone = false;
#pragma unroll 1
    for (int s = f_first(set, wave); s < NSEG; s = f_next(s, wave)) {
        STAMP(h == 0, 16, s);
        unsigned seen;
        if (!wdone) { if (!wait_flags<8>(rs, gfl, s + 1, seen)) return; wdone = __builtin_amdgcn_ballot_w64(seen < NSEG) == 0; }
        v4u x[2][14];
        v4f a[2][2] = {{{0, 0, 0, 0}, {0, 0, 0, 0}}, {{0, 0, 0, 0}, {0, 0, 0, 0}}};
#pragma unroll
        for (int hf = 0; hf < 2; ++hf) {
            v4u y[2][7];
#pragma unroll
            for (int k = 0; k < 7; ++k) {
#pragma unroll
                for (int qt = 0; qt < 2; ++qt) x[qt][hf * 7 + k] = ld16(rs, OFF_XF + qt * 14336 + g * 256 + (rho ^ (qt * 4)) * 16, s * 28672 + (hf * 7 + k) * 1024, 0);
                y[0][k] = ld16(rs, gl0, s * (NH * 28672) + (hf * 7 + k) * 2048, 1); y[1][k] = ld16(rs, gl0 + 64, s * (NH * 28672) + (hf * 7 + k) * 2048, 1);
            }
            SCHED();
#pragma unroll
            for (int k = 0; k < 7; ++k)
#pragma unroll
                for (int qt = 0; qt < 2; ++qt) { a[qt][0] = mfma32(y[0][k], x[qt][hf * 7 + k], a[qt][0]); a[qt][1] = mfma32(y[1][k], x[qt][hf * 7 + k], a[qt][1]); }
        }
        float v[2][8], m[2];
#pragma unroll
        for (int qt = 0; qt < 2; ++qt) {
            const float r[8] = {a[qt][0].x, a[qt][0].y, a[qt][0].z, a[qt][0].w, a[qt][1].x, a[qt][1].y, a[qt][1].z, a[qt][1].w};
            m[qt] = ninf;
#pragma unroll
            for (int k = 0; k < 8; ++k) { v[qt][k] = (8 * g + k <= qt * 16 + rho) ? r[k] : ninf; m[qt] = fmx(m[qt], v[qt][k]); }
        }
        asm volatile("" : "+v"(m[0]), "+v"(m[1]));
        STAMP(h == 0, 17, s);
        if (!wait_flags<2>(rs, tfl, s + 1, seen)) return;
        STAMP(h == 0, 18, s);
        v4f am[2] = {{0, 0, 0, 0}, {0, 0, 0, 0}};
        { v4u y[14];
#pragma unroll
          for (int ks = 0; ks < 14; ++ks) y[ks] = ld16(rs, tl, s * (NH * 7168) + ks * 512, 1);
          SCHED();
#pragma unroll
          for (int ks = 0; ks < 14; ++ks) { am[0] = mfma32(y[ks], x[0][ks], am[0]); am[1] = mfma32(y[ks], x[1][ks], am[1]); } }
        unsigned pm[2];
#pragma unroll
        for (int qt = 0; qt < 2; ++qt) {
            const float mm = max_g(fmx(m[qt], fmx(am[qt].x, am[qt].y)));
            const float e0 = __builtin_amdgcn_exp2f(am[qt].x - mm), e1 = __builtin_amdgcn_exp2f(am[qt].y - mm);
            float z = e0 + e1;
#pragma unroll
            for (int k = 0; k < 8; ++k) { v[qt][k] = __builtin_amdgcn_exp2f(v[qt][k] - mm); z += v[qt][k]; }
            const float rz = __builtin_amdgcn_rcpf(sum_g(z));
            st16s(rs, OFF_PX + (h * 2 + qt) * 1024 + lane * 16, s * (NH * 2048),
                  (v4u){pk2(v[qt][0] * rz, v[qt][1] * rz), pk2(v[qt][2] * rz, v[qt][3] * rz), pk2(v[qt][4] * rz, v[qt][5] * rz), pk2(v[qt][6] * rz, v[qt][7] * rz)});
            pm[qt] = pk2(e0 * rz, e1 * rz);
        }
        st16s(rs, (lane & 1) == 0 ? OFF_PM + h * 512 + lane * 8 : OOB, s * (NH * 512), (v4u){pm[0], pm[1], dppu<0xF5>(pm[0]), dppu<0xF5>(pm[1])});
        VMWAIT(0);
        if (lane == 0) st4s(rs, flagoff((set ? FL_P2 : FL_P) + h * 8 + wave), 0, s + 1);
        STAMP(h == 0, 19, s);
    }
}

DEV void make_tables(const Params& p, const Ctx& c, int t) {
    const int lane = c.lane, wave = c.wave, rho = c.rho, g = c.g;
    const rsrc_t rs = c.rs;
    for (int ch = t * 512 + wave * 64 + lane; ch < 2 * 28672; ch += 28 * 512) {
        float v[8];
        if (ch < 28672) { const int fr = ch >> 6, tl = fr / 14, d0 = (fr - tl * 14) * 32 + g * 8, ri = tl >> 4, k = (tl & 15) * 16 + rho;
#pragma unroll
            for (int j = 0; j < 8; ++j) { const float ph = (float)((k * (d0 + j)) % 448) * (1.f / 448.f); v[j] = k > 224 ? 0.f : ri ? -__builtin_amdgcn_sinf(ph) : __builtin_amdgcn_cosf(ph); }
        } else { const int fr = (ch - 28672) >> 6, d = (fr >> 4) * 16 + rho, ri = (fr >> 3) & 1, k0 = (fr & 7) * 32 + g * 8;
#pragma unroll
            for (int j = 0; j < 8; ++j) { const int k = k0 + j; const float ph = (float)((k * d) % 448) * (1.f / 448.f); const bool edge = k == 0 || k == 224;
                const float wgt = (edge ? 1.f : 2.f) / (448.f * 2048.f); v[j] = k > 224 ? 0.f : ri ? (edge ? 0.f : -wgt * __builtin_amdgcn_sinf(ph)) : wgt * __builtin_amdgcn_cosf(ph); }
        }
        st16s(rs, OFF_FD + ch * 16, 0, (v4u){pk2(v[0], v[1]), pk2(v[2], v[3]), pk2(v[4], v[5]), pk2(v[6], v[7])});
    }
    VMWAIT(0);
    BAR();
    if (wave == 0 && lane == 0) __hip_atomic_fetch_add((unsigned*)(p.ws + flagoff(FL_TAB)), 1u, __ATOMIC_RELAXED, __HIP_MEMORY_SCOPE_AGENT);
}
DEV void role_f2(const Params& p, const Ctx& c, int t, int set) {
    const int lane = c.lane, wave = c.wave, rho = c.rho, g = c.g;
    const rsrc_t rs = c.rs;
    const int ufl = flagoff(FL_W + rho * 7 + ((14 + (t >> 1)) >> 2)), pfl = flagoff(lane < 16 ? (set ? FL_P2 : FL_P) + lane * 8 + wave : FL_V + t);
    const int ul = OFF_U + t * 16384 + lane * 16;
    bool wdone = false;
#pragma unroll 1
    for (int s = f_first(set, wave); s < NSEG; s = f_next(s, wave)) {
        unsigned seen;
        if (!wdone) { if (!wait_flags<8>(rs, ufl, s + 1, seen)) return; wdone = __builtin_amdgcn_ballot_w64(seen < NSEG) == 0; }
        STAMP(t == 0, 20, s);
        v4u u[16];
#pragma unroll
        for (int h = 0; h < 16; ++h) u[h] = ld16(rs, ul, s * (NH * 28672) + h * 1024, 1);
        if (!wait_flags<2>(rs, pfl, s + 1, seen)) return;
        STAMP(t == 0, 21, s);
        v4f a[2] = {{0, 0, 0, 0}, {0, 0, 0, 0}}, m[2] = {{0, 0, 0, 0}, {0, 0, 0, 0}};
        v4u px[16]; v2u pm[16], vv[8];
#pragma unroll
        for (int h = 0; h < 16; ++h) px[h] = ld16(rs, OFF_PX + lane * 16, s * (NH * 2048) + h * 2048, 1);
#pragma unroll
        for (int h = 0; h < 16; ++h) { pm[h] = ld8s(rs, OFF_PM + lane * 8, s * (NH * 512) + h * 512); if (h & 1) vv[h >> 1] = ld8s(rs, OFF_V + t * 4096 + lane * 8, s * (NH * 7168) + (h >> 1) * 512); }
        SCHED();
#pragma unroll
        for (int h = 0; h < 16; ++h) { a[0] = mfma32(u[h], px[h], a[0]); SCHED(); px[h] = ld16(rs, OFF_PX + 1024 + lane * 16, s * (NH * 2048) + h * 2048, 1); SCHED(); }
#pragma unroll
        for (int k = 0; k < 8; ++k) { m[0] = mfma16(vv[k], (v2u){pm[2 * k].x, pm[2 * k + 1].x}, m[0]); m[1] = mfma16(vv[k], (v2u){pm[2 * k].y, pm[2 * k + 1].y}, m[1]); }
        SCHED();
#pragma unroll
        for (int h = 0; h < 16; ++h) a[1] = mfma32(u[h], px[h], a[1]);
#pragma unroll
        for (int qt = 0; qt < 2; ++qt) { const v4f o = a[qt] + m[qt];
            *(v2u*)(p.out + (size_t)(s * 32 + qt * 16 + rho) * 896 + DM + t * 16 + g * 4) = (v2u){pk2(o.x, o.y), pk2(o.z, o.w)}; }
        STAMP(t == 0, 22, s);
    }
}

DEV bool fft_sync(const Params& p, const Ctx& c, int fl) {
    VMWAIT(0);
    BAR();
    int* okw = (int*)(c.lds + LDS_MAIN - 16);
    if (c.wave == 0) {
        if (c.lane == 0) __hip_atomic_fetch_add((unsigned*)(p.ws + flagoff(fl)), 1u, __ATOMIC_RELAXED, __HIP_MEMORY_SCOPE_AGENT);
        unsigned seen; const bool ok = wait_flags<2>(c.rs, flagoff(fl), 64, seen);
        if (c.lane == 0) *okw = ok;
    }
    BAR();
    return *okw != 0;
}
DEV v4u neg8(v4u v) { return v ^ 0x80008000u; }
DEV void twid(int a, const int (&n)[8], int mask, float inv, v4u& C, v4u& S) {
    float cs[8], sn[8];
#pragma unroll
    for (int j = 0; j < 8; ++j) { const float ph = (float)((a * n[j]) & mask) * inv; cs[j] = __builtin_amdgcn_cosf(ph); sn[j] = __builtin_amdgcn_sinf(ph); }
    C = (v4u){pk2(cs[0], cs[1]), pk2(cs[2], cs[3]), pk2(cs[4], cs[5]), pk2(cs[6], cs[7])};
    S = (v4u){pk2(sn[0], sn[1]), pk2(sn[2], sn[3]), pk2(sn[4], sn[5]), pk2(sn[6], sn[7])};
}
DEV void role_fft(const Params& p, const Ctx& c, int job) {
    const int lane = c.lane, wave = c.wave, rho = c.rho, g = c.g;
    const rsrc_t rs = c.rs; char* lds = c.lds;
    const v4f z4 = {0, 0, 0, 0};
    STAMP(wave == 1, 23, job);
    {
        const rsrc_t rx = mkrsrc(p.xa, 2048 * DM * 2);
        for (int pc = wave; pc < 28; pc += 8) { const int nt = pc >= 14, ks = pc - nt * 14; dma16(rx, ((64 * (nt * 16 + rho) + job) * DM + ks * 32 + g * 8) * 2, 0, pc * 1024); }
        STAMP(wave == 1, 24, job);
        v4u C[2], S[2];
        { int n[8];
#pragma unroll
          for (int j = 0; j < 8; ++j) n[j] = 64 * (16 * (j >> 2) + 4 * g + (j & 3)) + job;
          twid(rho, n, 2047, 1.f / 2048.f, C[0], S[0]); twid(16 + rho, n, 2047, 1.f / 2048.f, C[1], S[1]); }
        VMWAIT(0);
        BAR();
        v4f zr[2][2], zi[2][2];
#pragma unroll
        for (int t = 0; t < 2; ++t) {
            const int fo = OFF_FD + (wave * 2 + (rho >> 3)) * 14336 + g * 256 + (pperm(rho, t) & 15) * 16;
            v4f r0 = z4, r1 = z4, i0 = z4, i1 = z4;
#pragma unroll
            for (int hf = 0; hf < 2; ++hf) {
                v4u fr[7], fi[7];
#pragma unroll
                for (int k = 0; k < 7; ++k) { fr[k] = ld16(rs, fo, (hf * 7 + k) * 1024, 0); fi[k] = ld16(rs, fo, 256 * DM * 2 + (hf * 7 + k) * 1024, 0); }
                SCHED();
#pragma unroll
                for (int k = 0; k < 7; ++k) { const v4u x0 = lds16(lds, (hf * 7 + k) * 1024 + lane * 16), x1 = lds16(lds, (14 + hf * 7 + k) * 1024 + lane * 16);
                    r0 = mfma32(x0, fr[k], r0); r1 = mfma32(x1, fr[k], r1); i0 = mfma32(x0, fi[k], i0); i1 = mfma32(x1, fi[k], i1); }
            }
            const v4u yr = pk8(r0, r1), yi = pk8(i0, i1), nr = neg8(yr);
#pragma unroll
            for (int ct = 0; ct < 2; ++ct) { zr[ct][t] = mfma32(yi, S[ct], mfma32(yr, C[ct], z4)); zi[ct][t] = mfma32(nr, S[ct], mfma32(yi, C[ct], z4)); }
        }
        char* O = lds + 32768;
#pragma unroll
        for (int ct = 0; ct < 2; ++ct) { const int o = (ct * 16 + rho) * 1040 + wave * 64 + g * 16;
            *(v4u*)(O + o) = pk8(zr[ct][0], zr[ct][1]); *(v4u*)(O + o + 512) = pk8(zi[ct][0], zi[ct][1]); }
        BAR();
#pragma unroll
        for (int k = 0; k < 4; ++k) st16s(rs, OFF_Z1 + job * 1024 + lane * 16, (wave * 4 + k) * 65536, lds16(O, (wave * 4 + k) * 1040 + lane * 16));
    }
    STAMP(job == 0 && wave == 1, 4, 3); STAMP(wave == 1, 25, job);
    if (!fft_sync(p, c, FL_FA)) return;
    STAMP(job == 0 && wave == 1, 4, 4); STAMP(wave == 1, 26, job);
    if (wave < 4) {
        const int k1 = job >> 1, P = (job & 1) * 4 + wave;
        char* L = lds + wave * 8192;
#pragma unroll
        for (int pc = 0; pc < 8; ++pc) dma16s(rs, OFF_Z1 + (k1 * 64 + (pc & 3) * 16 + (lane >> 2)) * 1024 + (pc >> 2) * 512 + P * 64 + (lane & 3) * 16, 0, wave * 8192 + pc * 1024);
        v4u C[4][2], S[4][2];
#pragma unroll
        for (int ks = 0; ks < 2; ++ks) { int n[8];
#pragma unroll
            for (int j = 0; j < 8; ++j) n[j] = 32 * ks + 16 * (j >> 2) + 4 * g + (j & 3);
#pragma unroll
            for (int kt = 0; kt < 4; ++kt) twid(16 * kt + rho, n, 63, 1.f / 64.f, C[kt][ks], S[kt][ks]); }
        VMWAIT(0);
        v2u hr[4], hi[4];
#pragma unroll
        for (int t = 0; t < 2; ++t) {
            const int k = P * 32 + pperm(rho, t);
            v4u xr[2], xi[2];
#pragma unroll
            for (int ks = 0; ks < 2; ++ks) { const int a = (32 * ks + 4 * g + (rho >> 2)) * 64 + (rho & 3) * 16 + t * 8;
                const v2u a0 = ldstr(L, a), a1 = ldstr(L, a + 1024), b0 = ldstr(L, a + 4096), b1 = ldstr(L, a + 5120);
                xr[ks] = (v4u){a0.x, a0.y, a1.x, a1.y}; xi[ks] = (v4u){b0.x, b0.y, b1.x, b1.y}; }
            const v4u n0 = neg8(xr[0]), n1 = neg8(xr[1]);
            unsigned short fr[16], fi[16];
#pragma unroll
            for (int j = 0; j < 16; ++j) { const int o = (k1 + 32 * (16 * (j >> 2) + 4 * g + (j & 3))) * 225 + min(k, 224); fr[j] = p.wr[o]; fi[j] = p.wi[o]; }
            SCHED();
            v4f vr[4], vi[4];
#pragma unroll
            for (int kt = 0; kt < 4; ++kt) {
                const v4f ar = mfma32(S[kt][1], xi[1], mfma32(S[kt][0], xi[0], mfma32(C[kt][1], xr[1], mfma32(C[kt][0], xr[0], z4))));
                const v4f ai = mfma32(S[kt][1], n1, mfma32(S[kt][0], n0, mfma32(C[kt][1], xi[1], mfma32(C[kt][0], xi[0], z4))));
                float wr[4], wi[4];
#pragma unroll
                for (int r = 0; r < 4; ++r) { wr[r] = __uint_as_float((unsigned)fr[kt * 4 + r] << 16); wi[r] = __uint_as_float((unsigned)fi[kt * 4 + r] << 16); }
                vr[kt] = (v4f){ar.x * wr[0] - ai.x * wi[0], ar.y * wr[1] - ai.y * wi[1], ar.z * wr[2] - ai.z * wi[2], ar.w * wr[3] - ai.w * wi[3]};
                vi[kt] = (v4f){ar.x * wi[0] + ai.x * wr[0], ar.y * wi[1] + ai.y * wr[1], ar.z * wi[2] + ai.z * wr[2], ar.w * wi[3] + ai.w * wr[3]};
            }
            const v4u pr0 = pk8(vr[0], vr[1]), pr1 = pk8(vr[2], vr[3]), pi0 = pk8(vi[0], vi[1]), pi1 = pk8(vi[2], vi[3]), ni0 = neg8(pi0), ni1 = neg8(pi1);
#pragma unroll
            for (int nt = 0; nt < 4; ++nt) {
                const v4f rr = mfma32(ni1, S[nt][1], mfma32(ni0, S[nt][0], mfma32(pr1, C[nt][1], mfma32(pr0, C[nt][0], z4))));
                const v4f ri = mfma32(pr1, S[nt][1], mfma32(pr0, S[nt][0], mfma32(pi1, C[nt][1], mfma32(pi0, C[nt][0], z4))));
                const v2u qr = {pk2(rr.x, rr.y), pk2(rr.z, rr.w)}, qi = {pk2(ri.x, ri.y), pk2(ri.z, ri.w)};
                if (t == 0) { hr[nt] = qr; hi[nt] = qi; }
                else { char* o = lds + 32768 + (nt * 16 + rho) * 528 + wave * 64 + g * 16;
                    *(v4u*)o = (v4u){hr[nt].x, hr[nt].y, qr.x, qr.y}; *(v4u*)(o + 256) = (v4u){hi[nt].x, hi[nt].y, qi.x, qi.y}; }
            }
        }
    }
    BAR();
#pragma unroll
    for (int k = 0; k < 4; ++k) { const int n2 = wave * 8 + k * 2 + (lane >> 5);
        st16s(rs, OFF_Z2 + (n2 * 32 + (job >> 1)) * 1024 + (g & 1) * 512 + (job & 1) * 256 + rho * 16, 0, lds16(lds, 32768 + n2 * 528 + (lane & 31) * 16)); }
    STAMP(job == 0 && wave == 1, 4, 5); STAMP(wave == 1, 27, job);
    if (!fft_sync(p, c, FL_FB)) return;
    STAMP(job == 0 && wave == 1, 4, 6); STAMP(wave == 1, 28, job);
    {
        char* L = lds + wave * 4096;
#pragma unroll
        for (int pc = 0; pc < 4; ++pc) dma16s(rs, OFF_Z2 + (job * 32 + (pc & 1) * 16 + (lane >> 2)) * 1024 + (pc >> 1) * 512 + wave * 64 + (lane & 3) * 16, 0, wave * 4096 + pc * 1024);
        v4u C[2], S[2];
        { int n[8];
#pragma unroll
          for (int j = 0; j < 8; ++j) n[j] = 8 * g + j;
          twid(64 * rho + job, n, 2047, 1.f / 2048.f, C[0], S[0]); twid(64 * (16 + rho) + job, n, 2047, 1.f / 2048.f, C[1], S[1]); }
        VMWAIT(0);
        v4f rr[2][2], ri[2][2];
#pragma unroll
        for (int t = 0; t < 2; ++t) {
            const int a = (8 * g + (rho >> 2)) * 64 + (rho & 3) * 16 + t * 8;
            const v2u a0 = ldstr(L, a), a1 = ldstr(L, a + 256), b0 = ldstr(L, a + 2048), b1 = ldstr(L, a + 2304);
            const v4u xr = {a0.x, a0.y, a1.x, a1.y}, xi = {b0.x, b0.y, b1.x, b1.y}, ni = neg8(xi);
#pragma unroll
            for (int nt = 0; nt < 2; ++nt) { rr[nt][t] = mfma32(ni, S[nt], mfma32(xr, C[nt], z4)); ri[nt][t] = mfma32(xr, S[nt], mfma32(xi, C[nt], z4)); }
        }
        char* R = lds + 32768;
#pragma unroll
        for (int nt = 0; nt < 2; ++nt) { *(v4u*)(R + (nt * 16 + wave) * 1024 + lane * 16) = pk8(rr[nt][0], rr[nt][1]); *(v4u*)(R + (nt * 16 + 8 + wave) * 1024 + lane * 16) = pk8(ri[nt][0], ri[nt][1]); }
        BAR();
#pragma unroll 1
        for (int Q = wave; Q < 14; Q += 8) {
            v4f y[2][2] = {{z4, z4}, {z4, z4}};
#pragma unroll
            for (int hf = 0; hf < 2; ++hf) {
                v4u f0[8], f1[8];
#pragma unroll
                for (int k = 0; k < 8; ++k) { const int fo = OFF_FI + (Q * 2 + (rho >> 3)) * 16384 + g * 256 + (pperm(rho, 0) & 15) * 16; f0[k] = ld16(rs, fo, (hf * 8 + k) * 1024, 0); f1[k] = ld16(rs, fo + 64, (hf * 8 + k) * 1024, 0); }
                SCHED();
#pragma unroll
                for (int k = 0; k < 8; ++k) { const v4u b0 = lds16(R, (hf * 8 + k) * 1024 + lane * 16), b1 = lds16(R, (16 + hf * 8 + k) * 1024 + lane * 16);
                    y[0][0] = mfma32(f0[k], b0, y[0][0]); y[0][1] = mfma32(f1[k], b0, y[0][1]); y[1][0] = mfma32(f0[k], b1, y[1][0]); y[1][1] = mfma32(f1[k], b1, y[1][1]); }
            }
#pragma unroll
            for (int nt = 0; nt < 2; ++nt) *(v4u*)(p.out + (size_t)(64 * (nt * 16 + rho) + job) * 896 + Q * 32 + g * 8) = pk8(y[nt][0], y[nt][1]);
        }
    }
}

extern "C" __global__ void __launch_bounds__(512, 2) k_main(Params p) {
    extern __shared__ __attribute__((aligned(16))) char lds[];
    Ctx c; c.rs = mkrsrc(p.ws, WS_BYTES); c.lds = lds; c.lane = threadIdx.x & 63; c.wave = __builtin_amdgcn_readfirstlane(threadIdx.x >> 6); c.rho = c.lane & 15; c.g = c.lane >> 4;
    { const int lane = c.lane; (void)lane; LSTAMP(blockIdx.x == 0 && c.wave == 7, 4, 0); }
    const int rot = *(const int*)(p.ws + flagoff(FL_ROT));
    const int x = (blockIdx.x + rot) & 7, y = blockIdx.x >> 3;
    if (x < 2) role_p(p, c, x * 32 + y);
    else if (x == 2) { if (y < 28) role_o(p, c, y); }
    else if (x == 3 && y == 28) role_cal(p, c);
    else {
        const int wi = (x - 4) * 32 + y - 16;
        if (x == 3) { if (y < 28) make_tables(p, c, y); } else if (wi >= 0) role_w(p, c, wi);
        if (wi >= 48) role_fft(p, c, wi - 48);
        else if (x == 3 ? y < 28 : wi >= 16 && wi < 44) role_f2(p, c, x == 3 ? y : wi - 16, x != 3);
        else if (x == 4) role_f1(p, c, y & 15, wi >= 0);
    }
    { const int lane = c.lane; (void)lane; LSTAMP(c.wave == 7, 8 + (blockIdx.x >> 6), blockIdx.x & 63); }
}

extern "C" void kernel_launch(void* const* d_in, const int* in_sizes, int n_in, void* d_out, int out_size, void* d_ws, size_t ws_size, hipStream_t stream) {
    Params p;
    p.xa = (const unsigned short*)d_in[0]; p.wr = (const unsigned short*)d_in[1]; p.wi = (const unsigned short*)d_in[2];
    p.xb = (const unsigned short*)d_in[3]; p.mem0 = (const unsigned short*)d_in[4];
    p.wq = (const unsigned short*)d_in[5]; p.wk = (const unsigned short*)d_in[6]; p.wv = (const unsigned short*)d_in[7]; p.wo = (const unsigned short*)d_in[8];
    p.out = (unsigned short*)d_out;
    p.ws = (char*)d_ws + (((size_t)2 << 20) - ((uintptr_t)d_ws & (((size_t)2 << 20) - 1))) % ((size_t)2 << 20);
    (void)hipFuncSetAttribute((const void*)k_main, hipFuncAttributeMaxDynamicSharedMemorySize, LDS_MAIN);
    (void)hipFuncSetAttribute((const void*)k_pro, hipFuncAttributeMaxDynamicSharedMemorySize, LDS_PRO);
    hipLaunchKernelGGL(k_pro, dim3(256), dim3(512), LDS_PRO, stream, p);
    hipLaunchKernelGGL(k_main, dim3(256), dim3(512), LDS_MAIN, stream, p);
}
```

```cpp
#include <hip/hip_runtime.h>
#include <hip/hip_bf16.h>
#include <stdint.h>

typedef unsigned v4u __attribute__((ext_vector_type(4)));
typedef unsigned v2u __attribute__((ext_vector_type(2)));
typedef float v4f __attribute__((ext_vector_type(4)));
typedef float v2f __attribute__((ext_vector_type(2)));
typedef short v4s __attribute__((ext_vector_type(4)));
typedef __bf16 v2bf __attribute__((ext_vector_type(2)));
typedef __bf16 v8bf __attribute__((ext_vector_type(8)));
typedef __bf16 v16bf __attribute__((ext_vector_type(16)));
typedef unsigned v8u __attribute__((ext_vector_type(8)));
typedef __amdgpu_buffer_rsrc_t rsrc_t;
#define LAS __attribute__((address_space(3)))
#define DEV __device__ __forceinline__

constexpr int NSEG = 64, NH = 16, DM = 448;
constexpr unsigned SENT = 0xFFFFFFFFu;
constexpr int OOB = 0x7fffffff;
constexpr int SC1 = 16;
constexpr int OFF_HO = 0;
constexpr int OFF_CTL = 4 << 20;
constexpr int OFF_AP = OFF_CTL + 65536;
constexpr int OFF_BT = OFF_AP + NH * DM * DM * 2;
constexpr int OFF_G = OFF_BT + NH * DM * DM * 2;
constexpr int OFF_U = OFF_G + NSEG * NH * 28672;
constexpr int OFF_T = OFF_U + NSEG * NH * 28672;
constexpr int OFF_V = OFF_T + NSEG * NH * 7168;
constexpr int OFF_PX = OFF_V + NSEG * NH * 7168;
constexpr int OFF_PM = OFF_PX + NSEG * NH * 2048;
constexpr int OFF_FD = OFF_PM + NSEG * NH * 512;
constexpr int OFF_FI = OFF_FD + 512 * DM * 2;
constexpr int OFF_Z1 = OFF_FI + 512 * DM * 2;
constexpr int OFF_Z2 = OFF_Z1 + (2 << 20);
constexpr int OFF_XF = OFF_Z2 + (2 << 20);
constexpr int OFF_DBG = OFF_XF + (2 << 20);
constexpr int WS_BYTES = OFF_DBG + (1 << 20);
constexpr int HO_MEM = 512, HO_PART = 16384, HO_E = 32768;
constexpr int OFF_PROBE = OFF_HO + 49152;
constexpr int FL_W = 0, FL_T = 112, FL_V = 176, FL_P = 208, FL_ROT = 336, FL_TAB = 337, FL_FA = 338, FL_FB = 339, FL_ABORT = 340, FL_CAL = 341, FL_P2 = 344;
constexpr int LDS_MAIN = 114688 + 2048;
constexpr int LDS_PRO = 114688;
constexpr int SPIN_CAP = 1 << 17;

struct Params {
    const unsigned short* xa; const unsigned short* wr; const unsigned short* wi;
    const unsigned short* xb; const unsigned short* mem0;
    const unsigned short* wq; const unsigned short* wk; const unsigned short* wv; const unsigned short* wo;
    unsigned short* out; char* ws;
};

DEV unsigned pk2(float a, float b) { v2bf r = {(__bf16)a, (__bf16)b}; return __builtin_bit_cast(unsigned, r); }
DEV v4u pk8(v4f a, v4f b) { return (v4u){pk2(a.x, a.y), pk2(a.z, a.w), pk2(b.x, b.y), pk2(b.z, b.w)}; }
DEV v4f mfma32(v4u a, v4u b, v4f c) { return __builtin_amdgcn_mfma_f32_16x16x32_bf16(__builtin_bit_cast(v8bf, a), __builtin_bit_cast(v8bf, b), c, 0, 0, 0); }
DEV v4f mfma16(v2u a, v2u b, v4f c) { return __builtin_amdgcn_mfma_f32_16x16x16bf16_1k(__builtin_bit_cast(v4s, a), __builtin_bit_cast(v4s, b), c, 0, 0, 0); }
DEV v4f smf(v4u a, v4u b0, v4u b1, v4f c, int idx) {
    v8u b = {b0.x, b0.y, b0.z, b0.w, b1.x, b1.y, b1.z, b1.w};
    return __builtin_amdgcn_smfmac_f32_16x16x64_bf16(__builtin_bit_cast(v8bf, a), __builtin_bit_cast(v16bf, b), c, idx, 0, 0);
}
DEV float fmx(float a, float b) { return __builtin_elementwise_maximum(a, b); }
DEV float max_g(float s) {
    auto r = __builtin_amdgcn_permlane16_swap(__float_as_uint(s), __float_as_uint(s), false, false);
    s = fmx(__uint_as_float(r[0]), __uint_as_float(r[1]));
    auto q = __builtin_amdgcn_permlane32_swap(__float_as_uint(s), __float_as_uint(s), false, false);
    return fmx(__uint_as_float(q[0]), __uint_as_float(q[1]));
}
DEV float sum_g(float s) {
    auto r = __builtin_amdgcn_permlane16_swap(__float_as_uint(s), __float_as_uint(s), false, false);
    s = __uint_as_float(r[0]) + __uint_as_float(r[1]);
    auto q = __builtin_amdgcn_permlane32_swap(__float_as_uint(s), __float_as_uint(s), false, false);
    return __uint_as_float(q[0]) + __uint_as_float(q[1]);
}
DEV float ror8(float s) { return __int_as_float(__builtin_amdgcn_mov_dpp(__float_as_int(s), 0x128, 0xf, 0xf, true)); }
template <int CTRL> DEV unsigned dppu(unsigned x) { return (unsigned)__builtin_amdgcn_mov_dpp((int)x, CTRL, 0xf, 0xf, true); }
DEV v4u quad4(unsigned x) { return (v4u){x, dppu<0x55>(x), dppu<0xAA>(x), dppu<0xFF>(x)}; }
DEV unsigned umax4(v4u v) { return max(max(v.x, v.y), max(v.z, v.w)); }
DEV rsrc_t mkrsrc(const void* p, unsigned bytes) { return __builtin_amdgcn_make_buffer_rsrc((void*)p, 0, bytes, 0x00020000); }
DEV v4u ld16(rsrc_t r, int vo, int so, int aux) { return aux ? __builtin_amdgcn_raw_buffer_load_b128(r, vo, so, SC1) : __builtin_amdgcn_raw_buffer_load_b128(r, vo, so, 0); }
DEV v2u ld8s(rsrc_t r, int vo, int so) { return __builtin_amdgcn_raw_buffer_load_b64(r, vo, so, SC1); }
DEV unsigned ld4s(rsrc_t r, int vo, int so) { return __builtin_amdgcn_raw_buffer_load_b32(r, vo, so, SC1); }
DEV void st16s(rsrc_t r, int vo, int so, v4u v) {
    __builtin_amdgcn_raw_buffer_store_b128(v, r, vo, so, SC1); asm volatile("s_nop 1" :: "v"(v) : "memory");
}
DEV void st8s(rsrc_t r, int vo, int so, v2u v) { __builtin_amdgcn_raw_buffer_store_b64(v, r, vo, so, SC1); }
DEV void st4s(rsrc_t r, int vo, int so, unsigned v) { __builtin_amdgcn_raw_buffer_store_b32(v, r, vo, so, SC1); }
DEV void dma16(rsrc_t r, int vo, int so, int ldsaddr) {
    unsigned keep;
    asm volatile("s_mov_b32 %0, m0\n\ts_mov_b32 m0, %4\n\ts_nop 4\n\tbuffer_load_dwordx4 %1, %2, %3 offen lds\n\ts_mov_b32 m0, %0"
                 : "=&s"(keep) : "v"(vo), "s"(r), "s"(__builtin_amdgcn_readfirstlane(so)), "s"(__builtin_amdgcn_readfirstlane(ldsaddr)) : "memory");
}
DEV void dma16s(rsrc_t r, int vo, int so, int ldsaddr) {
    unsigned keep;
    asm volatile("s_mov_b32 %0, m0\n\ts_mov_b32 m0, %4\n\ts_nop 4\n\tbuffer_load_dwordx4 %1, %2, %3 offen sc1 lds\n\ts_mov_b32 m0, %0"
                 : "=&s"(keep) : "v"(vo), "s"(r), "s"(__builtin_amdgcn_readfirstlane(so)), "s"(__builtin_amdgcn_readfirstlane(ldsaddr)) : "memory");
}
#define BAR() asm volatile("s_waitcnt lgkmcnt(0)\n\ts_barrier" ::: "memory")
#define BAR0() asm volatile("s_barrier" ::: "memory")
#define VMWAIT(n) asm volatile("s_waitcnt vmcnt(" #n ")" ::: "memory")
#define FENCE() asm volatile("" ::: "memory")
#define SCHED() __builtin_amdgcn_sched_barrier(0)
DEV v4u lds16(const char* l, int a) { return *(const v4u*)(l + a); }
DEV v2u lds8(const char* l, int a) { return *(const v2u*)(l + a); }
DEV v2u ldstr(const char* l, int a) { v4s r = __builtin_amdgcn_ds_read_tr16_b64_v4i16((LAS v4s*)(l + a)); return __builtin_bit_cast(v2u, r); }
template <int N> DEV void slp() { if constexpr (N > 0) __builtin_amdgcn_s_sleep(N); }
DEV int flagoff(int i) { return OFF_CTL + i * 128; }
template <int SL> DEV bool wait_flags(rsrc_t rs, int vo, unsigned need, unsigned& seen) {
    for (int sp = 0;; ++sp) {
        FENCE();
        seen = ld4s(rs, vo, 0);
        if (__builtin_amdgcn_ballot_w64(seen < need) == 0) return true;
        slp<SL>();
        if ((sp & 255) == 255) {
            if (sp > SPIN_CAP) st4s(rs, flagoff(FL_ABORT), 0, 1u);
            if (sp > SPIN_CAP || __builtin_amdgcn_readfirstlane(ld4s(rs, flagoff(FL_ABORT), 0))) return false;
        }
    }
}
DEV int img(int p) { return ((p & 7) >> 1) * 4 + (p >> 3) * 2 + (p & 1); }
DEV int pperm(int rho, int tau) { return (rho >> 2) * 8 + tau * 4 + (rho & 3); }

#define STAMP(cond, slot, s)
#define LSTAMP(cond, slot, s)
#define ACC_DECL
#define ACC_T(k)
#define ACC_OUT(slot)
#define CSTAMP(cond, slot, s)

extern "C" __global__ void __launch_bounds__(512, 2) k_pro(Params p) {
    extern __shared__ __attribute__((aligned(16))) char lds[];
    const int b = blockIdx.x, tid = threadIdx.x, lane = tid & 63, wave = __builtin_amdgcn_readfirstlane(tid >> 6);
    const int rho = lane & 15, g = lane >> 4;
    const int gemm = (b & 7) * 4 + (b >> 6), sub = (b >> 3) & 7, which = gemm & 1, h = gemm >> 1;
    const int b0 = 224 * (sub & 1), c0 = 112 * (sub >> 1);
    const rsrc_t rs = mkrsrc(p.ws, WS_BYTES);
    LSTAMP(b == 0 && wave == 0, 4, 8); STAMP(wave == 0 && b < 64, 29, b);
    const int lr = lane >> 2, lc = ((lane & 3) ^ ((0 - g) & 3)) * 8, rd = rho * 64 + ((g ^ ((0 - (rho >> 2)) & 3)) * 16);
    if (which == 0) {
        const rsrc_t rq = mkrsrc(p.wq, DM * 4096 * 2);
        for (int k = wave; k < 56; k += 8) dma16(rq, ((c0 + (k >> 3) * 16 + lr) * 4096 + h * 256 + (k & 7) * 32 + lc) * 2, 0, k * 1024);
    } else {
        const rsrc_t ro = mkrsrc(p.wo, DM * 4096 * 2);
        for (int k = wave; k < 56; k += 8) { const int ch = k * 64 + lane, e = ch / 14, cc = ch - e * 14; dma16(ro, ((h * 256 + e) * DM + c0 + cc * 8) * 2, 0, k * 1024); }
    }
    v4u a[2][8];
    char* la = lds + 57344 + wave * 8192;
    if (wave < 7) {
        const rsrc_t ra = mkrsrc(which ? p.wv : p.wk, DM * 4096 * 2);
#pragma unroll
        for (int t = 0; t < 2; ++t) {
            const int src = ((b0 + 32 * wave + pperm(lr, t)) * 4096 + h * 256 + lc) * 2;
#pragma unroll
            for (int ks = 0; ks < 4; ++ks) { dma16(ra, src, ks * 64, 57344 + wave * 8192 + (t * 4 + ks) * 1024); a[t][4 + ks] = ld16(ra, src, 256 + ks * 64, 0); }
        }
        VMWAIT(0);
#pragma unroll
        for (int k = 0; k < 8; ++k) a[k >> 2][k & 3] = lds16(la, k * 1024 + rd);
        asm volatile("s_waitcnt lgkmcnt(0)" : "+v"(a[0][0]), "+v"(a[0][1]), "+v"(a[0][2]), "+v"(a[0][3]), "+v"(a[1][0]), "+v"(a[1][1]), "+v"(a[1][2]), "+v"(a[1][3]) :: "memory");
#pragma unroll
        for (int k = 0; k < 8; ++k) *(v4u*)(la + k * 1024 + lane * 16) = a[k >> 2][4 + (k & 3)];
        FENCE();
#pragma unroll
        for (int k = 0; k < 8; ++k) a[k >> 2][4 + (k & 3)] = lds16(la, k * 1024 + rd);
    } else {
        const rsrc_t rx = mkrsrc(p.xb, 2048 * DM * 2);
        v4u xf[7], m0[7];
#pragma unroll
        for (int k = 0; k < 7; ++k) { const int pc = b * 7 + k, rt = pc / 14, ks = pc - rt * 14; xf[k] = ld16(rx, ((rt * 16 + (rho ^ ((rt & 1) * 4))) * DM + ks * 32 + g * 8) * 2, 0, 0); }
        const v4u sent = {SENT, SENT, SENT, SENT};
        if (b == 0) {
#pragma unroll
            for (int k = 0; k < 7; ++k) { const int c = k * 64 + lane; const unsigned* m = (const unsigned*)(p.mem0 + ((c >> 1) & 7) * DM + (c >> 4) * 16 + (c & 1) * 2); m0[k] = (v4u){m[0], m[2], m[4], m[6]}; }
        } else {
#pragma unroll
            for (int k = 0; k < 7; ++k) m0[k] = sent;
        }
        const unsigned rot = ((unsigned)__builtin_amdgcn_s_getreg(20 | (3 << 11)) - b) & 7u;
        if (lane < 16) st16s(rs, OFF_CTL + b * 256 + lane * 16, 0, (v4u){b * 2 == FL_ROT && lane == 0 ? rot : 0u, 0u, 0u, 0u});
#pragma unroll
        for (int k = 0; k < 16; ++k) {
            const int o = ((k & 7) * 1024 + lane * 16 + HO_MEM) & 8191;
            st16s(rs, (b & 3) == 3 && (b & 127) != 3 ? OOB : OFF_HO + b * 16384 + (k >> 3) * 8192 + o, 0, (k & 7) < 7 ? m0[k & 7] : sent);
        }
#pragma unroll
        for (int k = 0; k < 7; ++k) st16s(rs, OFF_XF + (b * 7 + k) * 1024 + lane * 16, 0, xf[k]);
        VMWAIT(24);
    }
    BAR();
    if (wave >= 7) return;
    STAMP(b == 0 && wave == 0, 4, 9); STAMP(wave == 0 && b < 64, 30, b);
    const float sc = which ? 1.f : 0.09016844005556021f;
    const int obase = (which ? OFF_BT : OFF_AP) + ((h * 28 + (c0 >> 4)) * 14 + (b0 >> 5) + wave) * 1024 + lane * 16;
    const int tra = (8 * g + (rho >> 2)) * 224 + (rho & 3) * 8;
    auto tile = [&](const v4u (&bf)[8], int ct) {
        v4f c0v = {0, 0, 0, 0}, c1v = {0, 0, 0, 0};
#pragma unroll
        for (int ks = 0; ks < 8; ++ks) { c0v = mfma32(a[0][ks], bf[ks], c0v); c1v = mfma32(a[1][ks], bf[ks], c1v); }
        st16s(rs, obase, ct * 14336, pk8(c0v * sc, c1v * sc));
    };
    auto fetch = [&](v4u (&bf)[8], int ct) {
        if (which == 0) {
#pragma unroll
            for (int ks = 0; ks < 8; ++ks) bf[ks] = lds16(lds, (ct * 8 + ks) * 1024 + rd);
        } else {
#pragma unroll
            for (int ks = 0; ks < 8; ++ks) { const v2u x = ldstr(lds, tra + ks * 7168 + ct * 32), y = ldstr(lds, tra + ks * 7168 + 896 + ct * 32); bf[ks] = (v4u){x.x, x.y, y.x, y.y}; }
        }
    };
    v4u f0[8], f1[8];
    fetch(f0, 0);
#pragma unroll 1
    for (int ct = 0; ct < 6; ct += 2) { fetch(f1, ct + 1); SCHED(); tile(f0, ct); fetch(f0, ct + 2); SCHED(); tile(f1, ct + 1); }
    tile(f0, 6);
    STAMP(b == 0 && wave == 0, 4, 10);
    STAMP(wave == 0 && b >= 192, 31, b & 63);
}

struct Ctx { rsrc_t rs; char* lds; int lane, wave, rho, g; };

DEV int slot_base(int s, int cal) { return OFF_HO + s * 65536 + (((cal >> (s >> 5)) & 1) << 13); }
#define STAGE_HEAD "s_add_i32 %[bud], %[bud], -1\n\ts_cmp_lt_i32 %[bud], 0\n\ts_cselect_b32 vcc_hi, -1, -2\n\tv_mov_b32 v233, vcc_hi\n\t"
#define STAGE_TAIL "v_cmp_gt_u32 vcc, v232, v233\n\ts_cbranch_vccnz 1b\n\ts_waitcnt lgkmcnt(0)\n\ts_barrier"
#define STAGE_CLOB "memory", "vcc", "scc", "v224", "v225", "v226", "v227", "v228", "v229", "v230", "v231", "v232", "v233"
DEV void stage1(rsrc_t rs, int vo, int so, int& budget) {
    asm volatile("1:\n\tbuffer_load_dwordx4 v[224:227], %[vo], %[rs], %[so] offen sc1\n\t" STAGE_HEAD
                 "s_waitcnt vmcnt(0)\n\tds_write_b128 %[vo], v[224:227]\n\tv_max3_u32 v232, v224, v225, v226\n\tv_max_u32 v232, v232, v227\n\t" STAGE_TAIL
                 : [bud] "+s"(budget) : [vo] "v"(vo), [rs] "s"(rs), [so] "s"(so) : STAGE_CLOB);
}
DEV void stage2(rsrc_t rs, int vo, int vo2, int so, int& budget) {
    asm volatile("1:\n\tbuffer_load_dwordx4 v[224:227], %[vo], %[rs], %[so] offen sc1\n\tbuffer_load_dwordx4 v[228:231], %[vo2], %[rs], %[so] offen sc1\n\t" STAGE_HEAD
                 "s_waitcnt vmcnt(1)\n\tds_write_b128 %[vo], v[224:227]\n\tv_max3_u32 v232, v224, v225, v226\n\t"
                 "s_waitcnt vmcnt(0)\n\tds_write_b128 %[vo2], v[228:231]\n\tv_max3_u32 v232, v232, v227, v228\n\tv_max3_u32 v232, v232, v229, v230\n\tv_max_u32 v232, v232, v231\n\t" STAGE_TAIL
                 : [bud] "+s"(budget) : [vo] "v"(vo), [vo2] "v"(vo2), [rs] "s"(rs), [so] "s"(so) : STAGE_CLOB);
}
DEV void give_up(rsrc_t rs) { st4s(rs, flagoff(FL_ABORT), 0, 1u); }
#define PIN2(a, b) asm volatile("" : "+v"(a), "+v"(b))
constexpr int CAL_STEP = 8;
#define TOUCH_LEAD 3
DEV void role_cal(const Params& p, const Ctx& c) {
    if (c.wave >= 2) return;
    unsigned best[2] = {0xffffffffu, 0xffffffffu};
    for (int r = 0; r < 20; ++r) {
#pragma unroll
        for (int k = 0; k < 2; ++k) {
            const int off = OFF_PROBE + c.wave * (2 << 20) + k * 8192 + r * 256;
            unsigned long long t0, t1; unsigned v;
            asm volatile("s_memtime %0\n\ts_waitcnt lgkmcnt(0)\n\tbuffer_load_dword %2, %3, %4, 0 offen sc1\n\ts_waitcnt vmcnt(0)\n\ts_memtime %1\n\ts_waitcnt lgkmcnt(0)"
                         : "=&s"(t0), "=&s"(t1), "=&v"(v) : "v"(off), "s"(c.rs) : "memory");
            best[k] = min(best[k], (unsigned)(t1 - t0));
        }
        if (r == 5 && c.wave == 0) { if (c.lane == 0) st4s(c.rs, flagoff(FL_CAL), 0, best[1] < best[0] ? 3u : 2u); VMWAIT(0); }
    }
    if (c.lane == 0) st4s(c.rs, flagoff(FL_CAL + 1 + c.wave), 0, best[1] < best[0] ? 3u : 2u);
    { const int k = c.lane & 3, o = (k == 0 ? 0 : k == 1 ? 7936 : 49152 + (k & 1) * 4096 + 2048) + (best[1] < best[0] ? 8192 : 0);
      for (int s = c.wave ? 32 : 12; s < 32 * (c.wave + 1); s += 4) {
          unsigned seen; if (!wait_flags<16>(c.rs, flagoff(FL_V), s - TOUCH_LEAD, seen)) return;
          unsigned v = ld4s(c.rs, OFF_HO + s * 65536 + o, 0); asm volatile("" :: "v"(v));
      } }
}
DEV int get_cal(const Ctx& c, int late, int& budget) {
    unsigned seen;
    if (!wait_flags<2>(c.rs, flagoff(FL_CAL + late + (late & c.lane)), 1u, seen)) budget = -1;
    return (__builtin_amdgcn_readlane(seen, 0) & 1) | ((__builtin_amdgcn_readlane(seen, 1) & 1) << 1);
}
DEV unsigned now() { return (unsigned)__builtin_amdgcn_s_memtime(); }
DEV void wait_until(unsigned t) {
    int rem = (int)(t - now());
    while (rem > 260) { __builtin_amdgcn_s_sleep(1); rem = (int)(t - now()); }
    rem -= 60;
    asm volatile("1:\n\ts_add_i32 %0, %0, -40\n\ts_cmp_gt_i32 %0, 0\n\ts_cbranch_scc1 1b" : "+s"(rem) :: "scc");
}
#define T_PM 2050
#define T_OS 2200
#define T_OM 400
#define T_OU 750
#define T_ST 0
#define T_PG 200
#define ABL(b) true
#define S_AT 6

#define MEM_TIMES_W(lbase)                                                                                           \
    v4u a_[7];                                                                                                       \
    _Pragma("unroll") for (int f = 0; f < 7; ++f) a_[f] = lds16(lds, (lbase) + f * 1024);                            \
    EXTRA_READS;                                                                                                     \
    SCHED();                                                                                                         \
    _Pragma("unroll") for (int f = 0; f < 7; ++f) { c0 = smf(a_[f], w[0][2 * f], w[0][2 * f + 1], c0, sidx); c1 = smf(a_[f], w[1][2 * f], w[1][2 * f + 1], c1, sidx); AFTER_STEP(f); } \
    const v2u tp = {pk2(c0.x + c0.z, c0.y + c0.w), pk2(c1.x + c1.z, c1.y + c1.w)};

#define VCLOB "v224", "v225", "v226", "v227", "v228", "v229", "v230", "v231", VCLOB_HI
#define VCLOB_HI "v232", "v233", "v234", "v235", "v236", "v237", "v238", "v239", \
              "v240", "v241", "v242", "v243", "v244", "v245", "v246", "v247", "v248", "v249", "v250", "v251", "v252", "v253", "v254", "v255"
#define PUBLISH_ASM(ST)                                                                                                                     \
    "s_cmp_lg_u32 %[pub], 7\n\ts_waitcnt lgkmcnt(0)\n\ts_barrier\n\ts_cbranch_scc1 9f\n\t"                                                  \
    "ds_read_b64 v[224:225], %[la]\n\tds_read_b64 v[226:227], %[la] offset:" #ST "*1\n\tds_read_b64 v[228:229], %[la] offset:" #ST "*2\n\t" \
    "ds_read_b64 v[230:231], %[la] offset:" #ST "*3\n\tds_read_b64 v[232:233], %[la] offset:" #ST "*4\n\t"                                  \
    "ds_read_b64 v[234:235], %[la] offset:" #ST "*5\n\tds_read_b64 v[236:237], %[la] offset:" #ST "*6\n\t"                                  \
    "ds_read_b64 v[238:239], %[la] offset:" #ST "*7\n\t"                                                                                    \
    "s_waitcnt lgkmcnt(6)\n\tv_pk_add_f32 v[224:225], v[224:225], v[226:227]\n\t"                                                           \
    "s_waitcnt lgkmcnt(4)\n\tv_pk_add_f32 v[228:229], v[228:229], v[230:231]\n\t"                                                           \
    "s_waitcnt lgkmcnt(2)\n\tv_pk_add_f32 v[232:233], v[232:233], v[234:235]\n\tv_pk_add_f32 v[224:225], v[224:225], v[228:229]\n\t"        \
    "s_waitcnt lgkmcnt(0)\n\tv_pk_add_f32 v[236:237], v[236:237], v[238:239]\n\tv_pk_add_f32 v[224:225], v[224:225], v[232:233]\n\t"        \
    "s_nop 0\n\tv_pk_add_f32 v[224:225], v[224:225], v[236:237]\n\t"                                                                        \
    "s_nop 0\n\tv_cvt_pk_bf16_f32 v224, v224, v225\n\ts_nop 1\n\t"                                                                          \
    "v_mov_b32_dpp v225, v224 quad_perm:[1,1,1,1] row_mask:0xf bank_mask:0xf\n\t"                                                           \
    "v_mov_b32_dpp v226, v224 quad_perm:[2,2,2,2] row_mask:0xf bank_mask:0xf\n\t"                                                           \
    "v_mov_b32_dpp v227, v224 quad_perm:[3,3,3,3] row_mask:0xf bank_mask:0xf\n\t"                                                           \
    "buffer_store_dwordx4 v[224:227], %[vo], %[rs], %[so] offen sc1\n\t"                                                                    \
    "9:\n\ts_barrier\n\ts_nop 0"

DEV void role_p(const Params& p, const Ctx& c, int idx) {
    const int h = idx >> 2, q = idx & 3, lane = c.lane, wave = c.wave, rho = c.rho, g = c.g;
    const rsrc_t rs = c.rs; char* lds = c.lds;
    const int om = q * 4 + wave;
    const bool isT = wave < 4 && om < 14, isX = wave < 4 && om >= 14;
    constexpr int L_MEM = 0, L_PART = 8192, L_DUMP = 10240;
    const int sidx = (rho & 2) ? 0xEEEE : 0x4444;
    const int fa = L_MEM + g * 256 + ((rho >> 2) * 2 + (rho & 1)) * 32 + ((rho >> 1) & 1) * 16;
    v2u sel; { const int r0 = rho - 2 * g; sel.x = (r0 == 0 ? 0x3f80u : 0u) | (r0 == 1 ? 0x3f800000u : 0u); sel.y = (r0 == 8 ? 0x3f80u : 0u) | (r0 == 9 ? 0x3f800000u : 0u); }
    const int piece = q < 3 ? wave : (wave < 2 ? wave : wave - 2);
    const int po = piece * 1024 + lane * 16;
    int budget = SPIN_CAP;
    if (isT) {
        v4u w[2][14];
#pragma unroll
        for (int t = 0; t < 2; ++t)
#pragma unroll
            for (int ks = 0; ks < 14; ++ks) w[t][ks] = ld16(rs, OFF_AP + (h * 28 + 2 * om + t) * 14336 + g * 256 + img(rho) * 16, ks * 1024, 0);
        int cal = get_cal(c, 0, budget);
        unsigned tref = now() - 65536;
        const int qa = L_MEM + (2 * om + (rho >> 3)) * 256 + (rho & 7) * 32 + g * 8;
        const int pw = (g >> 1) == (rho >> 3) ? L_PART + (2 * wave + (rho >> 3)) * 256 + (rho & 7) * 32 + (g & 1) * 16 : L_DUMP + lane * 16;
        const int tw = lane < 32 ? OFF_T + (h * 14 + om) * 512 + ((rho >> 3) * 2 + g) * 128 + (rho & 7) * 16 : OOB;
#pragma unroll 1
        for (int s = 0; s < NSEG && budget >= 0; ++s) {
            if (s == CAL_STEP) cal = get_cal(c, 1, budget);
            const int sb = slot_base(s, cal);
            v4f c0 = {0, 0, 0, 0}, c1 = {0, 0, 0, 0}; PIN2(c0, c1);
            wait_until(tref + T_PM);
            stage1(rs, po, sb + HO_MEM, budget);
            v2u mq;
#define EXTRA_READS mq = lds8(lds, qa)
#define AFTER_STEP(f)
            MEM_TIMES_W(fa)
#undef EXTRA_READS
            const v4f d2 = mfma16(tp, sel, (v4f){0, 0, 0, 0});
            const v2u d = {pk2(d2.x, d2.y), pk2(d2.z, d2.w)};
            *(v4f*)(lds + pw) = mfma16(d, mq, (v4f){0, 0, 0, 0});
            BAR();
            BAR0();
            tref = now();
            wait_until(tref + T_PG);
            if (ABL(8)) { auto x = __builtin_amdgcn_permlane32_swap(d.x, d.y, false, false);
              auto y = __builtin_amdgcn_permlane32_swap(x[0], x[0], false, false); auto z = __builtin_amdgcn_permlane32_swap(x[1], x[1], false, false);
              st16s(rs, tw, s * (NH * 7168), (v4u){y[0], z[0], y[1], z[1]}); }
        }
    } else if (isX) {
        v4u w[2][14];
        int cal = get_cal(c, 0, budget);
        const int gl0 = OFF_G + h * 28672 + (rho >> 3) * 1024 + g * 256 + (pperm(rho, 0) & 15) * 16;
        const int gfl = flagoff(FL_W + h * 7 + (lane & 3));
        const int ew = rho < 8 ? HO_E + h * 512 + g * 128 + rho * 16 : OOB, mw = g == 0 ? L_PART + 1024 + (rho & 7) * 32 + (rho >> 3) * 16 : L_DUMP + lane * 16;
        unsigned known = 0;
        if (om == 15) { BAR0(); BAR0(); BAR0(); }
#pragma unroll 1
        for (int s = om - 14; s < NSEG; s += 2) {
            if ((s | 1) == CAL_STEP + 1) cal = get_cal(c, 1, budget);
            const int sb = slot_base(s, cal);
            if (s < NSEG - 1 && (s < 2 || ABL(4))) {
                if (s >= 2) wait_until(now() + T_PG);
                if (known < (unsigned)s + 1) {
                    unsigned seen; if (!wait_flags<1>(rs, gfl, s + 1, seen)) break;
                    known = min(min((unsigned)__builtin_amdgcn_readlane(seen, 0), (unsigned)__builtin_amdgcn_readlane(seen, 1)), min((unsigned)__builtin_amdgcn_readlane(seen, 2), (unsigned)__builtin_amdgcn_readlane(seen, 3)));
                }
#pragma unroll
                for (int t = 0; t < 2; ++t)
#pragma unroll
                    for (int ks = 0; ks < 14; ++ks) w[t][ks] = ld16(rs, gl0 + t * 64, s * (NH * 28672) + ks * 2048, 1);
            }
            if (s >= 2) { BAR0(); BAR0(); BAR0(); }
            v4f c0 = {0, 0, 0, 0}, c1 = {0, 0, 0, 0}; PIN2(c0, c1);
            VMWAIT(0);
            STAMP(idx == 3 && s == 0, 4, 12);
            BAR();
            if (s < NSEG - 1) {
#define EXTRA_READS
                MEM_TIMES_W(fa)
#undef EXTRA_READS
                const v4f d2 = mfma16(tp, sel, (v4f){0, 0, 0, 0});
                float m = fmx(fmx(d2.x, d2.y), fmx(d2.z, d2.w));
                m = max_g(fmx(m, ror8(m)));
                *(v4f*)(lds + mw) = (v4f){-m, -m, -m, -m};
                SCHED();
                BAR();
                const float e0 = __builtin_amdgcn_exp2f(d2.x - m), e1 = __builtin_amdgcn_exp2f(d2.y - m), e2 = __builtin_amdgcn_exp2f(d2.z - m), e3 = __builtin_amdgcn_exp2f(d2.w - m);
                const unsigned u0 = pk2(e0, e1), u1 = pk2(e2, e3);
                st16s(rs, ew, sb, (v4u){u0, u1, dppu<0x128>(u0), dppu<0x128>(u1)});
            } else BAR0();
            BAR0();
        }
        if (om == 14) { BAR0(); BAR0(); BAR0(); }
    } else {
        if (wave == 4) { *(v4u*)(lds + L_PART + lane * 16) = (v4u){0u, 0u, 0u, 0u}; *(v4u*)(lds + L_PART + 1024 + lane * 16) = (v4u){0u, 0u, 0u, 0u}; }
        int cal = get_cal(c, 0, budget);
        unsigned tref = now() - 65536;
        STAMP(idx == 0 && wave == 7, 4, 1);
        const bool stg = q == 3 || wave != 7, two = q == 3 && wave == 5;
        const int pr = L_PART + (lane & 31) * 8, pubo = lane < 32 && (lane & 3) == 0 ? HO_PART + idx * 128 + lane * 4 : OOB;
#pragma unroll 1
        for (int s = 0; s < NSEG && budget >= 0; ++s) {
            if (s == CAL_STEP) cal = get_cal(c, 1, budget);
            const int sb = slot_base(s, cal);
            if (stg) {
                wait_until(tref + T_PM);
                if (two) stage2(rs, po, 6144 + lane * 16, sb + HO_MEM, budget); else stage1(rs, po, sb + HO_MEM, budget);
            } else BAR0();
            STAMP(idx == 0 && wave == 7, 0, s); STAMP(idx == 3 && wave == 6, 12, s);
            asm volatile(PUBLISH_ASM(256) :: [pub] "s"(wave), [la] "v"(pr), [vo] "v"(pubo), [rs] "s"(rs), [so] "s"(sb) : "memory", "scc", VCLOB);
            tref = now();
            if (wave == 7) {
                STAMP(idx == 0, 1, s);
                LSTAMP(idx == 0 && (s == 1 || s == 32 || s == 62), 14, s);
                if (lane == 0) st4s(rs, flagoff(FL_T + idx), 0, s);
            }
        }
    }
    if (budget < 0) give_up(rs);
    VMWAIT(0);
    BAR();
    if (wave == 7 && lane == 0) st4s(rs, flagoff(FL_T + idx), 0, NSEG);
}

DEV void role_o(const Params& p, const Ctx& c, int t) {
    const int lane = c.lane, wave = c.wave, rho = c.rho, g = c.g;
    const rsrc_t rs = c.rs; char* lds = c.lds;
    constexpr int L_MEM = 0, L_OUT = 8192, L_DUMP = 12288, L_V = 13312;
    v4u w[2][14];
#pragma unroll
    for (int k = 0; k < 2; ++k)
#pragma unroll
        for (int ks = 0; ks < 14; ++ks) w[k][ks] = ld16(rs, OFF_BT + ((2 * wave + k) * 28 + t) * 14336 + lane * 16, ks * 1024, 0);
    int budget = SPIN_CAP;
    int cal = get_cal(c, 0, budget), caln = cal;
    const int sidx = (rho & 2) ? 0xEEEE : 0x4444;
    const int fa = L_MEM + g * 256 + ((rho >> 2) * 2 + (rho & 1)) * 32 + ((rho >> 1) & 1) * 16;
    const int hb = rho >> 3, hd = 2 * wave + hb, i = rho & 7;
    const int pa = HO_PART + hd * 512 + g * 128 + i * 16, ea = HO_E + hd * 512 + g * 128 + i * 16;
    const int ul = OFF_U + (t * 16 + 2 * wave) * 1024 + lane * 16;
    const int ufl = flagoff(FL_W + rho * 7 + ((14 + (t >> 1)) >> 2));
    const int mo = (lane & 3) == 0 ? HO_MEM + t * 256 + lane * 4 : OOB;
    const int p2 = (lane & 4) ? 2 * (lane & 3) + 1 : 2 * (lane & 3);
    const int ra = L_OUT + (lane >> 3) * 64 + p2 * 8;
    const int ow = hb == 0 ? L_OUT + wave * 512 + i * 64 + g * 16 : L_DUMP + lane * 16;
    const int po = wave * 1024 + lane * 16, po2 = min(wave + 4, 6) * 1024 + lane * 16;
    const v4u ones = {0x3f803f80u, 0x3f803f80u, 0x3f803f80u, 0x3f803f80u};
    const v2u ones2 = {0x3f803f80u, 0x3f803f80u};
    const unsigned mlo = hb ? 0u : 0xffffffffu, mhi = ~mlo;
    const unsigned long long hbm = 0xff00ff00ff00ff00ull;
    v4u psel; { const int kq = (rho >> 2) * 2 + (rho & 1); const unsigned one = (kq & 1) ? 0x3f800000u : 0x3f80u;
        psel = (v4u){(kq >> 1) == 0 ? one : 0u, (kq >> 1) == 1 ? one : 0u, (kq >> 1) == 2 ? one : 0u, (kq >> 1) == 3 ? one : 0u}; }
    asm volatile("v_mov_b32 v215, 0" ::: "v215");
    auto check_u = [&](int s) {
        unsigned long long lack; unsigned seen;
        asm volatile("v_cmp_ge_u32 %0, %1, v215" : "=s"(lack) : "s"(s) : "memory");
        if (lack != 0 && !wait_flags<1>(rs, ufl, s + 1, seen)) budget = -1;
    };
    auto load_u = [&](int s) {
        asm volatile("s_nop 4\n\tbuffer_load_dwordx4 v[216:219], %0, %1, %2 offen sc1\n\tbuffer_load_dwordx4 v[220:223], %0, %1, %2 offen offset:1024 sc1"
                     :: "v"(ul), "s"(rs), "s"(s * (NH * 28672)) : "memory", "v216", "v217", "v218", "v219", "v220", "v221", "v222", "v223");
    };
    auto store_v = [&](int s) { st16s(rs, OFF_V + t * 4096 + (wave & 3) * 1024 + lane * 16, s * (NH * 7168), lds16(lds, L_V + (s & 1) * 4096 + (wave & 3) * 1024 + lane * 16)); };
    ACC_DECL;
    unsigned tref = now() - 65536;
    auto mem_times_b = [&](int sb, int s, bool last) -> v2u {
        v4f c0 = {0, 0, 0, 0}, c1 = {0, 0, 0, 0};
        if (wave < 4) stage2(rs, po, po2, sb + HO_MEM, budget);
        else BAR0();
        ACC_T(2);
#undef AFTER_STEP
#define AFTER_STEP(f) if (f == S_AT && !last) { SCHED(); ACC_T(4); if (T_OS) wait_until(tref + T_OS); ACC_T(5); asm volatile("buffer_load_dwordx4 v[224:227], %0, %2, %3 offen sc1\n\tbuffer_load_dwordx4 v[228:231], %1, %2, %3 offen sc1" \
                          :: "v"(pa), "v"(ea), "s"(rs), "s"(sb) : "memory", VCLOB); SCHED(); }
#define EXTRA_READS if (wave < 4 && !last && ABL(1)) load_u(s)
        MEM_TIMES_W(fa)
#undef EXTRA_READS
        *(v2u*)(lds + L_V + (s & 1) * 4096 + wave * 512 + lane * 8) = tp;
        return tp;
    };
    STAMP(t == 0 && wave == 0, 4, 13);
    check_u(1);
    STAMP(t == 0 && wave == 0, 4, 14);
    if (wave >= 4) load_u(0);
#pragma unroll 1
    for (int s = 0; s < NSEG - 1 && budget >= 0; ++s) {
        const int sb = slot_base(s, s < CAL_STEP ? cal : caln), sn = slot_base(s + 1, s + 1 < CAL_STEP ? cal : caln);
        ACC_T(0);
        const v2u vp = mem_times_b(sb, s, false);
        ACC_T(3);
        asm volatile(
            "s_branch 3f\n\t"
            "1:\n\t"
            "buffer_load_dwordx4 v[224:227], %[pa], %[rs], %[sb] offen sc1\n\t"
            "buffer_load_dwordx4 v[228:231], %[ea], %[rs], %[sb] offen sc1\n\t"
            "3:\n\t"
            "s_add_i32 %[bud], %[bud], -1\n\ts_cmp_lt_i32 %[bud], 0\n\ts_cselect_b32 vcc_hi, -1, -2\n\tv_mov_b32 v253, vcc_hi\n\t"
            "s_waitcnt vmcnt(1)\n\t"
            "v_mfma_f32_16x16x32_bf16 v[232:235], %[psel], v[224:227], 0\n\t"
            "v_max3_u32 v250, v224, v225, v226\n\t"
            "s_waitcnt vmcnt(0)\n\t"
            "v_mfma_f32_16x16x32_bf16 v[244:247], %[ones], v[228:231], 0\n\t"
            "v_max3_u32 v251, v228, v229, v230\n\t"
            "v_mfma_f32_16x16x32_bf16 v[236:239], v[216:219], v[228:231], 0\n\t"
            "v_max3_u32 v250, v250, v227, v231\n\t"
            "v_mfma_f32_16x16x32_bf16 v[240:243], v[220:223], v[228:231], 0\n\t"
            "v_max_u32 v250, v250, v251\n\t"
            "v_cmp_gt_u32 vcc, v250, v253\n\t"
            "s_cbranch_vccnz 1b\n\t"
            "v_exp_f32 v250, v232\n\tv_exp_f32 v251, v233\n\t"
            "v_max_f32 v252, v232, v233\n\t"
            "v_cvt_pk_bf16_f32 v250, v250, v251\n\t"
            "v_cmp_lt_f32 vcc, 0x42800000, v252\n\t"
            "v_and_b32 v248, v250, %[mlo]\n\tv_and_b32 v249, v250, %[mhi]\n\t"
            "v_cndmask_b32 v236, v236, v240, %[hbm]\n\tv_cndmask_b32 v237, v237, v241, %[hbm]\n\t"
            "v_mfma_f32_16x16x16_bf16 v[244:247], %[ones2], v[248:249], v[244:247]\n\t"
            "v_cndmask_b32 v238, v238, v242, %[hbm]\n\tv_cndmask_b32 v239, v239, v243, %[hbm]\n\t"
            "s_nop 1\n\t"
            "v_mfma_f32_16x16x16_bf16 v[236:239], %[vp], v[248:249], v[236:239]\n\t"
            "s_cbranch_vccz 2f\n\t"
            "s_nop 7\n\t"
            "v_max_f32 v252, 0, v252\n\tv_mov_b32 v253, v252\n\ts_nop 1\n\tv_permlane16_swap_b32 v252, v253\n\ts_nop 1\n\t"
            "v_max_f32 v252, v252, v253\n\tv_mov_b32 v253, v252\n\ts_nop 1\n\tv_permlane32_swap_b32 v252, v253\n\ts_nop 1\n\t"
            "v_max_f32 v252, v252, v253\n\t"
            "v_mfma_f32_16x16x32_bf16 v[244:247], %[ones], v[228:231], 0\n\t"
            "v_mfma_f32_16x16x32_bf16 v[236:239], v[216:219], v[228:231], 0\n\t"
            "v_sub_f32 v250, v232, v252\n\tv_sub_f32 v251, v233, v252\n\t"
            "v_exp_f32 v250, v250\n\tv_exp_f32 v251, v251\n\tv_exp_f32 v253, -v252\n\ts_nop 0\n\t"
            "v_cvt_pk_bf16_f32 v250, v250, v251\n\t"
            "v_and_b32 v248, v250, %[mlo]\n\tv_and_b32 v249, v250, %[mhi]\n\ts_nop 7\n\t"
            "v_cndmask_b32 v236, v236, v240, %[hbm]\n\tv_cndmask_b32 v237, v237, v241, %[hbm]\n\tv_cndmask_b32 v238, v238, v242, %[hbm]\n\tv_cndmask_b32 v239, v239, v243, %[hbm]\n\t"
            "v_mul_f32 v236, v236, v253\n\tv_mul_f32 v237, v237, v253\n\tv_mul_f32 v238, v238, v253\n\tv_mul_f32 v239, v239, v253\n\t"
            "v_mul_f32 v244, v244, v253\n\tv_mul_f32 v245, v245, v253\n\tv_mul_f32 v246, v246, v253\n\tv_mul_f32 v247, v247, v253\n\ts_nop 1\n\t"
            "v_mfma_f32_16x16x16_bf16 v[244:247], %[ones2], v[248:249], v[244:247]\n\t"
            "v_mfma_f32_16x16x16_bf16 v[236:239], %[vp], v[248:249], v[236:239]\n\t"
            "s_nop 7\n\t"
            "2:\n\t"
            "s_nop 1\n\t"
            "v_rcp_f32 v252, v244\n\t"
            "s_nop 3\n\t"
            "v_mul_f32 v236, v236, v252\n\tv_mul_f32 v237, v237, v252\n\tv_mul_f32 v238, v238, v252\n\tv_mul_f32 v239, v239, v252\n\t"
            "v_add_f32_dpp v240, v236, v236 row_ror:8 row_mask:0xf bank_mask:0xf\n\tv_add_f32_dpp v241, v237, v237 row_ror:8 row_mask:0xf bank_mask:0xf\n\t"
            "v_add_f32_dpp v242, v238, v238 row_ror:8 row_mask:0xf bank_mask:0xf\n\tv_add_f32_dpp v243, v239, v239 row_ror:8 row_mask:0xf bank_mask:0xf\n\t"
            "ds_write_b128 %[ow], v[240:243]\n\t"
            PUBLISH_ASM(512)
            : [bud] "+s"(budget)
            : [pa] "v"(pa), [ea] "v"(ea), [rs] "s"(rs), [sb] "s"(sb), [psel] "v"(psel), [ones] "v"(ones), [ones2] "v"(ones2), [vp] "v"(vp),
              [mlo] "v"(mlo), [mhi] "v"(mhi), [hbm] "s"(hbm), [ow] "v"(ow),
              [pub] "s"(wave), [la] "v"(ra), [vo] "v"(mo), [so] "s"(sn)
            : "memory", "vcc", "scc", VCLOB);
        tref = now();
        STAMP(t == 0 && wave == 7, 3, s);
        if (wave == 7 && lane == 0) st4s(rs, flagoff(FL_V + t), 0, s);
        if (s + 2 == CAL_STEP) caln = get_cal(c, 1, budget);
        if (wave == 4) check_u(min(s + 2, NSEG - 2));
        ACC_T(6);
        if (wave < 4) wait_until(tref + T_OM);
        else {
            wait_until(tref + T_OU + (wave - 4) * T_ST);
            if (s + 1 < NSEG - 1 && ABL(1)) load_u(s + 1);
            if (wave == 4) asm volatile("buffer_load_dword v215, %0, %1, 0 offen sc1" :: "v"(ufl), "s"(rs) : "memory", "v215");
            if (ABL(2)) store_v(s);
        }
        ACC_T(7);
    }
    VMWAIT(0);
    ACC_OUT(t * 8 + wave);
    mem_times_b(slot_base(NSEG - 1, caln), NSEG - 1, true);
    if (budget < 0) give_up(rs);
    BAR();
    if (wave >= 4) store_v(NSEG - 1);
    VMWAIT(0);
    BAR0();
    if (wave == 7 && lane == 0) st4s(rs, flagoff(FL_V + t), 0, NSEG);
}

DEV void role_w(const Params& p, const Ctx& c, int wi) {
    const int lane = c.lane, wave = c.wave, rho = c.rho, g = c.g;
    const rsrc_t rs = c.rs; char* lds = c.lds;
    const int h = wi / 7, unit = (wi - h * 7) * 4 + (wave & 3), set = wave >> 2;
    const bool isG = unit < 14;
    auto stage = [&](int it) {
        const int sg = 2 * it + set, lb = (set * 2 + (it & 1)) * 28672;
#pragma unroll
        for (int k = 0; k < 7; ++k) { const int pc = (wave & 3) * 7 + k; dma16(rs, OFF_XF + lane * 16, sg * 28672 + pc * 1024, lb + pc * 1024); }
    };
    v4u w[2][14];
    auto load_w = [&]() {
#pragma unroll
        for (int t = 0; t < 2; ++t) {
            const int row = isG ? unit * 32 + pperm(rho, t) : (unit - 14) * 32 + t * 16 + rho;
#pragma unroll
            for (int ks = 0; ks < 14; ++ks) w[t][ks] = ld16(rs, (isG ? OFF_AP : OFF_BT) + (h * 28 + (row >> 4)) * 14336 + g * 256 + (row & 15) * 16, ks * 1024, 0);
        }
    };
    int xa[2];
#pragma unroll
    for (int k = 0; k < 2; ++k) { const int key = isG ? k * 16 + rho : pperm(rho, k), kt = key >> 4; xa[k] = kt * 14336 + (g * 16 + ((key & 15) ^ (kt * 4))) * 16; }
    const int ob = (isG ? OFF_G + (h * 14 + unit) * 2048 : OFF_U + ((unit - 14) * 32 + h) * 1024) + lane * 16;
    const int ostep = isG ? 1024 : 16384;
    auto segment = [&](int buf, int sg) {
        const char* xb = lds + buf * 28672;
        v4f a00 = {0, 0, 0, 0}, a01 = a00, a10 = a00, a11 = a00;
        if (isG) {
#pragma unroll
            for (int ks = 0; ks < 14; ++ks) { const v4u x0 = lds16(xb, xa[0] + ks * 1024), x1 = lds16(xb, xa[1] + ks * 1024);
                a00 = mfma32(w[0][ks], x0, a00); a01 = mfma32(w[1][ks], x0, a01); a10 = mfma32(w[0][ks], x1, a10); a11 = mfma32(w[1][ks], x1, a11); }
        } else {
#pragma unroll
            for (int ks = 0; ks < 14; ++ks) { const v4u x0 = lds16(xb, xa[0] + ks * 1024), x1 = lds16(xb, xa[1] + ks * 1024);
                a00 = mfma32(x0, w[0][ks], a00); a01 = mfma32(x1, w[0][ks], a01); a10 = mfma32(x0, w[1][ks], a10); a11 = mfma32(x1, w[1][ks], a11); }
        }
        st16s(rs, ob, sg * (NH * 28672), pk8(a00, a01)); st16s(rs, ob + ostep, sg * (NH * 28672), pk8(a10, a11));
    };
    if (set == 0) load_w();
    if (wave == 7) *(int*)(lds + LDS_MAIN - 32) = 0;
    stage(0);
    VMWAIT(0);
    BAR();
    STAMP(wi == 20 && wave == 1, 4, 2);
#pragma unroll 1
    for (int it = -1; it < 32; ++it) {
        if (it == -1 && set == 1) load_w();
        if (it >= 0 && it + 1 < 32) stage(it + 1);
        if (it > 0 || set == 0) segment(it > 0 ? set * 2 + (it & 1) : 2 * (it + 1), it > 0 ? 2 * it + set : it + 1);
        if (it < 0) continue;
        if (it == 0 && set == 0) {
            VMWAIT(0);
            if (lane == 0 && __hip_atomic_fetch_add((int*)(lds + LDS_MAIN - 32), 1, __ATOMIC_RELAXED, __HIP_MEMORY_SCOPE_WORKGROUP) == 3) st4s(rs, flagoff(FL_W + wi), 0, 2);
            STAMP(wi == 20 && wave == 1, 4, 15);
        }
        const bool eager = it < 4 || it == 31;
        if (eager) VMWAIT(0); else VMWAIT(2);
        BAR();
        if (wave == 0 && lane == 0) st4s(rs, flagoff(FL_W + wi), 0, eager ? 2 * it + 2 : 2 * it);
        if (it == 12 && wave == 0 && wi >= 48) {
            unsigned seen; wait_flags<8>(rs, flagoff(FL_TAB), 28, seen);
            __builtin_amdgcn_fence(__ATOMIC_ACQUIRE, "agent");
            VMWAIT(0);
        }
        STAMP(wi == 20 && wave == 1, 5, it); CSTAMP(wi == 20 && wave == 1, 6, it);
    }
}

DEV int f_first(int set, int wave) { return set ? 33 + 2 * wave : wave; }
DEV int f_next(int s, int wave) { return s >= 32 ? s + 16 : s >= 24 ? 32 + 2 * wave : s + 8; }
DEV void role_f1(const Params& p, const Ctx& c, int h, int set) {
    const int lane = c.lane, wave = c.wave, rho = c.rho, g = c.g;
    const rsrc_t rs = c.rs;
    const int gfl = flagoff(FL_W + h * 7 + (lane & 3)), tfl = flagoff(FL_T + h * 4 + (lane & 3));
    const int gl0 = OFF_G + h * 28672 + (rho >> 3) * 1024 + g * 256 + (pperm(rho, 0) & 15) * 16;
    const int tl = OFF_T + h * 7168 + g * 128 + ((rho >> 2) * 2 + (rho & 1)) * 16;
    const float ninf = -__builtin_inff();
    bool wdone = false;
#pragma unroll 1
    for (int s = f_first(set, wave); s < NSEG; s = f_next(s, wave)) {
        STAMP(h == 0, 16, s);
        unsigned seen;
        if (!wdone) { if (!wait_flags<8>(rs, gfl, s + 1, seen)) return; wdone = __builtin_amdgcn_ballot_w64(seen < NSEG) == 0; }
        v4u x[2][14];
        v4f a[2][2] = {{{0, 0, 0, 0}, {0, 0, 0, 0}}, {{0, 0, 0, 0}, {0, 0, 0, 0}}};
#pragma unroll
        for (int hf = 0; hf < 2; ++hf) {
            v4u y[2][7];
#pragma unroll
            for (int k = 0; k < 7; ++k) {
#pragma unroll
                for (int qt = 0; qt < 2; ++qt) x[qt][hf * 7 + k] = ld16(rs, OFF_XF + qt * 14336 + g * 256 + (rho ^ (qt * 4)) * 16, s * 28672 + (hf * 7 + k) * 1024, 0);
                y[0][k] = ld16(rs, gl0, s * (NH * 28672) + (hf * 7 + k) * 2048, 1); y[1][k] = ld16(rs, gl0 + 64, s * (NH * 28672) + (hf * 7 + k) * 2048, 1);
            }
            SCHED();
#pragma unroll
            for (int k = 0; k < 7; ++k)
#pragma unroll
                for (int qt = 0; qt < 2; ++qt) { a[qt][0] = mfma32(y[0][k], x[qt][hf * 7 + k], a[qt][0]); a[qt][1] = mfma32(y[1][k], x[qt][hf * 7 + k], a[qt][1]); }
        }
        float v[2][8], m[2];
#pragma unroll
        for (int qt = 0; qt < 2; ++qt) {
            const float r[8] = {a[qt][0].x, a[qt][0].y, a[qt][0].z, a[qt][0].w, a[qt][1].x, a[qt][1].y, a[qt][1].z, a[qt][1].w};
            m[qt] = ninf;
#pragma unroll
            for (int k = 0; k < 8; ++k) { v[qt][k] = (8 * g + k <= qt * 16 + rho) ? r[k] : ninf; m[qt] = fmx(m[qt], v[qt][k]); }
        }
        asm volatile("" : "+v"(m[0]), "+v"(m[1]));
        STAMP(h == 0, 17, s);
        if (!wait_flags<2>(rs, tfl, s + 1, seen)) return;
        STAMP(h == 0, 18, s);
        v4f am[2] = {{0, 0, 0, 0}, {0, 0, 0, 0}};
        { v4u y[14];
#pragma unroll
          for (int ks = 0; ks < 14; ++ks) y[ks] = ld16(rs, tl, s * (NH * 7168) + ks * 512, 1);
          SCHED();
#pragma unroll
          for (int ks = 0; ks < 14; ++ks) { am[0] = mfma32(y[ks], x[0][ks], am[0]); am[1] = mfma32(y[ks], x[1][ks], am[1]); } }
        unsigned pm[2];
#pragma unroll
        for (int qt = 0; qt < 2; ++qt) {
            const float mm = max_g(fmx(m[qt], fmx(am[qt].x, am[qt].y)));
            const float e0 = __builtin_amdgcn_exp2f(am[qt].x - mm), e1 = __builtin_amdgcn_exp2f(am[qt].y - mm);
            float z = e0 + e1;
#pragma unroll
            for (int k = 0; k < 8; ++k) { v[qt][k] = __builtin_amdgcn_exp2f(v[qt][k] - mm); z += v[qt][k]; }
            const float rz = __builtin_amdgcn_rcpf(sum_g(z));
            st16s(rs, OFF_PX + (h * 2 + qt) * 1024 + lane * 16, s * (NH * 2048),
                  (v4u){pk2(v[qt][0] * rz, v[qt][1] * rz), pk2(v[qt][2] * rz, v[qt][3] * rz), pk2(v[qt][4] * rz, v[qt][5] * rz), pk2(v[qt][6] * rz, v[qt][7] * rz)});
            pm[qt] = pk2(e0 * rz, e1 * rz);
        }
        st16s(rs, (lane & 1) == 0 ? OFF_PM + h * 512 + lane * 8 : OOB, s * (NH * 512), (v4u){pm[0], pm[1], dppu<0xF5>(pm[0]), dppu<0xF5>(pm[1])});
        VMWAIT(0);
        if (lane == 0) st4s(rs, flagoff((set ? FL_P2 : FL_P) + h * 8 + wave), 0, s + 1);
        STAMP(h == 0, 19, s);
    }
}

DEV void make_tables(const Params& p, const Ctx& c, int t) {
    const int lane = c.lane, wave = c.wave, rho = c.rho, g = c.g;
    const rsrc_t rs = c.rs;
    for (int ch = t * 512 + wave * 64 + lane; ch < 2 * 28672; ch += 28 * 512) {
        float v[8];
        if (ch < 28672) { const int fr = ch >> 6, tl = fr / 14, d0 = (fr - tl * 14) * 32 + g * 8, ri = tl >> 4, k = (tl & 15) * 16 + rho;
#pragma unroll
            for (int j = 0; j < 8; ++j) { const float ph = (float)((k * (d0 + j)) % 448) * (1.f / 448.f); v[j] = k > 224 ? 0.f : ri ? -__builtin_amdgcn_sinf(ph) : __builtin_amdgcn_cosf(ph); }
        } else { const int fr = (ch - 28672) >> 6, d = (fr >> 4) * 16 + rho, ri = (fr >> 3) & 1, k0 = (fr & 7) * 32 + g * 8;
#pragma unroll
            for (int j = 0; j < 8; ++j) { const int k = k0 + j; const float ph = (float)((k * d) % 448) * (1.f / 448.f); const bool edge = k == 0 || k == 224;
                const float wgt = (edge ? 1.f : 2.f) / (448.f * 2048.f); v[j] = k > 224 ? 0.f : ri ? (edge ? 0.f : -wgt * __builtin_amdgcn_sinf(ph)) : wgt * __builtin_amdgcn_cosf(ph); }
        }
        st16s(rs, OFF_FD + ch * 16, 0, (v4u){pk2(v[0], v[1]), pk2(v[2], v[3]), pk2(v[4], v[5]), pk2(v[6], v[7])});
    }
    VMWAIT(0);
    BAR();
    if (wave == 0 && lane == 0) __hip_atomic_fetch_add((unsigned*)(p.ws + flagoff(FL_TAB)), 1u, __ATOMIC_RELAXED, __HIP_MEMORY_SCOPE_AGENT);
}
DEV void role_f2(const Params& p, const Ctx& c, int t, int set) {
    const int lane = c.lane, wave = c.wave, rho = c.rho, g = c.g;
    const rsrc_t rs = c.rs;
    const int ufl = flagoff(FL_W + rho * 7 + ((14 + (t >> 1)) >> 2)), pfl = flagoff(lane < 16 ? (set ? FL_P2 : FL_P) + lane * 8 + wave : FL_V + t);
    const int ul = OFF_U + t * 16384 + lane * 16;
    bool wdone = false;
#pragma unroll 1
    for (int s = f_first(set, wave); s < NSEG; s = f_next(s, wave)) {
        unsigned seen;
        if (!wdone) { if (!wait_flags<8>(rs, ufl, s + 1, seen)) return; wdone = __builtin_amdgcn_ballot_w64(seen < NSEG) == 0; }
        STAMP(t == 0, 20, s);
        v4u u[16];
#pragma unroll
        for (int h = 0; h < 16; ++h) u[h] = ld16(rs, ul, s * (NH * 28672) + h * 1024, 1);
        if (!wait_flags<2>(rs, pfl, s + 1, seen)) return;
        STAMP(t == 0, 21, s);
        v4f a[2] = {{0, 0, 0, 0}, {0, 0, 0, 0}}, m[2] = {{0, 0, 0, 0}, {0, 0, 0, 0}};
        v4u px[16]; v2u pm[16], vv[8];
#pragma unroll
        for (int h = 0; h < 16; ++h) px[h] = ld16(rs, OFF_PX + lane * 16, s * (NH * 2048) + h * 2048, 1);
#pragma unroll
        for (int h = 0; h < 16; ++h) { pm[h] = ld8s(rs, OFF_PM + lane * 8, s * (NH * 512) + h * 512); if (h & 1) vv[h >> 1] = ld8s(rs, OFF_V + t * 4096 + lane * 8, s * (NH * 7168) + (h >> 1) * 512); }
        SCHED();
#pragma unroll
        for (int h = 0; h < 16; ++h) { a[0] = mfma32(u[h], px[h], a[0]); SCHED(); px[h] = ld16(rs, OFF_PX + 1024 + lane * 16, s * (NH * 2048) + h * 2048, 1); SCHED(); }
#pragma unroll
        for (int k = 0; k < 8; ++k) { m[0] = mfma16(vv[k], (v2u){pm[2 * k].x, pm[2 * k + 1].x}, m[0]); m[1] = mfma16(vv[k], (v2u){pm[2 * k].y, pm[2 * k + 1].y}, m[1]); }
        SCHED();
#pragma unroll
        for (int h = 0; h < 16; ++h) a[1] = mfma32(u[h], px[h], a[1]);
#pragma unroll
        for (int qt = 0; qt < 2; ++qt) { const v4f o = a[qt] + m[qt];
            *(v2u*)(p.out + (size_t)(s * 32 + qt * 16 + rho) * 896 + DM + t * 16 + g * 4) = (v2u){pk2(o.x, o.y), pk2(o.z, o.w)}; }
        if (s == 56 + (t & 1) && t < 2) { VMWAIT(0); __builtin_amdgcn_fence(__ATOMIC_RELEASE, "agent"); }
        STAMP(t == 0, 22, s);
    }
}

DEV bool fft_sync(const Params& p, const Ctx& c, int fl) {
    VMWAIT(0);
    BAR();
    int* okw = (int*)(c.lds + LDS_MAIN - 16);
    if (c.wave == 0) {
        if (c.lane == 0) __hip_atomic_fetch_add((unsigned*)(p.ws + flagoff(fl)), 1u, __ATOMIC_RELAXED, __HIP_MEMORY_SCOPE_AGENT);
        unsigned seen; const bool ok = wait_flags<2>(c.rs, flagoff(fl), 64, seen);
        if (c.lane == 0) *okw = ok;
    }
    BAR();
    return *okw != 0;
}
DEV v4u neg8(v4u v) { return v ^ 0x80008000u; }
DEV void twid(int a, const int (&n)[8], int mask, float inv, v4u& C, v4u& S) {
    float cs[8], sn[8];
#pragma unroll
    for (int j = 0; j < 8; ++j) { const float ph = (float)((a * n[j]) & mask) * inv; cs[j] = __builtin_amdgcn_cosf(ph); sn[j] = __builtin_amdgcn_sinf(ph); }
    C = (v4u){pk2(cs[0], cs[1]), pk2(cs[2], cs[3]), pk2(cs[4], cs[5]), pk2(cs[6], cs[7])};
    S = (v4u){pk2(sn[0], sn[1]), pk2(sn[2], sn[3]), pk2(sn[4], sn[5]), pk2(sn[6], sn[7])};
}
DEV void role_fft(const Params& p, const Ctx& c, int job) {
    const int lane = c.lane, wave = c.wave, rho = c.rho, g = c.g;
    const rsrc_t rs = c.rs; char* lds = c.lds;
    const v4f z4 = {0, 0, 0, 0};
    STAMP(wave == 1, 23, job);
    {
        const rsrc_t rx = mkrsrc(p.xa, 2048 * DM * 2);
        for (int pc = wave; pc < 28; pc += 8) { const int nt = pc >= 14, ks = pc - nt * 14; dma16(rx, ((64 * (nt * 16 + rho) + job) * DM + ks * 32 + g * 8) * 2, 0, pc * 1024); }
        STAMP(wave == 1, 24, job);
        v4u C[2], S[2];
        { int n[8];
#pragma unroll
          for (int j = 0; j < 8; ++j) n[j] = 64 * (16 * (j >> 2) + 4 * g + (j & 3)) + job;
          twid(rho, n, 2047, 1.f / 2048.f, C[0], S[0]); twid(16 + rho, n, 2047, 1.f / 2048.f, C[1], S[1]); }
        VMWAIT(0);
        BAR();
        v4f zr[2][2], zi[2][2];
#pragma unroll
        for (int t = 0; t < 2; ++t) {
            const int fo = OFF_FD + (wave * 2 + (rho >> 3)) * 14336 + g * 256 + (pperm(rho, t) & 15) * 16;
            v4f r0 = z4, r1 = z4, i0 = z4, i1 = z4;
#pragma unroll
            for (int hf = 0; hf < 2; ++hf) {
                v4u fr[7], fi[7];
#pragma unroll
                for (int k = 0; k < 7; ++k) { fr[k] = ld16(rs, fo, (hf * 7 + k) * 1024, 0); fi[k] = ld16(rs, fo, 256 * DM * 2 + (hf * 7 + k) * 1024, 0); }
                SCHED();
#pragma unroll
                for (int k = 0; k < 7; ++k) { const v4u x0 = lds16(lds, (hf * 7 + k) * 1024 + lane * 16), x1 = lds16(lds, (14 + hf * 7 + k) * 1024 + lane * 16);
                    r0 = mfma32(x0, fr[k], r0); r1 = mfma32(x1, fr[k], r1); i0 = mfma32(x0, fi[k], i0); i1 = mfma32(x1, fi[k], i1); }
            }
            const v4u yr = pk8(r0, r1), yi = pk8(i0, i1), nr = neg8(yr);
#pragma unroll
            for (int ct = 0; ct < 2; ++ct) { zr[ct][t] = mfma32(yi, S[ct], mfma32(yr, C[ct], z4)); zi[ct][t] = mfma32(nr, S[ct], mfma32(yi, C[ct], z4)); }
        }
        char* O = lds + 32768;
#pragma unroll
        for (int ct = 0; ct < 2; ++ct) { const int o = (ct * 16 + rho) * 1040 + wave * 64 + g * 16;
            *(v4u*)(O + o) = pk8(zr[ct][0], zr[ct][1]); *(v4u*)(O + o + 512) = pk8(zi[ct][0], zi[ct][1]); }
        BAR();
#pragma unroll
        for (int k = 0; k < 4; ++k) st16s(rs, OFF_Z1 + job * 1024 + lane * 16, (wave * 4 + k) * 65536, lds16(O, (wave * 4 + k) * 1040 + lane * 16));
    }
    STAMP(job == 0 && wave == 1, 4, 3); STAMP(wave == 1, 25, job);
    if (!fft_sync(p, c, FL_FA)) return;
    STAMP(job == 0 && wave == 1, 4, 4); STAMP(wave == 1, 26, job);
    if (wave < 4) {
        const int k1 = job >> 1, P = (job & 1) * 4 + wave;
        char* L = lds + wave * 8192;
#pragma unroll
        for (int pc = 0; pc < 8; ++pc) dma16s(rs, OFF_Z1 + (k1 * 64 + (pc & 3) * 16 + (lane >> 2)) * 1024 + (pc >> 2) * 512 + P * 64 + (lane & 3) * 16, 0, wave * 8192 + pc * 1024);
        v4u C[4][2], S[4][2];
#pragma unroll
        for (int ks = 0; ks < 2; ++ks) { int n[8];
#pragma unroll
            for (int j = 0; j < 8; ++j) n[j] = 32 * ks + 16 * (j >> 2) + 4 * g + (j & 3);
#pragma unroll
            for (int kt = 0; kt < 4; ++kt) twid(16 * kt + rho, n, 63, 1.f / 64.f, C[kt][ks], S[kt][ks]); }
        VMWAIT(0);
        v2u hr[4], hi[4];
#pragma unroll
        for (int t = 0; t < 2; ++t) {
            const int k = P * 32 + pperm(rho, t);
            v4u xr[2], xi[2];
#pragma unroll
            for (int ks = 0; ks < 2; ++ks) { const int a = (32 * ks + 4 * g + (rho >> 2)) * 64 + (rho & 3) * 16 + t * 8;
                const v2u a0 = ldstr(L, a), a1 = ldstr(L, a + 1024), b0 = ldstr(L, a + 4096), b1 = ldstr(L, a + 5120);
                xr[ks] = (v4u){a0.x, a0.y, a1.x, a1.y}; xi[ks] = (v4u){b0.x, b0.y, b1.x, b1.y}; }
            const v4u n0 = neg8(xr[0]), n1 = neg8(xr[1]);
            unsigned short fr[16], fi[16];
#pragma unroll
            for (int j = 0; j < 16; ++j) { const int o = (k1 + 32 * (16 * (j >> 2) + 4 * g + (j & 3))) * 225 + min(k, 224); fr[j] = p.wr[o]; fi[j] = p.wi[o]; }
            SCHED();
            v4f vr[4], vi[4];
#pragma unroll
            for (int kt = 0; kt < 4; ++kt) {
                const v4f ar = mfma32(S[kt][1], xi[1], mfma32(S[kt][0], xi[0], mfma32(C[kt][1], xr[1], mfma32(C[kt][0], xr[0], z4))));
                const v4f ai = mfma32(S[kt][1], n1, mfma32(S[kt][0], n0, mfma32(C[kt][1], xi[1], mfma32(C[kt][0], xi[0], z4))));
                float wr[4], wi[4];
#pragma unroll
                for (int r = 0; r < 4; ++r) { wr[r] = __uint_as_float((unsigned)fr[kt * 4 + r] << 16); wi[r] = __uint_as_float((unsigned)fi[kt * 4 + r] << 16); }
                vr[kt] = (v4f){ar.x * wr[0] - ai.x * wi[0], ar.y * wr[1] - ai.y * wi[1], ar.z * wr[2] - ai.z * wi[2], ar.w * wr[3] - ai.w * wi[3]};
                vi[kt] = (v4f){ar.x * wi[0] + ai.x * wr[0], ar.y * wi[1] + ai.y * wr[1], ar.z * wi[2] + ai.z * wr[2], ar.w * wi[3] + ai.w * wr[3]};
            }
            const v4u pr0 = pk8(vr[0], vr[1]), pr1 = pk8(vr[2], vr[3]), pi0 = pk8(vi[0], vi[1]), pi1 = pk8(vi[2], vi[3]), ni0 = neg8(pi0), ni1 = neg8(pi1);
#pragma unroll
            for (int nt = 0; nt < 4; ++nt) {
                const v4f rr = mfma32(ni1, S[nt][1], mfma32(ni0, S[nt][0], mfma32(pr1, C[nt][1], mfma32(pr0, C[nt][0], z4))));
                const v4f ri = mfma32(pr1, S[nt][1], mfma32(pr0, S[nt][0], mfma32(pi1, C[nt][1], mfma32(pi0, C[nt][0], z4))));
                const v2u qr = {pk2(rr.x, rr.y), pk2(rr.z, rr.w)}, qi = {pk2(ri.x, ri.y), pk2(ri.z, ri.w)};
                if (t == 0) { hr[nt] = qr; hi[nt] = qi; }
                else { char* o = lds + 32768 + (nt * 16 + rho) * 528 + wave * 64 + g * 16;
                    *(v4u*)o = (v4u){hr[nt].x, hr[nt].y, qr.x, qr.y}; *(v4u*)(o + 256) = (v4u){hi[nt].x, hi[nt].y, qi.x, qi.y}; }
            }
        }
    }
    BAR();
#pragma unroll
    for (int k = 0; k < 4; ++k) { const int n2 = wave * 8 + k * 2 + (lane >> 5);
        st16s(rs, OFF_Z2 + (n2 * 32 + (job >> 1)) * 1024 + (g & 1) * 512 + (job & 1) * 256 + rho * 16, 0, lds16(lds, 32768 + n2 * 528 + (lane & 31) * 16)); }
    STAMP(job == 0 && wave == 1, 4, 5); STAMP(wave == 1, 27, job);
    if (!fft_sync(p, c, FL_FB)) return;
    STAMP(job == 0 && wave == 1, 4, 6); STAMP(wave == 1, 28, job);
    {
        char* L = lds + wave * 4096;
#pragma unroll
        for (int pc = 0; pc < 4; ++pc) dma16s(rs, OFF_Z2 + (job * 32 + (pc & 1) * 16 + (lane >> 2)) * 1024 + (pc >> 1) * 512 + wave * 64 + (lane & 3) * 16, 0, wave * 4096 + pc * 1024);
        v4u C[2], S[2];
        { int n[8];
#pragma unroll
          for (int j = 0; j < 8; ++j) n[j] = 8 * g + j;
          twid(64 * rho + job, n, 2047, 1.f / 2048.f, C[0], S[0]); twid(64 * (16 + rho) + job, n, 2047, 1.f / 2048.f, C[1], S[1]); }
        VMWAIT(0);
        v4f rr[2][2], ri[2][2];
#pragma unroll
        for (int t = 0; t < 2; ++t) {
            const int a = (8 * g + (rho >> 2)) * 64 + (rho & 3) * 16 + t * 8;
            const v2u a0 = ldstr(L, a), a1 = ldstr(L, a + 256), b0 = ldstr(L, a + 2048), b1 = ldstr(L, a + 2304);
            const v4u xr = {a0.x, a0.y, a1.x, a1.y}, xi = {b0.x, b0.y, b1.x, b1.y}, ni = neg8(xi);
#pragma unroll
            for (int nt = 0; nt < 2; ++nt) { rr[nt][t] = mfma32(ni, S[nt], mfma32(xr, C[nt], z4)); ri[nt][t] = mfma32(xr, S[nt], mfma32(xi, C[nt], z4)); }
        }
        char* R = lds + 32768;
#pragma unroll
        for (int nt = 0; nt < 2; ++nt) { *(v4u*)(R + (nt * 16 + wave) * 1024 + lane * 16) = pk8(rr[nt][0], rr[nt][1]); *(v4u*)(R + (nt * 16 + 8 + wave) * 1024 + lane * 16) = pk8(ri[nt][0], ri[nt][1]); }
        BAR();
#pragma unroll 1
        for (int Q = wave; Q < 14; Q += 8) {
            v4f y[2][2] = {{z4, z4}, {z4, z4}};
#pragma unroll
            for (int hf = 0; hf < 2; ++hf) {
                v4u f0[8], f1[8];
#pragma unroll
                for (int k = 0; k < 8; ++k) { const int fo = OFF_FI + (Q * 2 + (rho >> 3)) * 16384 + g * 256 + (pperm(rho, 0) & 15) * 16; f0[k] = ld16(rs, fo, (hf * 8 + k) * 1024, 0); f1[k] = ld16(rs, fo + 64, (hf * 8 + k) * 1024, 0); }
                SCHED();
#pragma unroll
                for (int k = 0; k < 8; ++k) { const v4u b0 = lds16(R, (hf * 8 + k) * 1024 + lane * 16), b1 = lds16(R, (16 + hf * 8 + k) * 1024 + lane * 16);
                    y[0][0] = mfma32(f0[k], b0, y[0][0]); y[0][1] = mfma32(f1[k], b0, y[0][1]); y[1][0] = mfma32(f0[k], b1, y[1][0]); y[1][1] = mfma32(f1[k], b1, y[1][1]); }
            }
#pragma unroll
            for (int nt = 0; nt < 2; ++nt) *(v4u*)(p.out + (size_t)(64 * (nt * 16 + rho) + job) * 896 + Q * 32 + g * 8) = pk8(y[nt][0], y[nt][1]);
        }
        VMWAIT(0); BAR(); if (wave == 0 && (job & 31) == 31) { __builtin_amdgcn_s_sleep(100); __builtin_amdgcn_fence(__ATOMIC_RELEASE, "agent"); }
    }
}

extern "C" __global__ void __launch_bounds__(512, 2) k_main(Params p) {
    extern __shared__ __attribute__((aligned(16))) char lds[];
    Ctx c; c.rs = mkrsrc(p.ws, WS_BYTES); c.lds = lds; c.lane = threadIdx.x & 63; c.wave = __builtin_amdgcn_readfirstlane(threadIdx.x >> 6); c.rho = c.lane & 15; c.g = c.lane >> 4;
    { const int lane = c.lane; (void)lane; STAMP(blockIdx.x == 0 && c.wave == 7, 4, 0); }
    const int rot = *(const int*)(p.ws + flagoff(FL_ROT));
    const int x = (blockIdx.x + rot) & 7, y = blockIdx.x >> 3;
    if (x < 2) role_p(p, c, x * 32 + y);
    else if (x == 2) { if (y < 28) role_o(p, c, y); }
    else if (x == 3 && y == 28) role_cal(p, c);
    else {
        const int wi = (x - 4) * 32 + y - 16;
        if (x == 3) { if (y < 28) make_tables(p, c, y); } else if (wi >= 0) role_w(p, c, wi);
        if (wi >= 48) role_fft(p, c, wi - 48);
        else if (x == 3 ? y < 28 : wi >= 16 && wi < 44) role_f2(p, c, x == 3 ? y : wi - 16, x != 3);
        else if (x == 4) role_f1(p, c, y & 15, wi >= 0);
    }
    { const int lane = c.lane; (void)lane; LSTAMP(c.wave == 7, 8 + (blockIdx.x >> 6), blockIdx.x & 63); }
}

extern "C" void kernel_launch(void* const* d_in, const int* in_sizes, int n_in, void* d_out, int out_size, void* d_ws, size_t ws_size, hipStream_t stream) {
    Params p;
    p.xa = (const unsigned short*)d_in[0]; p.wr = (const unsigned short*)d_in[1]; p.wi = (const unsigned short*)d_in[2];
    p.xb = (const unsigned short*)d_in[3]; p.mem0 = (const unsigned short*)d_in[4];
    p.wq = (const unsigned short*)d_in[5]; p.wk = (const unsigned short*)d_in[6]; p.wv = (const unsigned short*)d_in[7]; p.wo = (const unsigned short*)d_in[8];
    p.out = (unsigned short*)d_out;
    p.ws = (char*)d_ws + (((size_t)2 << 20) - ((uintptr_t)d_ws & (((size_t)2 << 20) - 1))) % ((size_t)2 << 20);
    (void)hipFuncSetAttribute((const void*)k_main, hipFuncAttributeMaxDynamicSharedMemorySize, LDS_MAIN);
    (void)hipFuncSetAttribute((const void*)k_pro, hipFuncAttributeMaxDynamicSharedMemorySize, LDS_PRO);
    hipLaunchKernelGGL(k_pro, dim3(256), dim3(512), LDS_PRO, stream, p);
    hipLaunchKernelGGL(k_main, dim3(256), dim3(512), LDS_MAIN, stream, p);
}
```

```cpp
#include <hip/hip_runtime.h>
#include <hip/hip_bf16.h>
#include <stdint.h>

typedef unsigned v4u __attribute__((ext_vector_type(4)));
typedef unsigned v2u __attribute__((ext_vector_type(2)));
typedef float v4f __attribute__((ext_vector_type(4)));
typedef float v2f __attribute__((ext_vector_type(2)));
typedef short v4s __attribute__((ext_vector_type(4)));
typedef __bf16 v2bf __attribute__((ext_vector_type(2)));
typedef __bf16 v8bf __attribute__((ext_vector_type(8)));
typedef __bf16 v16bf __attribute__((ext_vector_type(16)));
typedef unsigned v8u __attribute__((ext_vector_type(8)));
typedef __amdgpu_buffer_rsrc_t rsrc_t;
#define LAS __attribute__((address_space(3)))
#define DEV __device__ __forceinline__

constexpr int NSEG = 64, NH = 16, DM = 448;
constexpr unsigned SENT = 0xFFFFFFFFu;
constexpr int OOB = 0x7fffffff;
constexpr int SC1 = 16;
constexpr int OFF_HO = 0;
constexpr int OFF_CTL = 4 << 20;
constexpr int OFF_AP = OFF_CTL + 131072;
constexpr int OFF_BT = OFF_AP + NH * DM * DM * 2;
constexpr int OFF_G = OFF_BT + NH * DM * DM * 2;
constexpr int OFF_U = OFF_G + NSEG * NH * 28672;
constexpr int OFF_T = OFF_U + NSEG * NH * 28672;
constexpr int OFF_V = OFF_T + NSEG * NH * 7168;
constexpr int OFF_PX = OFF_V + NSEG * NH * 7168;
constexpr int OFF_PM = OFF_PX + NSEG * NH * 2048;
constexpr int OFF_FD = OFF_PM + NSEG * NH * 512;
constexpr int OFF_FI = OFF_FD + 512 * DM * 2;
constexpr int OFF_Z1 = OFF_FI + 512 * DM * 2;
constexpr int OFF_Z2 = OFF_Z1 + (2 << 20);
constexpr int OFF_XF = OFF_Z2 + (2 << 20);
constexpr int OFF_G0 = OFF_XF + (2 << 20);
constexpr int OFF_DBG = OFF_G0 + (1 << 20);
constexpr int WS_BYTES = OFF_DBG + (1 << 20);
constexpr int HO_MEM = 512, HO_PART = 16384, HO_E = 32768;
constexpr int OFF_PROBE = OFF_HO + 49152;
constexpr int FL_W = 0, FL_T = 112, FL_V = 176, FL_P = 208, FL_ROT = 336, FL_TAB = 337, FL_FA = 338, FL_FB = 339, FL_ABORT = 340, FL_CAL = 341, FL_P2 = 344;
constexpr int LDS_MAIN = 114688 + 2048;
constexpr int LDS_PRO = 143360;
constexpr int SPIN_CAP = 1 << 17;

struct Params {
    const unsigned short* xa; const unsigned short* wr; const unsigned short* wi;
    const unsigned short* xb; const unsigned short* mem0;
    const unsigned short* wq; const unsigned short* wk; const unsigned short* wv; const unsigned short* wo;
    unsigned short* out; char* ws;
};

DEV unsigned pk2(float a, float b) { v2bf r = {(__bf16)a, (__bf16)b}; return __builtin_bit_cast(unsigned, r); }
DEV v4u pk8(v4f a, v4f b) { return (v4u){pk2(a.x, a.y), pk2(a.z, a.w), pk2(b.x, b.y), pk2(b.z, b.w)}; }
DEV v4f mfma32(v4u a, v4u b, v4f c) { return __builtin_amdgcn_mfma_f32_16x16x32_bf16(__builtin_bit_cast(v8bf, a), __builtin_bit_cast(v8bf, b), c, 0, 0, 0); }
DEV v4f mfma16(v2u a, v2u b, v4f c) { return __builtin_amdgcn_mfma_f32_16x16x16bf16_1k(__builtin_bit_cast(v4s, a), __builtin_bit_cast(v4s, b), c, 0, 0, 0); }
DEV v4f smf(v4u a, v4u b0, v4u b1, v4f c, int idx) {
    v8u b = {b0.x, b0.y, b0.z, b0.w, b1.x, b1.y, b1.z, b1.w};
    return __builtin_amdgcn_smfmac_f32_16x16x64_bf16(__builtin_bit_cast(v8bf, a), __builtin_bit_cast(v16bf, b), c, idx, 0, 0);
}
DEV float fmx(float a, float b) { return __builtin_elementwise_maximum(a, b); }
DEV float max_g(float s) {
    auto r = __builtin_amdgcn_permlane16_swap(__float_as_uint(s), __float_as_uint(s), false, false);
    s = fmx(__uint_as_float(r[0]), __uint_as_float(r[1]));
    auto q = __builtin_amdgcn_permlane32_swap(__float_as_uint(s), __float_as_uint(s), false, false);
    return fmx(__uint_as_float(q[0]), __uint_as_float(q[1]));
}
DEV float sum_g(float s) {
    auto r = __builtin_amdgcn_permlane16_swap(__float_as_uint(s), __float_as_uint(s), false, false);
    s = __uint_as_float(r[0]) + __uint_as_float(r[1]);
    auto q = __builtin_amdgcn_permlane32_swap(__float_as_uint(s), __float_as_uint(s), false, false);
    return __uint_as_float(q[0]) + __uint_as_float(q[1]);
}
DEV float ror8(float s) { return __int_as_float(__builtin_amdgcn_mov_dpp(__float_as_int(s), 0x128, 0xf, 0xf, true)); }
template <int CTRL> DEV unsigned dppu(unsigned x) { return (unsigned)__builtin_amdgcn_mov_dpp((int)x, CTRL, 0xf, 0xf, true); }
DEV v4u quad4(unsigned x) { return (v4u){x, dppu<0x55>(x), dppu<0xAA>(x), dppu<0xFF>(x)}; }
DEV unsigned umax4(v4u v) { return max(max(v.x, v.y), max(v.z, v.w)); }
DEV rsrc_t mkrsrc(const void* p, unsigned bytes) { return __builtin_amdgcn_make_buffer_rsrc((void*)p, 0, bytes, 0x00020000); }
DEV v4u ld16(rsrc_t r, int vo, int so, int aux) { return aux ? __builtin_amdgcn_raw_buffer_load_b128(r, vo, so, SC1) : __builtin_amdgcn_raw_buffer_load_b128(r, vo, so, 0); }
DEV v2u ld8s(rsrc_t r, int vo, int so) { return __builtin_amdgcn_raw_buffer_load_b64(r, vo, so, SC1); }
DEV unsigned ld4s(rsrc_t r, int vo, int so) { return __builtin_amdgcn_raw_buffer_load_b32(r, vo, so, SC1); }
DEV void st16s(rsrc_t r, int vo, int so, v4u v) {
    __builtin_amdgcn_raw_buffer_store_b128(v, r, vo, so, SC1); asm volatile("s_nop 1" :: "v"(v) : "memory");
}
DEV void st8s(rsrc_t r, int vo, int so, v2u v) { __builtin_amdgcn_raw_buffer_store_b64(v, r, vo, so, SC1); }
DEV void st4s(rsrc_t r, int vo, int so, unsigned v) { __builtin_amdgcn_raw_buffer_store_b32(v, r, vo, so, SC1); }
DEV void dma16(rsrc_t r, int vo, int so, int ldsaddr) {
    unsigned keep;
    asm volatile("s_mov_b32 %0, m0\n\ts_mov_b32 m0, %4\n\ts_nop 4\n\tbuffer_load_dwordx4 %1, %2, %3 offen lds\n\ts_mov_b32 m0, %0"
                 : "=&s"(keep) : "v"(vo), "s"(r), "s"(__builtin_amdgcn_readfirstlane(so)), "s"(__builtin_amdgcn_readfirstlane(ldsaddr)) : "memory");
}
DEV void dma16s(rsrc_t r, int vo, int so, int ldsaddr) {
    unsigned keep;
    asm volatile("s_mov_b32 %0, m0\n\ts_mov_b32 m0, %4\n\ts_nop 4\n\tbuffer_load_dwordx4 %1, %2, %3 offen sc1 lds\n\ts_mov_b32 m0, %0"
                 : "=&s"(keep) : "v"(vo), "s"(r), "s"(__builtin_amdgcn_readfirstlane(so)), "s"(__builtin_amdgcn_readfirstlane(ldsaddr)) : "memory");
}
#define BAR() asm volatile("s_waitcnt lgkmcnt(0)\n\ts_barrier" ::: "memory")
#define BAR0() asm volatile("s_barrier" ::: "memory")
#define VMWAIT(n) asm volatile("s_waitcnt vmcnt(" #n ")" ::: "memory")
#define FENCE() asm volatile("" ::: "memory")
#define SCHED() __builtin_amdgcn_sched_barrier(0)
DEV v4u lds16(const char* l, int a) { return *(const v4u*)(l + a); }
DEV v2u lds8(const char* l, int a) { return *(const v2u*)(l + a); }
DEV v2u ldstr(const char* l, int a) { v4s r = __builtin_amdgcn_ds_read_tr16_b64_v4i16((LAS v4s*)(l + a)); return __builtin_bit_cast(v2u, r); }
template <int N> DEV void slp() { if constexpr (N > 0) __builtin_amdgcn_s_sleep(N); }
DEV int flagoff(int i) { return OFF_CTL + i * 128; }
template <int SL> DEV bool wait_flags(rsrc_t rs, int vo, unsigned need, unsigned& seen) {
    for (int sp = 0;; ++sp) {
        FENCE();
        seen = ld4s(rs, vo, 0);
        if (__builtin_amdgcn_ballot_w64(seen < need) == 0) return true;
        slp<SL>();
        if ((sp & 255) == 255) {
            if (sp > SPIN_CAP) st4s(rs, flagoff(FL_ABORT), 0, 1u);
            if (sp > SPIN_CAP || __builtin_amdgcn_readfirstlane(ld4s(rs, flagoff(FL_ABORT), 0))) return false;
        }
    }
}
DEV int img(int p) { return ((p & 7) >> 1) * 4 + (p >> 3) * 2 + (p & 1); }
DEV int pperm(int rho, int tau) { return (rho >> 2) * 8 + tau * 4 + (rho & 3); }

#define STAMP(cond, slot, s)
#define LSTAMP(cond, slot, s)
#define ACC_DECL
#define ACC_T(k)
#define ACC_OUT(slot)
#define CSTAMP(cond, slot, s)

template <int which> DEV void pro_tile(const Params& p, char* lds, rsrc_t rs, int b, int h, int b0, int c0, int lane, int wave) {
    const int rho = lane & 15, g = lane >> 4;
    const rsrc_t ra = mkrsrc(which ? p.wv : p.wk, DM * 4096 * 2), rb = mkrsrc(which ? p.wo : p.wq, DM * 4096 * 2);
    int sa[4], sb[2];
#pragma unroll
    for (int k = 0; k < 4; ++k) { const int r = (k & 1) * 8 + (lane >> 3); sa[k] = ((b0 + 32 * wave + pperm(r, k >> 1)) * 4096 + h * 256 + ((lane & 7) ^ (r >> 1)) * 8) * 2; }
#pragma unroll
    for (int k = 0; k < 2; ++k) { const int r = k * 8 + (lane >> 3), ch = (2 * wave + k) * 64 + lane, e = ch / 14;
        sb[k] = which ? ((h * 256 + e) * DM + c0 + (ch - e * 14) * 8) * 2 : ((c0 + wave * 16 + r) * 4096 + h * 256 + ((lane & 7) ^ (r >> 1)) * 8) * 2; }
    const int bstep = which ? 64 * DM * 2 : 128, la = 57344 + wave * 4096;
    v4u a3[4];
#pragma unroll
    for (int c = 0; c < 4; ++c) {
#pragma unroll
        for (int k = 0; k < 2; ++k) dma16(rb, sb[k], c * bstep, c * 14336 + (2 * wave + k) * 1024);
#pragma unroll
        for (int k = 0; k < 4; ++k) { if (c < 3) dma16(ra, sa[k], c * 128, la + c * 28672 + k * 1024); else a3[k] = ld16(ra, sa[k], 384, 0); }
    }
    const int rd = (rho >> 3) * 1024 + (rho & 7) * 128, rx0 = (g ^ (rho >> 1)) * 16, rx1 = ((4 + g) ^ (rho >> 1)) * 16;
    const int tra = (8 * g + (rho >> 2)) * 224 + (rho & 3) * 8;
    v4f acc[7][2];
#pragma unroll
    for (int ct = 0; ct < 7; ++ct) { acc[ct][0] = (v4f){0, 0, 0, 0}; acc[ct][1] = acc[ct][0]; }
    const float sc = which ? 1.f : 0.09016844005556021f;
    const int obase = (which ? OFF_BT : OFF_AP) + ((h * 28 + (c0 >> 4)) * 14 + (b0 >> 5) + wave) * 1024 + lane * 16;
    v4u af[2][4], f[2][7][2];
#pragma unroll
    for (int c = 0; c <= 4; ++c) {
        if (c < 4) {
            if (c == 0) VMWAIT(18); else if (c == 1) VMWAIT(12); else if (c == 2) VMWAIT(6); else VMWAIT(0);
            if (c == 3) {
#pragma unroll
                for (int k = 0; k < 4; ++k) *(v4u*)(lds + la + k * 1024 + lane * 16) = a3[k];
            }
            BAR();
            if (c == 3) { STAMP(b == 0 && wave == 0, 4, 9); STAMP(wave == 0 && b < 64, 30, b); }
        }
        const char* A = lds + la + (c % 3) * 28672; const char* B = lds + c * 14336;
        if (c < 4) { af[c & 1][0] = lds16(A, rd + rx0); af[c & 1][1] = lds16(A, rd + rx1); af[c & 1][2] = lds16(A, 2048 + rd + rx0); af[c & 1][3] = lds16(A, 2048 + rd + rx1); }
#pragma unroll
        for (int ct = 0; ct < 7; ++ct) {
            if (c < 4) {
                if (which == 0) { f[c & 1][ct][0] = lds16(B, ct * 2048 + rd + rx0); f[c & 1][ct][1] = lds16(B, ct * 2048 + rd + rx1); }
                else {
#pragma unroll
                    for (int kk = 0; kk < 2; ++kk) { const v2u x = ldstr(B, tra + kk * 7168 + ct * 32), y = ldstr(B, tra + kk * 7168 + 896 + ct * 32); f[c & 1][ct][kk] = (v4u){x.x, x.y, y.x, y.y}; }
                }
            }
            SCHED();
            if (c > 0) { const int q = (c - 1) & 1;
                acc[ct][0] = mfma32(af[q][1], f[q][ct][1], mfma32(af[q][0], f[q][ct][0], acc[ct][0])); acc[ct][1] = mfma32(af[q][3], f[q][ct][1], mfma32(af[q][2], f[q][ct][0], acc[ct][1])); }
            if (c == 4) st16s(rs, obase, ct * 14336, pk8(acc[ct][0] * sc, acc[ct][1] * sc));
            SCHED();
        }
    }
    STAMP(b == 0 && wave == 0, 4, 10);
    STAMP(wave == 0 && b >= 192, 31, b & 63);
}
extern "C" __global__ void __launch_bounds__(512, 2) k_pro(Params p) {
    extern __shared__ __attribute__((aligned(16))) char lds[];
    const int b = blockIdx.x, tid = threadIdx.x, lane = tid & 63, wave = __builtin_amdgcn_readfirstlane(tid >> 6);
    const int rho = lane & 15, g = lane >> 4;
    const int gemm = (b & 7) * 4 + (b >> 6), sub = (b >> 3) & 7, which = gemm & 1, h = gemm >> 1;
    const int b0 = 224 * (sub & 1), c0 = 112 * (sub >> 1);
    const rsrc_t rs = mkrsrc(p.ws, WS_BYTES);
    LSTAMP(b == 0 && wave == 0, 4, 8); STAMP(wave == 0 && b < 64, 29, b);
    if (wave == 7) {
        const rsrc_t rx = mkrsrc(p.xb, 2048 * DM * 2);
        v4u xf[7], m0[7];
#pragma unroll
        for (int k = 0; k < 7; ++k) { const int pc = b * 7 + k, rt = pc / 14, ks = pc - rt * 14; xf[k] = ld16(rx, ((rt * 16 + (rho ^ ((rt & 1) * 4))) * DM + ks * 32 + g * 8) * 2, 0, 0); }
        const v4u sent = {SENT, SENT, SENT, SENT};
        if (b == 0) {
#pragma unroll
            for (int k = 0; k < 7; ++k) { const int c = k * 64 + lane; const unsigned* m = (const unsigned*)(p.mem0 + ((c >> 1) & 7) * DM + (c >> 4) * 16 + (c & 1) * 2); m0[k] = (v4u){m[0], m[2], m[4], m[6]}; }
        } else {
#pragma unroll
            for (int k = 0; k < 7; ++k) m0[k] = sent;
        }
        SCHED();
        BAR0(); BAR0(); BAR0(); BAR0();
        const unsigned rot = ((unsigned)__builtin_amdgcn_s_getreg(20 | (3 << 11)) - b) & 7u;
        if (lane < 32) st16s(rs, OFF_CTL + b * 512 + lane * 16, 0, (v4u){b * 4 == FL_ROT && lane == 0 ? rot : 0u, 0u, 0u, 0u});
#pragma unroll
        for (int k = 0; k < 16; ++k) {
            const int o = ((k & 7) * 1024 + lane * 16 + HO_MEM) & 8191;
            st16s(rs, (b & 3) == 3 && (b & 127) != 3 ? OOB : OFF_HO + b * 16384 + (k >> 3) * 8192 + o, 0, (k & 7) < 7 ? m0[k & 7] : sent);
        }
#pragma unroll
        for (int k = 0; k < 4; ++k) st16s(rs, OFF_G0 + (b * 4 + k) * 1024 + lane * 16, 0, sent);
#pragma unroll
        for (int k = 0; k < 7; ++k) st16s(rs, OFF_XF + (b * 7 + k) * 1024 + lane * 16, 0, xf[k]);
        return;
    }
    if (which) pro_tile<1>(p, lds, rs, b, h, b0, c0, lane, wave); else pro_tile<0>(p, lds, rs, b, h, b0, c0, lane, wave);
}

struct Ctx { rsrc_t rs; char* lds; int lane, wave, rho, g; };

DEV int slot_base(int s, int cal) { return OFF_HO + s * 65536 + (((cal >> (s >> 5)) & 1) << 13); }
#define STAGE_HEAD "s_add_i32 %[bud], %[bud], -1\n\ts_cmp_lt_i32 %[bud], 0\n\ts_cselect_b32 vcc_hi, -1, -2\n\tv_mov_b32 v233, vcc_hi\n\t"
#define STAGE_TAIL "v_cmp_gt_u32 vcc, v232, v233\n\ts_cbranch_vccnz 1b\n\ts_waitcnt lgkmcnt(0)\n\ts_barrier"
#define STAGE_CLOB "memory", "vcc", "scc", "v224", "v225", "v226", "v227", "v228", "v229", "v230", "v231", "v232", "v233"
DEV void stage1(rsrc_t rs, int vo, int so, int& budget) {
    asm volatile("1:\n\tbuffer_load_dwordx4 v[224:227], %[vo], %[rs], %[so] offen sc1\n\t" STAGE_HEAD
                 "s_waitcnt vmcnt(0)\n\tds_write_b128 %[vo], v[224:227]\n\tv_max3_u32 v232, v224, v225, v226\n\tv_max_u32 v232, v232, v227\n\t" STAGE_TAIL
                 : [bud] "+s"(budget) : [vo] "v"(vo), [rs] "s"(rs), [so] "s"(so) : STAGE_CLOB);
}
DEV void stage2(rsrc_t rs, int vo, int vo2, int so, int& budget) {
    asm volatile("1:\n\tbuffer_load_dwordx4 v[224:227], %[vo], %[rs], %[so] offen sc1\n\tbuffer_load_dwordx4 v[228:231], %[vo2], %[rs], %[so] offen sc1\n\t" STAGE_HEAD
                 "s_waitcnt vmcnt(1)\n\tds_write_b128 %[vo], v[224:227]\n\tv_max3_u32 v232, v224, v225, v226\n\t"
                 "s_waitcnt vmcnt(0)\n\tds_write_b128 %[vo2], v[228:231]\n\tv_max3_u32 v232, v232, v227, v228\n\tv_max3_u32 v232, v232, v229, v230\n\tv_max_u32 v232, v232, v231\n\t" STAGE_TAIL
                 : [bud] "+s"(budget) : [vo] "v"(vo), [vo2] "v"(vo2), [rs] "s"(rs), [so] "s"(so) : STAGE_CLOB);
}
DEV void give_up(rsrc_t rs) { st4s(rs, flagoff(FL_ABORT), 0, 1u); }
#define PIN2(a, b) asm volatile("" : "+v"(a), "+v"(b))
constexpr int CAL_STEP = 8;
#define TOUCH_LEAD 3
DEV void role_cal(const Params& p, const Ctx& c) {
    if (c.wave >= 2) return;
    unsigned best[2] = {0xffffffffu, 0xffffffffu};
    for (int r = 0; r < 20; ++r) {
#pragma unroll
        for (int k = 0; k < 2; ++k) {
            const int off = OFF_PROBE + c.wave * (2 << 20) + k * 8192 + r * 256;
            unsigned long long t0, t1; unsigned v;
            asm volatile("s_memtime %0\n\ts_waitcnt lgkmcnt(0)\n\tbuffer_load_dword %2, %3, %4, 0 offen sc1\n\ts_waitcnt vmcnt(0)\n\ts_memtime %1\n\ts_waitcnt lgkmcnt(0)"
                         : "=&s"(t0), "=&s"(t1), "=&v"(v) : "v"(off), "s"(c.rs) : "memory");
            best[k] = min(best[k], (unsigned)(t1 - t0));
        }
        if (r == 5 && c.wave == 0) { if (c.lane == 0) st4s(c.rs, flagoff(FL_CAL), 0, best[1] < best[0] ? 3u : 2u); VMWAIT(0); }
    }
    if (c.lane == 0) st4s(c.rs, flagoff(FL_CAL + 1 + c.wave), 0, best[1] < best[0] ? 3u : 2u);
    { const int k = c.lane & 3, o = (k == 0 ? 0 : k == 1 ? 7936 : 49152 + (k & 1) * 4096 + 2048) + (best[1] < best[0] ? 8192 : 0);
      for (int s = c.wave ? 32 : 12; s < 32 * (c.wave + 1); s += 4) {
          unsigned seen; if (!wait_flags<16>(c.rs, flagoff(FL_V), s - TOUCH_LEAD, seen)) return;
          unsigned v = ld4s(c.rs, OFF_HO + s * 65536 + o, 0); asm volatile("" :: "v"(v));
      } }
}
DEV void touch_window(rsrc_t rs) { const unsigned v = ld4s(rs, OFF_PROBE + (2 << 20) + 8064, 0); asm volatile("" :: "v"(v)); }
DEV int get_cal(const Ctx& c, int late, int& budget) {
    unsigned seen;
    if (!wait_flags<2>(c.rs, flagoff(FL_CAL + late + (late & c.lane)), 1u, seen)) budget = -1;
    return (__builtin_amdgcn_readlane(seen, 0) & 1) | ((__builtin_amdgcn_readlane(seen, 1) & 1) << 1);
}
DEV unsigned now() { return (unsigned)__builtin_amdgcn_s_memtime(); }
DEV void wait_until(unsigned t) {
    int rem = (int)(t - now());
    while (rem > 260) { __builtin_amdgcn_s_sleep(1); rem = (int)(t - now()); }
    rem -= 60;
    asm volatile("1:\n\ts_add_i32 %0, %0, -40\n\ts_cmp_gt_i32 %0, 0\n\ts_cbranch_scc1 1b" : "+s"(rem) :: "scc");
}
#define T_PM 2050
#define T_OS 2200
#define T_OM 400
#define T_OU 750
#define T_G0 600
#define T_ST 0
#define T_PG 200
#define ABL(b) true
#define S_AT 6

#define MEM_TIMES_W(lbase)                                                                                           \
    v4u a_[7];                                                                                                       \
    _Pragma("unroll") for (int f = 0; f < 7; ++f) a_[f] = lds16(lds, (lbase) + f * 1024);                            \
    EXTRA_READS;                                                                                                     \
    SCHED();                                                                                                         \
    _Pragma("unroll") for (int f = 0; f < 7; ++f) { c0 = smf(a_[f], w[0][2 * f], w[0][2 * f + 1], c0, sidx); c1 = smf(a_[f], w[1][2 * f], w[1][2 * f + 1], c1, sidx); AFTER_STEP(f); } \
    const v2u tp = {pk2(c0.x + c0.z, c0.y + c0.w), pk2(c1.x + c1.z, c1.y + c1.w)};

#define VCLOB "v224", "v225", "v226", "v227", "v228", "v229", "v230", "v231", VCLOB_HI
#define VCLOB_HI "v232", "v233", "v234", "v235", "v236", "v237", "v238", "v239", \
              "v240", "v241", "v242", "v243", "v244", "v245", "v246", "v247", "v248", "v249", "v250", "v251", "v252", "v253", "v254", "v255"
#define PUBLISH_ASM(ST)                                                                                                                     \
    "s_cmp_lg_u32 %[pub], 7\n\ts_waitcnt lgkmcnt(0)\n\ts_barrier\n\ts_cbranch_scc1 9f\n\t"                                                  \
    "ds_read_b64 v[224:225], %[la]\n\tds_read_b64 v[226:227], %[la] offset:" #ST "*1\n\tds_read_b64 v[228:229], %[la] offset:" #ST "*2\n\t" \
    "ds_read_b64 v[230:231], %[la] offset:" #ST "*3\n\tds_read_b64 v[232:233], %[la] offset:" #ST "*4\n\t"                                  \
    "ds_read_b64 v[234:235], %[la] offset:" #ST "*5\n\tds_read_b64 v[236:237], %[la] offset:" #ST "*6\n\t"                                  \
    "ds_read_b64 v[238:239], %[la] offset:" #ST "*7\n\t"                                                                                    \
    "s_waitcnt lgkmcnt(6)\n\tv_pk_add_f32 v[224:225], v[224:225], v[226:227]\n\t"                                                           \
    "s_waitcnt lgkmcnt(4)\n\tv_pk_add_f32 v[228:229], v[228:229], v[230:231]\n\t"                                                           \
    "s_waitcnt lgkmcnt(2)\n\tv_pk_add_f32 v[232:233], v[232:233], v[234:235]\n\tv_pk_add_f32 v[224:225], v[224:225], v[228:229]\n\t"        \
    "s_waitcnt lgkmcnt(0)\n\tv_pk_add_f32 v[236:237], v[236:237], v[238:239]\n\tv_pk_add_f32 v[224:225], v[224:225], v[232:233]\n\t"        \
    "s_nop 0\n\tv_pk_add_f32 v[224:225], v[224:225], v[236:237]\n\t"                                                                        \
    "s_nop 0\n\tv_cvt_pk_bf16_f32 v224, v224, v225\n\ts_nop 1\n\t"                                                                          \
    "v_mov_b32_dpp v225, v224 quad_perm:[1,1,1,1] row_mask:0xf bank_mask:0xf\n\t"                                                           \
    "v_mov_b32_dpp v226, v224 quad_perm:[2,2,2,2] row_mask:0xf bank_mask:0xf\n\t"                                                           \
    "v_mov_b32_dpp v227, v224 quad_perm:[3,3,3,3] row_mask:0xf bank_mask:0xf\n\t"                                                           \
    "buffer_store_dwordx4 v[224:227], %[vo], %[rs], %[so] offen sc1\n\t"                                                                    \
    "9:\n\ts_barrier\n\ts_nop 0"

DEV void role_p(const Params& p, const Ctx& c, int idx) {
    const int h = idx >> 2, q = idx & 3, lane = c.lane, wave = c.wave, rho = c.rho, g = c.g;
    const rsrc_t rs = c.rs; char* lds = c.lds;
    const int om = q * 4 + wave;
    const bool isT = wave < 4 && om < 14, isX = wave < 4 && om >= 14;
    constexpr int L_MEM = 0, L_PART = 8192, L_DUMP = 10240, L_CAL = 11264, L_X = 16384;
    const int sidx = (rho & 2) ? 0xEEEE : 0x4444;
    const int fa = L_MEM + g * 256 + ((rho >> 2) * 2 + (rho & 1)) * 32 + ((rho >> 1) & 1) * 16;
    v2u sel; { const int r0 = rho - 2 * g; sel.x = (r0 == 0 ? 0x3f80u : 0u) | (r0 == 1 ? 0x3f800000u : 0u); sel.y = (r0 == 8 ? 0x3f80u : 0u) | (r0 == 9 ? 0x3f800000u : 0u); }
    const int piece = q < 3 ? wave : (wave < 2 ? wave : wave - 2);
    const int po = piece * 1024 + lane * 16;
    int budget = SPIN_CAP;
#pragma unroll
    for (int k = 0; k < 7; ++k) dma16(rs, OFF_XF + lane * 16, (wave * 7 + k) * 1024, L_X + (wave * 7 + k) * 1024);
    if (isT) {
        v4u w[2][14];
#pragma unroll
        for (int t = 0; t < 2; ++t)
#pragma unroll
            for (int ks = 0; ks < 14; ++ks) w[t][ks] = ld16(rs, OFF_AP + (h * 28 + 2 * om + t) * 14336 + g * 256 + img(rho) * 16, ks * 1024, 0);
        VMWAIT(28);
        BAR0();
        STAMP(wave == 0, 23, idx);
#pragma unroll 1
        for (int sg = 0; sg < 2; ++sg) {
            v4f a[2][2] = {{{0, 0, 0, 0}, {0, 0, 0, 0}}, {{0, 0, 0, 0}, {0, 0, 0, 0}}};
#pragma unroll
            for (int ks = 0; ks < 14; ++ks)
#pragma unroll
                for (int kt = 0; kt < 2; ++kt) { const v4u x = lds16(lds, L_X + sg * 28672 + kt * 14336 + ks * 1024 + g * 256 + (rho ^ (kt * 4)) * 16);
                    a[kt][0] = mfma32(w[0][ks], x, a[kt][0]); a[kt][1] = mfma32(w[1][ks], x, a[kt][1]); }
#pragma unroll
            for (int kt = 0; kt < 2; ++kt) {
                auto x = __builtin_amdgcn_permlane32_swap(pk2(a[kt][0].x, a[kt][0].y), pk2(a[kt][1].x, a[kt][1].y), false, false);
                auto y = __builtin_amdgcn_permlane32_swap(pk2(a[kt][0].z, a[kt][0].w), pk2(a[kt][1].z, a[kt][1].w), false, false);
                st16s(rs, OFF_G0 + (h * 14 + om) * 2048 + kt * 1024 + lane * 16, sg * (NH * 28672), (v4u){x[0], x[1], y[0], y[1]});
            }
            BAR0();
            STAMP(wave == 0, 24 + sg, idx);
        }
        int cal = get_cal(c, 0, budget);
        STAMP(wave == 0, 26, idx);
        unsigned tref = now() - 65536;
        const int qa = L_MEM + (2 * om + (rho >> 3)) * 256 + (rho & 7) * 32 + g * 8;
        const int pw = (g >> 1) == (rho >> 3) ? L_PART + (2 * wave + (rho >> 3)) * 256 + (rho & 7) * 32 + (g & 1) * 16 : L_DUMP + lane * 16;
        const int tw = lane < 32 ? OFF_T + (h * 14 + om) * 512 + ((rho >> 3) * 2 + g) * 128 + (rho & 7) * 16 : OOB;
#pragma unroll 1
        for (int s = 0; s < NSEG && budget >= 0; ++s) {
            if (s == CAL_STEP) cal = __builtin_amdgcn_readfirstlane(*(const int*)(lds + L_CAL));
            const int sb = slot_base(s, cal);
            v4f c0 = {0, 0, 0, 0}, c1 = {0, 0, 0, 0}; PIN2(c0, c1);
            wait_until(tref + T_PM);
            stage1(rs, po, sb + HO_MEM, budget);
            v2u mq;
#define EXTRA_READS mq = lds8(lds, qa)
#define AFTER_STEP(f)
            MEM_TIMES_W(fa)
#undef EXTRA_READS
            const v4f d2 = mfma16(tp, sel, (v4f){0, 0, 0, 0});
            const v2u d = {pk2(d2.x, d2.y), pk2(d2.z, d2.w)};
            *(v4f*)(lds + pw) = mfma16(d, mq, (v4f){0, 0, 0, 0});
            BAR();
            BAR0();
            tref = now();
            wait_until(tref + T_PG);
            if (ABL(8)) { auto x = __builtin_amdgcn_permlane32_swap(d.x, d.y, false, false);
              auto y = __builtin_amdgcn_permlane32_swap(x[0], x[0], false, false); auto z = __builtin_amdgcn_permlane32_swap(x[1], x[1], false, false);
              st16s(rs, tw, s * (NH * 7168), (v4u){y[0], z[0], y[1], z[1]}); }
        }
    } else if (isX) {
        v4u w[2][14];
        int cal = get_cal(c, 0, budget);
        BAR0(); BAR0();
        if (om == 14) wait_until(now() + T_G0);
        else BAR0();
        const int gl0 = OFF_G + h * 28672 + (rho >> 3) * 1024 + g * 256 + (pperm(rho, 0) & 15) * 16;
        const int gfl = flagoff(FL_W + h * 7 + (lane & 3));
        const int ew = rho < 8 ? HO_E + h * 512 + g * 128 + rho * 16 : OOB, mw = g == 0 ? L_PART + 1024 + (rho & 7) * 32 + (rho >> 3) * 16 : L_DUMP + lane * 16;
        auto load_g = [&](int s, int t) {
            FENCE();
#pragma unroll
            for (int ks = 0; ks < 14; ++ks) w[t][ks] = ld16(rs, gl0 + t * 64, s * (NH * 28672) + (s < 2 ? OFF_G0 - OFF_G : 0) + ks * 2048, 1);
            FENCE();
        };
        bool lack = false;
#pragma unroll 1
        for (int s = om - 14; s < NSEG; s += 2) {
            const int sl = min(s, NSEG - 2);
            if (s >= 1) wait_until(now() + (s == 1 ? T_G0 : T_PG));
            unsigned seen;
            if (lack && !wait_flags<1>(rs, gfl, sl, seen)) budget = -1;
            load_g(sl, 0);
            if (s >= 2) { BAR0(); BAR0(); BAR0(); wait_until(now() + T_PG); }
            load_g(sl, 1);
            seen = ld4s(rs, gfl, 0);
            if (s == 1) { BAR0(); BAR0(); BAR0(); }
            if (s < 2) {
#pragma unroll 1
                for (;;) {
                    unsigned m = 0;
#pragma unroll
                    for (int ks = 0; ks < 14; ++ks) m = max(m, max(umax4(w[0][ks]), umax4(w[1][ks])));
                    if (__builtin_amdgcn_ballot_w64(m == SENT) == 0 || --budget < 0) break;
                    load_g(s, 0); load_g(s, 1);
                }
                if (s == 0) BAR0();
            }
            if ((s | 1) == CAL_STEP + 1) cal = __builtin_amdgcn_readfirstlane(*(const int*)(lds + L_CAL));
            const int sb = slot_base(s, cal);
            v4f c0 = {0, 0, 0, 0}, c1 = {0, 0, 0, 0}; PIN2(c0, c1);
            lack = __builtin_amdgcn_ballot_w64(seen < (unsigned)min(s + 2, NSEG - 2)) != 0;
            SCHED();
            VMWAIT(0);
            STAMP(idx == 3 && s == 0, 4, 12); STAMP(s == 0, 27, h); STAMP(s == 1, 27, 16 + h);
            BAR();
            if (s < NSEG - 1) {
#define EXTRA_READS
                MEM_TIMES_W(fa)
#undef EXTRA_READS
                const v4f d2 = mfma16(tp, sel, (v4f){0, 0, 0, 0});
                float m = fmx(fmx(d2.x, d2.y), fmx(d2.z, d2.w));
                m = max_g(fmx(m, ror8(m)));
                *(v4f*)(lds + mw) = (v4f){-m, -m, -m, -m};
                SCHED();
                BAR();
                const float e0 = __builtin_amdgcn_exp2f(d2.x - m), e1 = __builtin_amdgcn_exp2f(d2.y - m), e2 = __builtin_amdgcn_exp2f(d2.z - m), e3 = __builtin_amdgcn_exp2f(d2.w - m);
                const unsigned u0 = pk2(e0, e1), u1 = pk2(e2, e3);
                st16s(rs, ew, sb, (v4u){u0, u1, dppu<0x128>(u0), dppu<0x128>(u1)});
            } else BAR0();
            BAR0();
        }
        if (om == 14) { BAR0(); BAR0(); BAR0(); }
    } else {
        if (wave == 4) { *(v4u*)(lds + L_PART + lane * 16) = (v4u){0u, 0u, 0u, 0u}; *(v4u*)(lds + L_PART + 1024 + lane * 16) = (v4u){0u, 0u, 0u, 0u}; }
        BAR();
        int cal = get_cal(c, 0, budget);
        BAR0(); BAR0();
        unsigned tref = now() - 65536;
        STAMP(idx == 0 && wave == 7, 4, 1);
        const bool stg = q == 3 || wave != 7, two = q == 3 && wave == 5;
        const int pr = L_PART + (lane & 31) * 8, pubo = lane < 32 && (lane & 3) == 0 ? HO_PART + idx * 128 + lane * 4 : OOB;
#pragma unroll 1
        for (int s = 0; s < NSEG && budget >= 0; ++s) {
            if (s == CAL_STEP - 1 && wave == 6) *(int*)(lds + L_CAL) = get_cal(c, 1, budget);
            if (s == CAL_STEP) cal = __builtin_amdgcn_readfirstlane(*(const int*)(lds + L_CAL));
            if (s == 30 && wave == 6) touch_window(rs);
            const int sb = slot_base(s, cal);
            if (stg) {
                wait_until(tref + T_PM);
                if (two) stage2(rs, po, 6144 + lane * 16, sb + HO_MEM, budget); else stage1(rs, po, sb + HO_MEM, budget);
            } else BAR0();
            STAMP(idx == 0 && wave == 7, 0, s); STAMP(idx == 3 && wave == 6, 12, s);
            asm volatile(PUBLISH_ASM(256) :: [pub] "s"(wave), [la] "v"(pr), [vo] "v"(pubo), [rs] "s"(rs), [so] "s"(sb) : "memory", "scc", VCLOB);
            tref = now();
            if (wave == 7) {
                STAMP(idx == 0, 1, s);
                LSTAMP(idx == 0 && (s == 1 || s == 32 || s == 62), 14, s);
                if (lane == 0) st4s(rs, flagoff(FL_T + idx), 0, s);
            }
        }
    }
    if (budget < 0) give_up(rs);
    VMWAIT(0);
    BAR();
    if (wave == 7 && lane == 0) st4s(rs, flagoff(FL_T + idx), 0, NSEG);
}

DEV void role_o(const Params& p, const Ctx& c, int t) {
    const int lane = c.lane, wave = c.wave, rho = c.rho, g = c.g;
    const rsrc_t rs = c.rs; char* lds = c.lds;
    constexpr int L_MEM = 0, L_OUT = 8192, L_DUMP = 12288, L_V = 13312, L_CAL = 21504, L_X = 24576;
#pragma unroll
    for (int k = 0; k < 4; ++k) dma16(rs, wave * 4 + k < 28 ? OFF_XF + lane * 16 : OOB, (wave * 4 + k) * 1024, L_X + (wave * 4 + k) * 1024);
    v4u w[2][14];
#pragma unroll
    for (int k = 0; k < 2; ++k)
#pragma unroll
        for (int ks = 0; ks < 14; ++ks) w[k][ks] = ld16(rs, OFF_BT + ((2 * wave + k) * 28 + t) * 14336 + lane * 16, ks * 1024, 0);
    VMWAIT(28);
    BAR0();
    {
        v4u u[2];
#pragma unroll
        for (int k = 0; k < 2; ++k) {
            v4f a0 = {0, 0, 0, 0}, a1 = a0;
#pragma unroll
            for (int ks = 0; ks < 14; ++ks) { const int xo = L_X + (rho >> 3) * 14336 + ks * 1024 + g * 256;
                a0 = mfma32(lds16(lds, xo + ((pperm(rho, 0) & 15) ^ ((rho >> 3) * 4)) * 16), w[k][ks], a0); a1 = mfma32(lds16(lds, xo + ((pperm(rho, 1) & 15) ^ ((rho >> 3) * 4)) * 16), w[k][ks], a1); }
            u[k] = pk8(a0, a1);
        }
        asm volatile("v_mov_b32 v216, %0\n\tv_mov_b32 v217, %1\n\tv_mov_b32 v218, %2\n\tv_mov_b32 v219, %3\n\tv_mov_b32 v220, %4\n\tv_mov_b32 v221, %5\n\tv_mov_b32 v222, %6\n\tv_mov_b32 v223, %7"
                     :: "v"(u[0].x), "v"(u[0].y), "v"(u[0].z), "v"(u[0].w), "v"(u[1].x), "v"(u[1].y), "v"(u[1].z), "v"(u[1].w) : "v216", "v217", "v218", "v219", "v220", "v221", "v222", "v223");
    }
    int budget = SPIN_CAP;
    int cal = get_cal(c, 0, budget), caln = cal;
    const int sidx = (rho & 2) ? 0xEEEE : 0x4444;
    const int fa = L_MEM + g * 256 + ((rho >> 2) * 2 + (rho & 1)) * 32 + ((rho >> 1) & 1) * 16;
    const int hb = rho >> 3, hd = 2 * wave + hb, i = rho & 7;
    const int pa = HO_PART + hd * 512 + g * 128 + i * 16, ea = HO_E + hd * 512 + g * 128 + i * 16;
    const int ul = OFF_U + (t * 16 + 2 * wave) * 1024 + lane * 16;
    const int ufl = flagoff(FL_W + rho * 7 + ((14 + (t >> 1)) >> 2));
    const int mo = (lane & 3) == 0 ? HO_MEM + t * 256 + lane * 4 : OOB;
    const int p2 = (lane & 4) ? 2 * (lane & 3) + 1 : 2 * (lane & 3);
    const int ra = L_OUT + (lane >> 3) * 64 + p2 * 8;
    const int ow = hb == 0 ? L_OUT + wave * 512 + i * 64 + g * 16 : L_DUMP + lane * 16;
    const int po = wave * 1024 + lane * 16, po2 = min(wave + 4, 6) * 1024 + lane * 16;
    const v4u ones = {0x3f803f80u, 0x3f803f80u, 0x3f803f80u, 0x3f803f80u};
    const v2u ones2 = {0x3f803f80u, 0x3f803f80u};
    const unsigned mlo = hb ? 0u : 0xffffffffu, mhi = ~mlo;
    const unsigned long long hbm = 0xff00ff00ff00ff00ull;
    v4u psel; { const int kq = (rho >> 2) * 2 + (rho & 1); const unsigned one = (kq & 1) ? 0x3f800000u : 0x3f80u;
        psel = (v4u){(kq >> 1) == 0 ? one : 0u, (kq >> 1) == 1 ? one : 0u, (kq >> 1) == 2 ? one : 0u, (kq >> 1) == 3 ? one : 0u}; }
    asm volatile("v_mov_b32 v215, 0" ::: "v215");
    auto check_u = [&](int s) {
        unsigned long long lack; unsigned seen;
        asm volatile("v_cmp_gt_u32 %0, %1, v215" : "=s"(lack) : "s"(s) : "memory");
        if (lack != 0 && !wait_flags<1>(rs, ufl, s, seen)) budget = -1;
    };
    auto load_u = [&](int s) {
        asm volatile("s_nop 4\n\tbuffer_load_dwordx4 v[216:219], %0, %1, %2 offen sc1\n\tbuffer_load_dwordx4 v[220:223], %0, %1, %2 offen offset:1024 sc1"
                     :: "v"(ul), "s"(rs), "s"(s * (NH * 28672)) : "memory", "v216", "v217", "v218", "v219", "v220", "v221", "v222", "v223");
    };
    auto store_v = [&](int s) { st16s(rs, OFF_V + t * 4096 + (wave & 3) * 1024 + lane * 16, s * (NH * 7168), lds16(lds, L_V + (s & 1) * 4096 + (wave & 3) * 1024 + lane * 16)); };
    ACC_DECL;
    unsigned tref = now() - 65536;
    auto mem_times_b = [&](int sb, int s, bool last) -> v2u {
        v4f c0 = {0, 0, 0, 0}, c1 = {0, 0, 0, 0};
        if (wave < 4) stage2(rs, po, po2, sb + HO_MEM, budget);
        else BAR0();
        ACC_T(2);
#undef AFTER_STEP
#define AFTER_STEP(f) if (f == S_AT && !last) { SCHED(); ACC_T(4); if (T_OS) wait_until(tref + T_OS); ACC_T(5); asm volatile("buffer_load_dwordx4 v[224:227], %0, %2, %3 offen sc1\n\tbuffer_load_dwordx4 v[228:231], %1, %2, %3 offen sc1" \
                          :: "v"(pa), "v"(ea), "s"(rs), "s"(sb) : "memory", VCLOB); SCHED(); }
#define EXTRA_READS if (wave < 4 && !last && s > 0 && ABL(1)) load_u(s)
        MEM_TIMES_W(fa)
#undef EXTRA_READS
        *(v2u*)(lds + L_V + (s & 1) * 4096 + wave * 512 + lane * 8) = tp;
        return tp;
    };
    STAMP(t == 0 && wave == 0, 4, 13); STAMP(wave == 0, 28, t); STAMP(wave == 7, 28, 32 + t);
    STAMP(t == 0 && wave == 0, 4, 14);
#pragma unroll 1
    for (int s = 0; s < NSEG - 1 && budget >= 0; ++s) {
        const int sb = slot_base(s, s < CAL_STEP ? cal : caln), sn = slot_base(s + 1, s + 1 < CAL_STEP ? cal : caln);
        ACC_T(0);
        const v2u vp = mem_times_b(sb, s, false);
        ACC_T(3);
        asm volatile(
            "s_branch 3f\n\t"
            "1:\n\t"
            "buffer_load_dwordx4 v[224:227], %[pa], %[rs], %[sb] offen sc1\n\t"
            "buffer_load_dwordx4 v[228:231], %[ea], %[rs], %[sb] offen sc1\n\t"
            "3:\n\t"
            "s_add_i32 %[bud], %[bud], -1\n\ts_cmp_lt_i32 %[bud], 0\n\ts_cselect_b32 vcc_hi, -1, -2\n\tv_mov_b32 v253, vcc_hi\n\t"
            "s_waitcnt vmcnt(1)\n\t"
            "v_mfma_f32_16x16x32_bf16 v[232:235], %[psel], v[224:227], 0\n\t"
            "v_max3_u32 v250, v224, v225, v226\n\t"
            "s_waitcnt vmcnt(0)\n\t"
            "v_mfma_f32_16x16x32_bf16 v[244:247], %[ones], v[228:231], 0\n\t"
            "v_max3_u32 v251, v228, v229, v230\n\t"
            "v_mfma_f32_16x16x32_bf16 v[236:239], v[216:219], v[228:231], 0\n\t"
            "v_max3_u32 v250, v250, v227, v231\n\t"
            "v_mfma_f32_16x16x32_bf16 v[240:243], v[220:223], v[228:231], 0\n\t"
            "v_max_u32 v250, v250, v251\n\t"
            "v_cmp_gt_u32 vcc, v250, v253\n\t"
            "s_cbranch_vccnz 1b\n\t"
            "v_exp_f32 v250, v232\n\tv_exp_f32 v251, v233\n\t"
            "v_max_f32 v252, v232, v233\n\t"
            "v_cvt_pk_bf16_f32 v250, v250, v251\n\t"
            "v_cmp_lt_f32 vcc, 0x42800000, v252\n\t"
            "v_and_b32 v248, v250, %[mlo]\n\tv_and_b32 v249, v250, %[mhi]\n\t"
            "v_cndmask_b32 v236, v236, v240, %[hbm]\n\tv_cndmask_b32 v237, v237, v241, %[hbm]\n\t"
            "v_mfma_f32_16x16x16_bf16 v[244:247], %[ones2], v[248:249], v[244:247]\n\t"
            "v_cndmask_b32 v238, v238, v242, %[hbm]\n\tv_cndmask_b32 v239, v239, v243, %[hbm]\n\t"
            "s_nop 1\n\t"
            "v_mfma_f32_16x16x16_bf16 v[236:239], %[vp], v[248:249], v[236:239]\n\t"
            "s_cbranch_vccz 2f\n\t"
            "s_nop 7\n\t"
            "v_max_f32 v252, 0, v252\n\tv_mov_b32 v253, v252\n\ts_nop 1\n\tv_permlane16_swap_b32 v252, v253\n\ts_nop 1\n\t"
            "v_max_f32 v252, v252, v253\n\tv_mov_b32 v253, v252\n\ts_nop 1\n\tv_permlane32_swap_b32 v252, v253\n\ts_nop 1\n\t"
            "v_max_f32 v252, v252, v253\n\t"
            "v_mfma_f32_16x16x32_bf16 v[244:247], %[ones], v[228:231], 0\n\t"
            "v_mfma_f32_16x16x32_bf16 v[236:239], v[216:219], v[228:231], 0\n\t"
            "v_sub_f32 v250, v232, v252\n\tv_sub_f32 v251, v233, v252\n\t"
            "v_exp_f32 v250, v250\n\tv_exp_f32 v251, v251\n\tv_exp_f32 v253, -v252\n\ts_nop 0\n\t"
            "v_cvt_pk_bf16_f32 v250, v250, v251\n\t"
            "v_and_b32 v248, v250, %[mlo]\n\tv_and_b32 v249, v250, %[mhi]\n\ts_nop 7\n\t"
            "v_cndmask_b32 v236, v236, v240, %[hbm]\n\tv_cndmask_b32 v237, v237, v241, %[hbm]\n\tv_cndmask_b32 v238, v238, v242, %[hbm]\n\tv_cndmask_b32 v239, v239, v243, %[hbm]\n\t"
            "v_mul_f32 v236, v236, v253\n\tv_mul_f32 v237, v237, v253\n\tv_mul_f32 v238, v238, v253\n\tv_mul_f32 v239, v239, v253\n\t"
            "v_mul_f32 v244, v244, v253\n\tv_mul_f32 v245, v245, v253\n\tv_mul_f32 v246, v246, v253\n\tv_mul_f32 v247, v247, v253\n\ts_nop 1\n\t"
            "v_mfma_f32_16x16x16_bf16 v[244:247], %[ones2], v[248:249], v[244:247]\n\t"
            "v_mfma_f32_16x16x16_bf16 v[236:239], %[vp], v[248:249], v[236:239]\n\t"
            "s_nop 7\n\t"
            "2:\n\t"
            "s_nop 1\n\t"
            "v_rcp_f32 v252, v244\n\t"
            "s_nop 3\n\t"
            "v_mul_f32 v236, v236, v252\n\tv_mul_f32 v237, v237, v252\n\tv_mul_f32 v238, v238, v252\n\tv_mul_f32 v239, v239, v252\n\t"
            "v_add_f32_dpp v240, v236, v236 row_ror:8 row_mask:0xf bank_mask:0xf\n\tv_add_f32_dpp v241, v237, v237 row_ror:8 row_mask:0xf bank_mask:0xf\n\t"
            "v_add_f32_dpp v242, v238, v238 row_ror:8 row_mask:0xf bank_mask:0xf\n\tv_add_f32_dpp v243, v239, v239 row_ror:8 row_mask:0xf bank_mask:0xf\n\t"
            "ds_write_b128 %[ow], v[240:243]\n\t"
            PUBLISH_ASM(512)
            : [bud] "+s"(budget)
            : [pa] "v"(pa), [ea] "v"(ea), [rs] "s"(rs), [sb] "s"(sb), [psel] "v"(psel), [ones] "v"(ones), [ones2] "v"(ones2), [vp] "v"(vp),
              [mlo] "v"(mlo), [mhi] "v"(mhi), [hbm] "s"(hbm), [ow] "v"(ow),
              [pub] "s"(wave), [la] "v"(ra), [vo] "v"(mo), [so] "s"(sn)
            : "memory", "vcc", "scc", VCLOB);
        tref = now();
        STAMP(t == 0 && wave == 7, 3, s);
        if (wave == 7 && lane == 0) st4s(rs, flagoff(FL_V + t), 0, s);
        if (s + 2 == CAL_STEP) caln = __builtin_amdgcn_readfirstlane(*(const int*)(lds + L_CAL));
        if (wave == 5 && s + 3 == CAL_STEP) *(int*)(lds + L_CAL) = get_cal(c, 1, budget);
        if (wave == 5 && s == 29) touch_window(rs);
        if (wave == 4 || (s == 0 && wave > 4)) check_u(min(s + 2, NSEG - 2));
        ACC_T(6);
        if (wave < 4) wait_until(tref + T_OM);
        else {
            wait_until(tref + T_OU + (wave - 4) * T_ST);
            if (s + 1 < NSEG - 1 && ABL(1)) load_u(s + 1);
            if (wave == 4) asm volatile("buffer_load_dword v215, %0, %1, 0 offen sc1" :: "v"(ufl), "s"(rs) : "memory", "v215");
            if (ABL(2)) store_v(s);
        }
        ACC_T(7);
    }
    VMWAIT(0);
    ACC_OUT(t * 8 + wave);
    mem_times_b(slot_base(NSEG - 1, caln), NSEG - 1, true);
    if (budget < 0) give_up(rs);
    BAR();
    if (wave >= 4) store_v(NSEG - 1);
    VMWAIT(0);
    BAR0();
    if (wave == 7 && lane == 0) st4s(rs, flagoff(FL_V + t), 0, NSEG);
}

DEV void role_w(const Params& p, const Ctx& c, int wi) {
    const int lane = c.lane, wave = c.wave, rho = c.rho, g = c.g;
    const rsrc_t rs = c.rs; char* lds = c.lds;
    const int h = wi / 7, unit = (wi - h * 7) * 4 + (wave & 3), set = wave >> 2;
    const bool isG = unit < 14;
    auto stage = [&](int it) {
        const int sg = (2 * it + set + 1) & 63, lb = (set * 2 + (it & 1)) * 28672;
#pragma unroll
        for (int k = 0; k < 7; ++k) { const int pc = (wave & 3) * 7 + k; dma16(rs, OFF_XF + lane * 16, sg * 28672 + pc * 1024, lb + pc * 1024); }
    };
    v4u w[2][14];
    auto load_w = [&]() {
#pragma unroll
        for (int t = 0; t < 2; ++t) {
            const int row = isG ? unit * 32 + pperm(rho, t) : (unit - 14) * 32 + t * 16 + rho;
#pragma unroll
            for (int ks = 0; ks < 14; ++ks) w[t][ks] = ld16(rs, (isG ? OFF_AP : OFF_BT) + (h * 28 + (row >> 4)) * 14336 + g * 256 + (row & 15) * 16, ks * 1024, 0);
        }
    };
    int xa[2];
#pragma unroll
    for (int k = 0; k < 2; ++k) { const int key = isG ? k * 16 + rho : pperm(rho, k), kt = key >> 4; xa[k] = kt * 14336 + (g * 16 + ((key & 15) ^ (kt * 4))) * 16; }
    const int ob = (isG ? OFF_G + (h * 14 + unit) * 2048 : OFF_U + ((unit - 14) * 32 + h) * 1024) + lane * 16;
    const int ostep = isG ? 1024 : 16384;
    auto segment = [&](int buf, int sg) {
        const char* xb = lds + buf * 28672;
        v4f a00 = {0, 0, 0, 0}, a01 = a00, a10 = a00, a11 = a00;
        if (isG) {
#pragma unroll
            for (int ks = 0; ks < 14; ++ks) { const v4u x0 = lds16(xb, xa[0] + ks * 1024), x1 = lds16(xb, xa[1] + ks * 1024);
                a00 = mfma32(w[0][ks], x0, a00); a01 = mfma32(w[1][ks], x0, a01); a10 = mfma32(w[0][ks], x1, a10); a11 = mfma32(w[1][ks], x1, a11); }
        } else {
#pragma unroll
            for (int ks = 0; ks < 14; ++ks) { const v4u x0 = lds16(xb, xa[0] + ks * 1024), x1 = lds16(xb, xa[1] + ks * 1024);
                a00 = mfma32(x0, w[0][ks], a00); a01 = mfma32(x1, w[0][ks], a01); a10 = mfma32(x0, w[1][ks], a10); a11 = mfma32(x1, w[1][ks], a11); }
        }
        st16s(rs, ob, sg * (NH * 28672), pk8(a00, a01)); st16s(rs, ob + ostep, sg * (NH * 28672), pk8(a10, a11));
    };
    if (set == 0) load_w();
    if (wave == 7) *(int*)(lds + LDS_MAIN - 32) = 0;
    stage(0);
    VMWAIT(0);
    BAR();
    STAMP(wi == 20 && wave == 1, 4, 2);
#pragma unroll 1
    for (int it = -1; it < 32; ++it) {
        if (it == -1 && set == 1) load_w();
        if (it >= 0 && it + 1 < 32) stage(it + 1);
        if (it > 0 || set == 0) segment(it > 0 ? set * 2 + (it & 1) : 2 * (it + 1), ((it > 0 ? 2 * it + set : it + 1) + 1) & 63);
        if (it < 0) continue;
        if (it == 0 && set == 0) {
            VMWAIT(0);
            if (lane == 0 && __hip_atomic_fetch_add((int*)(lds + LDS_MAIN - 32), 1, __ATOMIC_RELAXED, __HIP_MEMORY_SCOPE_WORKGROUP) == 3) st4s(rs, flagoff(FL_W + wi), 0, 2);
            STAMP(wi == 20 && wave == 1, 4, 15);
        }
        const bool eager = it < 4 || it == 31;
        if (eager) VMWAIT(0); else VMWAIT(2);
        BAR();
        if (wave == 0 && lane == 0) st4s(rs, flagoff(FL_W + wi), 0, eager ? 2 * it + 2 : 2 * it);
        if (it == 12 && wave == 0 && wi >= 48) {
            unsigned seen; wait_flags<8>(rs, flagoff(FL_TAB), 28, seen);
            __builtin_amdgcn_fence(__ATOMIC_ACQUIRE, "agent");
            VMWAIT(0);
        }
        STAMP(wi == 20 && wave == 1, 5, it); CSTAMP(wi == 20 && wave == 1, 6, it);
    }
}

DEV int f_first(int set, int wave) { return set ? 33 + 2 * wave : wave; }
DEV int f_next(int s, int wave) { return s >= 32 ? s + 16 : s >= 24 ? 32 + 2 * wave : s + 8; }
DEV void role_f1(const Params& p, const Ctx& c, int h, int set) {
    const int lane = c.lane, wave = c.wave, rho = c.rho, g = c.g;
    const rsrc_t rs = c.rs;
    const int gfl = flagoff(FL_W + h * 7 + (lane & 3)), tfl = flagoff(FL_T + h * 4 + (lane & 3));
    const int gl0 = OFF_G + h * 28672 + (rho >> 3) * 1024 + g * 256 + (pperm(rho, 0) & 15) * 16;
    const int tl = OFF_T + h * 7168 + g * 128 + ((rho >> 2) * 2 + (rho & 1)) * 16;
    const float ninf = -__builtin_inff();
    bool wdone = false;
#pragma unroll 1
    for (int s = f_first(set, wave); s < NSEG; s = f_next(s, wave)) {
        STAMP(h == 0, 16, s);
        unsigned seen;
        if (!wdone) { if (!wait_flags<8>(rs, gfl, s ? s : NSEG, seen)) return; wdone = __builtin_amdgcn_ballot_w64(seen < NSEG) == 0; }
        v4u x[2][14];
        v4f a[2][2] = {{{0, 0, 0, 0}, {0, 0, 0, 0}}, {{0, 0, 0, 0}, {0, 0, 0, 0}}};
#pragma unroll
        for (int hf = 0; hf < 2; ++hf) {
            v4u y[2][7];
#pragma unroll
            for (int k = 0; k < 7; ++k) {
#pragma unroll
                for (int qt = 0; qt < 2; ++qt) x[qt][hf * 7 + k] = ld16(rs, OFF_XF + qt * 14336 + g * 256 + (rho ^ (qt * 4)) * 16, s * 28672 + (hf * 7 + k) * 1024, 0);
                y[0][k] = ld16(rs, gl0, s * (NH * 28672) + (hf * 7 + k) * 2048, 1); y[1][k] = ld16(rs, gl0 + 64, s * (NH * 28672) + (hf * 7 + k) * 2048, 1);
            }
            SCHED();
#pragma unroll
            for (int k = 0; k < 7; ++k)
#pragma unroll
                for (int qt = 0; qt < 2; ++qt) { a[qt][0] = mfma32(y[0][k], x[qt][hf * 7 + k], a[qt][0]); a[qt][1] = mfma32(y[1][k], x[qt][hf * 7 + k], a[qt][1]); }
        }
        float v[2][8], m[2];
#pragma unroll
        for (int qt = 0; qt < 2; ++qt) {
            const float r[8] = {a[qt][0].x, a[qt][0].y, a[qt][0].z, a[qt][0].w, a[qt][1].x, a[qt][1].y, a[qt][1].z, a[qt][1].w};
            m[qt] = ninf;
#pragma unroll
            for (int k = 0; k < 8; ++k) { v[qt][k] = (8 * g + k <= qt * 16 + rho) ? r[k] : ninf; m[qt] = fmx(m[qt], v[qt][k]); }
        }
        asm volatile("" : "+v"(m[0]), "+v"(m[1]));
        STAMP(h == 0, 17, s);
        if (!wait_flags<2>(rs, tfl, s + 1, seen)) return;
        STAMP(h == 0, 18, s);
        v4f am[2] = {{0, 0, 0, 0}, {0, 0, 0, 0}};
        { v4u y[14];
#pragma unroll
          for (int ks = 0; ks < 14; ++ks) y[ks] = ld16(rs, tl, s * (NH * 7168) + ks * 512, 1);
          SCHED();
#pragma unroll
          for (int ks = 0; ks < 14; ++ks) { am[0] = mfma32(y[ks], x[0][ks], am[0]); am[1] = mfma32(y[ks], x[1][ks], am[1]); } }
        unsigned pm[2];
#pragma unroll
        for (int qt = 0; qt < 2; ++qt) {
            const float mm = max_g(fmx(m[qt], fmx(am[qt].x, am[qt].y)));
            const float e0 = __builtin_amdgcn_exp2f(am[qt].x - mm), e1 = __builtin_amdgcn_exp2f(am[qt].y - mm);
            float z = e0 + e1;
#pragma unroll
            for (int k = 0; k < 8; ++k) { v[qt][k] = __builtin_amdgcn_exp2f(v[qt][k] - mm); z += v[qt][k]; }
            const float rz = __builtin_amdgcn_rcpf(sum_g(z));
            st16s(rs, OFF_PX + (h * 2 + qt) * 1024 + lane * 16, s * (NH * 2048),
                  (v4u){pk2(v[qt][0] * rz, v[qt][1] * rz), pk2(v[qt][2] * rz, v[qt][3] * rz), pk2(v[qt][4] * rz, v[qt][5] * rz), pk2(v[qt][6] * rz, v[qt][7] * rz)});
            pm[qt] = pk2(e0 * rz, e1 * rz);
        }
        st16s(rs, (lane & 1) == 0 ? OFF_PM + h * 512 + lane * 8 : OOB, s * (NH * 512), (v4u){pm[0], pm[1], dppu<0xF5>(pm[0]), dppu<0xF5>(pm[1])});
        VMWAIT(0);
        if (lane == 0) st4s(rs, flagoff((set ? FL_P2 : FL_P) + h * 8 + wave), 0, s + 1);
        STAMP(h == 0, 19, s);
    }
}

DEV void make_tables(const Params& p, const Ctx& c, int t) {
    const int lane = c.lane, wave = c.wave, rho = c.rho, g = c.g;
    const rsrc_t rs = c.rs;
    for (int ch = t * 512 + wave * 64 + lane; ch < 2 * 28672; ch += 28 * 512) {
        float v[8];
        if (ch < 28672) { const int fr = ch >> 6, tl = fr / 14, d0 = (fr - tl * 14) * 32 + g * 8, ri = tl >> 4, k = (tl & 15) * 16 + rho;
#pragma unroll
            for (int j = 0; j < 8; ++j) { const float ph = (float)((k * (d0 + j)) % 448) * (1.f / 448.f); v[j] = k > 224 ? 0.f : ri ? -__builtin_amdgcn_sinf(ph) : __builtin_amdgcn_cosf(ph); }
        } else { const int fr = (ch - 28672) >> 6, d = (fr >> 4) * 16 + rho, ri = (fr >> 3) & 1, k0 = (fr & 7) * 32 + g * 8;
#pragma unroll
            for (int j = 0; j < 8; ++j) { const int k = k0 + j; const float ph = (float)((k * d) % 448) * (1.f / 448.f); const bool edge = k == 0 || k == 224;
                const float wgt = (edge ? 1.f : 2.f) / (448.f * 2048.f); v[j] = k > 224 ? 0.f : ri ? (edge ? 0.f : -wgt * __builtin_amdgcn_sinf(ph)) : wgt * __builtin_amdgcn_cosf(ph); }
        }
        st16s(rs, OFF_FD + ch * 16, 0, (v4u){pk2(v[0], v[1]), pk2(v[2], v[3]), pk2(v[4], v[5]), pk2(v[6], v[7])});
    }
    VMWAIT(0);
    BAR();
    if (wave == 0 && lane == 0) __hip_atomic_fetch_add((unsigned*)(p.ws + flagoff(FL_TAB)), 1u, __ATOMIC_RELAXED, __HIP_MEMORY_SCOPE_AGENT);
}
DEV void role_f2(const Params& p, const Ctx& c, int t, int set) {
    const int lane = c.lane, wave = c.wave, rho = c.rho, g = c.g;
    const rsrc_t rs = c.rs;
    const int ufl = flagoff(FL_W + rho * 7 + ((14 + (t >> 1)) >> 2)), pfl = flagoff(lane < 16 ? (set ? FL_P2 : FL_P) + lane * 8 + wave : FL_V + t);
    const int ul = OFF_U + t * 16384 + lane * 16;
    bool wdone = false;
#pragma unroll 1
    for (int s = f_first(set, wave); s < NSEG; s = f_next(s, wave)) {
        unsigned seen;
        if (!wdone) { if (!wait_flags<8>(rs, ufl, s ? s : NSEG, seen)) return; wdone = __builtin_amdgcn_ballot_w64(seen < NSEG) == 0; }
        STAMP(t == 0, 20, s);
        v4u u[16];
#pragma unroll
        for (int h = 0; h < 16; ++h) u[h] = ld16(rs, ul, s * (NH * 28672) + h * 1024, 1);
        if (!wait_flags<2>(rs, pfl, s + 1, seen)) return;
        STAMP(t == 0, 21, s);
        v4f a[2] = {{0, 0, 0, 0}, {0, 0, 0, 0}}, m[2] = {{0, 0, 0, 0}, {0, 0, 0, 0}};
        v4u px[16]; v2u pm[16], vv[8];
#pragma unroll
        for (int h = 0; h < 16; ++h) px[h] = ld16(rs, OFF_PX + lane * 16, s * (NH * 2048) + h * 2048, 1);
#pragma unroll
        for (int h = 0; h < 16; ++h) { pm[h] = ld8s(rs, OFF_PM + lane * 8, s * (NH * 512) + h * 512); if (h & 1) vv[h >> 1] = ld8s(rs, OFF_V + t * 4096 + lane * 8, s * (NH * 7168) + (h >> 1) * 512); }
        SCHED();
#pragma unroll
        for (int h = 0; h < 16; ++h) { a[0] = mfma32(u[h], px[h], a[0]); SCHED(); px[h] = ld16(rs, OFF_PX + 1024 + lane * 16, s * (NH * 2048) + h * 2048, 1); SCHED(); }
#pragma unroll
        for (int k = 0; k < 8; ++k) { m[0] = mfma16(vv[k], (v2u){pm[2 * k].x, pm[2 * k + 1].x}, m[0]); m[1] = mfma16(vv[k], (v2u){pm[2 * k].y, pm[2 * k + 1].y}, m[1]); }
        SCHED();
#pragma unroll
        for (int h = 0; h < 16; ++h) a[1] = mfma32(u[h], px[h], a[1]);
#pragma unroll
        for (int qt = 0; qt < 2; ++qt) { const v4f o = a[qt] + m[qt];
            *(v2u*)(p.out + (size_t)(s * 32 + qt * 16 + rho) * 896 + DM + t * 16 + g * 4) = (v2u){pk2(o.x, o.y), pk2(o.z, o.w)}; }
        STAMP(t == 0, 22, s);
    }
}

DEV bool fft_sync(const Params& p, const Ctx& c, int fl) {
    VMWAIT(0);
    BAR();
    int* okw = (int*)(c.lds + LDS_MAIN - 16);
    if (c.wave == 0) {
        if (c.lane == 0) __hip_atomic_fetch_add((unsigned*)(p.ws + flagoff(fl)), 1u, __ATOMIC_RELAXED, __HIP_MEMORY_SCOPE_AGENT);
        unsigned seen; const bool ok = wait_flags<2>(c.rs, flagoff(fl), 64, seen);
        if (c.lane == 0) *okw = ok;
    }
    BAR();
    return *okw != 0;
}
DEV v4u neg8(v4u v) { return v ^ 0x80008000u; }
DEV void twid(int a, const int (&n)[8], int mask, float inv, v4u& C, v4u& S) {
    float cs[8], sn[8];
#pragma unroll
    for (int j = 0; j < 8; ++j) { const float ph = (float)((a * n[j]) & mask) * inv; cs[j] = __builtin_amdgcn_cosf(ph); sn[j] = __builtin_amdgcn_sinf(ph); }
    C = (v4u){pk2(cs[0], cs[1]), pk2(cs[2], cs[3]), pk2(cs[4], cs[5]), pk2(cs[6], cs[7])};
    S = (v4u){pk2(sn[0], sn[1]), pk2(sn[2], sn[3]), pk2(sn[4], sn[5]), pk2(sn[6], sn[7])};
}
DEV void role_fft(const Params& p, const Ctx& c, int job) {
    const int lane = c.lane, wave = c.wave, rho = c.rho, g = c.g;
    const rsrc_t rs = c.rs; char* lds = c.lds;
    const v4f z4 = {0, 0, 0, 0};

    {
        const rsrc_t rx = mkrsrc(p.xa, 2048 * DM * 2);
        for (int pc = wave; pc < 28; pc += 8) { const int nt = pc >= 14, ks = pc - nt * 14; dma16(rx, ((64 * (nt * 16 + rho) + job) * DM + ks * 32 + g * 8) * 2, 0, pc * 1024); }

        v4u C[2], S[2];
        { int n[8];
#pragma unroll
          for (int j = 0; j < 8; ++j) n[j] = 64 * (16 * (j >> 2) + 4 * g + (j & 3)) + job;
          twid(rho, n, 2047, 1.f / 2048.f, C[0], S[0]); twid(16 + rho, n, 2047, 1.f / 2048.f, C[1], S[1]); }
        VMWAIT(0);
        BAR();
        v4f zr[2][2], zi[2][2];
#pragma unroll
        for (int t = 0; t < 2; ++t) {
            const int fo = OFF_FD + (wave * 2 + (rho >> 3)) * 14336 + g * 256 + (pperm(rho, t) & 15) * 16;
            v4f r0 = z4, r1 = z4, i0 = z4, i1 = z4;
#pragma unroll
            for (int hf = 0; hf < 2; ++hf) {
                v4u fr[7], fi[7];
#pragma unroll
                for (int k = 0; k < 7; ++k) { fr[k] = ld16(rs, fo, (hf * 7 + k) * 1024, 0); fi[k] = ld16(rs, fo, 256 * DM * 2 + (hf * 7 + k) * 1024, 0); }
                SCHED();
#pragma unroll
                for (int k = 0; k < 7; ++k) { const v4u x0 = lds16(lds, (hf * 7 + k) * 1024 + lane * 16), x1 = lds16(lds, (14 + hf * 7 + k) * 1024 + lane * 16);
                    r0 = mfma32(x0, fr[k], r0); r1 = mfma32(x1, fr[k], r1); i0 = mfma32(x0, fi[k], i0); i1 = mfma32(x1, fi[k], i1); }
            }
            const v4u yr = pk8(r0, r1), yi = pk8(i0, i1), nr = neg8(yr);
#pragma unroll
            for (int ct = 0; ct < 2; ++ct) { zr[ct][t] = mfma32(yi, S[ct], mfma32(yr, C[ct], z4)); zi[ct][t] = mfma32(nr, S[ct], mfma32(yi, C[ct], z4)); }
        }
        char* O = lds + 32768;
#pragma unroll
        for (int ct = 0; ct < 2; ++ct) { const int o = (ct * 16 + rho) * 1040 + wave * 64 + g * 16;
            *(v4u*)(O + o) = pk8(zr[ct][0], zr[ct][1]); *(v4u*)(O + o + 512) = pk8(zi[ct][0], zi[ct][1]); }
        BAR();
#pragma unroll
        for (int k = 0; k < 4; ++k) st16s(rs, OFF_Z1 + job * 1024 + lane * 16, (wave * 4 + k) * 65536, lds16(O, (wave * 4 + k) * 1040 + lane * 16));
    }
    STAMP(job == 0 && wave == 1, 4, 3);
    if (!fft_sync(p, c, FL_FA)) return;
    STAMP(job == 0 && wave == 1, 4, 4);
    if (wave < 4) {
        const int k1 = job >> 1, P = (job & 1) * 4 + wave;
        char* L = lds + wave * 8192;
#pragma unroll
        for (int pc = 0; pc < 8; ++pc) dma16s(rs, OFF_Z1 + (k1 * 64 + (pc & 3) * 16 + (lane >> 2)) * 1024 + (pc >> 2) * 512 + P * 64 + (lane & 3) * 16, 0, wave * 8192 + pc * 1024);
        v4u C[4][2], S[4][2];
#pragma unroll
        for (int ks = 0; ks < 2; ++ks) { int n[8];
#pragma unroll
            for (int j = 0; j < 8; ++j) n[j] = 32 * ks + 16 * (j >> 2) + 4 * g + (j & 3);
#pragma unroll
            for (int kt = 0; kt < 4; ++kt) twid(16 * kt + rho, n, 63, 1.f / 64.f, C[kt][ks], S[kt][ks]); }
        VMWAIT(0);
        v2u hr[4], hi[4];
#pragma unroll
        for (int t = 0; t < 2; ++t) {
            const int k = P * 32 + pperm(rho, t);
            v4u xr[2], xi[2];
#pragma unroll
            for (int ks = 0; ks < 2; ++ks) { const int a = (32 * ks + 4 * g + (rho >> 2)) * 64 + (rho & 3) * 16 + t * 8;
                const v2u a0 = ldstr(L, a), a1 = ldstr(L, a + 1024), b0 = ldstr(L, a + 4096), b1 = ldstr(L, a + 5120);
                xr[ks] = (v4u){a0.x, a0.y, a1.x, a1.y}; xi[ks] = (v4u){b0.x, b0.y, b1.x, b1.y}; }
            const v4u n0 = neg8(xr[0]), n1 = neg8(xr[1]);
            unsigned short fr[16], fi[16];
#pragma unroll
            for (int j = 0; j < 16; ++j) { const int o = (k1 + 32 * (16 * (j >> 2) + 4 * g + (j & 3))) * 225 + min(k, 224); fr[j] = p.wr[o]; fi[j] = p.wi[o]; }
            SCHED();
            v4f vr[4], vi[4];
#pragma unroll
            for (int kt = 0; kt < 4; ++kt) {
                const v4f ar = mfma32(S[kt][1], xi[1], mfma32(S[kt][0], xi[0], mfma32(C[kt][1], xr[1], mfma32(C[kt][0], xr[0], z4))));
                const v4f ai = mfma32(S[kt][1], n1, mfma32(S[kt][0], n0, mfma32(C[kt][1], xi[1], mfma32(C[kt][0], xi[0], z4))));
                float wr[4], wi[4];
#pragma unroll
                for (int r = 0; r < 4; ++r) { wr[r] = __uint_as_float((unsigned)fr[kt * 4 + r] << 16); wi[r] = __uint_as_float((unsigned)fi[kt * 4 + r] << 16); }
                vr[kt] = (v4f){ar.x * wr[0] - ai.x * wi[0], ar.y * wr[1] - ai.y * wi[1], ar.z * wr[2] - ai.z * wi[2], ar.w * wr[3] - ai.w * wi[3]};
                vi[kt] = (v4f){ar.x * wi[0] + ai.x * wr[0], ar.y * wi[1] + ai.y * wr[1], ar.z * wi[2] + ai.z * wr[2], ar.w * wi[3] + ai.w * wr[3]};
            }
            const v4u pr0 = pk8(vr[0], vr[1]), pr1 = pk8(vr[2], vr[3]), pi0 = pk8(vi[0], vi[1]), pi1 = pk8(vi[2], vi[3]), ni0 = neg8(pi0), ni1 = neg8(pi1);
#pragma unroll
            for (int nt = 0; nt < 4; ++nt) {
                const v4f rr = mfma32(ni1, S[nt][1], mfma32(ni0, S[nt][0], mfma32(pr1, C[nt][1], mfma32(pr0, C[nt][0], z4))));
                const v4f ri = mfma32(pr1, S[nt][1], mfma32(pr0, S[nt][0], mfma32(pi1, C[nt][1], mfma32(pi0, C[nt][0], z4))));
                const v2u qr = {pk2(rr.x, rr.y), pk2(rr.z, rr.w)}, qi = {pk2(ri.x, ri.y), pk2(ri.z, ri.w)};
                if (t == 0) { hr[nt] = qr; hi[nt] = qi; }
                else { char* o = lds + 32768 + (nt * 16 + rho) * 528 + wave * 64 + g * 16;
                    *(v4u*)o = (v4u){hr[nt].x, hr[nt].y, qr.x, qr.y}; *(v4u*)(o + 256) = (v4u){hi[nt].x, hi[nt].y, qi.x, qi.y}; }
            }
        }
    }
    BAR();
#pragma unroll
    for (int k = 0; k < 4; ++k) { const int n2 = wave * 8 + k * 2 + (lane >> 5);
        st16s(rs, OFF_Z2 + (n2 * 32 + (job >> 1)) * 1024 + (g & 1) * 512 + (job & 1) * 256 + rho * 16, 0, lds16(lds, 32768 + n2 * 528 + (lane & 31) * 16)); }
    STAMP(job == 0 && wave == 1, 4, 5);
    if (!fft_sync(p, c, FL_FB)) return;
    STAMP(job == 0 && wave == 1, 4, 6);
    {
        char* L = lds + wave * 4096;
#pragma unroll
        for (int pc = 0; pc < 4; ++pc) dma16s(rs, OFF_Z2 + (job * 32 + (pc & 1) * 16 + (lane >> 2)) * 1024 + (pc >> 1) * 512 + wave * 64 + (lane & 3) * 16, 0, wave * 4096 + pc * 1024);
        v4u C[2], S[2];
        { int n[8];
#pragma unroll
          for (int j = 0; j < 8; ++j) n[j] = 8 * g + j;
          twid(64 * rho + job, n, 2047, 1.f / 2048.f, C[0], S[0]); twid(64 * (16 + rho) + job, n, 2047, 1.f / 2048.f, C[1], S[1]); }
        VMWAIT(0);
        v4f rr[2][2], ri[2][2];
#pragma unroll
        for (int t = 0; t < 2; ++t) {
            const int a = (8 * g + (rho >> 2)) * 64 + (rho & 3) * 16 + t * 8;
            const v2u a0 = ldstr(L, a), a1 = ldstr(L, a + 256), b0 = ldstr(L, a + 2048), b1 = ldstr(L, a + 2304);
            const v4u xr = {a0.x, a0.y, a1.x, a1.y}, xi = {b0.x, b0.y, b1.x, b1.y}, ni = neg8(xi);
#pragma unroll
            for (int nt = 0; nt < 2; ++nt) { rr[nt][t] = mfma32(ni, S[nt], mfma32(xr, C[nt], z4)); ri[nt][t] = mfma32(xr, S[nt], mfma32(xi, C[nt], z4)); }
        }
        char* R = lds + 32768;
#pragma unroll
        for (int nt = 0; nt < 2; ++nt) { *(v4u*)(R + (nt * 16 + wave) * 1024 + lane * 16) = pk8(rr[nt][0], rr[nt][1]); *(v4u*)(R + (nt * 16 + 8 + wave) * 1024 + lane * 16) = pk8(ri[nt][0], ri[nt][1]); }
        BAR();
#pragma unroll 1
        for (int Q = wave; Q < 14; Q += 8) {
            v4f y[2][2] = {{z4, z4}, {z4, z4}};
#pragma unroll
            for (int hf = 0; hf < 2; ++hf) {
                v4u f0[8], f1[8];
#pragma unroll
                for (int k = 0; k < 8; ++k) { const int fo = OFF_FI + (Q * 2 + (rho >> 3)) * 16384 + g * 256 + (pperm(rho, 0) & 15) * 16; f0[k] = ld16(rs, fo, (hf * 8 + k) * 1024, 0); f1[k] = ld16(rs, fo + 64, (hf * 8 + k) * 1024, 0); }
                SCHED();
#pragma unroll
                for (int k = 0; k < 8; ++k) { const v4u b0 = lds16(R, (hf * 8 + k) * 1024 + lane * 16), b1 = lds16(R, (16 + hf * 8 + k) * 1024 + lane * 16);
                    y[0][0] = mfma32(f0[k], b0, y[0][0]); y[0][1] = mfma32(f1[k], b0, y[0][1]); y[1][0] = mfma32(f0[k], b1, y[1][0]); y[1][1] = mfma32(f1[k], b1, y[1][1]); }
            }
#pragma unroll
            for (int nt = 0; nt < 2; ++nt) *(v4u*)(p.out + (size_t)(64 * (nt * 16 + rho) + job) * 896 + Q * 32 + g * 8) = pk8(y[nt][0], y[nt][1]);
        }
    }
}

extern "C" __global__ void __launch_bounds__(512, 2) k_main(Params p) {
    extern __shared__ __attribute__((aligned(16))) char lds[];
    Ctx c; c.rs = mkrsrc(p.ws, WS_BYTES); c.lds = lds; c.lane = threadIdx.x & 63; c.wave = __builtin_amdgcn_readfirstlane(threadIdx.x >> 6); c.rho = c.lane & 15; c.g = c.lane >> 4;
    { const int lane = c.lane; (void)lane; STAMP(blockIdx.x == 0 && c.wave == 7, 4, 0); }
    const int rot = *(const int*)(p.ws + flagoff(FL_ROT));
    const int x = (blockIdx.x + rot) & 7, y = blockIdx.x >> 3;
    if (x < 2) role_p(p, c, x * 32 + y);
    else if (x == 2) { if (y < 28) role_o(p, c, y); }
    else if (x == 3 && y == 28) role_cal(p, c);
    else {
        const int wi = (x - 4) * 32 + y - 16;
        if (x == 3) { if (y < 28) make_tables(p, c, y); } else if (wi >= 0) role_w(p, c, wi);
        if (wi >= 48) role_fft(p, c, wi - 48);
        else if (x == 3 ? y < 28 : wi >= 16 && wi < 44) role_f2(p, c, x == 3 ? y : wi - 16, x != 3);
        else if (x == 4) role_f1(p, c, y & 15, wi >= 0);
    }
    { const int lane = c.lane; (void)lane; LSTAMP(c.wave == 7, 8 + (blockIdx.x >> 6), blockIdx.x & 63); }
}

extern "C" void kernel_launch(void* const* d_in, const int* in_sizes, int n_in, void* d_out, int out_size, void* d_ws, size_t ws_size, hipStream_t stream) {
    Params p;
    p.xa = (const unsigned short*)d_in[0]; p.wr = (const unsigned short*)d_in[1]; p.wi = (const unsigned short*)d_in[2];
    p.xb = (const unsigned short*)d_in[3]; p.mem0 = (const unsigned short*)d_in[4];
    p.wq = (const unsigned short*)d_in[5]; p.wk = (const unsigned short*)d_in[6]; p.wv = (const unsigned short*)d_in[7]; p.wo = (const unsigned short*)d_in[8];
    p.out = (unsigned short*)d_out;
    p.ws = (char*)d_ws + (((size_t)2 << 20) - ((uintptr_t)d_ws & (((size_t)2 << 20) - 1))) % ((size_t)2 << 20);
    (void)hipFuncSetAttribute((const void*)k_main, hipFuncAttributeMaxDynamicSharedMemorySize, LDS_MAIN);
    (void)hipFuncSetAttribute((const void*)k_pro, hipFuncAttributeMaxDynamicSharedMemorySize, LDS_PRO);
    hipLaunchKernelGGL(k_pro, dim3(256), dim3(512), LDS_PRO, stream, p);
    hipLaunchKernelGGL(k_main, dim3(256), dim3(512), LDS_MAIN, stream, p);
}
```

```cpp
#define LITE
#include <hip/hip_runtime.h>
#include <hip/hip_bf16.h>
#include <stdint.h>

typedef unsigned v4u __attribute__((ext_vector_type(4)));
typedef unsigned v2u __attribute__((ext_vector_type(2)));
typedef float v4f __attribute__((ext_vector_type(4)));
typedef float v2f __attribute__((ext_vector_type(2)));
typedef short v4s __attribute__((ext_vector_type(4)));
typedef __bf16 v2bf __attribute__((ext_vector_type(2)));
typedef __bf16 v8bf __attribute__((ext_vector_type(8)));
typedef __bf16 v16bf __attribute__((ext_vector_type(16)));
typedef unsigned v8u __attribute__((ext_vector_type(8)));
typedef __amdgpu_buffer_rsrc_t rsrc_t;
#define LAS __attribute__((address_space(3)))
#define DEV __device__ __forceinline__

constexpr int NSEG = 64, NH = 16, DM = 448;
constexpr unsigned SENT = 0xFFFFFFFFu;
constexpr int OOB = 0x7fffffff;
constexpr int SC1 = 16;
constexpr int OFF_HO = 0;
constexpr int OFF_CTL = 4 << 20;
constexpr int OFF_AP = OFF_CTL + 131072;
constexpr int OFF_BT = OFF_AP + NH * DM * DM * 2;
constexpr int OFF_G = OFF_BT + NH * DM * DM * 2;
constexpr int OFF_U = OFF_G + NSEG * NH * 28672;
constexpr int OFF_T = OFF_U + NSEG * NH * 28672;
constexpr int OFF_V = OFF_T + NSEG * NH * 7168;
constexpr int OFF_PX = OFF_V + NSEG * NH * 7168;
constexpr int OFF_PM = OFF_PX + NSEG * NH * 2048;
constexpr int OFF_FD = OFF_PM + NSEG * NH * 512;
constexpr int OFF_FI = OFF_FD + 512 * DM * 2;
constexpr int OFF_Z1 = OFF_FI + 512 * DM * 2;
constexpr int OFF_Z2 = OFF_Z1 + (2 << 20);
constexpr int OFF_XF = OFF_Z2 + (2 << 20);
constexpr int OFF_G0 = OFF_XF + (2 << 20);
constexpr int OFF_DBG = OFF_G0 + (1 << 20);
constexpr int WS_BYTES = OFF_DBG + (1 << 20);
constexpr int HO_MEM = 512, HO_PART = 16384, HO_E = 32768;
constexpr int OFF_PROBE = OFF_HO + 49152;
constexpr int FL_W = 0, FL_T = 112, FL_V = 176, FL_P = 208, FL_ROT = 336, FL_TAB = 337, FL_FA = 338, FL_FB = 339, FL_ABORT = 340, FL_CAL = 341, FL_P2 = 344;
constexpr int LDS_MAIN = 114688 + 2048;
constexpr int LDS_PRO = 143360;
constexpr int SPIN_CAP = 1 << 17;

struct Params {
    const unsigned short* xa; const unsigned short* wr; const unsigned short* wi;
    const unsigned short* xb; const unsigned short* mem0;
    const unsigned short* wq; const unsigned short* wk; const unsigned short* wv; const unsigned short* wo;
    unsigned short* out; char* ws;
};

DEV unsigned pk2(float a, float b) { v2bf r = {(__bf16)a, (__bf16)b}; return __builtin_bit_cast(unsigned, r); }
DEV v4u pk8(v4f a, v4f b) { return (v4u){pk2(a.x, a.y), pk2(a.z, a.w), pk2(b.x, b.y), pk2(b.z, b.w)}; }
DEV v4f mfma32(v4u a, v4u b, v4f c) { return __builtin_amdgcn_mfma_f32_16x16x32_bf16(__builtin_bit_cast(v8bf, a), __builtin_bit_cast(v8bf, b), c, 0, 0, 0); }
DEV v4f mfma16(v2u a, v2u b, v4f c) { return __builtin_amdgcn_mfma_f32_16x16x16bf16_1k(__builtin_bit_cast(v4s, a), __builtin_bit_cast(v4s, b), c, 0, 0, 0); }
DEV v4f smf(v4u a, v4u b0, v4u b1, v4f c, int idx) {
    v8u b = {b0.x, b0.y, b0.z, b0.w, b1.x, b1.y, b1.z, b1.w};
    return __builtin_amdgcn_smfmac_f32_16x16x64_bf16(__builtin_bit_cast(v8bf, a), __builtin_bit_cast(v16bf, b), c, idx, 0, 0);
}
DEV float fmx(float a, float b) { return __builtin_elementwise_maximum(a, b); }
DEV float max_g(float s) {
    auto r = __builtin_amdgcn_permlane16_swap(__float_as_uint(s), __float_as_uint(s), false, false);
    s = fmx(__uint_as_float(r[0]), __uint_as_float(r[1]));
    auto q = __builtin_amdgcn_permlane32_swap(__float_as_uint(s), __float_as_uint(s), false, false);
    return fmx(__uint_as_float(q[0]), __uint_as_float(q[1]));
}
DEV float sum_g(float s) {
    auto r = __builtin_amdgcn_permlane16_swap(__float_as_uint(s), __float_as_uint(s), false, false);
    s = __uint_as_float(r[0]) + __uint_as_float(r[1]);
    auto q = __builtin_amdgcn_permlane32_swap(__float_as_uint(s), __float_as_uint(s), false, false);
    return __uint_as_float(q[0]) + __uint_as_float(q[1]);
}
DEV float ror8(float s) { return __int_as_float(__builtin_amdgcn_mov_dpp(__float_as_int(s), 0x128, 0xf, 0xf, true)); }
template <int CTRL> DEV unsigned dppu(unsigned x) { return (unsigned)__builtin_amdgcn_mov_dpp((int)x, CTRL, 0xf, 0xf, true); }
DEV v4u quad4(unsigned x) { return (v4u){x, dppu<0x55>(x), dppu<0xAA>(x), dppu<0xFF>(x)}; }
DEV unsigned umax4(v4u v) { return max(max(v.x, v.y), max(v.z, v.w)); }
DEV rsrc_t mkrsrc(const void* p, unsigned bytes) { return __builtin_amdgcn_make_buffer_rsrc((void*)p, 0, bytes, 0x00020000); }
DEV v4u ld16(rsrc_t r, int vo, int so, int aux) { return aux ? __builtin_amdgcn_raw_buffer_load_b128(r, vo, so, SC1) : __builtin_amdgcn_raw_buffer_load_b128(r, vo, so, 0); }
DEV v2u ld8s(rsrc_t r, int vo, int so) { return __builtin_amdgcn_raw_buffer_load_b64(r, vo, so, SC1); }
DEV unsigned ld4s(rsrc_t r, int vo, int so) { return __builtin_amdgcn_raw_buffer_load_b32(r, vo, so, SC1); }
DEV void st16s(rsrc_t r, int vo, int so, v4u v) {
    __builtin_amdgcn_raw_buffer_store_b128(v, r, vo, so, SC1); asm volatile("s_nop 1" :: "v"(v) : "memory");
}
DEV void st8s(rsrc_t r, int vo, int so, v2u v) { __builtin_amdgcn_raw_buffer_store_b64(v, r, vo, so, SC1); }
DEV void st4s(rsrc_t r, int vo, int so, unsigned v) { __builtin_amdgcn_raw_buffer_store_b32(v, r, vo, so, SC1); }
DEV void dma16(rsrc_t r, int vo, int so, int ldsaddr) {
    unsigned keep;
    asm volatile("s_mov_b32 %0, m0\n\ts_mov_b32 m0, %4\n\ts_nop 4\n\tbuffer_load_dwordx4 %1, %2, %3 offen lds\n\ts_mov_b32 m0, %0"
                 : "=&s"(keep) : "v"(vo), "s"(r), "s"(__builtin_amdgcn_readfirstlane(so)), "s"(__builtin_amdgcn_readfirstlane(ldsaddr)) : "memory");
}
DEV void dma16s(rsrc_t r, int vo, int so, int ldsaddr) {
    unsigned keep;
    asm volatile("s_mov_b32 %0, m0\n\ts_mov_b32 m0, %4\n\ts_nop 4\n\tbuffer_load_dwordx4 %1, %2, %3 offen sc1 lds\n\ts_mov_b32 m0, %0"
                 : "=&s"(keep) : "v"(vo), "s"(r), "s"(__builtin_amdgcn_readfirstlane(so)), "s"(__builtin_amdgcn_readfirstlane(ldsaddr)) : "memory");
}
#define BAR() asm volatile("s_waitcnt lgkmcnt(0)\n\ts_barrier" ::: "memory")
#define BAR0() asm volatile("s_barrier" ::: "memory")
#define VMWAIT(n) asm volatile("s_waitcnt vmcnt(" #n ")" ::: "memory")
#define FENCE() asm volatile("" ::: "memory")
#define SCHED() __builtin_amdgcn_sched_barrier(0)
DEV v4u lds16(const char* l, int a) { return *(const v4u*)(l + a); }
DEV v2u lds8(const char* l, int a) { return *(const v2u*)(l + a); }
DEV v2u ldstr(const char* l, int a) { v4s r = __builtin_amdgcn_ds_read_tr16_b64_v4i16((LAS v4s*)(l + a)); return __builtin_bit_cast(v2u, r); }
template <int N> DEV void slp() { if constexpr (N > 0) __builtin_amdgcn_s_sleep(N); }
DEV int flagoff(int i) { return OFF_CTL + i * 128; }
template <int SL> DEV bool wait_flags(rsrc_t rs, int vo, unsigned need, unsigned& seen) {
    for (int sp = 0;; ++sp) {
        FENCE();
        seen = ld4s(rs, vo, 0);
        if (__builtin_amdgcn_ballot_w64(seen < need) == 0) return true;
        slp<SL>();
        if ((sp & 255) == 255) {
            if (sp > SPIN_CAP) st4s(rs, flagoff(FL_ABORT), 0, 1u);
            if (sp > SPIN_CAP || __builtin_amdgcn_readfirstlane(ld4s(rs, flagoff(FL_ABORT), 0))) return false;
        }
    }
}
DEV int img(int p) { return ((p & 7) >> 1) * 4 + (p >> 3) * 2 + (p & 1); }
DEV int pperm(int rho, int tau) { return (rho >> 2) * 8 + tau * 4 + (rho & 3); }

#define STAMP(cond, slot, s)
#define LSTAMP(cond, slot, s) do { if ((cond) && lane == 0) { unsigned long long t_ = __builtin_amdgcn_s_memrealtime(); __hip_atomic_store((unsigned long long*)(p.ws + OFF_DBG + ((slot) * 64 + (s)) * 8), t_, __ATOMIC_RELAXED, __HIP_MEMORY_SCOPE_AGENT); } } while (0)
#define ACC_DECL
#define ACC_T(k)
#define ACC_OUT(slot)
#define CSTAMP(cond, slot, s)

template <int which> DEV void pro_tile(const Params& p, char* lds, rsrc_t rs, int b, int h, int b0, int c0, int lane, int wave) {
    const int rho = lane & 15, g = lane >> 4;
    const rsrc_t ra = mkrsrc(which ? p.wv : p.wk, DM * 4096 * 2), rb = mkrsrc(which ? p.wo : p.wq, DM * 4096 * 2);
    int sa[4], sb[2];
#pragma unroll
    for (int k = 0; k < 4; ++k) { const int r = (k & 1) * 8 + (lane >> 3); sa[k] = ((b0 + 32 * wave + pperm(r, k >> 1)) * 4096 + h * 256 + ((lane & 7) ^ (r >> 1)) * 8) * 2; }
#pragma unroll
    for (int k = 0; k < 2; ++k) { const int r = k * 8 + (lane >> 3), ch = (2 * wave + k) * 64 + lane, e = ch / 14;
        sb[k] = which ? ((h * 256 + e) * DM + c0 + (ch - e * 14) * 8) * 2 : ((c0 + wave * 16 + r) * 4096 + h * 256 + ((lane & 7) ^ (r >> 1)) * 8) * 2; }
    const int bstep = which ? 64 * DM * 2 : 128, la = 57344 + wave * 4096;
    v4u a3[4];
#pragma unroll
    for (int c = 0; c < 4; ++c) {
#pragma unroll
        for (int k = 0; k < 2; ++k) dma16(rb, sb[k], c * bstep, c * 14336 + (2 * wave + k) * 1024);
#pragma unroll
        for (int k = 0; k < 4; ++k) { if (c < 3) dma16(ra, sa[k], c * 128, la + c * 28672 + k * 1024); else a3[k] = ld16(ra, sa[k], 384, 0); }
    }
    const int rd = (rho >> 3) * 1024 + (rho & 7) * 128, rx0 = (g ^ (rho >> 1)) * 16, rx1 = ((4 + g) ^ (rho >> 1)) * 16;
    const int tra = (8 * g + (rho >> 2)) * 224 + (rho & 3) * 8;
    v4f acc[7][2];
#pragma unroll
    for (int ct = 0; ct < 7; ++ct) { acc[ct][0] = (v4f){0, 0, 0, 0}; acc[ct][1] = acc[ct][0]; }
    const float sc = which ? 1.f : 0.09016844005556021f;
    const int obase = (which ? OFF_BT : OFF_AP) + ((h * 28 + (c0 >> 4)) * 14 + (b0 >> 5) + wave) * 1024 + lane * 16;
    v4u af[2][4], f[2][7][2];
#pragma unroll
    for (int c = 0; c <= 4; ++c) {
        if (c < 4) {
            if (c == 0) VMWAIT(18); else if (c == 1) VMWAIT(12); else if (c == 2) VMWAIT(6); else VMWAIT(0);
            if (c == 3) {
#pragma unroll
                for (int k = 0; k < 4; ++k) *(v4u*)(lds + la + k * 1024 + lane * 16) = a3[k];
            }
            BAR();
            if (c == 3) { STAMP(b == 0 && wave == 0, 4, 9); STAMP(wave == 0 && b < 64, 30, b); }
        }
        const char* A = lds + la + (c % 3) * 28672; const char* B = lds + c * 14336;
        if (c < 4) { af[c & 1][0] = lds16(A, rd + rx0); af[c & 1][1] = lds16(A, rd + rx1); af[c & 1][2] = lds16(A, 2048 + rd + rx0); af[c & 1][3] = lds16(A, 2048 + rd + rx1); }
#pragma unroll
        for (int ct = 0; ct < 7; ++ct) {
            if (c < 4) {
                if (which == 0) { f[c & 1][ct][0] = lds16(B, ct * 2048 + rd + rx0); f[c & 1][ct][1] = lds16(B, ct * 2048 + rd + rx1); }
                else {
#pragma unroll
                    for (int kk = 0; kk < 2; ++kk) { const v2u x = ldstr(B, tra + kk * 7168 + ct * 32), y = ldstr(B, tra + kk * 7168 + 896 + ct * 32); f[c & 1][ct][kk] = (v4u){x.x, x.y, y.x, y.y}; }
                }
            }
            SCHED();
            if (c > 0) { const int q = (c - 1) & 1;
                acc[ct][0] = mfma32(af[q][1], f[q][ct][1], mfma32(af[q][0], f[q][ct][0], acc[ct][0])); acc[ct][1] = mfma32(af[q][3], f[q][ct][1], mfma32(af[q][2], f[q][ct][0], acc[ct][1])); }
            if (c == 4) st16s(rs, obase, ct * 14336, pk8(acc[ct][0] * sc, acc[ct][1] * sc));
            SCHED();
        }
    }
    STAMP(b == 0 && wave == 0, 4, 10);
    STAMP(wave == 0 && b >= 192, 31, b & 63);
}
extern "C" __global__ void __launch_bounds__(512, 2) k_pro(Params p) {
    extern __shared__ __attribute__((aligned(16))) char lds[];
    const int b = blockIdx.x, tid = threadIdx.x, lane = tid & 63, wave = __builtin_amdgcn_readfirstlane(tid >> 6);
    const int rho = lane & 15, g = lane >> 4;
    const int gemm = (b & 7) * 4 + (b >> 6), sub = (b >> 3) & 7, which = gemm & 1, h = gemm >> 1;
    const int b0 = 224 * (sub & 1), c0 = 112 * (sub >> 1);
    const rsrc_t rs = mkrsrc(p.ws, WS_BYTES);
    LSTAMP(b == 0 && wave == 0, 4, 8); STAMP(wave == 0 && b < 64, 29, b);
    if (wave == 7) {
        const rsrc_t rx = mkrsrc(p.xb, 2048 * DM * 2);
        v4u xf[7], m0[7];
#pragma unroll
        for (int k = 0; k < 7; ++k) { const int pc = b * 7 + k, rt = pc / 14, ks = pc - rt * 14; xf[k] = ld16(rx, ((rt * 16 + (rho ^ ((rt & 1) * 4))) * DM + ks * 32 + g * 8) * 2, 0, 0); }
        const v4u sent = {SENT, SENT, SENT, SENT};
        if (b == 0) {
#pragma unroll
            for (int k = 0; k < 7; ++k) { const int c = k * 64 + lane; const unsigned* m = (const unsigned*)(p.mem0 + ((c >> 1) & 7) * DM + (c >> 4) * 16 + (c & 1) * 2); m0[k] = (v4u){m[0], m[2], m[4], m[6]}; }
        } else {
#pragma unroll
            for (int k = 0; k < 7; ++k) m0[k] = sent;
        }
        SCHED();
        BAR0(); BAR0(); BAR0(); BAR0();
        const unsigned rot = ((unsigned)__builtin_amdgcn_s_getreg(20 | (3 << 11)) - b) & 7u;
        if (lane < 32) st16s(rs, OFF_CTL + b * 512 + lane * 16, 0, (v4u){b * 4 == FL_ROT && lane == 0 ? rot : 0u, 0u, 0u, 0u});
#pragma unroll
        for (int k = 0; k < 16; ++k) {
            const int o = ((k & 7) * 1024 + lane * 16 + HO_MEM) & 8191;
            st16s(rs, (b & 3) == 3 && (b & 127) != 3 ? OOB : OFF_HO + b * 16384 + (k >> 3) * 8192 + o, 0, (k & 7) < 7 ? m0[k & 7] : sent);
        }
#pragma unroll
        for (int k = 0; k < 4; ++k) st16s(rs, OFF_G0 + (b * 4 + k) * 1024 + lane * 16, 0, sent);
#pragma unroll
        for (int k = 0; k < 7; ++k) st16s(rs, OFF_XF + (b * 7 + k) * 1024 + lane * 16, 0, xf[k]);
        return;
    }
    if (which) pro_tile<1>(p, lds, rs, b, h, b0, c0, lane, wave); else pro_tile<0>(p, lds, rs, b, h, b0, c0, lane, wave);
}

struct Ctx { rsrc_t rs; char* lds; int lane, wave, rho, g; };

DEV int slot_base(int s, int cal) { return OFF_HO + s * 65536 + (((cal >> (s >> 5)) & 1) << 13); }
#define STAGE_HEAD "s_add_i32 %[bud], %[bud], -1\n\ts_cmp_lt_i32 %[bud], 0\n\ts_cselect_b32 vcc_hi, -1, -2\n\tv_mov_b32 v233, vcc_hi\n\t"
#define STAGE_TAIL "v_cmp_gt_u32 vcc, v232, v233\n\ts_cbranch_vccnz 1b\n\ts_waitcnt lgkmcnt(0)\n\ts_barrier"
#define STAGE_CLOB "memory", "vcc", "scc", "v224", "v225", "v226", "v227", "v228", "v229", "v230", "v231", "v232", "v233"
DEV void stage1(rsrc_t rs, int vo, int so, int& budget) {
    asm volatile("1:\n\tbuffer_load_dwordx4 v[224:227], %[vo], %[rs], %[so] offen sc1\n\t" STAGE_HEAD
                 "s_waitcnt vmcnt(0)\n\tds_write_b128 %[vo], v[224:227]\n\tv_max3_u32 v232, v224, v225, v226\n\tv_max_u32 v232, v232, v227\n\t" STAGE_TAIL
                 : [bud] "+s"(budget) : [vo] "v"(vo), [rs] "s"(rs), [so] "s"(so) : STAGE_CLOB);
}
DEV void stage2(rsrc_t rs, int vo, int vo2, int so, int& budget) {
    asm volatile("1:\n\tbuffer_load_dwordx4 v[224:227], %[vo], %[rs], %[so] offen sc1\n\tbuffer_load_dwordx4 v[228:231], %[vo2], %[rs], %[so] offen sc1\n\t" STAGE_HEAD
                 "s_waitcnt vmcnt(1)\n\tds_write_b128 %[vo], v[224:227]\n\tv_max3_u32 v232, v224, v225, v226\n\t"
                 "s_waitcnt vmcnt(0)\n\tds_write_b128 %[vo2], v[228:231]\n\tv_max3_u32 v232, v232, v227, v228\n\tv_max3_u32 v232, v232, v229, v230\n\tv_max_u32 v232, v232, v231\n\t" STAGE_TAIL
                 : [bud] "+s"(budget) : [vo] "v"(vo), [vo2] "v"(vo2), [rs] "s"(rs), [so] "s"(so) : STAGE_CLOB);
}
DEV void give_up(rsrc_t rs) { st4s(rs, flagoff(FL_ABORT), 0, 1u); }
#define PIN2(a, b) asm volatile("" : "+v"(a), "+v"(b))
constexpr int CAL_STEP = 8;
#define TOUCH_LEAD 3
DEV void role_cal(const Params& p, const Ctx& c) {
    if (c.wave >= 2) return;
    unsigned best[2] = {0xffffffffu, 0xffffffffu};
    for (int r = 0; r < 20; ++r) {
#pragma unroll
        for (int k = 0; k < 2; ++k) {
            const int off = OFF_PROBE + c.wave * (2 << 20) + k * 8192 + r * 256;
            unsigned long long t0, t1; unsigned v;
            asm volatile("s_memtime %0\n\ts_waitcnt lgkmcnt(0)\n\tbuffer_load_dword %2, %3, %4, 0 offen sc1\n\ts_waitcnt vmcnt(0)\n\ts_memtime %1\n\ts_waitcnt lgkmcnt(0)"
                         : "=&s"(t0), "=&s"(t1), "=&v"(v) : "v"(off), "s"(c.rs) : "memory");
            best[k] = min(best[k], (unsigned)(t1 - t0));
        }
        if (r == 5 && c.wave == 0) { if (c.lane == 0) st4s(c.rs, flagoff(FL_CAL), 0, best[1] < best[0] ? 3u : 2u); VMWAIT(0); }
    }
    if (c.lane == 0) st4s(c.rs, flagoff(FL_CAL + 1 + c.wave), 0, best[1] < best[0] ? 3u : 2u);
    { const int k = c.lane & 3, o = (k == 0 ? 0 : k == 1 ? 7936 : 49152 + (k & 1) * 4096 + 2048) + (best[1] < best[0] ? 8192 : 0);
      for (int s = c.wave ? 32 : 12; s < 32 * (c.wave + 1); s += 4) {
          unsigned seen; if (!wait_flags<16>(c.rs, flagoff(FL_V), s - TOUCH_LEAD, seen)) return;
          unsigned v = ld4s(c.rs, OFF_HO + s * 65536 + o, 0); asm volatile("" :: "v"(v));
      } }
    if (c.wave == 0) { unsigned seen; wait_flags<16>(c.rs, flagoff(FL_V), 64, seen);
      auto S = [&](int sl, int k) { return (long long)__hip_atomic_load((unsigned long long*)(p.ws + OFF_DBG + (sl * 64 + k) * 8), __ATOMIC_RELAXED, __HIP_MEMORY_SCOPE_AGENT); };
      long long I = 1000; I = I < 0 ? 0 : I > 20000 ? 20000 : I;
      const long long until = (long long)__builtin_amdgcn_s_memrealtime() + 4 * I;
      while ((long long)__builtin_amdgcn_s_memrealtime() < until) __builtin_amdgcn_s_sleep(1); }
}
DEV void touch_window(rsrc_t rs) { const unsigned v = ld4s(rs, OFF_PROBE + (2 << 20) + 8064, 0); asm volatile("" :: "v"(v)); }
DEV int get_cal(const Ctx& c, int late, int& budget) {
    unsigned seen;
    if (!wait_flags<2>(c.rs, flagoff(FL_CAL + late + (late & c.lane)), 1u, seen)) budget = -1;
    return (__builtin_amdgcn_readlane(seen, 0) & 1) | ((__builtin_amdgcn_readlane(seen, 1) & 1) << 1);
}
DEV unsigned now() { return (unsigned)__builtin_amdgcn_s_memtime(); }
DEV void wait_until(unsigned t) {
    int rem = (int)(t - now());
    while (rem > 260) { __builtin_amdgcn_s_sleep(1); rem = (int)(t - now()); }
    rem -= 60;
    asm volatile("1:\n\ts_add_i32 %0, %0, -40\n\ts_cmp_gt_i32 %0, 0\n\ts_cbranch_scc1 1b" : "+s"(rem) :: "scc");
}
#define T_PM 2050
#define T_OS 2200
#define T_OM 400
#define T_OU 750
#define T_G0 600
#define T_ST 0
#define T_PG 200
#define ABL(b) true
#define S_AT 6

#define MEM_TIMES_W(lbase)                                                                                           \
    v4u a_[7];                                                                                                       \
    _Pragma("unroll") for (int f = 0; f < 7; ++f) a_[f] = lds16(lds, (lbase) + f * 1024);                            \
    EXTRA_READS;                                                                                                     \
    SCHED();                                                                                                         \
    _Pragma("unroll") for (int f = 0; f < 7; ++f) { c0 = smf(a_[f], w[0][2 * f], w[0][2 * f + 1], c0, sidx); c1 = smf(a_[f], w[1][2 * f], w[1][2 * f + 1], c1, sidx); AFTER_STEP(f); } \
    const v2u tp = {pk2(c0.x + c0.z, c0.y + c0.w), pk2(c1.x + c1.z, c1.y + c1.w)};

#define VCLOB "v224", "v225", "v226", "v227", "v228", "v229", "v230", "v231", VCLOB_HI
#define VCLOB_HI "v232", "v233", "v234", "v235", "v236", "v237", "v238", "v239", \
              "v240", "v241", "v242", "v243", "v244", "v245", "v246", "v247", "v248", "v249", "v250", "v251", "v252", "v253", "v254", "v255"
#define PUBLISH_ASM(ST)                                                                                                                     \
    "s_cmp_lg_u32 %[pub], 7\n\ts_waitcnt lgkmcnt(0)\n\ts_barrier\n\ts_cbranch_scc1 9f\n\t"                                                  \
    "ds_read_b64 v[224:225], %[la]\n\tds_read_b64 v[226:227], %[la] offset:" #ST "*1\n\tds_read_b64 v[228:229], %[la] offset:" #ST "*2\n\t" \
    "ds_read_b64 v[230:231], %[la] offset:" #ST "*3\n\tds_read_b64 v[232:233], %[la] offset:" #ST "*4\n\t"                                  \
    "ds_read_b64 v[234:235], %[la] offset:" #ST "*5\n\tds_read_b64 v[236:237], %[la] offset:" #ST "*6\n\t"                                  \
    "ds_read_b64 v[238:239], %[la] offset:" #ST "*7\n\t"                                                                                    \
    "s_waitcnt lgkmcnt(6)\n\tv_pk_add_f32 v[224:225], v[224:225], v[226:227]\n\t"                                                           \
    "s_waitcnt lgkmcnt(4)\n\tv_pk_add_f32 v[228:229], v[228:229], v[230:231]\n\t"                                                           \
    "s_waitcnt lgkmcnt(2)\n\tv_pk_add_f32 v[232:233], v[232:233], v[234:235]\n\tv_pk_add_f32 v[224:225], v[224:225], v[228:229]\n\t"        \
    "s_waitcnt lgkmcnt(0)\n\tv_pk_add_f32 v[236:237], v[236:237], v[238:239]\n\tv_pk_add_f32 v[224:225], v[224:225], v[232:233]\n\t"        \
    "s_nop 0\n\tv_pk_add_f32 v[224:225], v[224:225], v[236:237]\n\t"                                                                        \
    "s_nop 0\n\tv_cvt_pk_bf16_f32 v224, v224, v225\n\ts_nop 1\n\t"                                                                          \
    "v_mov_b32_dpp v225, v224 quad_perm:[1,1,1,1] row_mask:0xf bank_mask:0xf\n\t"                                                           \
    "v_mov_b32_dpp v226, v224 quad_perm:[2,2,2,2] row_mask:0xf bank_mask:0xf\n\t"                                                           \
    "v_mov_b32_dpp v227, v224 quad_perm:[3,3,3,3] row_mask:0xf bank_mask:0xf\n\t"                                                           \
    "buffer_store_dwordx4 v[224:227], %[vo], %[rs], %[so] offen sc1\n\t"                                                                    \
    "9:\n\ts_barrier\n\ts_nop 0"

DEV void role_p(const Params& p, const Ctx& c, int idx) {
    const int h = idx >> 2, q = idx & 3, lane = c.lane, wave = c.wave, rho = c.rho, g = c.g;
    const rsrc_t rs = c.rs; char* lds = c.lds;
    const int om = q * 4 + wave;
    const bool isT = wave < 4 && om < 14, isX = wave < 4 && om >= 14;
    constexpr int L_MEM = 0, L_PART = 8192, L_DUMP = 10240, L_CAL = 11264, L_X = 16384;
    const int sidx = (rho & 2) ? 0xEEEE : 0x4444;
    const int fa = L_MEM + g * 256 + ((rho >> 2) * 2 + (rho & 1)) * 32 + ((rho >> 1) & 1) * 16;
    v2u sel; { const int r0 = rho - 2 * g; sel.x = (r0 == 0 ? 0x3f80u : 0u) | (r0 == 1 ? 0x3f800000u : 0u); sel.y = (r0 == 8 ? 0x3f80u : 0u) | (r0 == 9 ? 0x3f800000u : 0u); }
    const int piece = q < 3 ? wave : (wave < 2 ? wave : wave - 2);
    const int po = piece * 1024 + lane * 16;
    int budget = SPIN_CAP;
#pragma unroll
    for (int k = 0; k < 7; ++k) dma16(rs, OFF_XF + lane * 16, (wave * 7 + k) * 1024, L_X + (wave * 7 + k) * 1024);
    if (isT) {
        v4u w[2][14];
#pragma unroll
        for (int t = 0; t < 2; ++t)
#pragma unroll
            for (int ks = 0; ks < 14; ++ks) w[t][ks] = ld16(rs, OFF_AP + (h * 28 + 2 * om + t) * 14336 + g * 256 + img(rho) * 16, ks * 1024, 0);
        VMWAIT(28);
        BAR0();
        STAMP(wave == 0, 23, idx);
#pragma unroll 1
        for (int sg = 0; sg < 2; ++sg) {
            v4f a[2][2] = {{{0, 0, 0, 0}, {0, 0, 0, 0}}, {{0, 0, 0, 0}, {0, 0, 0, 0}}};
#pragma unroll
            for (int ks = 0; ks < 14; ++ks)
#pragma unroll
                for (int kt = 0; kt < 2; ++kt) { const v4u x = lds16(lds, L_X + sg * 28672 + kt * 14336 + ks * 1024 + g * 256 + (rho ^ (kt * 4)) * 16);
                    a[kt][0] = mfma32(w[0][ks], x, a[kt][0]); a[kt][1] = mfma32(w[1][ks], x, a[kt][1]); }
#pragma unroll
            for (int kt = 0; kt < 2; ++kt) {
                auto x = __builtin_amdgcn_permlane32_swap(pk2(a[kt][0].x, a[kt][0].y), pk2(a[kt][1].x, a[kt][1].y), false, false);
                auto y = __builtin_amdgcn_permlane32_swap(pk2(a[kt][0].z, a[kt][0].w), pk2(a[kt][1].z, a[kt][1].w), false, false);
                st16s(rs, OFF_G0 + (h * 14 + om) * 2048 + kt * 1024 + lane * 16, sg * (NH * 28672), (v4u){x[0], x[1], y[0], y[1]});
            }
            BAR0();
            STAMP(wave == 0, 24 + sg, idx);
        }
        int cal = get_cal(c, 0, budget);
        STAMP(wave == 0, 26, idx);
        unsigned tref = now() - 65536;
        const int qa = L_MEM + (2 * om + (rho >> 3)) * 256 + (rho & 7) * 32 + g * 8;
        const int pw = (g >> 1) == (rho >> 3) ? L_PART + (2 * wave + (rho >> 3)) * 256 + (rho & 7) * 32 + (g & 1) * 16 : L_DUMP + lane * 16;
        const int tw = lane < 32 ? OFF_T + (h * 14 + om) * 512 + ((rho >> 3) * 2 + g) * 128 + (rho & 7) * 16 : OOB;
#pragma unroll 1
        for (int s = 0; s < NSEG && budget >= 0; ++s) {
            if (s == CAL_STEP) cal = __builtin_amdgcn_readfirstlane(*(const int*)(lds + L_CAL));
            const int sb = slot_base(s, cal);
            v4f c0 = {0, 0, 0, 0}, c1 = {0, 0, 0, 0}; PIN2(c0, c1);
            wait_until(tref + T_PM);
            stage1(rs, po, sb + HO_MEM, budget);
            v2u mq;
#define EXTRA_READS mq = lds8(lds, qa)
#define AFTER_STEP(f)
            MEM_TIMES_W(fa)
#undef EXTRA_READS
            const v4f d2 = mfma16(tp, sel, (v4f){0, 0, 0, 0});
            const v2u d = {pk2(d2.x, d2.y), pk2(d2.z, d2.w)};
            *(v4f*)(lds + pw) = mfma16(d, mq, (v4f){0, 0, 0, 0});
            BAR();
            BAR0();
            tref = now();
            wait_until(tref + T_PG);
            if (ABL(8)) { auto x = __builtin_amdgcn_permlane32_swap(d.x, d.y, false, false);
              auto y = __builtin_amdgcn_permlane32_swap(x[0], x[0], false, false); auto z = __builtin_amdgcn_permlane32_swap(x[1], x[1], false, false);
              st16s(rs, tw, s * (NH * 7168), (v4u){y[0], z[0], y[1], z[1]}); }
        }
    } else if (isX) {
        v4u w[2][14];
        int cal = get_cal(c, 0, budget);
        BAR0(); BAR0();
        if (om == 14) wait_until(now() + T_G0);
        else BAR0();
        const int gl0 = OFF_G + h * 28672 + (rho >> 3) * 1024 + g * 256 + (pperm(rho, 0) & 15) * 16;
        const int gfl = flagoff(FL_W + h * 7 + (lane & 3));
        const int ew = rho < 8 ? HO_E + h * 512 + g * 128 + rho * 16 : OOB, mw = g == 0 ? L_PART + 1024 + (rho & 7) * 32 + (rho >> 3) * 16 : L_DUMP + lane * 16;
        auto load_g = [&](int s, int t) {
            FENCE();
#pragma unroll
            for (int ks = 0; ks < 14; ++ks) w[t][ks] = ld16(rs, gl0 + t * 64, s * (NH * 28672) + (s < 2 ? OFF_G0 - OFF_G : 0) + ks * 2048, 1);
            FENCE();
        };
        bool lack = false;
#pragma unroll 1
        for (int s = om - 14; s < NSEG; s += 2) {
            const int sl = min(s, NSEG - 2);
            if (s >= 1) wait_until(now() + (s == 1 ? T_G0 : T_PG));
            unsigned seen;
            if (lack && !wait_flags<1>(rs, gfl, sl, seen)) budget = -1;
            load_g(sl, 0);
            if (s >= 2) { BAR0(); BAR0(); BAR0(); wait_until(now() + T_PG); }
            load_g(sl, 1);
            seen = ld4s(rs, gfl, 0);
            if (s == 1) { BAR0(); BAR0(); BAR0(); }
            if (s < 2) {
#pragma unroll 1
                for (;;) {
                    unsigned m = 0;
#pragma unroll
                    for (int ks = 0; ks < 14; ++ks) m = max(m, max(umax4(w[0][ks]), umax4(w[1][ks])));
                    if (__builtin_amdgcn_ballot_w64(m == SENT) == 0 || --budget < 0) break;
                    load_g(s, 0); load_g(s, 1);
                }
                if (s == 0) BAR0();
            }
            if ((s | 1) == CAL_STEP + 1) cal = __builtin_amdgcn_readfirstlane(*(const int*)(lds + L_CAL));
            const int sb = slot_base(s, cal);
            v4f c0 = {0, 0, 0, 0}, c1 = {0, 0, 0, 0}; PIN2(c0, c1);
            lack = __builtin_amdgcn_ballot_w64(seen < (unsigned)min(s + 2, NSEG - 2)) != 0;
            SCHED();
            VMWAIT(0);
            STAMP(idx == 3 && s == 0, 4, 12); STAMP(s == 0, 27, h); STAMP(s == 1, 27, 16 + h);
            BAR();
            if (s < NSEG - 1) {
#define EXTRA_READS
                MEM_TIMES_W(fa)
#undef EXTRA_READS
                const v4f d2 = mfma16(tp, sel, (v4f){0, 0, 0, 0});
                float m = fmx(fmx(d2.x, d2.y), fmx(d2.z, d2.w));
                m = max_g(fmx(m, ror8(m)));
                *(v4f*)(lds + mw) = (v4f){-m, -m, -m, -m};
                SCHED();
                BAR();
                const float e0 = __builtin_amdgcn_exp2f(d2.x - m), e1 = __builtin_amdgcn_exp2f(d2.y - m), e2 = __builtin_amdgcn_exp2f(d2.z - m), e3 = __builtin_amdgcn_exp2f(d2.w - m);
                const unsigned u0 = pk2(e0, e1), u1 = pk2(e2, e3);
                st16s(rs, ew, sb, (v4u){u0, u1, dppu<0x128>(u0), dppu<0x128>(u1)});
            } else BAR0();
            BAR0();
        }
        if (om == 14) { BAR0(); BAR0(); BAR0(); }
    } else {
        if (wave == 4) { *(v4u*)(lds + L_PART + lane * 16) = (v4u){0u, 0u, 0u, 0u}; *(v4u*)(lds + L_PART + 1024 + lane * 16) = (v4u){0u, 0u, 0u, 0u}; }
        BAR();
        int cal = get_cal(c, 0, budget);
        BAR0(); BAR0();
        unsigned tref = now() - 65536;
        STAMP(idx == 0 && wave == 7, 4, 1);
        const bool stg = q == 3 || wave != 7, two = q == 3 && wave == 5;
        const int pr = L_PART + (lane & 31) * 8, pubo = lane < 32 && (lane & 3) == 0 ? HO_PART + idx * 128 + lane * 4 : OOB;
#pragma unroll 1
        for (int s = 0; s < NSEG && budget >= 0; ++s) {
            if (s == CAL_STEP - 1 && wave == 6) *(int*)(lds + L_CAL) = get_cal(c, 1, budget);
            if (s == CAL_STEP) cal = __builtin_amdgcn_readfirstlane(*(const int*)(lds + L_CAL));
            if (s == 30 && wave == 6) touch_window(rs);
            const int sb = slot_base(s, cal);
            if (stg) {
                wait_until(tref + T_PM);
                if (two) stage2(rs, po, 6144 + lane * 16, sb + HO_MEM, budget); else stage1(rs, po, sb + HO_MEM, budget);
            } else BAR0();
            STAMP(idx == 0 && wave == 7, 0, s); STAMP(idx == 3 && wave == 6, 12, s);
            asm volatile(PUBLISH_ASM(256) :: [pub] "s"(wave), [la] "v"(pr), [vo] "v"(pubo), [rs] "s"(rs), [so] "s"(sb) : "memory", "scc", VCLOB);
            tref = now();
            if (wave == 7) {
                STAMP(idx == 0, 1, s);
                LSTAMP(idx == 0 && (s == 1 || s == 32 || s == 62), 14, s);
                if (lane == 0) st4s(rs, flagoff(FL_T + idx), 0, s);
            }
        }
    }
    if (budget < 0) give_up(rs);
    VMWAIT(0);
    BAR();
    if (wave == 7 && lane == 0) st4s(rs, flagoff(FL_T + idx), 0, NSEG);
}

DEV void role_o(const Params& p, const Ctx& c, int t) {
    const int lane = c.lane, wave = c.wave, rho = c.rho, g = c.g;
    const rsrc_t rs = c.rs; char* lds = c.lds;
    constexpr int L_MEM = 0, L_OUT = 8192, L_DUMP = 12288, L_V = 13312, L_CAL = 21504, L_X = 24576;
#pragma unroll
    for (int k = 0; k < 4; ++k) dma16(rs, wave * 4 + k < 28 ? OFF_XF + lane * 16 : OOB, (wave * 4 + k) * 1024, L_X + (wave * 4 + k) * 1024);
    v4u w[2][14];
#pragma unroll
    for (int k = 0; k < 2; ++k)
#pragma unroll
        for (int ks = 0; ks < 14; ++ks) w[k][ks] = ld16(rs, OFF_BT + ((2 * wave + k) * 28 + t) * 14336 + lane * 16, ks * 1024, 0);
    VMWAIT(28);
    BAR0();
    {
        v4u u[2];
#pragma unroll
        for (int k = 0; k < 2; ++k) {
            v4f a0 = {0, 0, 0, 0}, a1 = a0;
#pragma unroll
            for (int ks = 0; ks < 14; ++ks) { const int xo = L_X + (rho >> 3) * 14336 + ks * 1024 + g * 256;
                a0 = mfma32(lds16(lds, xo + ((pperm(rho, 0) & 15) ^ ((rho >> 3) * 4)) * 16), w[k][ks], a0); a1 = mfma32(lds16(lds, xo + ((pperm(rho, 1) & 15) ^ ((rho >> 3) * 4)) * 16), w[k][ks], a1); }
            u[k] = pk8(a0, a1);
        }
        asm volatile("v_mov_b32 v216, %0\n\tv_mov_b32 v217, %1\n\tv_mov_b32 v218, %2\n\tv_mov_b32 v219, %3\n\tv_mov_b32 v220, %4\n\tv_mov_b32 v221, %5\n\tv_mov_b32 v222, %6\n\tv_mov_b32 v223, %7"
                     :: "v"(u[0].x), "v"(u[0].y), "v"(u[0].z), "v"(u[0].w), "v"(u[1].x), "v"(u[1].y), "v"(u[1].z), "v"(u[1].w) : "v216", "v217", "v218", "v219", "v220", "v221", "v222", "v223");
    }
    int budget = SPIN_CAP;
    int cal = get_cal(c, 0, budget), caln = cal;
    const int sidx = (rho & 2) ? 0xEEEE : 0x4444;
    const int fa = L_MEM + g * 256 + ((rho >> 2) * 2 + (rho & 1)) * 32 + ((rho >> 1) & 1) * 16;
    const int hb = rho >> 3, hd = 2 * wave + hb, i = rho & 7;
    const int pa = HO_PART + hd * 512 + g * 128 + i * 16, ea = HO_E + hd * 512 + g * 128 + i * 16;
    const int ul = OFF_U + (t * 16 + 2 * wave) * 1024 + lane * 16;
    const int ufl = flagoff(FL_W + rho * 7 + ((14 + (t >> 1)) >> 2));
    const int mo = (lane & 3) == 0 ? HO_MEM + t * 256 + lane * 4 : OOB;
    const int p2 = (lane & 4) ? 2 * (lane & 3) + 1 : 2 * (lane & 3);
    const int ra = L_OUT + (lane >> 3) * 64 + p2 * 8;
    const int ow = hb == 0 ? L_OUT + wave * 512 + i * 64 + g * 16 : L_DUMP + lane * 16;
    const int po = wave * 1024 + lane * 16, po2 = min(wave + 4, 6) * 1024 + lane * 16;
    const v4u ones = {0x3f803f80u, 0x3f803f80u, 0x3f803f80u, 0x3f803f80u};
    const v2u ones2 = {0x3f803f80u, 0x3f803f80u};
    const unsigned mlo = hb ? 0u : 0xffffffffu, mhi = ~mlo;
    const unsigned long long hbm = 0xff00ff00ff00ff00ull;
    v4u psel; { const int kq = (rho >> 2) * 2 + (rho & 1); const unsigned one = (kq & 1) ? 0x3f800000u : 0x3f80u;
        psel = (v4u){(kq >> 1) == 0 ? one : 0u, (kq >> 1) == 1 ? one : 0u, (kq >> 1) == 2 ? one : 0u, (kq >> 1) == 3 ? one : 0u}; }
    asm volatile("v_mov_b32 v215, 0" ::: "v215");
    auto check_u = [&](int s) {
        unsigned long long lack; unsigned seen;
        asm volatile("v_cmp_gt_u32 %0, %1, v215" : "=s"(lack) : "s"(s) : "memory");
        if (lack != 0 && !wait_flags<1>(rs, ufl, s, seen)) budget = -1;
    };
    auto load_u = [&](int s) {
        asm volatile("s_nop 4\n\tbuffer_load_dwordx4 v[216:219], %0, %1, %2 offen sc1\n\tbuffer_load_dwordx4 v[220:223], %0, %1, %2 offen offset:1024 sc1"
                     :: "v"(ul), "s"(rs), "s"(s * (NH * 28672)) : "memory", "v216", "v217", "v218", "v219", "v220", "v221", "v222", "v223");
    };
    auto store_v = [&](int s) { st16s(rs, OFF_V + t * 4096 + (wave & 3) * 1024 + lane * 16, s * (NH * 7168), lds16(lds, L_V + (s & 1) * 4096 + (wave & 3) * 1024 + lane * 16)); };
    ACC_DECL;
    unsigned tref = now() - 65536;
    auto mem_times_b = [&](int sb, int s, bool last) -> v2u {
        v4f c0 = {0, 0, 0, 0}, c1 = {0, 0, 0, 0};
        if (wave < 4) stage2(rs, po, po2, sb + HO_MEM, budget);
        else BAR0();
        ACC_T(2);
#undef AFTER_STEP
#define AFTER_STEP(f) if (f == S_AT && !last) { SCHED(); ACC_T(4); if (T_OS) wait_until(tref + T_OS); ACC_T(5); asm volatile("buffer_load_dwordx4 v[224:227], %0, %2, %3 offen sc1\n\tbuffer_load_dwordx4 v[228:231], %1, %2, %3 offen sc1" \
                          :: "v"(pa), "v"(ea), "s"(rs), "s"(sb) : "memory", VCLOB); SCHED(); }
#define EXTRA_READS if (wave < 4 && !last && s > 0 && ABL(1)) load_u(s)
        MEM_TIMES_W(fa)
#undef EXTRA_READS
        *(v2u*)(lds + L_V + (s & 1) * 4096 + wave * 512 + lane * 8) = tp;
        return tp;
    };
    STAMP(t == 0 && wave == 0, 4, 13); STAMP(wave == 0, 28, t); STAMP(wave == 7, 28, 32 + t);
    STAMP(t == 0 && wave == 0, 4, 14);
#pragma unroll 1
    for (int s = 0; s < NSEG - 1 && budget >= 0; ++s) {
        const int sb = slot_base(s, s < CAL_STEP ? cal : caln), sn = slot_base(s + 1, s + 1 < CAL_STEP ? cal : caln);
        ACC_T(0);
        const v2u vp = mem_times_b(sb, s, false);
        ACC_T(3);
        asm volatile(
            "s_branch 3f\n\t"
            "1:\n\t"
            "buffer_load_dwordx4 v[224:227], %[pa], %[rs], %[sb] offen sc1\n\t"
            "buffer_load_dwordx4 v[228:231], %[ea], %[rs], %[sb] offen sc1\n\t"
            "3:\n\t"
            "s_add_i32 %[bud], %[bud], -1\n\ts_cmp_lt_i32 %[bud], 0\n\ts_cselect_b32 vcc_hi, -1, -2\n\tv_mov_b32 v253, vcc_hi\n\t"
            "s_waitcnt vmcnt(1)\n\t"
            "v_mfma_f32_16x16x32_bf16 v[232:235], %[psel], v[224:227], 0\n\t"
            "v_max3_u32 v250, v224, v225, v226\n\t"
            "s_waitcnt vmcnt(0)\n\t"
            "v_mfma_f32_16x16x32_bf16 v[244:247], %[ones], v[228:231], 0\n\t"
            "v_max3_u32 v251, v228, v229, v230\n\t"
            "v_mfma_f32_16x16x32_bf16 v[236:239], v[216:219], v[228:231], 0\n\t"
            "v_max3_u32 v250, v250, v227, v231\n\t"
            "v_mfma_f32_16x16x32_bf16 v[240:243], v[220:223], v[228:231], 0\n\t"
            "v_max_u32 v250, v250, v251\n\t"
            "v_cmp_gt_u32 vcc, v250, v253\n\t"
            "s_cbranch_vccnz 1b\n\t"
            "v_exp_f32 v250, v232\n\tv_exp_f32 v251, v233\n\t"
            "v_max_f32 v252, v232, v233\n\t"
            "v_cvt_pk_bf16_f32 v250, v250, v251\n\t"
            "v_cmp_lt_f32 vcc, 0x42800000, v252\n\t"
            "v_and_b32 v248, v250, %[mlo]\n\tv_and_b32 v249, v250, %[mhi]\n\t"
            "v_cndmask_b32 v236, v236, v240, %[hbm]\n\tv_cndmask_b32 v237, v237, v241, %[hbm]\n\t"
            "v_mfma_f32_16x16x16_bf16 v[244:247], %[ones2], v[248:249], v[244:247]\n\t"
            "v_cndmask_b32 v238, v238, v242, %[hbm]\n\tv_cndmask_b32 v239, v239, v243, %[hbm]\n\t"
            "s_nop 1\n\t"
            "v_mfma_f32_16x16x16_bf16 v[236:239], %[vp], v[248:249], v[236:239]\n\t"
            "s_cbranch_vccz 2f\n\t"
            "s_nop 7\n\t"
            "v_max_f32 v252, 0, v252\n\tv_mov_b32 v253, v252\n\ts_nop 1\n\tv_permlane16_swap_b32 v252, v253\n\ts_nop 1\n\t"
            "v_max_f32 v252, v252, v253\n\tv_mov_b32 v253, v252\n\ts_nop 1\n\tv_permlane32_swap_b32 v252, v253\n\ts_nop 1\n\t"
            "v_max_f32 v252, v252, v253\n\t"
            "v_mfma_f32_16x16x32_bf16 v[244:247], %[ones], v[228:231], 0\n\t"
            "v_mfma_f32_16x16x32_bf16 v[236:239], v[216:219], v[228:231], 0\n\t"
            "v_sub_f32 v250, v232, v252\n\tv_sub_f32 v251, v233, v252\n\t"
            "v_exp_f32 v250, v250\n\tv_exp_f32 v251, v251\n\tv_exp_f32 v253, -v252\n\ts_nop 0\n\t"
            "v_cvt_pk_bf16_f32 v250, v250, v251\n\t"
            "v_and_b32 v248, v250, %[mlo]\n\tv_and_b32 v249, v250, %[mhi]\n\ts_nop 7\n\t"
            "v_cndmask_b32 v236, v236, v240, %[hbm]\n\tv_cndmask_b32 v237, v237, v241, %[hbm]\n\tv_cndmask_b32 v238, v238, v242, %[hbm]\n\tv_cndmask_b32 v239, v239, v243, %[hbm]\n\t"
            "v_mul_f32 v236, v236, v253\n\tv_mul_f32 v237, v237, v253\n\tv_mul_f32 v238, v238, v253\n\tv_mul_f32 v239, v239, v253\n\t"
            "v_mul_f32 v244, v244, v253\n\tv_mul_f32 v245, v245, v253\n\tv_mul_f32 v246, v246, v253\n\tv_mul_f32 v247, v247, v253\n\ts_nop 1\n\t"
            "v_mfma_f32_16x16x16_bf16 v[244:247], %[ones2], v[248:249], v[244:247]\n\t"
            "v_mfma_f32_16x16x16_bf16 v[236:239], %[vp], v[248:249], v[236:239]\n\t"
            "s_nop 7\n\t"
            "2:\n\t"
            "s_nop 1\n\t"
            "v_rcp_f32 v252, v244\n\t"
            "s_nop 3\n\t"
            "v_mul_f32 v236, v236, v252\n\tv_mul_f32 v237, v237, v252\n\tv_mul_f32 v238, v238, v252\n\tv_mul_f32 v239, v239, v252\n\t"
            "v_add_f32_dpp v240, v236, v236 row_ror:8 row_mask:0xf bank_mask:0xf\n\tv_add_f32_dpp v241, v237, v237 row_ror:8 row_mask:0xf bank_mask:0xf\n\t"
            "v_add_f32_dpp v242, v238, v238 row_ror:8 row_mask:0xf bank_mask:0xf\n\tv_add_f32_dpp v243, v239, v239 row_ror:8 row_mask:0xf bank_mask:0xf\n\t"
            "ds_write_b128 %[ow], v[240:243]\n\t"
            PUBLISH_ASM(512)
            : [bud] "+s"(budget)
            : [pa] "v"(pa), [ea] "v"(ea), [rs] "s"(rs), [sb] "s"(sb), [psel] "v"(psel), [ones] "v"(ones), [ones2] "v"(ones2), [vp] "v"(vp),
              [mlo] "v"(mlo), [mhi] "v"(mhi), [hbm] "s"(hbm), [ow] "v"(ow),
              [pub] "s"(wave), [la] "v"(ra), [vo] "v"(mo), [so] "s"(sn)
            : "memory", "vcc", "scc", VCLOB);
        tref = now();
        STAMP(t == 0 && wave == 7, 3, s);
        if (wave == 7 && lane == 0) st4s(rs, flagoff(FL_V + t), 0, s);
        if (s + 2 == CAL_STEP) caln = __builtin_amdgcn_readfirstlane(*(const int*)(lds + L_CAL));
        if (wave == 5 && s + 3 == CAL_STEP) *(int*)(lds + L_CAL) = get_cal(c, 1, budget);
        if (wave == 5 && s == 29) touch_window(rs);
        if (wave == 4 || (s == 0 && wave > 4)) check_u(min(s + 2, NSEG - 2));
        ACC_T(6);
        if (wave < 4) wait_until(tref + T_OM);
        else {
            wait_until(tref + T_OU + (wave - 4) * T_ST);
            if (s + 1 < NSEG - 1 && ABL(1)) load_u(s + 1);
            if (wave == 4) asm volatile("buffer_load_dword v215, %0, %1, 0 offen sc1" :: "v"(ufl), "s"(rs) : "memory", "v215");
            if (ABL(2)) store_v(s);
        }
        ACC_T(7);
    }
    VMWAIT(0);
    ACC_OUT(t * 8 + wave);
    mem_times_b(slot_base(NSEG - 1, caln), NSEG - 1, true);
    if (budget < 0) give_up(rs);
    BAR();
    if (wave >= 4) store_v(NSEG - 1);
    VMWAIT(0);
    BAR0();
    if (wave == 7 && lane == 0) st4s(rs, flagoff(FL_V + t), 0, NSEG);
}

DEV void role_w(const Params& p, const Ctx& c, int wi) {
    const int lane = c.lane, wave = c.wave, rho = c.rho, g = c.g;
    const rsrc_t rs = c.rs; char* lds = c.lds;
    const int h = wi / 7, unit = (wi - h * 7) * 4 + (wave & 3), set = wave >> 2;
    const bool isG = unit < 14;
    auto stage = [&](int it) {
        const int sg = (2 * it + set + 1) & 63, lb = (set * 2 + (it & 1)) * 28672;
#pragma unroll
        for (int k = 0; k < 7; ++k) { const int pc = (wave & 3) * 7 + k; dma16(rs, OFF_XF + lane * 16, sg * 28672 + pc * 1024, lb + pc * 1024); }
    };
    v4u w[2][14];
    auto load_w = [&]() {
#pragma unroll
        for (int t = 0; t < 2; ++t) {
            const int row = isG ? unit * 32 + pperm(rho, t) : (unit - 14) * 32 + t * 16 + rho;
#pragma unroll
            for (int ks = 0; ks < 14; ++ks) w[t][ks] = ld16(rs, (isG ? OFF_AP : OFF_BT) + (h * 28 + (row >> 4)) * 14336 + g * 256 + (row & 15) * 16, ks * 1024, 0);
        }
    };
    int xa[2];
#pragma unroll
    for (int k = 0; k < 2; ++k) { const int key = isG ? k * 16 + rho : pperm(rho, k), kt = key >> 4; xa[k] = kt * 14336 + (g * 16 + ((key & 15) ^ (kt * 4))) * 16; }
    const int ob = (isG ? OFF_G + (h * 14 + unit) * 2048 : OFF_U + ((unit - 14) * 32 + h) * 1024) + lane * 16;
    const int ostep = isG ? 1024 : 16384;
    auto segment = [&](int buf, int sg) {
        const char* xb = lds + buf * 28672;
        v4f a00 = {0, 0, 0, 0}, a01 = a00, a10 = a00, a11 = a00;
        if (isG) {
#pragma unroll
            for (int ks = 0; ks < 14; ++ks) { const v4u x0 = lds16(xb, xa[0] + ks * 1024), x1 = lds16(xb, xa[1] + ks * 1024);
                a00 = mfma32(w[0][ks], x0, a00); a01 = mfma32(w[1][ks], x0, a01); a10 = mfma32(w[0][ks], x1, a10); a11 = mfma32(w[1][ks], x1, a11); }
        } else {
#pragma unroll
            for (int ks = 0; ks < 14; ++ks) { const v4u x0 = lds16(xb, xa[0] + ks * 1024), x1 = lds16(xb, xa[1] + ks * 1024);
                a00 = mfma32(x0, w[0][ks], a00); a01 = mfma32(x1, w[0][ks], a01); a10 = mfma32(x0, w[1][ks], a10); a11 = mfma32(x1, w[1][ks], a11); }
        }
        st16s(rs, ob, sg * (NH * 28672), pk8(a00, a01)); st16s(rs, ob + ostep, sg * (NH * 28672), pk8(a10, a11));
    };
    if (set == 0) load_w();
    if (wave == 7) *(int*)(lds + LDS_MAIN - 32) = 0;
    stage(0);
    VMWAIT(0);
    BAR();
    STAMP(wi == 20 && wave == 1, 4, 2);
#pragma unroll 1
    for (int it = -1; it < 32; ++it) {
        if (it == -1 && set == 1) load_w();
        if (it >= 0 && it + 1 < 32) stage(it + 1);
        if (it > 0 || set == 0) segment(it > 0 ? set * 2 + (it & 1) : 2 * (it + 1), ((it > 0 ? 2 * it + set : it + 1) + 1) & 63);
        if (it < 0) continue;
        if (it == 0 && set == 0) {
            VMWAIT(0);
            if (lane == 0 && __hip_atomic_fetch_add((int*)(lds + LDS_MAIN - 32), 1, __ATOMIC_RELAXED, __HIP_MEMORY_SCOPE_WORKGROUP) == 3) st4s(rs, flagoff(FL_W + wi), 0, 2);
            STAMP(wi == 20 && wave == 1, 4, 15);
        }
        const bool eager = it < 4 || it == 31;
        if (eager) VMWAIT(0); else VMWAIT(2);
        BAR();
        if (wave == 0 && lane == 0) st4s(rs, flagoff(FL_W + wi), 0, eager ? 2 * it + 2 : 2 * it);
        if (it == 12 && wave == 0 && wi >= 48) {
            unsigned seen; wait_flags<8>(rs, flagoff(FL_TAB), 28, seen);
            __builtin_amdgcn_fence(__ATOMIC_ACQUIRE, "agent");
            VMWAIT(0);
        }
        STAMP(wi == 20 && wave == 1, 5, it); CSTAMP(wi == 20 && wave == 1, 6, it);
    }
}

DEV int f_first(int set, int wave) { return set ? 33 + 2 * wave : wave; }
DEV int f_next(int s, int wave) { return s >= 32 ? s + 16 : s >= 24 ? 32 + 2 * wave : s + 8; }
DEV void role_f1(const Params& p, const Ctx& c, int h, int set) {
    const int lane = c.lane, wave = c.wave, rho = c.rho, g = c.g;
    const rsrc_t rs = c.rs;
    const int gfl = flagoff(FL_W + h * 7 + (lane & 3)), tfl = flagoff(FL_T + h * 4 + (lane & 3));
    const int gl0 = OFF_G + h * 28672 + (rho >> 3) * 1024 + g * 256 + (pperm(rho, 0) & 15) * 16;
    const int tl = OFF_T + h * 7168 + g * 128 + ((rho >> 2) * 2 + (rho & 1)) * 16;
    const float ninf = -__builtin_inff();
    bool wdone = false;
#pragma unroll 1
    for (int s = f_first(set, wave); s < NSEG; s = f_next(s, wave)) {
        STAMP(h == 0, 16, s);
        unsigned seen;
        if (!wdone) { if (!wait_flags<8>(rs, gfl, s ? s : NSEG, seen)) return; wdone = __builtin_amdgcn_ballot_w64(seen < NSEG) == 0; }
        v4u x[2][14];
        v4f a[2][2] = {{{0, 0, 0, 0}, {0, 0, 0, 0}}, {{0, 0, 0, 0}, {0, 0, 0, 0}}};
#pragma unroll
        for (int hf = 0; hf < 2; ++hf) {
            v4u y[2][7];
#pragma unroll
            for (int k = 0; k < 7; ++k) {
#pragma unroll
                for (int qt = 0; qt < 2; ++qt) x[qt][hf * 7 + k] = ld16(rs, OFF_XF + qt * 14336 + g * 256 + (rho ^ (qt * 4)) * 16, s * 28672 + (hf * 7 + k) * 1024, 0);
                y[0][k] = ld16(rs, gl0, s * (NH * 28672) + (hf * 7 + k) * 2048, 1); y[1][k] = ld16(rs, gl0 + 64, s * (NH * 28672) + (hf * 7 + k) * 2048, 1);
            }
            SCHED();
#pragma unroll
            for (int k = 0; k < 7; ++k)
#pragma unroll
                for (int qt = 0; qt < 2; ++qt) { a[qt][0] = mfma32(y[0][k], x[qt][hf * 7 + k], a[qt][0]); a[qt][1] = mfma32(y[1][k], x[qt][hf * 7 + k], a[qt][1]); }
        }
        float v[2][8], m[2];
#pragma unroll
        for (int qt = 0; qt < 2; ++qt) {
            const float r[8] = {a[qt][0].x, a[qt][0].y, a[qt][0].z, a[qt][0].w, a[qt][1].x, a[qt][1].y, a[qt][1].z, a[qt][1].w};
            m[qt] = ninf;
#pragma unroll
            for (int k = 0; k < 8; ++k) { v[qt][k] = (8 * g + k <= qt * 16 + rho) ? r[k] : ninf; m[qt] = fmx(m[qt], v[qt][k]); }
        }
        asm volatile("" : "+v"(m[0]), "+v"(m[1]));
        STAMP(h == 0, 17, s);
        if (!wait_flags<2>(rs, tfl, s + 1, seen)) return;
        STAMP(h == 0, 18, s);
        v4f am[2] = {{0, 0, 0, 0}, {0, 0, 0, 0}};
        { v4u y[14];
#pragma unroll
          for (int ks = 0; ks < 14; ++ks) y[ks] = ld16(rs, tl, s * (NH * 7168) + ks * 512, 1);
          SCHED();
#pragma unroll
          for (int ks = 0; ks < 14; ++ks) { am[0] = mfma32(y[ks], x[0][ks], am[0]); am[1] = mfma32(y[ks], x[1][ks], am[1]); } }
        unsigned pm[2];
#pragma unroll
        for (int qt = 0; qt < 2; ++qt) {
            const float mm = max_g(fmx(m[qt], fmx(am[qt].x, am[qt].y)));
            const float e0 = __builtin_amdgcn_exp2f(am[qt].x - mm), e1 = __builtin_amdgcn_exp2f(am[qt].y - mm);
            float z = e0 + e1;
#pragma unroll
            for (int k = 0; k < 8; ++k) { v[qt][k] = __builtin_amdgcn_exp2f(v[qt][k] - mm); z += v[qt][k]; }
            const float rz = __builtin_amdgcn_rcpf(sum_g(z));
            st16s(rs, OFF_PX + (h * 2 + qt) * 1024 + lane * 16, s * (NH * 2048),
                  (v4u){pk2(v[qt][0] * rz, v[qt][1] * rz), pk2(v[qt][2] * rz, v[qt][3] * rz), pk2(v[qt][4] * rz, v[qt][5] * rz), pk2(v[qt][6] * rz, v[qt][7] * rz)});
            pm[qt] = pk2(e0 * rz, e1 * rz);
        }
        st16s(rs, (lane & 1) == 0 ? OFF_PM + h * 512 + lane * 8 : OOB, s * (NH * 512), (v4u){pm[0], pm[1], dppu<0xF5>(pm[0]), dppu<0xF5>(pm[1])});
        VMWAIT(0);
        if (lane == 0) st4s(rs, flagoff((set ? FL_P2 : FL_P) + h * 8 + wave), 0, s + 1);
        STAMP(h == 0, 19, s);
    }
}

DEV void make_tables(const Params& p, const Ctx& c, int t) {
    const int lane = c.lane, wave = c.wave, rho = c.rho, g = c.g;
    const rsrc_t rs = c.rs;
    for (int ch = t * 512 + wave * 64 + lane; ch < 2 * 28672; ch += 28 * 512) {
        float v[8];
        if (ch < 28672) { const int fr = ch >> 6, tl = fr / 14, d0 = (fr - tl * 14) * 32 + g * 8, ri = tl >> 4, k = (tl & 15) * 16 + rho;
#pragma unroll
            for (int j = 0; j < 8; ++j) { const float ph = (float)((k * (d0 + j)) % 448) * (1.f / 448.f); v[j] = k > 224 ? 0.f : ri ? -__builtin_amdgcn_sinf(ph) : __builtin_amdgcn_cosf(ph); }
        } else { const int fr = (ch - 28672) >> 6, d = (fr >> 4) * 16 + rho, ri = (fr >> 3) & 1, k0 = (fr & 7) * 32 + g * 8;
#pragma unroll
            for (int j = 0; j < 8; ++j) { const int k = k0 + j; const float ph = (float)((k * d) % 448) * (1.f / 448.f); const bool edge = k == 0 || k == 224;
                const float wgt = (edge ? 1.f : 2.f) / (448.f * 2048.f); v[j] = k > 224 ? 0.f : ri ? (edge ? 0.f : -wgt * __builtin_amdgcn_sinf(ph)) : wgt * __builtin_amdgcn_cosf(ph); }
        }
        st16s(rs, OFF_FD + ch * 16, 0, (v4u){pk2(v[0], v[1]), pk2(v[2], v[3]), pk2(v[4], v[5]), pk2(v[6], v[7])});
    }
    VMWAIT(0);
    BAR();
    if (wave == 0 && lane == 0) __hip_atomic_fetch_add((unsigned*)(p.ws + flagoff(FL_TAB)), 1u, __ATOMIC_RELAXED, __HIP_MEMORY_SCOPE_AGENT);
}
DEV void role_f2(const Params& p, const Ctx& c, int t, int set) {
    const int lane = c.lane, wave = c.wave, rho = c.rho, g = c.g;
    const rsrc_t rs = c.rs;
    const int ufl = flagoff(FL_W + rho * 7 + ((14 + (t >> 1)) >> 2)), pfl = flagoff(lane < 16 ? (set ? FL_P2 : FL_P) + lane * 8 + wave : FL_V + t);
    const int ul = OFF_U + t * 16384 + lane * 16;
    bool wdone = false;
#pragma unroll 1
    for (int s = f_first(set, wave); s < NSEG; s = f_next(s, wave)) {
        unsigned seen;
        if (!wdone) { if (!wait_flags<8>(rs, ufl, s ? s : NSEG, seen)) return; wdone = __builtin_amdgcn_ballot_w64(seen < NSEG) == 0; }
        STAMP(t == 0, 20, s);
        v4u u[16];
#pragma unroll
        for (int h = 0; h < 16; ++h) u[h] = ld16(rs, ul, s * (NH * 28672) + h * 1024, 1);
        if (!wait_flags<2>(rs, pfl, s + 1, seen)) return;
        STAMP(t == 0, 21, s);
        v4f a[2] = {{0, 0, 0, 0}, {0, 0, 0, 0}}, m[2] = {{0, 0, 0, 0}, {0, 0, 0, 0}};
        v4u px[16]; v2u pm[16], vv[8];
#pragma unroll
        for (int h = 0; h < 16; ++h) px[h] = ld16(rs, OFF_PX + lane * 16, s * (NH * 2048) + h * 2048, 1);
#pragma unroll
        for (int h = 0; h < 16; ++h) { pm[h] = ld8s(rs, OFF_PM + lane * 8, s * (NH * 512) + h * 512); if (h & 1) vv[h >> 1] = ld8s(rs, OFF_V + t * 4096 + lane * 8, s * (NH * 7168) + (h >> 1) * 512); }
        SCHED();
#pragma unroll
        for (int h = 0; h < 16; ++h) { a[0] = mfma32(u[h], px[h], a[0]); SCHED(); px[h] = ld16(rs, OFF_PX + 1024 + lane * 16, s * (NH * 2048) + h * 2048, 1); SCHED(); }
#pragma unroll
        for (int k = 0; k < 8; ++k) { m[0] = mfma16(vv[k], (v2u){pm[2 * k].x, pm[2 * k + 1].x}, m[0]); m[1] = mfma16(vv[k], (v2u){pm[2 * k].y, pm[2 * k + 1].y}, m[1]); }
        SCHED();
#pragma unroll
        for (int h = 0; h < 16; ++h) a[1] = mfma32(u[h], px[h], a[1]);
#pragma unroll
        for (int qt = 0; qt < 2; ++qt) { const v4f o = a[qt] + m[qt];
            *(v2u*)(p.out + (size_t)(s * 32 + qt * 16 + rho) * 896 + DM + t * 16 + g * 4) = (v2u){pk2(o.x, o.y), pk2(o.z, o.w)}; }
        STAMP(t == 0, 22, s);
    }
}

DEV bool fft_sync(const Params& p, const Ctx& c, int fl) {
    VMWAIT(0);
    BAR();
    int* okw = (int*)(c.lds + LDS_MAIN - 16);
    if (c.wave == 0) {
        if (c.lane == 0) __hip_atomic_fetch_add((unsigned*)(p.ws + flagoff(fl)), 1u, __ATOMIC_RELAXED, __HIP_MEMORY_SCOPE_AGENT);
        unsigned seen; const bool ok = wait_flags<2>(c.rs, flagoff(fl), 64, seen);
        if (c.lane == 0) *okw = ok;
    }
    BAR();
    return *okw != 0;
}
DEV v4u neg8(v4u v) { return v ^ 0x80008000u; }
DEV void twid(int a, const int (&n)[8], int mask, float inv, v4u& C, v4u& S) {
    float cs[8], sn[8];
#pragma unroll
    for (int j = 0; j < 8; ++j) { const float ph = (float)((a * n[j]) & mask) * inv; cs[j] = __builtin_amdgcn_cosf(ph); sn[j] = __builtin_amdgcn_sinf(ph); }
    C = (v4u){pk2(cs[0], cs[1]), pk2(cs[2], cs[3]), pk2(cs[4], cs[5]), pk2(cs[6], cs[7])};
    S = (v4u){pk2(sn[0], sn[1]), pk2(sn[2], sn[3]), pk2(sn[4], sn[5]), pk2(sn[6], sn[7])};
}
DEV void role_fft(const Params& p, const Ctx& c, int job) {
    const int lane = c.lane, wave = c.wave, rho = c.rho, g = c.g;
    const rsrc_t rs = c.rs; char* lds = c.lds;
    const v4f z4 = {0, 0, 0, 0};

    {
        const rsrc_t rx = mkrsrc(p.xa, 2048 * DM * 2);
        for (int pc = wave; pc < 28; pc += 8) { const int nt = pc >= 14, ks = pc - nt * 14; dma16(rx, ((64 * (nt * 16 + rho) + job) * DM + ks * 32 + g * 8) * 2, 0, pc * 1024); }

        v4u C[2], S[2];
        { int n[8];
#pragma unroll
          for (int j = 0; j < 8; ++j) n[j] = 64 * (16 * (j >> 2) + 4 * g + (j & 3)) + job;
          twid(rho, n, 2047, 1.f / 2048.f, C[0], S[0]); twid(16 + rho, n, 2047, 1.f / 2048.f, C[1], S[1]); }
        VMWAIT(0);
        BAR();
        v4f zr[2][2], zi[2][2];
#pragma unroll
        for (int t = 0; t < 2; ++t) {
            const int fo = OFF_FD + (wave * 2 + (rho >> 3)) * 14336 + g * 256 + (pperm(rho, t) & 15) * 16;
            v4f r0 = z4, r1 = z4, i0 = z4, i1 = z4;
#pragma unroll
            for (int hf = 0; hf < 2; ++hf) {
                v4u fr[7], fi[7];
#pragma unroll
                for (int k = 0; k < 7; ++k) { fr[k] = ld16(rs, fo, (hf * 7 + k) * 1024, 0); fi[k] = ld16(rs, fo, 256 * DM * 2 + (hf * 7 + k) * 1024, 0); }
                SCHED();
#pragma unroll
                for (int k = 0; k < 7; ++k) { const v4u x0 = lds16(lds, (hf * 7 + k) * 1024 + lane * 16), x1 = lds16(lds, (14 + hf * 7 + k) * 1024 + lane * 16);
                    r0 = mfma32(x0, fr[k], r0); r1 = mfma32(x1, fr[k], r1); i0 = mfma32(x0, fi[k], i0); i1 = mfma32(x1, fi[k], i1); }
            }
            const v4u yr = pk8(r0, r1), yi = pk8(i0, i1), nr = neg8(yr);
#pragma unroll
            for (int ct = 0; ct < 2; ++ct) { zr[ct][t] = mfma32(yi, S[ct], mfma32(yr, C[ct], z4)); zi[ct][t] = mfma32(nr, S[ct], mfma32(yi, C[ct], z4)); }
        }
        char* O = lds + 32768;
#pragma unroll
        for (int ct = 0; ct < 2; ++ct) { const int o = (ct * 16 + rho) * 1040 + wave * 64 + g * 16;
            *(v4u*)(O + o) = pk8(zr[ct][0], zr[ct][1]); *(v4u*)(O + o + 512) = pk8(zi[ct][0], zi[ct][1]); }
        BAR();
#pragma unroll
        for (int k = 0; k < 4; ++k) st16s(rs, OFF_Z1 + job * 1024 + lane * 16, (wave * 4 + k) * 65536, lds16(O, (wave * 4 + k) * 1040 + lane * 16));
    }
    STAMP(job == 0 && wave == 1, 4, 3);
    if (!fft_sync(p, c, FL_FA)) return;
    STAMP(job == 0 && wave == 1, 4, 4);
    if (wave < 4) {
        const int k1 = job >> 1, P = (job & 1) * 4 + wave;
        char* L = lds + wave * 8192;
#pragma unroll
        for (int pc = 0; pc < 8; ++pc) dma16s(rs, OFF_Z1 + (k1 * 64 + (pc & 3) * 16 + (lane >> 2)) * 1024 + (pc >> 2) * 512 + P * 64 + (lane & 3) * 16, 0, wave * 8192 + pc * 1024);
        v4u C[4][2], S[4][2];
#pragma unroll
        for (int ks = 0; ks < 2; ++ks) { int n[8];
#pragma unroll
            for (int j = 0; j < 8; ++j) n[j] = 32 * ks + 16 * (j >> 2) + 4 * g + (j & 3);
#pragma unroll
            for (int kt = 0; kt < 4; ++kt) twid(16 * kt + rho, n, 63, 1.f / 64.f, C[kt][ks], S[kt][ks]); }
        VMWAIT(0);
        v2u hr[4], hi[4];
#pragma unroll
        for (int t = 0; t < 2; ++t) {
            const int k = P * 32 + pperm(rho, t);
            v4u xr[2], xi[2];
#pragma unroll
            for (int ks = 0; ks < 2; ++ks) { const int a = (32 * ks + 4 * g + (rho >> 2)) * 64 + (rho & 3) * 16 + t * 8;
                const v2u a0 = ldstr(L, a), a1 = ldstr(L, a + 1024), b0 = ldstr(L, a + 4096), b1 = ldstr(L, a + 5120);
                xr[ks] = (v4u){a0.x, a0.y, a1.x, a1.y}; xi[ks] = (v4u){b0.x, b0.y, b1.x, b1.y}; }
            const v4u n0 = neg8(xr[0]), n1 = neg8(xr[1]);
            unsigned short fr[16], fi[16];
#pragma unroll
            for (int j = 0; j < 16; ++j) { const int o = (k1 + 32 * (16 * (j >> 2) + 4 * g + (j & 3))) * 225 + min(k, 224); fr[j] = p.wr[o]; fi[j] = p.wi[o]; }
            SCHED();
            v4f vr[4], vi[4];
#pragma unroll
            for (int kt = 0; kt < 4; ++kt) {
                const v4f ar = mfma32(S[kt][1], xi[1], mfma32(S[kt][0], xi[0], mfma32(C[kt][1], xr[1], mfma32(C[kt][0], xr[0], z4))));
                const v4f ai = mfma32(S[kt][1], n1, mfma32(S[kt][0], n0, mfma32(C[kt][1], xi[1], mfma32(C[kt][0], xi[0], z4))));
                float wr[4], wi[4];
#pragma unroll
                for (int r = 0; r < 4; ++r) { wr[r] = __uint_as_float((unsigned)fr[kt * 4 + r] << 16); wi[r] = __uint_as_float((unsigned)fi[kt * 4 + r] << 16); }
                vr[kt] = (v4f){ar.x * wr[0] - ai.x * wi[0], ar.y * wr[1] - ai.y * wi[1], ar.z * wr[2] - ai.z * wi[2], ar.w * wr[3] - ai.w * wi[3]};
                vi[kt] = (v4f){ar.x * wi[0] + ai.x * wr[0], ar.y * wi[1] + ai.y * wr[1], ar.z * wi[2] + ai.z * wr[2], ar.w * wi[3] + ai.w * wr[3]};
            }
            const v4u pr0 = pk8(vr[0], vr[1]), pr1 = pk8(vr[2], vr[3]), pi0 = pk8(vi[0], vi[1]), pi1 = pk8(vi[2], vi[3]), ni0 = neg8(pi0), ni1 = neg8(pi1);
#pragma unroll
            for (int nt = 0; nt < 4; ++nt) {
                const v4f rr = mfma32(ni1, S[nt][1], mfma32(ni0, S[nt][0], mfma32(pr1, C[nt][1], mfma32(pr0, C[nt][0], z4))));
                const v4f ri = mfma32(pr1, S[nt][1], mfma32(pr0, S[nt][0], mfma32(pi1, C[nt][1], mfma32(pi0, C[nt][0], z4))));
                const v2u qr = {pk2(rr.x, rr.y), pk2(rr.z, rr.w)}, qi = {pk2(ri.x, ri.y), pk2(ri.z, ri.w)};
                if (t == 0) { hr[nt] = qr; hi[nt] = qi; }
                else { char* o = lds + 32768 + (nt * 16 + rho) * 528 + wave * 64 + g * 16;
                    *(v4u*)o = (v4u){hr[nt].x, hr[nt].y, qr.x, qr.y}; *(v4u*)(o + 256) = (v4u){hi[nt].x, hi[nt].y, qi.x, qi.y}; }
            }
        }
    }
    BAR();
#pragma unroll
    for (int k = 0; k < 4; ++k) { const int n2 = wave * 8 + k * 2 + (lane >> 5);
        st16s(rs, OFF_Z2 + (n2 * 32 + (job >> 1)) * 1024 + (g & 1) * 512 + (job & 1) * 256 + rho * 16, 0, lds16(lds, 32768 + n2 * 528 + (lane & 31) * 16)); }
    STAMP(job == 0 && wave == 1, 4, 5);
    if (!fft_sync(p, c, FL_FB)) return;
    STAMP(job == 0 && wave == 1, 4, 6);
    {
        char* L = lds + wave * 4096;
#pragma unroll
        for (int pc = 0; pc < 4; ++pc) dma16s(rs, OFF_Z2 + (job * 32 + (pc & 1) * 16 + (lane >> 2)) * 1024 + (pc >> 1) * 512 + wave * 64 + (lane & 3) * 16, 0, wave * 4096 + pc * 1024);
        v4u C[2], S[2];
        { int n[8];
#pragma unroll
          for (int j = 0; j < 8; ++j) n[j] = 8 * g + j;
          twid(64 * rho + job, n, 2047, 1.f / 2048.f, C[0], S[0]); twid(64 * (16 + rho) + job, n, 2047, 1.f / 2048.f, C[1], S[1]); }
        VMWAIT(0);
        v4f rr[2][2], ri[2][2];
#pragma unroll
        for (int t = 0; t < 2; ++t) {
            const int a = (8 * g + (rho >> 2)) * 64 + (rho & 3) * 16 + t * 8;
            const v2u a0 = ldstr(L, a), a1 = ldstr(L, a + 256), b0 = ldstr(L, a + 2048), b1 = ldstr(L, a + 2304);
            const v4u xr = {a0.x, a0.y, a1.x, a1.y}, xi = {b0.x, b0.y, b1.x, b1.y}, ni = neg8(xi);
#pragma unroll
            for (int nt = 0; nt < 2; ++nt) { rr[nt][t] = mfma32(ni, S[nt], mfma32(xr, C[nt], z4)); ri[nt][t] = mfma32(xr, S[nt], mfma32(xi, C[nt], z4)); }
        }
        char* R = lds + 32768;
#pragma unroll
        for (int nt = 0; nt < 2; ++nt) { *(v4u*)(R + (nt * 16 + wave) * 1024 + lane * 16) = pk8(rr[nt][0], rr[nt][1]); *(v4u*)(R + (nt * 16 + 8 + wave) * 1024 + lane * 16) = pk8(ri[nt][0], ri[nt][1]); }
        BAR();
#pragma unroll 1
        for (int Q = wave; Q < 14; Q += 8) {
            v4f y[2][2] = {{z4, z4}, {z4, z4}};
#pragma unroll
            for (int hf = 0; hf < 2; ++hf) {
                v4u f0[8], f1[8];
#pragma unroll
                for (int k = 0; k < 8; ++k) { const int fo = OFF_FI + (Q * 2 + (rho >> 3)) * 16384 + g * 256 + (pperm(rho, 0) & 15) * 16; f0[k] = ld16(rs, fo, (hf * 8 + k) * 1024, 0); f1[k] = ld16(rs, fo + 64, (hf * 8 + k) * 1024, 0); }
                SCHED();
#pragma unroll
                for (int k = 0; k < 8; ++k) { const v4u b0 = lds16(R, (hf * 8 + k) * 1024 + lane * 16), b1 = lds16(R, (16 + hf * 8 + k) * 1024 + lane * 16);
                    y[0][0] = mfma32(f0[k], b0, y[0][0]); y[0][1] = mfma32(f1[k], b0, y[0][1]); y[1][0] = mfma32(f0[k], b1, y[1][0]); y[1][1] = mfma32(f1[k], b1, y[1][1]); }
            }
#pragma unroll
            for (int nt = 0; nt < 2; ++nt) *(v4u*)(p.out + (size_t)(64 * (nt * 16 + rho) + job) * 896 + Q * 32 + g * 8) = pk8(y[nt][0], y[nt][1]);
        }
    }
}

extern "C" __global__ void __launch_bounds__(512, 2) k_main(Params p) {
    extern __shared__ __attribute__((aligned(16))) char lds[];
    Ctx c; c.rs = mkrsrc(p.ws, WS_BYTES); c.lds = lds; c.lane = threadIdx.x & 63; c.wave = __builtin_amdgcn_readfirstlane(threadIdx.x >> 6); c.rho = c.lane & 15; c.g = c.lane >> 4;
    { const int lane = c.lane; (void)lane; LSTAMP(blockIdx.x == 0 && c.wave == 7, 4, 0); }
    const int rot = *(const int*)(p.ws + flagoff(FL_ROT));
    const int x = (blockIdx.x + rot) & 7, y = blockIdx.x >> 3;
    if (x < 2) role_p(p, c, x * 32 + y);
    else if (x == 2) { if (y < 28) role_o(p, c, y); }
    else if (x == 3 && y == 28) role_cal(p, c);
    else {
        const int wi = (x - 4) * 32 + y - 16;
        if (x == 3) { if (y < 28) make_tables(p, c, y); } else if (wi >= 0) role_w(p, c, wi);
        if (wi >= 48) role_fft(p, c, wi - 48);
        else if (x == 3 ? y < 28 : wi >= 16 && wi < 44) role_f2(p, c, x == 3 ? y : wi - 16, x != 3);
        else if (x == 4) role_f1(p, c, y & 15, wi >= 0);
    }
    { const int lane = c.lane; (void)lane; LSTAMP(c.wave == 7, 8 + (blockIdx.x >> 6), blockIdx.x & 63); }
}

extern "C" void kernel_launch(void* const* d_in, const int* in_sizes, int n_in, void* d_out, int out_size, void* d_ws, size_t ws_size, hipStream_t stream) {
    Params p;
    p.xa = (const unsigned short*)d_in[0]; p.wr = (const unsigned short*)d_in[1]; p.wi = (const unsigned short*)d_in[2];
    p.xb = (const unsigned short*)d_in[3]; p.mem0 = (const unsigned short*)d_in[4];
    p.wq = (const unsigned short*)d_in[5]; p.wk = (const unsigned short*)d_in[6]; p.wv = (const unsigned short*)d_in[7]; p.wo = (const unsigned short*)d_in[8];
    p.out = (unsigned short*)d_out;
    p.ws = (char*)d_ws + (((size_t)2 << 20) - ((uintptr_t)d_ws & (((size_t)2 << 20) - 1))) % ((size_t)2 << 20);
    (void)hipFuncSetAttribute((const void*)k_main, hipFuncAttributeMaxDynamicSharedMemorySize, LDS_MAIN);
    (void)hipFuncSetAttribute((const void*)k_pro, hipFuncAttributeMaxDynamicSharedMemorySize, LDS_PRO);
    hipLaunchKernelGGL(k_pro, dim3(256), dim3(512), LDS_PRO, stream, p);
    hipLaunchKernelGGL(k_main, dim3(256), dim3(512), LDS_MAIN, stream, p);
}
```

```cpp
#include <hip/hip_runtime.h>
#include <hip/hip_bf16.h>
#include <stdint.h>

typedef unsigned v4u __attribute__((ext_vector_type(4)));
typedef unsigned v2u __attribute__((ext_vector_type(2)));
typedef float v4f __attribute__((ext_vector_type(4)));
typedef float v2f __attribute__((ext_vector_type(2)));
typedef short v4s __attribute__((ext_vector_type(4)));
typedef __bf16 v2bf __attribute__((ext_vector_type(2)));
typedef __bf16 v8bf __attribute__((ext_vector_type(8)));
typedef __bf16 v16bf __attribute__((ext_vector_type(16)));
typedef unsigned v8u __attribute__((ext_vector_type(8)));
typedef __amdgpu_buffer_rsrc_t rsrc_t;
#define LAS __attribute__((address_space(3)))
#define DEV __device__ __forceinline__

constexpr int NSEG = 64, NH = 16, DM = 448;
constexpr unsigned SENT = 0xFFFFFFFFu;
constexpr int OOB = 0x7fffffff;
constexpr int SC1 = 16;
constexpr int OFF_HO = 0;
constexpr int OFF_CTL = 4 << 20;
constexpr int OFF_AP = OFF_CTL + 131072;
constexpr int OFF_BT = OFF_AP + NH * DM * DM * 2;
constexpr int OFF_G = OFF_BT + NH * DM * DM * 2;
constexpr int OFF_U = OFF_G + NSEG * NH * 28672;
constexpr int OFF_T = OFF_U + NSEG * NH * 28672;
constexpr int OFF_V = OFF_T + NSEG * NH * 7168;
constexpr int OFF_PX = OFF_V + NSEG * NH * 7168;
constexpr int OFF_PM = OFF_PX + NSEG * NH * 2048;
constexpr int OFF_FD = OFF_PM + NSEG * NH * 512;
constexpr int OFF_FI = OFF_FD + 512 * DM * 2;
constexpr int OFF_Z1 = OFF_FI + 512 * DM * 2;
constexpr int OFF_Z2 = OFF_Z1 + (2 << 20);
constexpr int OFF_XF = OFF_Z2 + (2 << 20);
constexpr int OFF_G0 = OFF_XF + (2 << 20);
constexpr int OFF_DBG = OFF_G0 + (1 << 20);
constexpr int WS_BYTES = OFF_DBG + (1 << 20);
constexpr int HO_MEM = 512, HO_PART = 16384, HO_E = 32768;
constexpr int OFF_PROBE = OFF_HO + 49152;
constexpr int FL_W = 0, FL_T = 112, FL_V = 176, FL_P = 208, FL_ROT = 336, FL_TAB = 337, FL_FA = 338, FL_FB = 339, FL_ABORT = 340, FL_CAL = 341, FL_P2 = 344;
constexpr int LDS_MAIN = 114688 + 2048;
constexpr int LDS_PRO = 143360;
constexpr int SPIN_CAP = 1 << 17;

struct Params {
    const unsigned short* xa; const unsigned short* wr; const unsigned short* wi;
    const unsigned short* xb; const unsigned short* mem0;
    const unsigned short* wq; const unsigned short* wk; const unsigned short* wv; const unsigned short* wo;
    unsigned short* out; char* ws;
};

DEV unsigned pk2(float a, float b) { v2bf r = {(__bf16)a, (__bf16)b}; return __builtin_bit_cast(unsigned, r); }
DEV v4u pk8(v4f a, v4f b) { return (v4u){pk2(a.x, a.y), pk2(a.z, a.w), pk2(b.x, b.y), pk2(b.z, b.w)}; }
DEV v4f mfma32(v4u a, v4u b, v4f c) { return __builtin_amdgcn_mfma_f32_16x16x32_bf16(__builtin_bit_cast(v8bf, a), __builtin_bit_cast(v8bf, b), c, 0, 0, 0); }
DEV v4f mfma16(v2u a, v2u b, v4f c) { return __builtin_amdgcn_mfma_f32_16x16x16bf16_1k(__builtin_bit_cast(v4s, a), __builtin_bit_cast(v4s, b), c, 0, 0, 0); }
DEV v4f smf(v4u a, v4u b0, v4u b1, v4f c, int idx) {
    v8u b = {b0.x, b0.y, b0.z, b0.w, b1.x, b1.y, b1.z, b1.w};
    return __builtin_amdgcn_smfmac_f32_16x16x64_bf16(__builtin_bit_cast(v8bf, a), __builtin_bit_cast(v16bf, b), c, idx, 0, 0);
}
DEV float fmx(float a, float b) { return __builtin_elementwise_maximum(a, b); }
DEV float max_g(float s) {
    auto r = __builtin_amdgcn_permlane16_swap(__float_as_uint(s), __float_as_uint(s), false, false);
    s = fmx(__uint_as_float(r[0]), __uint_as_float(r[1]));
    auto q = __builtin_amdgcn_permlane32_swap(__float_as_uint(s), __float_as_uint(s), false, false);
    return fmx(__uint_as_float(q[0]), __uint_as_float(q[1]));
}
DEV float sum_g(float s) {
    auto r = __builtin_amdgcn_permlane16_swap(__float_as_uint(s), __float_as_uint(s), false, false);
    s = __uint_as_float(r[0]) + __uint_as_float(r[1]);
    auto q = __builtin_amdgcn_permlane32_swap(__float_as_uint(s), __float_as_uint(s), false, false);
    return __uint_as_float(q[0]) + __uint_as_float(q[1]);
}
DEV float ror8(float s) { return __int_as_float(__builtin_amdgcn_mov_dpp(__float_as_int(s), 0x128, 0xf, 0xf, true)); }
template <int CTRL> DEV unsigned dppu(unsigned x) { return (unsigned)__builtin_amdgcn_mov_dpp((int)x, CTRL, 0xf, 0xf, true); }
DEV v4u quad4(unsigned x) { return (v4u){x, dppu<0x55>(x), dppu<0xAA>(x), dppu<0xFF>(x)}; }
DEV unsigned umax4(v4u v) { return max(max(v.x, v.y), max(v.z, v.w)); }
DEV rsrc_t mkrsrc(const void* p, unsigned bytes) { return __builtin_amdgcn_make_buffer_rsrc((void*)p, 0, bytes, 0x00020000); }
DEV v4u ld16(rsrc_t r, int vo, int so, int aux) { return aux ? __builtin_amdgcn_raw_buffer_load_b128(r, vo, so, SC1) : __builtin_amdgcn_raw_buffer_load_b128(r, vo, so, 0); }
DEV v2u ld8s(rsrc_t r, int vo, int so) { return __builtin_amdgcn_raw_buffer_load_b64(r, vo, so, SC1); }
DEV unsigned ld4s(rsrc_t r, int vo, int so) { return __builtin_amdgcn_raw_buffer_load_b32(r, vo, so, SC1); }
DEV void st16s(rsrc_t r, int vo, int so, v4u v) {
    __builtin_amdgcn_raw_buffer_store_b128(v, r, vo, so, SC1); asm volatile("s_nop 1" :: "v"(v) : "memory");
}
DEV void st8s(rsrc_t r, int vo, int so, v2u v) { __builtin_amdgcn_raw_buffer_store_b64(v, r, vo, so, SC1); }
DEV void st4s(rsrc_t r, int vo, int so, unsigned v) { __builtin_amdgcn_raw_buffer_store_b32(v, r, vo, so, SC1); }
DEV void dma16(rsrc_t r, int vo, int so, int ldsaddr) {
    unsigned keep;
    asm volatile("s_mov_b32 %0, m0\n\ts_mov_b32 m0, %4\n\ts_nop 4\n\tbuffer_load_dwordx4 %1, %2, %3 offen lds\n\ts_mov_b32 m0, %0"
                 : "=&s"(keep) : "v"(vo), "s"(r), "s"(__builtin_amdgcn_readfirstlane(so)), "s"(__builtin_amdgcn_readfirstlane(ldsaddr)) : "memory");
}
DEV void dma16s(rsrc_t r, int vo, int so, int ldsaddr) {
    unsigned keep;
    asm volatile("s_mov_b32 %0, m0\n\ts_mov_b32 m0, %4\n\ts_nop 4\n\tbuffer_load_dwordx4 %1, %2, %3 offen sc1 lds\n\ts_mov_b32 m0, %0"
                 : "=&s"(keep) : "v"(vo), "s"(r), "s"(__builtin_amdgcn_readfirstlane(so)), "s"(__builtin_amdgcn_readfirstlane(ldsaddr)) : "memory");
}
#define BAR() asm volatile("s_waitcnt lgkmcnt(0)\n\ts_barrier" ::: "memory")
#define BAR0() asm volatile("s_barrier" ::: "memory")
#define VMWAIT(n) asm volatile("s_waitcnt vmcnt(" #n ")" ::: "memory")
#define FENCE() asm volatile("" ::: "memory")
#define SCHED() __builtin_amdgcn_sched_barrier(0)
DEV v4u lds16(const char* l, int a) { return *(const v4u*)(l + a); }
DEV v2u lds8(const char* l, int a) { return *(const v2u*)(l + a); }
DEV v2u ldstr(const char* l, int a) { v4s r = __builtin_amdgcn_ds_read_tr16_b64_v4i16((LAS v4s*)(l + a)); return __builtin_bit_cast(v2u, r); }
template <int N> DEV void slp() { if constexpr (N > 0) __builtin_amdgcn_s_sleep(N); }
DEV int flagoff(int i) { return OFF_CTL + i * 128; }
template <int SL> DEV bool wait_flags(rsrc_t rs, int vo, unsigned need, unsigned& seen) {
    for (int sp = 0;; ++sp) {
        FENCE();
        seen = ld4s(rs, vo, 0);
        if (__builtin_amdgcn_ballot_w64(seen < need) == 0) return true;
        slp<SL>();
        if ((sp & 255) == 255) {
            if (sp > SPIN_CAP) st4s(rs, flagoff(FL_ABORT), 0, 1u);
            if (sp > SPIN_CAP || __builtin_amdgcn_readfirstlane(ld4s(rs, flagoff(FL_ABORT), 0))) return false;
        }
    }
}
DEV int img(int p) { return ((p & 7) >> 1) * 4 + (p >> 3) * 2 + (p & 1); }
DEV int pperm(int rho, int tau) { return (rho >> 2) * 8 + tau * 4 + (rho & 3); }

#define STAMP(cond, slot, s)
#define LSTAMP(cond, slot, s)
#define ACC_DECL
#define ACC_T(k)
#define ACC_OUT(slot)
#define CSTAMP(cond, slot, s)

template <int which> DEV void pro_tile(const Params& p, char* lds, rsrc_t rs, int b, int h, int b0, int c0, int lane, int wave) {
    const int rho = lane & 15, g = lane >> 4;
    const rsrc_t ra = mkrsrc(which ? p.wv : p.wk, DM * 4096 * 2), rb = mkrsrc(which ? p.wo : p.wq, DM * 4096 * 2);
    int sa[4], sb[2];
#pragma unroll
    for (int k = 0; k < 4; ++k) { const int r = (k & 1) * 8 + (lane >> 3); sa[k] = ((b0 + 32 * wave + pperm(r, k >> 1)) * 4096 + h * 256 + ((lane & 7) ^ (r >> 1)) * 8) * 2; }
#pragma unroll
    for (int k = 0; k < 2; ++k) { const int r = k * 8 + (lane >> 3), ch = (2 * wave + k) * 64 + lane, e = ch / 14;
        sb[k] = which ? ((h * 256 + e) * DM + c0 + (ch - e * 14) * 8) * 2 : ((c0 + wave * 16 + r) * 4096 + h * 256 + ((lane & 7) ^ (r >> 1)) * 8) * 2; }
    const int bstep = which ? 64 * DM * 2 : 128, la = 57344 + wave * 4096;
    v4u a3[4];
#pragma unroll
    for (int c = 0; c < 4; ++c) {
#pragma unroll
        for (int k = 0; k < 2; ++k) dma16(rb, sb[k], c * bstep, c * 14336 + (2 * wave + k) * 1024);
#pragma unroll
        for (int k = 0; k < 4; ++k) { if (c < 3) dma16(ra, sa[k], c * 128, la + c * 28672 + k * 1024); else a3[k] = ld16(ra, sa[k], 384, 0); }
    }
    const int rd = (rho >> 3) * 1024 + (rho & 7) * 128, rx0 = (g ^ (rho >> 1)) * 16, rx1 = ((4 + g) ^ (rho >> 1)) * 16;
    const int tra = (8 * g + (rho >> 2)) * 224 + (rho & 3) * 8;
    v4f acc[7][2];
#pragma unroll
    for (int ct = 0; ct < 7; ++ct) { acc[ct][0] = (v4f){0, 0, 0, 0}; acc[ct][1] = acc[ct][0]; }
    const float sc = which ? 1.f : 0.09016844005556021f;
    const int obase = (which ? OFF_BT : OFF_AP) + ((h * 28 + (c0 >> 4)) * 14 + (b0 >> 5) + wave) * 1024 + lane * 16;
    v4u af[2][4], f[2][7][2];
#pragma unroll
    for (int c = 0; c <= 4; ++c) {
        if (c < 4) {
            if (c == 0) VMWAIT(18); else if (c == 1) VMWAIT(12); else if (c == 2) VMWAIT(6); else VMWAIT(0);
            if (c == 3) {
#pragma unroll
                for (int k = 0; k < 4; ++k) *(v4u*)(lds + la + k * 1024 + lane * 16) = a3[k];
            }
            BAR();
            if (c == 3) { STAMP(b == 0 && wave == 0, 4, 9); STAMP(wave == 0 && b < 64, 30, b); }
        }
        const char* A = lds + la + (c % 3) * 28672; const char* B = lds + c * 14336;
        if (c < 4) { af[c & 1][0] = lds16(A, rd + rx0); af[c & 1][1] = lds16(A, rd + rx1); af[c & 1][2] = lds16(A, 2048 + rd + rx0); af[c & 1][3] = lds16(A, 2048 + rd + rx1); }
#pragma unroll
        for (int ct = 0; ct < 7; ++ct) {
            if (c < 4) {
                if (which == 0) { f[c & 1][ct][0] = lds16(B, ct * 2048 + rd + rx0); f[c & 1][ct][1] = lds16(B, ct * 2048 + rd + rx1); }
                else {
#pragma unroll
                    for (int kk = 0; kk < 2; ++kk) { const v2u x = ldstr(B, tra + kk * 7168 + ct * 32), y = ldstr(B, tra + kk * 7168 + 896 + ct * 32); f[c & 1][ct][kk] = (v4u){x.x, x.y, y.x, y.y}; }
                }
            }
            SCHED();
            if (c > 0) { const int q = (c - 1) & 1;
                acc[ct][0] = mfma32(af[q][1], f[q][ct][1], mfma32(af[q][0], f[q][ct][0], acc[ct][0])); acc[ct][1] = mfma32(af[q][3], f[q][ct][1], mfma32(af[q][2], f[q][ct][0], acc[ct][1])); }
            if (c == 4) st16s(rs, obase, ct * 14336, pk8(acc[ct][0] * sc, acc[ct][1] * sc));
            SCHED();
        }
    }
    STAMP(b == 0 && wave == 0, 4, 10);
    STAMP(wave == 0 && b >= 192, 31, b & 63);
}
extern "C" __global__ void __launch_bounds__(512, 2) k_pro(Params p) {
    extern __shared__ __attribute__((aligned(16))) char lds[];
    const int b = blockIdx.x, tid = threadIdx.x, lane = tid & 63, wave = __builtin_amdgcn_readfirstlane(tid >> 6);
    const int rho = lane & 15, g = lane >> 4;
    const int gemm = (b & 7) * 4 + (b >> 6), sub = (b >> 3) & 7, which = gemm & 1, h = gemm >> 1;
    const int b0 = 224 * (sub & 1), c0 = 112 * (sub >> 1);
    const rsrc_t rs = mkrsrc(p.ws, WS_BYTES);
    LSTAMP(b == 0 && wave == 0, 4, 8); STAMP(wave == 0 && b < 64, 29, b);
    if (wave == 7) {
        const rsrc_t rx = mkrsrc(p.xb, 2048 * DM * 2);
        v4u xf[7], m0[7];
#pragma unroll
        for (int k = 0; k < 7; ++k) { const int pc = b * 7 + k, rt = pc / 14, ks = pc - rt * 14; xf[k] = ld16(rx, ((rt * 16 + (rho ^ ((rt & 1) * 4))) * DM + ks * 32 + g * 8) * 2, 0, 0); }
        const v4u sent = {SENT, SENT, SENT, SENT};
        if (b == 0) {
#pragma unroll
            for (int k = 0; k < 7; ++k) { const int c = k * 64 + lane; const unsigned* m = (const unsigned*)(p.mem0 + ((c >> 1) & 7) * DM + (c >> 4) * 16 + (c & 1) * 2); m0[k] = (v4u){m[0], m[2], m[4], m[6]}; }
        } else {
#pragma unroll
            for (int k = 0; k < 7; ++k) m0[k] = sent;
        }
        SCHED();
        BAR0(); BAR0(); BAR0(); BAR0();
        const unsigned rot = ((unsigned)__builtin_amdgcn_s_getreg(20 | (3 << 11)) - b) & 7u;
        if (lane < 32) st16s(rs, OFF_CTL + b * 512 + lane * 16, 0, (v4u){b * 4 == FL_ROT && lane == 0 ? rot : 0u, 0u, 0u, 0u});
#pragma unroll
        for (int k = 0; k < 16; ++k) {
            const int o = ((k & 7) * 1024 + lane * 16 + HO_MEM) & 8191;
            st16s(rs, (b & 3) == 3 && (b & 127) != 3 ? OOB : OFF_HO + b * 16384 + (k >> 3) * 8192 + o, 0, (k & 7) < 7 ? m0[k & 7] : sent);
        }
#pragma unroll
        for (int k = 62; k < 64; ++k)
            st16s(rs, (b < 112 ? OFF_T + k * NH * 7168 + b * 1024 : b < 144 ? OFF_PX + k * NH * 2048 + (b - 112) * 1024 : b < 152 ? OFF_PM + k * NH * 512 + (b - 144) * 1024 : OOB - 1024) + lane * 16, 0, sent);
#pragma unroll
        for (int k = 0; k < 4; ++k) st16s(rs, OFF_G0 + (b * 4 + k) * 1024 + lane * 16, 0, sent);
#pragma unroll
        for (int k = 0; k < 7; ++k) st16s(rs, OFF_XF + (b * 7 + k) * 1024 + lane * 16, 0, xf[k]);
        return;
    }
    if (which) pro_tile<1>(p, lds, rs, b, h, b0, c0, lane, wave); else pro_tile<0>(p, lds, rs, b, h, b0, c0, lane, wave);
}

struct Ctx { rsrc_t rs; char* lds; int lane, wave, rho, g; };

DEV int slot_base(int s, int cal) { return OFF_HO + s * 65536 + (((cal >> (s >> 5)) & 1) << 13); }
#define STAGE_HEAD "s_add_i32 %[bud], %[bud], -1\n\ts_cmp_lt_i32 %[bud], 0\n\ts_cselect_b32 vcc_hi, -1, -2\n\tv_mov_b32 v233, vcc_hi\n\t"
#define STAGE_TAIL "v_cmp_gt_u32 vcc, v232, v233\n\ts_cbranch_vccnz 1b\n\ts_waitcnt lgkmcnt(0)\n\ts_barrier"
#define STAGE_CLOB "memory", "vcc", "scc", "v224", "v225", "v226", "v227", "v228", "v229", "v230", "v231", "v232", "v233"
DEV void stage1(rsrc_t rs, int vo, int so, int& budget) {
    asm volatile("1:\n\tbuffer_load_dwordx4 v[224:227], %[vo], %[rs], %[so] offen sc1\n\t" STAGE_HEAD
                 "s_waitcnt vmcnt(0)\n\tds_write_b128 %[vo], v[224:227]\n\tv_max3_u32 v232, v224, v225, v226\n\tv_max_u32 v232, v232, v227\n\t" STAGE_TAIL
                 : [bud] "+s"(budget) : [vo] "v"(vo), [rs] "s"(rs), [so] "s"(so) : STAGE_CLOB);
}
DEV void stage2(rsrc_t rs, int vo, int vo2, int so, int& budget) {
    asm volatile("1:\n\tbuffer_load_dwordx4 v[224:227], %[vo], %[rs], %[so] offen sc1\n\tbuffer_load_dwordx4 v[228:231], %[vo2], %[rs], %[so] offen sc1\n\t" STAGE_HEAD
                 "s_waitcnt vmcnt(1)\n\tds_write_b128 %[vo], v[224:227]\n\tv_max3_u32 v232, v224, v225, v226\n\t"
                 "s_waitcnt vmcnt(0)\n\tds_write_b128 %[vo2], v[228:231]\n\tv_max3_u32 v232, v232, v227, v228\n\tv_max3_u32 v232, v232, v229, v230\n\tv_max_u32 v232, v232, v231\n\t" STAGE_TAIL
                 : [bud] "+s"(budget) : [vo] "v"(vo), [vo2] "v"(vo2), [rs] "s"(rs), [so] "s"(so) : STAGE_CLOB);
}
DEV void give_up(rsrc_t rs) { st4s(rs, flagoff(FL_ABORT), 0, 1u); }
#define PIN1(a) asm volatile("" : "+v"(a))
#define PIN2(a, b) asm volatile("" : "+v"(a), "+v"(b))
constexpr int CAL_STEP = 8;
#define TOUCH_LEAD 3
DEV void role_cal(const Params& p, const Ctx& c) {
    if (c.wave >= 2) return;
    unsigned best[2] = {0xffffffffu, 0xffffffffu};
    for (int r = 0; r < 20; ++r) {
#pragma unroll
        for (int k = 0; k < 2; ++k) {
            const int off = OFF_PROBE + c.wave * (2 << 20) + k * 8192 + r * 256;
            unsigned long long t0, t1; unsigned v;
            asm volatile("s_memtime %0\n\ts_waitcnt lgkmcnt(0)\n\tbuffer_load_dword %2, %3, %4, 0 offen sc1\n\ts_waitcnt vmcnt(0)\n\ts_memtime %1\n\ts_waitcnt lgkmcnt(0)"
                         : "=&s"(t0), "=&s"(t1), "=&v"(v) : "v"(off), "s"(c.rs) : "memory");
            best[k] = min(best[k], (unsigned)(t1 - t0));
        }
        if (r == 5 && c.wave == 0) { if (c.lane == 0) st4s(c.rs, flagoff(FL_CAL), 0, best[1] < best[0] ? 3u : 2u); VMWAIT(0); }
    }
    if (c.lane == 0) st4s(c.rs, flagoff(FL_CAL + 1 + c.wave), 0, best[1] < best[0] ? 3u : 2u);
    { const int k = c.lane & 3, o = (k == 0 ? 0 : k == 1 ? 7936 : 49152 + (k & 1) * 4096 + 2048) + (best[1] < best[0] ? 8192 : 0);
      for (int s = c.wave ? 32 : 12; s < 32 * (c.wave + 1); s += 4) {
          unsigned seen; if (!wait_flags<16>(c.rs, flagoff(FL_V), s - TOUCH_LEAD, seen)) return;
          unsigned v = ld4s(c.rs, OFF_HO + s * 65536 + o, 0); asm volatile("" :: "v"(v));
      } }
}
DEV void touch_window(rsrc_t rs) { const unsigned v = ld4s(rs, OFF_PROBE + (2 << 20) + 8064, 0); asm volatile("" :: "v"(v)); }
DEV int get_cal(const Ctx& c, int late, int& budget) {
    unsigned seen;
    if (!wait_flags<2>(c.rs, flagoff(FL_CAL + late + (late & c.lane)), 1u, seen)) budget = -1;
    return (__builtin_amdgcn_readlane(seen, 0) & 1) | ((__builtin_amdgcn_readlane(seen, 1) & 1) << 1);
}
DEV unsigned now() { return (unsigned)__builtin_amdgcn_s_memtime(); }
DEV void wait_until(unsigned t) {
    int rem = (int)(t - now());
    while (rem > 260) { __builtin_amdgcn_s_sleep(1); rem = (int)(t - now()); }
    rem -= 60;
    asm volatile("1:\n\ts_add_i32 %0, %0, -40\n\ts_cmp_gt_i32 %0, 0\n\ts_cbranch_scc1 1b" : "+s"(rem) :: "scc");
}
#define T_PM 2025
#define T_OS 2175
#define T_OM 400
#define T_OU 750
#define T_G0 600
#define T_PG 200
#define ABL(b) true
#define S_AT 6

#define MEM_TIMES_W(lbase)                                                                                           \
    v4u a_[7];                                                                                                       \
    _Pragma("unroll") for (int f = 0; f < 7; ++f) a_[f] = lds16(lds, (lbase) + f * 1024);                            \
    EXTRA_READS;                                                                                                     \
    SCHED();                                                                                                         \
    _Pragma("unroll") for (int f = 0; f < 7; ++f) { c0 = smf(a_[f], w[0][2 * f], w[0][2 * f + 1], c0, sidx); c1 = smf(a_[f], w[1][2 * f], w[1][2 * f + 1], c1, sidx); AFTER_STEP(f); } \
    const v2u tp = {pk2(c0.x + c0.z, c0.y + c0.w), pk2(c1.x + c1.z, c1.y + c1.w)};

#define VCLOB "v224", "v225", "v226", "v227", "v228", "v229", "v230", "v231", VCLOB_HI
#define VCLOB_HI "v232", "v233", "v234", "v235", "v236", "v237", "v238", "v239", \
              "v240", "v241", "v242", "v243", "v244", "v245", "v246", "v247", "v248", "v249", "v250", "v251", "v252", "v253", "v254", "v255"
#define PUBLISH_ASM(ST)                                                                                                                     \
    "s_cmp_lg_u32 %[pub], 7\n\ts_waitcnt lgkmcnt(0)\n\ts_barrier\n\ts_cbranch_scc1 9f\n\t"                                                  \
    "ds_read_b64 v[224:225], %[la]\n\tds_read_b64 v[226:227], %[la] offset:" #ST "*1\n\tds_read_b64 v[228:229], %[la] offset:" #ST "*2\n\t" \
    "ds_read_b64 v[230:231], %[la] offset:" #ST "*3\n\tds_read_b64 v[232:233], %[la] offset:" #ST "*4\n\t"                                  \
    "ds_read_b64 v[234:235], %[la] offset:" #ST "*5\n\tds_read_b64 v[236:237], %[la] offset:" #ST "*6\n\t"                                  \
    "ds_read_b64 v[238:239], %[la] offset:" #ST "*7\n\t"                                                                                    \
    "s_waitcnt lgkmcnt(6)\n\tv_pk_add_f32 v[224:225], v[224:225], v[226:227]\n\t"                                                           \
    "s_waitcnt lgkmcnt(4)\n\tv_pk_add_f32 v[228:229], v[228:229], v[230:231]\n\t"                                                           \
    "s_waitcnt lgkmcnt(2)\n\tv_pk_add_f32 v[232:233], v[232:233], v[234:235]\n\tv_pk_add_f32 v[224:225], v[224:225], v[228:229]\n\t"        \
    "s_waitcnt lgkmcnt(0)\n\tv_pk_add_f32 v[236:237], v[236:237], v[238:239]\n\tv_pk_add_f32 v[224:225], v[224:225], v[232:233]\n\t"        \
    "s_nop 0\n\tv_pk_add_f32 v[224:225], v[224:225], v[236:237]\n\t"                                                                        \
    "s_nop 0\n\tv_cvt_pk_bf16_f32 v224, v224, v225\n\ts_nop 1\n\t"                                                                          \
    "v_mov_b32_dpp v225, v224 quad_perm:[1,1,1,1] row_mask:0xf bank_mask:0xf\n\t"                                                           \
    "v_mov_b32_dpp v226, v224 quad_perm:[2,2,2,2] row_mask:0xf bank_mask:0xf\n\t"                                                           \
    "v_mov_b32_dpp v227, v224 quad_perm:[3,3,3,3] row_mask:0xf bank_mask:0xf\n\t"                                                           \
    "buffer_store_dwordx4 v[224:227], %[vo], %[rs], %[so] offen sc1\n\t"                                                                    \
    "9:\n\ts_barrier\n\ts_nop 0"

DEV void role_p(const Params& p, const Ctx& c, int idx) {
    const int h = idx >> 2, q = idx & 3, lane = c.lane, wave = c.wave, rho = c.rho, g = c.g;
    const rsrc_t rs = c.rs; char* lds = c.lds;
    const int om = q * 4 + wave;
    const bool isT = wave < 4 && om < 14, isX = wave < 4 && om >= 14;
    constexpr int L_MEM = 0, L_PART = 8192, L_DUMP = 10240, L_CAL = 11264, L_X = 16384;
    const int sidx = (rho & 2) ? 0xEEEE : 0x4444;
    const int fa = L_MEM + g * 256 + ((rho >> 2) * 2 + (rho & 1)) * 32 + ((rho >> 1) & 1) * 16;
    v2u sel; { const int r0 = rho - 2 * g; sel.x = (r0 == 0 ? 0x3f80u : 0u) | (r0 == 1 ? 0x3f800000u : 0u); sel.y = (r0 == 8 ? 0x3f80u : 0u) | (r0 == 9 ? 0x3f800000u : 0u); }
    const int piece = q < 3 ? wave : (wave < 2 ? wave : wave - 2);
    const int po = piece * 1024 + lane * 16;
    int budget = SPIN_CAP;
#pragma unroll
    for (int k = 0; k < 7; ++k) dma16(rs, wave < 4 || ABL(256) ? OFF_XF + lane * 16 : OOB, (wave * 7 + k) * 1024, L_X + (wave * 7 + k) * 1024);
    if (isT) {
        v4u w[2][14];
#pragma unroll
        for (int t = 0; t < 2; ++t)
#pragma unroll
            for (int ks = 0; ks < 14; ++ks) w[t][ks] = ld16(rs, OFF_AP + (h * 28 + 2 * om + t) * 14336 + g * 256 + (ABL(128) ? img(rho) : rho) * 16, ks * 1024, 0);
        VMWAIT(28);
        BAR0();
        STAMP(wave == 0, 23, idx);
#pragma unroll 1
        for (int sg = 0; sg < 2; ++sg) {
            v4f a[2][2] = {{{0, 0, 0, 0}, {0, 0, 0, 0}}, {{0, 0, 0, 0}, {0, 0, 0, 0}}};
#pragma unroll
            for (int ks = 0; ks < 14; ++ks)
#pragma unroll
                for (int kt = 0; kt < 2; ++kt) { const v4u x = lds16(lds, L_X + sg * 28672 + kt * 14336 + ks * 1024 + g * 256 + (rho ^ (kt * 4)) * 16);
                    a[kt][0] = mfma32(w[0][ks], x, a[kt][0]); a[kt][1] = mfma32(w[1][ks], x, a[kt][1]); }
#pragma unroll
            for (int kt = 0; kt < 2; ++kt) {
                auto x = __builtin_amdgcn_permlane32_swap(pk2(a[kt][0].x, a[kt][0].y), pk2(a[kt][1].x, a[kt][1].y), false, false);
                auto y = __builtin_amdgcn_permlane32_swap(pk2(a[kt][0].z, a[kt][0].w), pk2(a[kt][1].z, a[kt][1].w), false, false);
                st16s(rs, OFF_G0 + (h * 14 + om) * 2048 + kt * 1024 + lane * 16, sg * (NH * 28672), (v4u){x[0], x[1], y[0], y[1]});
            }
            BAR0();
            STAMP(wave == 0, 24 + sg, idx);
        }
        int cal = get_cal(c, 0, budget);
        STAMP(wave == 0, 26, idx);
        const int qa = L_MEM + (2 * om + (rho >> 3)) * 256 + (rho & 7) * 32 + g * 8;
        const int pw = (g >> 1) == (rho >> 3) ? L_PART + (2 * wave + (rho >> 3)) * 256 + (rho & 7) * 32 + (g & 1) * 16 : L_DUMP + lane * 16;
        const int tw = lane < 32 ? OFF_T + (h * 14 + om) * 512 + ((rho >> 3) * 2 + g) * 128 + (rho & 7) * 16 : OOB;
#pragma unroll 1
        for (int s = 0; s < NSEG && budget >= 0; ++s) {
            if (s == CAL_STEP) cal = __builtin_amdgcn_readfirstlane(*(const int*)(lds + L_CAL));
            const int sb = slot_base(s, cal);
            v4f c0 = {0, 0, 0, 0}, c1 = {0, 0, 0, 0}; PIN2(c0, c1);
            BAR0();
            stage1(rs, po, sb + HO_MEM, budget);
            v2u mq;
#define EXTRA_READS mq = lds8(lds, qa)
#define AFTER_STEP(f)
            MEM_TIMES_W(fa)
#undef EXTRA_READS
            const v4f d2 = mfma16(tp, sel, (v4f){0, 0, 0, 0});
            const v2u d = {pk2(d2.x, d2.y), pk2(d2.z, d2.w)};
            *(v4f*)(lds + pw) = mfma16(d, mq, (v4f){0, 0, 0, 0});
            BAR();
            BAR0();
            if (s < NSEG - 1) wait_until(now() + T_PG);
            if (ABL(8)) { auto x = __builtin_amdgcn_permlane32_swap(d.x, d.y, false, false);
              auto y = __builtin_amdgcn_permlane32_swap(x[0], x[0], false, false); auto z = __builtin_amdgcn_permlane32_swap(x[1], x[1], false, false);
              st16s(rs, tw, s * (NH * 7168), (v4u){y[0], z[0], y[1], z[1]}); }
        }
    } else if (isX) {
        v4u w[2][14];
        int cal = get_cal(c, 0, budget);
        BAR0(); BAR0();
        if (om == 14) wait_until(now() + T_G0);
        else BAR0();
        const int gl0 = OFF_G + h * 28672 + (rho >> 3) * 1024 + g * 256 + (pperm(rho, 0) & 15) * 16;
        const int gfl = flagoff(FL_W + h * 7 + (lane & 3));
        const int ew = rho < 8 ? HO_E + h * 512 + g * 128 + rho * 16 : OOB, mw = g == 0 ? L_PART + 1024 + (rho & 7) * 32 + (rho >> 3) * 16 : L_DUMP + lane * 16;
        auto load_g = [&](int s, int t) {
            FENCE();
#pragma unroll
            for (int ks = 0; ks < 14; ++ks) w[t][ks] = ld16(rs, gl0 + t * 64, s * (NH * 28672) + (s < 2 ? OFF_G0 - OFF_G : 0) + ks * 2048, 1);
            FENCE();
        };
        bool lack = false;
#pragma unroll 1
        for (int s = om - 14; s < NSEG; s += 2) {
            const int sl = min(s, NSEG - 2);
            if (s >= 1) wait_until(now() + (s == 1 ? T_G0 : T_PG));
            unsigned seen;
            if (lack && !wait_flags<1>(rs, gfl, sl, seen)) budget = -1;
            load_g(sl, 0);
            if (s >= 2) { BAR0(); BAR0(); BAR0(); BAR0(); wait_until(now() + T_PG); }
            load_g(sl, 1);
            seen = ld4s(rs, gfl, 0);
            if (s == 1) { BAR0(); BAR0(); BAR0(); BAR0(); }
            if (s < 2) {
#pragma unroll 1
                for (;;) {
                    unsigned m = 0;
#pragma unroll
                    for (int ks = 0; ks < 14; ++ks) m = max(m, max(umax4(w[0][ks]), umax4(w[1][ks])));
                    if (__builtin_amdgcn_ballot_w64(m == SENT) == 0 || --budget < 0) break;
                    load_g(s, 0); load_g(s, 1);
                }
                if (s == 0) BAR0();
            }
            if ((s | 1) == CAL_STEP + 1) cal = __builtin_amdgcn_readfirstlane(*(const int*)(lds + L_CAL));
            const int sb = slot_base(s, cal);
            v4f c0 = {0, 0, 0, 0}, c1 = {0, 0, 0, 0}; PIN2(c0, c1);
            lack = __builtin_amdgcn_ballot_w64(seen < (unsigned)min(s + 2, NSEG - 2)) != 0;
            SCHED();
            BAR0();
            VMWAIT(0);
            STAMP(idx == 3 && s == 0, 4, 12); STAMP(s == 0, 27, h); STAMP(s == 1, 27, 16 + h);
            BAR();
            if (s < NSEG - 1) {
#define EXTRA_READS
                MEM_TIMES_W(fa)
#undef EXTRA_READS
                const v4f d2 = mfma16(tp, sel, (v4f){0, 0, 0, 0});
                float m = fmx(fmx(d2.x, d2.y), fmx(d2.z, d2.w));
                m = max_g(fmx(m, ror8(m)));
                *(v4f*)(lds + mw) = (v4f){-m, -m, -m, -m};
                SCHED();
                BAR();
                const float e0 = __builtin_amdgcn_exp2f(d2.x - m), e1 = __builtin_amdgcn_exp2f(d2.y - m), e2 = __builtin_amdgcn_exp2f(d2.z - m), e3 = __builtin_amdgcn_exp2f(d2.w - m);
                const unsigned u0 = pk2(e0, e1), u1 = pk2(e2, e3);
                st16s(rs, ew, sb, (v4u){u0, u1, dppu<0x128>(u0), dppu<0x128>(u1)});
            } else BAR0();
            BAR0();
        }
        if (om == 14) { BAR0(); BAR0(); BAR0(); BAR0(); }
    } else {
        if (wave == 4) { *(v4u*)(lds + L_PART + lane * 16) = (v4u){0u, 0u, 0u, 0u}; *(v4u*)(lds + L_PART + 1024 + lane * 16) = (v4u){0u, 0u, 0u, 0u}; }
        BAR();
        int cal = get_cal(c, 0, budget);
        BAR0(); BAR0();
        unsigned tref = now() - 65536;
        STAMP(idx == 0 && wave == 7, 4, 1);
        const bool stg = q == 3 || wave != 7, two = q == 3 && wave == 5;
        const int pr = L_PART + (lane & 31) * 8, pubo = lane < 32 && (lane & 3) == 0 ? HO_PART + idx * 128 + lane * 4 : OOB;
#pragma unroll 1
        for (int s = 0; s < NSEG && budget >= 0; ++s) {
            if (s == CAL_STEP - 1 && wave == 6) *(int*)(lds + L_CAL) = get_cal(c, 1, budget);
            if (s == CAL_STEP) cal = __builtin_amdgcn_readfirstlane(*(const int*)(lds + L_CAL));
            if (s == 30 && wave == 6) touch_window(rs);
            const int sb = slot_base(s, cal);
            if (wave == 7) wait_until(tref + T_PM);
            BAR0();
            if (stg) {
                if (two) stage2(rs, po, 6144 + lane * 16, sb + HO_MEM, budget); else stage1(rs, po, sb + HO_MEM, budget);
            } else BAR0();
            STAMP(idx == 0 && wave == 7, 0, s); STAMP(idx == 3 && wave == 6, 12, s);
            asm volatile(PUBLISH_ASM(256) :: [pub] "s"(wave), [la] "v"(pr), [vo] "v"(pubo), [rs] "s"(rs), [so] "s"(sb) : "memory", "scc", VCLOB);
            tref = now();
            if (wave == 7) {
                STAMP(idx == 0, 1, s);
                LSTAMP(idx == 0 && (s == 1 || s == 32 || s == 62), 14, s);
                if (lane == 0) st4s(rs, flagoff(FL_T + idx), 0, s);
            }
        }
    }
    if (budget < 0) give_up(rs);
    VMWAIT(0);
    BAR();
    if (wave == 7 && lane == 0) st4s(rs, flagoff(FL_T + idx), 0, NSEG);
}

DEV void role_o(const Params& p, const Ctx& c, int t) {
    const int lane = c.lane, wave = c.wave, rho = c.rho, g = c.g;
    const rsrc_t rs = c.rs; char* lds = c.lds;
    constexpr int L_MEM = 0, L_OUT = 8192, L_DUMP = 12288, L_V = 13312, L_CAL = 21504, L_X = 24576;
#pragma unroll
    for (int k = 0; k < 4; ++k) dma16(rs, wave * 4 + k < 28 ? OFF_XF + lane * 16 : OOB, (wave * 4 + k) * 1024, L_X + (wave * 4 + k) * 1024);
    v4u w[2][14];
#pragma unroll
    for (int k = 0; k < 2; ++k)
#pragma unroll
        for (int ks = 0; ks < 14; ++ks) w[k][ks] = ld16(rs, OFF_BT + ((2 * wave + k) * 28 + t) * 14336 + lane * 16, ks * 1024, 0);
    VMWAIT(28);
    BAR0();
    {
        v4u u[2];
#pragma unroll
        for (int k = 0; k < 2; ++k) {
            v4f a0 = {0, 0, 0, 0}, a1 = a0;
#pragma unroll
            for (int ks = 0; ks < 14; ++ks) { const int xo = L_X + (rho >> 3) * 14336 + ks * 1024 + g * 256;
                a0 = mfma32(lds16(lds, xo + ((pperm(rho, 0) & 15) ^ ((rho >> 3) * 4)) * 16), w[k][ks], a0); a1 = mfma32(lds16(lds, xo + ((pperm(rho, 1) & 15) ^ ((rho >> 3) * 4)) * 16), w[k][ks], a1); }
            u[k] = pk8(a0, a1);
        }
        asm volatile("v_mov_b32 v216, %0\n\tv_mov_b32 v217, %1\n\tv_mov_b32 v218, %2\n\tv_mov_b32 v219, %3\n\tv_mov_b32 v220, %4\n\tv_mov_b32 v221, %5\n\tv_mov_b32 v222, %6\n\tv_mov_b32 v223, %7"
                     :: "v"(u[0].x), "v"(u[0].y), "v"(u[0].z), "v"(u[0].w), "v"(u[1].x), "v"(u[1].y), "v"(u[1].z), "v"(u[1].w) : "v216", "v217", "v218", "v219", "v220", "v221", "v222", "v223");
    }
    int budget = SPIN_CAP;
    int cal = get_cal(c, 0, budget), caln = cal;
    const int sidx = (rho & 2) ? 0xEEEE : 0x4444;
    const int fa = L_MEM + g * 256 + ((rho >> 2) * 2 + (rho & 1)) * 32 + ((rho >> 1) & 1) * 16;
    const int hb = rho >> 3, hd = 2 * wave + hb, i = rho & 7;
    const int pa = HO_PART + hd * 512 + g * 128 + i * 16, ea = HO_E + hd * 512 + g * 128 + i * 16;
    const int ul = OFF_U + (t * 16 + 2 * wave) * 1024 + lane * 16;
    const int ufl = flagoff(FL_W + rho * 7 + ((14 + (t >> 1)) >> 2));
    const int mo = (lane & 3) == 0 ? HO_MEM + t * 256 + lane * 4 : OOB;
    const int p2 = (lane & 4) ? 2 * (lane & 3) + 1 : 2 * (lane & 3);
    const int ra = L_OUT + (lane >> 3) * 64 + p2 * 8;
    const int ow = hb == 0 ? L_OUT + wave * 512 + i * 64 + g * 16 : L_DUMP + lane * 16;
    const int po = wave * 1024 + lane * 16, po2 = min(wave + 4, 6) * 1024 + lane * 16;
    const v4u ones = {0x3f803f80u, 0x3f803f80u, 0x3f803f80u, 0x3f803f80u};
    const v2u ones2 = {0x3f803f80u, 0x3f803f80u};
    const unsigned mlo = hb ? 0u : 0xffffffffu, mhi = ~mlo;
    const unsigned long long hbm = 0xff00ff00ff00ff00ull;
    v4u psel; { const int kq = (rho >> 2) * 2 + (rho & 1); const unsigned one = (kq & 1) ? 0x3f800000u : 0x3f80u;
        psel = (v4u){(kq >> 1) == 0 ? one : 0u, (kq >> 1) == 1 ? one : 0u, (kq >> 1) == 2 ? one : 0u, (kq >> 1) == 3 ? one : 0u}; }
    asm volatile("v_mov_b32 v215, 0" ::: "v215");
    auto check_u = [&](int s) {
        unsigned long long lack; unsigned seen;
        asm volatile("v_cmp_gt_u32 %0, %1, v215" : "=s"(lack) : "s"(s) : "memory");
        if (lack != 0 && !wait_flags<1>(rs, ufl, s, seen)) budget = -1;
    };
    auto load_u = [&](int s) {
        asm volatile("s_nop 4\n\tbuffer_load_dwordx4 v[216:219], %0, %1, %2 offen sc1\n\tbuffer_load_dwordx4 v[220:223], %0, %1, %2 offen offset:1024 sc1"
                     :: "v"(ul), "s"(rs), "s"(s * (NH * 28672)) : "memory", "v216", "v217", "v218", "v219", "v220", "v221", "v222", "v223");
    };
    auto store_v = [&](int s) { st16s(rs, OFF_V + t * 4096 + (wave & 3) * 1024 + lane * 16, s * (NH * 7168), lds16(lds, L_V + (s & 1) * 4096 + (wave & 3) * 1024 + lane * 16)); };
    ACC_DECL;
    unsigned tref = now() - 65536;
    auto mem_times_b = [&](int sb, int s, bool last) -> v2u {
        v4f c0 = {0, 0, 0, 0}, c1 = {0, 0, 0, 0};
        if (wave < 4) stage2(rs, po, po2, sb + HO_MEM, budget);
        else BAR0();
        if (last && wave == 7 && lane == 0) st4s(rs, flagoff(FL_V + t), 0, s);
        ACC_T(2);
#undef AFTER_STEP
#define AFTER_STEP(f) if (f == S_AT && !last) { SCHED(); ACC_T(4); if (wave == 0) wait_until(tref + T_OS); ACC_T(5); asm volatile("s_barrier\n\tbuffer_load_dwordx4 v[224:227], %0, %2, %3 offen sc1\n\tbuffer_load_dwordx4 v[228:231], %1, %2, %3 offen sc1" \
                          :: "v"(pa), "v"(ea), "s"(rs), "s"(sb) : "memory", VCLOB); SCHED(); }
#define EXTRA_READS if (wave < 4 && !last && s > 0 && ABL(1)) load_u(s)
        MEM_TIMES_W(fa)
#undef EXTRA_READS
        *(v2u*)(lds + L_V + (s & 1) * 4096 + wave * 512 + lane * 8) = tp;
        return tp;
    };
    STAMP(t == 0 && wave == 0, 4, 13); STAMP(wave == 0, 28, t); STAMP(wave == 7, 28, 32 + t);
    STAMP(t == 0 && wave == 0, 4, 14);
#pragma unroll 1
    for (int s = 0; s < NSEG - 1 && budget >= 0; ++s) {
        const int sb = slot_base(s, s < CAL_STEP ? cal : caln), sn = slot_base(s + 1, s + 1 < CAL_STEP ? cal : caln);
        ACC_T(0);
        const v2u vp = mem_times_b(sb, s, false);
        ACC_T(3);
        asm volatile(
            "s_branch 3f\n\t"
            "1:\n\t"
            "buffer_load_dwordx4 v[224:227], %[pa], %[rs], %[sb] offen sc1\n\t"
            "buffer_load_dwordx4 v[228:231], %[ea], %[rs], %[sb] offen sc1\n\t"
            "3:\n\t"
            "s_add_i32 %[bud], %[bud], -1\n\ts_cmp_lt_i32 %[bud], 0\n\ts_cselect_b32 vcc_hi, -1, -2\n\tv_mov_b32 v253, vcc_hi\n\t"
            "s_waitcnt vmcnt(1)\n\t"
            "v_mfma_f32_16x16x32_bf16 v[232:235], %[psel], v[224:227], 0\n\t"
            "v_max3_u32 v250, v224, v225, v226\n\t"
            "s_waitcnt vmcnt(0)\n\t"
            "v_mfma_f32_16x16x32_bf16 v[244:247], %[ones], v[228:231], 0\n\t"
            "v_max3_u32 v251, v228, v229, v230\n\t"
            "v_mfma_f32_16x16x32_bf16 v[236:239], v[216:219], v[228:231], 0\n\t"
            "v_max3_u32 v250, v250, v227, v231\n\t"
            "v_mfma_f32_16x16x32_bf16 v[240:243], v[220:223], v[228:231], 0\n\t"
            "v_max_u32 v250, v250, v251\n\t"
            "v_cmp_gt_u32 vcc, v250, v253\n\t"
            "s_cbranch_vccnz 1b\n\t"
            "v_exp_f32 v250, v232\n\tv_exp_f32 v251, v233\n\t"
            "v_max_f32 v252, v232, v233\n\t"
            "v_cvt_pk_bf16_f32 v250, v250, v251\n\t"
            "v_cmp_lt_f32 vcc, 0x42800000, v252\n\t"
            "v_and_b32 v248, v250, %[mlo]\n\tv_and_b32 v249, v250, %[mhi]\n\t"
            "v_cndmask_b32 v236, v236, v240, %[hbm]\n\tv_cndmask_b32 v237, v237, v241, %[hbm]\n\t"
            "v_mfma_f32_16x16x16_bf16 v[244:247], %[ones2], v[248:249], v[244:247]\n\t"
            "v_cndmask_b32 v238, v238, v242, %[hbm]\n\tv_cndmask_b32 v239, v239, v243, %[hbm]\n\t"
            "s_nop 1\n\t"
            "v_mfma_f32_16x16x16_bf16 v[236:239], %[vp], v[248:249], v[236:239]\n\t"
            "s_cbranch_vccz 2f\n\t"
            "s_nop 7\n\t"
            "v_max_f32 v252, 0, v252\n\tv_mov_b32 v253, v252\n\ts_nop 1\n\tv_permlane16_swap_b32 v252, v253\n\ts_nop 1\n\t"
            "v_max_f32 v252, v252, v253\n\tv_mov_b32 v253, v252\n\ts_nop 1\n\tv_permlane32_swap_b32 v252, v253\n\ts_nop 1\n\t"
            "v_max_f32 v252, v252, v253\n\t"
            "v_mfma_f32_16x16x32_bf16 v[244:247], %[ones], v[228:231], 0\n\t"
            "v_mfma_f32_16x16x32_bf16 v[236:239], v[216:219], v[228:231], 0\n\t"
            "v_sub_f32 v250, v232, v252\n\tv_sub_f32 v251, v233, v252\n\t"
            "v_exp_f32 v250, v250\n\tv_exp_f32 v251, v251\n\tv_exp_f32 v253, -v252\n\ts_nop 0\n\t"
            "v_cvt_pk_bf16_f32 v250, v250, v251\n\t"
            "v_and_b32 v248, v250, %[mlo]\n\tv_and_b32 v249, v250, %[mhi]\n\ts_nop 7\n\t"
            "v_cndmask_b32 v236, v236, v240, %[hbm]\n\tv_cndmask_b32 v237, v237, v241, %[hbm]\n\tv_cndmask_b32 v238, v238, v242, %[hbm]\n\tv_cndmask_b32 v239, v239, v243, %[hbm]\n\t"
            "v_mul_f32 v236, v236, v253\n\tv_mul_f32 v237, v237, v253\n\tv_mul_f32 v238, v238, v253\n\tv_mul_f32 v239, v239, v253\n\t"
            "v_mul_f32 v244, v244, v253\n\tv_mul_f32 v245, v245, v253\n\tv_mul_f32 v246, v246, v253\n\tv_mul_f32 v247, v247, v253\n\ts_nop 1\n\t"
            "v_mfma_f32_16x16x16_bf16 v[244:247], %[ones2], v[248:249], v[244:247]\n\t"
            "v_mfma_f32_16x16x16_bf16 v[236:239], %[vp], v[248:249], v[236:239]\n\t"
            "s_nop 7\n\t"
            "2:\n\t"
            "s_nop 1\n\t"
            "v_rcp_f32 v252, v244\n\t"
            "s_nop 3\n\t"
            "v_mul_f32 v236, v236, v252\n\tv_mul_f32 v237, v237, v252\n\tv_mul_f32 v238, v238, v252\n\tv_mul_f32 v239, v239, v252\n\t"
            "v_add_f32_dpp v240, v236, v236 row_ror:8 row_mask:0xf bank_mask:0xf\n\tv_add_f32_dpp v241, v237, v237 row_ror:8 row_mask:0xf bank_mask:0xf\n\t"
            "v_add_f32_dpp v242, v238, v238 row_ror:8 row_mask:0xf bank_mask:0xf\n\tv_add_f32_dpp v243, v239, v239 row_ror:8 row_mask:0xf bank_mask:0xf\n\t"
            "ds_write_b128 %[ow], v[240:243]\n\t"
            PUBLISH_ASM(512)
            : [bud] "+s"(budget)
            : [pa] "v"(pa), [ea] "v"(ea), [rs] "s"(rs), [sb] "s"(sb), [psel] "v"(psel), [ones] "v"(ones), [ones2] "v"(ones2), [vp] "v"(vp),
              [mlo] "v"(mlo), [mhi] "v"(mhi), [hbm] "s"(hbm), [ow] "v"(ow),
              [pub] "s"(wave), [la] "v"(ra), [vo] "v"(mo), [so] "s"(sn)
            : "memory", "vcc", "scc", VCLOB);
        tref = now();
        STAMP(t == 0 && wave == 7, 3, s);
        if (wave == 7 && lane == 0) st4s(rs, flagoff(FL_V + t), 0, s);
        if (s + 2 == CAL_STEP) caln = __builtin_amdgcn_readfirstlane(*(const int*)(lds + L_CAL));
        if (wave == 5 && s + 3 == CAL_STEP) *(int*)(lds + L_CAL) = get_cal(c, 1, budget);
        if (wave == 5 && s == 29) touch_window(rs);
        if (wave == 4 || (s == 0 && wave > 4)) check_u(min(s + 2, NSEG - 2));
        ACC_T(6);
        if (wave < 4) wait_until(tref + T_OM);
        else {
            wait_until(tref + T_OU);
            if (s + 1 < NSEG - 1 && ABL(1)) load_u(s + 1);
            if (wave == 4) asm volatile("buffer_load_dword v215, %0, %1, 0 offen sc1" :: "v"(ufl), "s"(rs) : "memory", "v215");
            if (ABL(2)) store_v(s);
        }
        ACC_T(7);
    }
    VMWAIT(0);
    ACC_OUT(t * 8 + wave);
    mem_times_b(slot_base(NSEG - 1, caln), NSEG - 1, true);
    if (budget < 0) give_up(rs);
    BAR();
    if (wave >= 4) store_v(NSEG - 1);
    VMWAIT(0);
    BAR0();
    if (wave == 7 && lane == 0) st4s(rs, flagoff(FL_V + t), 0, NSEG);
}

DEV void role_w(const Params& p, const Ctx& c, int wi) {
    const int lane = c.lane, wave = c.wave, rho = c.rho, g = c.g;
    const rsrc_t rs = c.rs; char* lds = c.lds;
    const int h = wi / 7, unit = (wi - h * 7) * 4 + (wave & 3), set = wave >> 2;
    const bool isG = unit < 14;
    auto stage = [&](int it) {
        const int sg = (2 * it + set + 1) & 63, lb = (set * 2 + (it & 1)) * 28672;
#pragma unroll
        for (int k = 0; k < 7; ++k) { const int pc = (wave & 3) * 7 + k; dma16(rs, OFF_XF + lane * 16, sg * 28672 + pc * 1024, lb + pc * 1024); }
    };
    v4u w[2][14];
    auto load_w = [&]() {
#pragma unroll
        for (int t = 0; t < 2; ++t) {
            const int row = isG ? unit * 32 + pperm(rho, t) : (unit - 14) * 32 + t * 16 + rho;
#pragma unroll
            for (int ks = 0; ks < 14; ++ks) w[t][ks] = ld16(rs, (isG ? OFF_AP : OFF_BT) + (h * 28 + (row >> 4)) * 14336 + g * 256 + (row & 15) * 16, ks * 1024, 0);
        }
    };
    int xa[2];
#pragma unroll
    for (int k = 0; k < 2; ++k) { const int key = isG ? k * 16 + rho : pperm(rho, k), kt = key >> 4; xa[k] = kt * 14336 + (g * 16 + ((key & 15) ^ (kt * 4))) * 16; }
    const int ob = (isG ? OFF_G + (h * 14 + unit) * 2048 : OFF_U + ((unit - 14) * 32 + h) * 1024) + lane * 16;
    const int ostep = isG ? 1024 : 16384;
    auto segment = [&](int buf, int sg) {
        const char* xb = lds + buf * 28672;
        v4f a00 = {0, 0, 0, 0}, a01 = a00, a10 = a00, a11 = a00;
        if (isG) {
#pragma unroll
            for (int ks = 0; ks < 14; ++ks) { const v4u x0 = lds16(xb, xa[0] + ks * 1024), x1 = lds16(xb, xa[1] + ks * 1024);
                a00 = mfma32(w[0][ks], x0, a00); a01 = mfma32(w[1][ks], x0, a01); a10 = mfma32(w[0][ks], x1, a10); a11 = mfma32(w[1][ks], x1, a11); }
        } else {
#pragma unroll
            for (int ks = 0; ks < 14; ++ks) { const v4u x0 = lds16(xb, xa[0] + ks * 1024), x1 = lds16(xb, xa[1] + ks * 1024);
                a00 = mfma32(x0, w[0][ks], a00); a01 = mfma32(x1, w[0][ks], a01); a10 = mfma32(x0, w[1][ks], a10); a11 = mfma32(x1, w[1][ks], a11); }
        }
        st16s(rs, ob, sg * (NH * 28672), pk8(a00, a01)); st16s(rs, ob + ostep, sg * (NH * 28672), pk8(a10, a11));
    };
    if (!ABL(512)) wait_until(now() + 7000);
    if (set == 0) load_w();
    if (wave == 7) *(int*)(lds + LDS_MAIN - 32) = 0;
    stage(0);
    VMWAIT(0);
    BAR();
    STAMP(wi == 20 && wave == 1, 4, 2);
#pragma unroll 1
    for (int it = -1; it < 32; ++it) {
        if (it == -1 && set == 1) load_w();
        if (it >= 0 && it + 1 < 32) stage(it + 1);
        if (it > 0 || set == 0) segment(it > 0 ? set * 2 + (it & 1) : 2 * (it + 1), ((it > 0 ? 2 * it + set : it + 1) + 1) & 63);
        if (it < 0) continue;
        if (it == 0 && set == 0) {
            VMWAIT(0);
            if (lane == 0 && __hip_atomic_fetch_add((int*)(lds + LDS_MAIN - 32), 1, __ATOMIC_RELAXED, __HIP_MEMORY_SCOPE_WORKGROUP) == 3) st4s(rs, flagoff(FL_W + wi), 0, 2);
            STAMP(wi == 20 && wave == 1, 4, 15);
        }
        const bool eager = it < 4 || it == 31;
        if (eager) VMWAIT(0); else VMWAIT(2);
        BAR();
        if (wave == 0 && lane == 0) st4s(rs, flagoff(FL_W + wi), 0, eager ? 2 * it + 2 : 2 * it);
        if (it == 12 && wave == 0 && wi >= 48) {
            unsigned seen; wait_flags<8>(rs, flagoff(FL_TAB), 28, seen);
            __builtin_amdgcn_fence(__ATOMIC_ACQUIRE, "agent");
            VMWAIT(0);
        }
        STAMP(wi == 20 && wave == 1, 5, it); CSTAMP(wi == 20 && wave == 1, 6, it);
    }
}

DEV int f_first(int set, int wave) { return set ? 33 + 2 * wave : wave; }
DEV int f_next(int s, int wave) { return s >= 32 ? s + 16 : s >= 24 ? 32 + 2 * wave : s + 8; }
DEV void role_f1(const Params& p, const Ctx& c, int h, int set) {
    const int lane = c.lane, wave = c.wave, rho = c.rho, g = c.g;
    const rsrc_t rs = c.rs;
    const int gfl = flagoff(FL_W + h * 7 + (lane & 3)), tfl = flagoff(FL_T + h * 4 + (lane & 3));
    const int gl0 = OFF_G + h * 28672 + (rho >> 3) * 1024 + g * 256 + (pperm(rho, 0) & 15) * 16;
    const int tl = OFF_T + h * 7168 + g * 128 + ((rho >> 2) * 2 + (rho & 1)) * 16;
    const float ninf = -__builtin_inff();
    bool wdone = false;
#pragma unroll 1
    for (int s = f_first(set, wave); s < NSEG; s = f_next(s, wave)) {
        STAMP(h == 0, 16, s);
        unsigned seen;
        if (!wdone) { if (!wait_flags<8>(rs, gfl, s ? s : NSEG, seen)) return; wdone = __builtin_amdgcn_ballot_w64(seen < NSEG) == 0; }
        v4u x[2][14];
        v4f a[2][2] = {{{0, 0, 0, 0}, {0, 0, 0, 0}}, {{0, 0, 0, 0}, {0, 0, 0, 0}}};
#pragma unroll
        for (int hf = 0; hf < 2; ++hf) {
            v4u y[2][7];
#pragma unroll
            for (int k = 0; k < 7; ++k) {
#pragma unroll
                for (int qt = 0; qt < 2; ++qt) x[qt][hf * 7 + k] = ld16(rs, OFF_XF + qt * 14336 + g * 256 + (rho ^ (qt * 4)) * 16, s * 28672 + (hf * 7 + k) * 1024, 0);
                y[0][k] = ld16(rs, gl0, s * (NH * 28672) + (hf * 7 + k) * 2048, 1); y[1][k] = ld16(rs, gl0 + 64, s * (NH * 28672) + (hf * 7 + k) * 2048, 1);
            }
            SCHED();
#pragma unroll
            for (int k = 0; k < 7; ++k)
#pragma unroll
                for (int qt = 0; qt < 2; ++qt) { a[qt][0] = mfma32(y[0][k], x[qt][hf * 7 + k], a[qt][0]); a[qt][1] = mfma32(y[1][k], x[qt][hf * 7 + k], a[qt][1]); }
        }
        float v[2][8], m[2];
#pragma unroll
        for (int qt = 0; qt < 2; ++qt) {
            const float r[8] = {a[qt][0].x, a[qt][0].y, a[qt][0].z, a[qt][0].w, a[qt][1].x, a[qt][1].y, a[qt][1].z, a[qt][1].w};
            m[qt] = ninf;
#pragma unroll
            for (int k = 0; k < 8; ++k) { v[qt][k] = (8 * g + k <= qt * 16 + rho) ? r[k] : ninf; m[qt] = fmx(m[qt], v[qt][k]); }
        }
        asm volatile("" : "+v"(m[0]), "+v"(m[1]));
        STAMP(h == 0, 17, s);
        const bool last = s >= NSEG - 2;
        if (!wait_flags<2>(rs, tfl, last ? s : s + 1, seen)) return;
        STAMP(h == 0, 18, s);
        v4f am[2];
#pragma unroll 1
        for (int sp = 0; sp < SPIN_CAP; ++sp) {
            FENCE();
            v4u y[14]; unsigned mx = 0;
#pragma unroll
            for (int ks = 0; ks < 14; ++ks) y[ks] = ld16(rs, tl, s * (NH * 7168) + ks * 512, 1);
            SCHED();
            am[0] = am[1] = (v4f){0, 0, 0, 0};
#pragma unroll
            for (int ks = 0; ks < 14; ++ks) { am[0] = mfma32(y[ks], x[0][ks], am[0]); am[1] = mfma32(y[ks], x[1][ks], am[1]); mx = max(mx, umax4(y[ks])); PIN1(mx); PIN2(am[0], am[1]); }
            if (!last || __builtin_amdgcn_ballot_w64(mx == SENT) == 0) break;
        }
        unsigned pm[2];
#pragma unroll
        for (int qt = 0; qt < 2; ++qt) {
            const float mm = max_g(fmx(m[qt], fmx(am[qt].x, am[qt].y)));
            const float e0 = __builtin_amdgcn_exp2f(am[qt].x - mm), e1 = __builtin_amdgcn_exp2f(am[qt].y - mm);
            float z = e0 + e1;
#pragma unroll
            for (int k = 0; k < 8; ++k) { v[qt][k] = __builtin_amdgcn_exp2f(v[qt][k] - mm); z += v[qt][k]; }
            const float rz = __builtin_amdgcn_rcpf(sum_g(z));
            st16s(rs, OFF_PX + (h * 2 + qt) * 1024 + lane * 16, s * (NH * 2048),
                  (v4u){pk2(v[qt][0] * rz, v[qt][1] * rz), pk2(v[qt][2] * rz, v[qt][3] * rz), pk2(v[qt][4] * rz, v[qt][5] * rz), pk2(v[qt][6] * rz, v[qt][7] * rz)});
            pm[qt] = pk2(e0 * rz, e1 * rz);
        }
        st16s(rs, (lane & 1) == 0 ? OFF_PM + h * 512 + lane * 8 : OOB, s * (NH * 512), (v4u){pm[0], pm[1], dppu<0xF5>(pm[0]), dppu<0xF5>(pm[1])});
        if (!last) VMWAIT(0);
        if (lane == 0) st4s(rs, flagoff((set ? FL_P2 : FL_P) + h * 8 + wave), 0, s + 1);
        STAMP(h == 0, 19, s);
    }
}

DEV void make_tables(const Params& p, const Ctx& c, int t) {
    const int lane = c.lane, wave = c.wave, rho = c.rho, g = c.g;
    const rsrc_t rs = c.rs;
    for (int ch = t * 512 + wave * 64 + lane; ch < 2 * 28672; ch += 28 * 512) {
        float v[8];
        if (ch < 28672) { const int fr = ch >> 6, tl = fr / 14, d0 = (fr - tl * 14) * 32 + g * 8, ri = tl >> 4, k = (tl & 15) * 16 + rho;
#pragma unroll
            for (int j = 0; j < 8; ++j) { const float ph = (float)((k * (d0 + j)) % 448) * (1.f / 448.f); v[j] = k > 224 ? 0.f : ri ? -__builtin_amdgcn_sinf(ph) : __builtin_amdgcn_cosf(ph); }
        } else { const int fr = (ch - 28672) >> 6, d = (fr >> 4) * 16 + rho, ri = (fr >> 3) & 1, k0 = (fr & 7) * 32 + g * 8;
#pragma unroll
            for (int j = 0; j < 8; ++j) { const int k = k0 + j; const float ph = (float)((k * d) % 448) * (1.f / 448.f); const bool edge = k == 0 || k == 224;
                const float wgt = (edge ? 1.f : 2.f) / (448.f * 2048.f); v[j] = k > 224 ? 0.f : ri ? (edge ? 0.f : -wgt * __builtin_amdgcn_sinf(ph)) : wgt * __builtin_amdgcn_cosf(ph); }
        }
        st16s(rs, OFF_FD + ch * 16, 0, (v4u){pk2(v[0], v[1]), pk2(v[2], v[3]), pk2(v[4], v[5]), pk2(v[6], v[7])});
    }
    VMWAIT(0);
    BAR();
    if (wave == 0 && lane == 0) __hip_atomic_fetch_add((unsigned*)(p.ws + flagoff(FL_TAB)), 1u, __ATOMIC_RELAXED, __HIP_MEMORY_SCOPE_AGENT);
}
DEV void role_f2(const Params& p, const Ctx& c, int t, int set) {
    const int lane = c.lane, wave = c.wave, rho = c.rho, g = c.g;
    const rsrc_t rs = c.rs;
    const int ufl = flagoff(FL_W + rho * 7 + ((14 + (t >> 1)) >> 2)), pfl = flagoff(lane < 16 ? (set ? FL_P2 : FL_P) + lane * 8 + wave : FL_V + t);
    const int ul = OFF_U + t * 16384 + lane * 16;
    bool wdone = false;
#pragma unroll 1
    for (int s = f_first(set, wave); s < NSEG; s = f_next(s, wave)) {
        unsigned seen;
        if (!wdone) { if (!wait_flags<8>(rs, ufl, s ? s : NSEG, seen)) return; wdone = __builtin_amdgcn_ballot_w64(seen < NSEG) == 0; }

        v4u u[16];
#pragma unroll
        for (int h = 0; h < 16; ++h) u[h] = ld16(rs, ul, s * (NH * 28672) + h * 1024, 1);
        if (!wait_flags<2>(rs, pfl, s + 1, seen)) return;

        v4f a[2], m[2];
        const bool last = s >= NSEG - 2;
#pragma unroll 1
        for (int sp = 0; sp < SPIN_CAP; ++sp) {
            FENCE();
            a[0] = a[1] = m[0] = m[1] = (v4f){0, 0, 0, 0};
            v4u px[16]; v2u pm[16], vv[8]; unsigned mx = 0;
#pragma unroll
            for (int h = 0; h < 16; ++h) { pm[h] = ld8s(rs, OFF_PM + lane * 8, s * (NH * 512) + h * 512); if (h & 1) vv[h >> 1] = ld8s(rs, OFF_V + t * 4096 + lane * 8, s * (NH * 7168) + (h >> 1) * 512); }
#pragma unroll
            for (int h = 0; h < 16; ++h) px[h] = ld16(rs, OFF_PX + lane * 16, s * (NH * 2048) + h * 2048, 1);
            SCHED();
#pragma unroll
            for (int k = 0; k < 8; ++k) { m[0] = mfma16(vv[k], (v2u){pm[2 * k].x, pm[2 * k + 1].x}, m[0]); m[1] = mfma16(vv[k], (v2u){pm[2 * k].y, pm[2 * k + 1].y}, m[1]);
                mx = max(max(mx, max(pm[2 * k].x, pm[2 * k].y)), max(pm[2 * k + 1].x, pm[2 * k + 1].y)); PIN1(mx); PIN2(m[0], m[1]); SCHED(); }
#pragma unroll
            for (int h = 0; h < 16; ++h) { a[0] = mfma32(u[h], px[h], a[0]); mx = max(mx, umax4(px[h])); PIN2(mx, a[0]); SCHED(); px[h] = ld16(rs, OFF_PX + 1024 + lane * 16, s * (NH * 2048) + h * 2048, 1); SCHED(); }
#pragma unroll
            for (int h = 0; h < 16; ++h) { a[1] = mfma32(u[h], px[h], a[1]); mx = max(mx, umax4(px[h])); PIN2(mx, a[1]); }
            if (!last || __builtin_amdgcn_ballot_w64(mx == SENT) == 0) break;
        }
#pragma unroll
        for (int qt = 0; qt < 2; ++qt) { const v4f o = a[qt] + m[qt];
            *(v2u*)(p.out + (size_t)(s * 32 + qt * 16 + rho) * 896 + DM + t * 16 + g * 4) = (v2u){pk2(o.x, o.y), pk2(o.z, o.w)}; }

    }
}

DEV bool fft_sync(const Params& p, const Ctx& c, int fl) {
    VMWAIT(0);
    BAR();
    int* okw = (int*)(c.lds + LDS_MAIN - 16);
    if (c.wave == 0) {
        if (c.lane == 0) __hip_atomic_fetch_add((unsigned*)(p.ws + flagoff(fl)), 1u, __ATOMIC_RELAXED, __HIP_MEMORY_SCOPE_AGENT);
        unsigned seen; const bool ok = wait_flags<2>(c.rs, flagoff(fl), 64, seen);
        if (c.lane == 0) *okw = ok;
    }
    BAR();
    return *okw != 0;
}
DEV v4u neg8(v4u v) { return v ^ 0x80008000u; }
DEV void twid(int a, const int (&n)[8], int mask, float inv, v4u& C, v4u& S) {
    float cs[8], sn[8];
#pragma unroll
    for (int j = 0; j < 8; ++j) { const float ph = (float)((a * n[j]) & mask) * inv; cs[j] = __builtin_amdgcn_cosf(ph); sn[j] = __builtin_amdgcn_sinf(ph); }
    C = (v4u){pk2(cs[0], cs[1]), pk2(cs[2], cs[3]), pk2(cs[4], cs[5]), pk2(cs[6], cs[7])};
    S = (v4u){pk2(sn[0], sn[1]), pk2(sn[2], sn[3]), pk2(sn[4], sn[5]), pk2(sn[6], sn[7])};
}
DEV void role_fft(const Params& p, const Ctx& c, int job) {
    const int lane = c.lane, wave = c.wave, rho = c.rho, g = c.g;
    const rsrc_t rs = c.rs; char* lds = c.lds;
    const v4f z4 = {0, 0, 0, 0};

    {
        const rsrc_t rx = mkrsrc(p.xa, 2048 * DM * 2);
        for (int pc = wave; pc < 28; pc += 8) { const int nt = pc >= 14, ks = pc - nt * 14; dma16(rx, ((64 * (nt * 16 + rho) + job) * DM + ks * 32 + g * 8) * 2, 0, pc * 1024); }

        v4u C[2], S[2];
        { int n[8];
#pragma unroll
          for (int j = 0; j < 8; ++j) n[j] = 64 * (16 * (j >> 2) + 4 * g + (j & 3)) + job;
          twid(rho, n, 2047, 1.f / 2048.f, C[0], S[0]); twid(16 + rho, n, 2047, 1.f / 2048.f, C[1], S[1]); }
        VMWAIT(0);
        BAR();
        v4f zr[2][2], zi[2][2];
#pragma unroll
        for (int t = 0; t < 2; ++t) {
            const int fo = OFF_FD + (wave * 2 + (rho >> 3)) * 14336 + g * 256 + (pperm(rho, t) & 15) * 16;
            v4f r0 = z4, r1 = z4, i0 = z4, i1 = z4;
#pragma unroll
            for (int hf = 0; hf < 2; ++hf) {
                v4u fr[7], fi[7];
#pragma unroll
                for (int k = 0; k < 7; ++k) { fr[k] = ld16(rs, fo, (hf * 7 + k) * 1024, 0); fi[k] = ld16(rs, fo, 256 * DM * 2 + (hf * 7 + k) * 1024, 0); }
                SCHED();
#pragma unroll
                for (int k = 0; k < 7; ++k) { const v4u x0 = lds16(lds, (hf * 7 + k) * 1024 + lane * 16), x1 = lds16(lds, (14 + hf * 7 + k) * 1024 + lane * 16);
                    r0 = mfma32(x0, fr[k], r0); r1 = mfma32(x1, fr[k], r1); i0 = mfma32(x0, fi[k], i0); i1 = mfma32(x1, fi[k], i1); }
            }
            const v4u yr = pk8(r0, r1), yi = pk8(i0, i1), nr = neg8(yr);
#pragma unroll
            for (int ct = 0; ct < 2; ++ct) { zr[ct][t] = mfma32(yi, S[ct], mfma32(yr, C[ct], z4)); zi[ct][t] = mfma32(nr, S[ct], mfma32(yi, C[ct], z4)); }
        }
        char* O = lds + 32768;
#pragma unroll
        for (int ct = 0; ct < 2; ++ct) { const int o = (ct * 16 + rho) * 1040 + wave * 64 + g * 16;
            *(v4u*)(O + o) = pk8(zr[ct][0], zr[ct][1]); *(v4u*)(O + o + 512) = pk8(zi[ct][0], zi[ct][1]); }
        BAR();
#pragma unroll
        for (int k = 0; k < 4; ++k) st16s(rs, OFF_Z1 + job * 1024 + lane * 16, (wave * 4 + k) * 65536, lds16(O, (wave * 4 + k) * 1040 + lane * 16));
    }
    STAMP(job == 0 && wave == 1, 4, 3);
    if (!fft_sync(p, c, FL_FA)) return;
    STAMP(job == 0 && wave == 1, 4, 4);
    if (wave < 4) {
        const int k1 = job >> 1, P = (job & 1) * 4 + wave;
        char* L = lds + wave * 8192;
#pragma unroll
        for (int pc = 0; pc < 8; ++pc) dma16s(rs, OFF_Z1 + (k1 * 64 + (pc & 3) * 16 + (lane >> 2)) * 1024 + (pc >> 2) * 512 + P * 64 + (lane & 3) * 16, 0, wave * 8192 + pc * 1024);
        v4u C[4][2], S[4][2];
#pragma unroll
        for (int ks = 0; ks < 2; ++ks) { int n[8];
#pragma unroll
            for (int j = 0; j < 8; ++j) n[j] = 32 * ks + 16 * (j >> 2) + 4 * g + (j & 3);
#pragma unroll
            for (int kt = 0; kt < 4; ++kt) twid(16 * kt + rho, n, 63, 1.f / 64.f, C[kt][ks], S[kt][ks]); }
        VMWAIT(0);
        v2u hr[4], hi[4];
#pragma unroll
        for (int t = 0; t < 2; ++t) {
            const int k = P * 32 + pperm(rho, t);
            v4u xr[2], xi[2];
#pragma unroll
            for (int ks = 0; ks < 2; ++ks) { const int a = (32 * ks + 4 * g + (rho >> 2)) * 64 + (rho & 3) * 16 + t * 8;
                const v2u a0 = ldstr(L, a), a1 = ldstr(L, a + 1024), b0 = ldstr(L, a + 4096), b1 = ldstr(L, a + 5120);
                xr[ks] = (v4u){a0.x, a0.y, a1.x, a1.y}; xi[ks] = (v4u){b0.x, b0.y, b1.x, b1.y}; }
            const v4u n0 = neg8(xr[0]), n1 = neg8(xr[1]);
            unsigned short fr[16], fi[16];
#pragma unroll
            for (int j = 0; j < 16; ++j) { const int o = (k1 + 32 * (16 * (j >> 2) + 4 * g + (j & 3))) * 225 + min(k, 224); fr[j] = p.wr[o]; fi[j] = p.wi[o]; }
            SCHED();
            v4f vr[4], vi[4];
#pragma unroll
            for (int kt = 0; kt < 4; ++kt) {
                const v4f ar = mfma32(S[kt][1], xi[1], mfma32(S[kt][0], xi[0], mfma32(C[kt][1], xr[1], mfma32(C[kt][0], xr[0], z4))));
                const v4f ai = mfma32(S[kt][1], n1, mfma32(S[kt][0], n0, mfma32(C[kt][1], xi[1], mfma32(C[kt][0], xi[0], z4))));
                float wr[4], wi[4];
#pragma unroll
                for (int r = 0; r < 4; ++r) { wr[r] = __uint_as_float((unsigned)fr[kt * 4 + r] << 16); wi[r] = __uint_as_float((unsigned)fi[kt * 4 + r] << 16); }
                vr[kt] = (v4f){ar.x * wr[0] - ai.x * wi[0], ar.y * wr[1] - ai.y * wi[1], ar.z * wr[2] - ai.z * wi[2], ar.w * wr[3] - ai.w * wi[3]};
                vi[kt] = (v4f){ar.x * wi[0] + ai.x * wr[0], ar.y * wi[1] + ai.y * wr[1], ar.z * wi[2] + ai.z * wr[2], ar.w * wi[3] + ai.w * wr[3]};
            }
            const v4u pr0 = pk8(vr[0], vr[1]), pr1 = pk8(vr[2], vr[3]), pi0 = pk8(vi[0], vi[1]), pi1 = pk8(vi[2], vi[3]), ni0 = neg8(pi0), ni1 = neg8(pi1);
#pragma unroll
            for (int nt = 0; nt < 4; ++nt) {
                const v4f rr = mfma32(ni1, S[nt][1], mfma32(ni0, S[nt][0], mfma32(pr1, C[nt][1], mfma32(pr0, C[nt][0], z4))));
                const v4f ri = mfma32(pr1, S[nt][1], mfma32(pr0, S[nt][0], mfma32(pi1, C[nt][1], mfma32(pi0, C[nt][0], z4))));
                const v2u qr = {pk2(rr.x, rr.y), pk2(rr.z, rr.w)}, qi = {pk2(ri.x, ri.y), pk2(ri.z, ri.w)};
                if (t == 0) { hr[nt] = qr; hi[nt] = qi; }
                else { char* o = lds + 32768 + (nt * 16 + rho) * 528 + wave * 64 + g * 16;
                    *(v4u*)o = (v4u){hr[nt].x, hr[nt].y, qr.x, qr.y}; *(v4u*)(o + 256) = (v4u){hi[nt].x, hi[nt].y, qi.x, qi.y}; }
            }
        }
    }
    BAR();
#pragma unroll
    for (int k = 0; k < 4; ++k) { const int n2 = wave * 8 + k * 2 + (lane >> 5);
        st16s(rs, OFF_Z2 + (n2 * 32 + (job >> 1)) * 1024 + (g & 1) * 512 + (job & 1) * 256 + rho * 16, 0, lds16(lds, 32768 + n2 * 528 + (lane & 31) * 16)); }
    STAMP(job == 0 && wave == 1, 4, 5);
    if (!fft_sync(p, c, FL_FB)) return;
    STAMP(job == 0 && wave == 1, 4, 6);
    {
        char* L = lds + wave * 4096;
#pragma unroll
        for (int pc = 0; pc < 4; ++pc) dma16s(rs, OFF_Z2 + (job * 32 + (pc & 1) * 16 + (lane >> 2)) * 1024 + (pc >> 1) * 512 + wave * 64 + (lane & 3) * 16, 0, wave * 4096 + pc * 1024);
        v4u C[2], S[2];
        { int n[8];
#pragma unroll
          for (int j = 0; j < 8; ++j) n[j] = 8 * g + j;
          twid(64 * rho + job, n, 2047, 1.f / 2048.f, C[0], S[0]); twid(64 * (16 + rho) + job, n, 2047, 1.f / 2048.f, C[1], S[1]); }
        VMWAIT(0);
        v4f rr[2][2], ri[2][2];
#pragma unroll
        for (int t = 0; t < 2; ++t) {
            const int a = (8 * g + (rho >> 2)) * 64 + (rho & 3) * 16 + t * 8;
            const v2u a0 = ldstr(L, a), a1 = ldstr(L, a + 256), b0 = ldstr(L, a + 2048), b1 = ldstr(L, a + 2304);
            const v4u xr = {a0.x, a0.y, a1.x, a1.y}, xi = {b0.x, b0.y, b1.x, b1.y}, ni = neg8(xi);
#pragma unroll
            for (int nt = 0; nt < 2; ++nt) { rr[nt][t] = mfma32(ni, S[nt], mfma32(xr, C[nt], z4)); ri[nt][t] = mfma32(xr, S[nt], mfma32(xi, C[nt], z4)); }
        }
        char* R = lds + 32768;
#pragma unroll
        for (int nt = 0; nt < 2; ++nt) { *(v4u*)(R + (nt * 16 + wave) * 1024 + lane * 16) = pk8(rr[nt][0], rr[nt][1]); *(v4u*)(R + (nt * 16 + 8 + wave) * 1024 + lane * 16) = pk8(ri[nt][0], ri[nt][1]); }
        BAR();
#pragma unroll 1
        for (int Q = wave; Q < 14; Q += 8) {
            v4f y[2][2] = {{z4, z4}, {z4, z4}};
#pragma unroll
            for (int hf = 0; hf < 2; ++hf) {
                v4u f0[8], f1[8];
#pragma unroll
                for (int k = 0; k < 8; ++k) { const int fo = OFF_FI + (Q * 2 + (rho >> 3)) * 16384 + g * 256 + (pperm(rho, 0) & 15) * 16; f0[k] = ld16(rs, fo, (hf * 8 + k) * 1024, 0); f1[k] = ld16(rs, fo + 64, (hf * 8 + k) * 1024, 0); }
                SCHED();
#pragma unroll
                for (int k = 0; k < 8; ++k) { const v4u b0 = lds16(R, (hf * 8 + k) * 1024 + lane * 16), b1 = lds16(R, (16 + hf * 8 + k) * 1024 + lane * 16);
                    y[0][0] = mfma32(f0[k], b0, y[0][0]); y[0][1] = mfma32(f1[k], b0, y[0][1]); y[1][0] = mfma32(f0[k], b1, y[1][0]); y[1][1] = mfma32(f1[k], b1, y[1][1]); }
            }
#pragma unroll
            for (int nt = 0; nt < 2; ++nt) *(v4u*)(p.out + (size_t)(64 * (nt * 16 + rho) + job) * 896 + Q * 32 + g * 8) = pk8(y[nt][0], y[nt][1]);
        }
    }
}

extern "C" __global__ void __launch_bounds__(512, 2) k_main(Params p) {
    extern __shared__ __attribute__((aligned(16))) char lds[];
    Ctx c; c.rs = mkrsrc(p.ws, WS_BYTES); c.lds = lds; c.lane = threadIdx.x & 63; c.wave = __builtin_amdgcn_readfirstlane(threadIdx.x >> 6); c.rho = c.lane & 15; c.g = c.lane >> 4;
    { const int lane = c.lane; (void)lane; STAMP(blockIdx.x == 0 && c.wave == 7, 4, 0); }
    const int rot = *(const int*)(p.ws + flagoff(FL_ROT));
    const int x = (blockIdx.x + rot) & 7, y = blockIdx.x >> 3;
    if (x < 2) role_p(p, c, x * 32 + y);
    else if (x == 2) { if (y < 28) role_o(p, c, y); }
    else if (x == 3 && y == 28) role_cal(p, c);
    else {
        const int wi = (x - 4) * 32 + y - 16;
        if (x == 3) { if (y < 28) make_tables(p, c, y); } else if (wi >= 0) role_w(p, c, wi);
        if (wi >= 48) role_fft(p, c, wi - 48);
        else if (x == 3 ? y < 28 : wi >= 16 && wi < 44) role_f2(p, c, x == 3 ? y : wi - 16, x != 3);
        else if (x == 4) role_f1(p, c, y & 15, wi >= 0);
    }
    { const int lane = c.lane; (void)lane; LSTAMP(c.wave == 7, 8 + (blockIdx.x >> 6), blockIdx.x & 63); }
}

extern "C" void kernel_launch(void* const* d_in, const int* in_sizes, int n_in, void* d_out, int out_size, void* d_ws, size_t ws_size, hipStream_t stream) {
    Params p;
    p.xa = (const unsigned short*)d_in[0]; p.wr = (const unsigned short*)d_in[1]; p.wi = (const unsigned short*)d_in[2];
    p.xb = (const unsigned short*)d_in[3]; p.mem0 = (const unsigned short*)d_in[4];
    p.wq = (const unsigned short*)d_in[5]; p.wk = (const unsigned short*)d_in[6]; p.wv = (const unsigned short*)d_in[7]; p.wo = (const unsigned short*)d_in[8];
    p.out = (unsigned short*)d_out;
    p.ws = (char*)d_ws + (((size_t)2 << 20) - ((uintptr_t)d_ws & (((size_t)2 << 20) - 1))) % ((size_t)2 << 20);
    (void)hipFuncSetAttribute((const void*)k_main, hipFuncAttributeMaxDynamicSharedMemorySize, LDS_MAIN);
    (void)hipFuncSetAttribute((const void*)k_pro, hipFuncAttributeMaxDynamicSharedMemorySize, LDS_PRO);
    hipLaunchKernelGGL(k_pro, dim3(256), dim3(512), LDS_PRO, stream, p);
    hipLaunchKernelGGL(k_main, dim3(256), dim3(512), LDS_MAIN, stream, p);
}
```

```cpp
#include <hip/hip_runtime.h>
#include <hip/hip_bf16.h>
#include <stdint.h>

typedef unsigned v4u __attribute__((ext_vector_type(4)));
typedef unsigned v2u __attribute__((ext_vector_type(2)));
typedef float v4f __attribute__((ext_vector_type(4)));
typedef float v2f __attribute__((ext_vector_type(2)));
typedef short v4s __attribute__((ext_vector_type(4)));
typedef __bf16 v2bf __attribute__((ext_vector_type(2)));
typedef __bf16 v8bf __attribute__((ext_vector_type(8)));
typedef __bf16 v16bf __attribute__((ext_vector_type(16)));
typedef unsigned v8u __attribute__((ext_vector_type(8)));
typedef __amdgpu_buffer_rsrc_t rsrc_t;
#define LAS __attribute__((address_space(3)))
#define DEV __device__ __forceinline__

constexpr int NSEG = 64, NH = 16, DM = 448;
constexpr unsigned SENT = 0xFFFFFFFFu;
constexpr int OOB = 0x7fffffff;
constexpr int SC1 = 16;
constexpr int OFF_HO = 0;
constexpr int OFF_CTL = 4 << 20;
constexpr int OFF_AP = OFF_CTL + 131072;
constexpr int OFF_BT = OFF_AP + NH * DM * DM * 2;
constexpr int OFF_G = OFF_BT + NH * DM * DM * 2;
constexpr int OFF_U = OFF_G + NSEG * NH * 28672;
constexpr int OFF_T = OFF_U + NSEG * NH * 28672;
constexpr int OFF_V = OFF_T + NSEG * NH * 7168;
constexpr int OFF_PX = OFF_V + NSEG * NH * 7168;
constexpr int OFF_PM = OFF_PX + NSEG * NH * 2048;
constexpr int OFF_FD = OFF_PM + NSEG * NH * 512;
constexpr int OFF_FI = OFF_FD + 512 * DM * 2;
constexpr int OFF_Z1 = OFF_FI + 512 * DM * 2;
constexpr int OFF_Z2 = OFF_Z1 + (2 << 20);
constexpr int OFF_XF = OFF_Z2 + (2 << 20);
constexpr int OFF_G0 = OFF_XF + (2 << 20);
constexpr int OFF_DBG = OFF_G0 + (1 << 20);
constexpr int WS_BYTES = OFF_DBG + (1 << 20);
constexpr int HO_MEM = 0, HO_PART = 32768, HO_E = 65536, HO_SLOT = 98304;
constexpr int OFF_MEM0 = 4 * HO_SLOT, OFF_PROBE = OFF_MEM0 + 32768;
constexpr int FL_W = 0, FL_T = 112, FL_V = 176, FL_P = 208, FL_ROT = 336, FL_TAB = 337, FL_FA = 338, FL_FB = 339, FL_ABORT = 340, FL_CAL = 341, FL_P2 = 344;
constexpr int LDS_MAIN = 114688 + 2048;
constexpr int LDS_PRO = 143360;
constexpr int SPIN_CAP = 1 << 17;

struct Params {
    const unsigned short* xa; const unsigned short* wr; const unsigned short* wi;
    const unsigned short* xb; const unsigned short* mem0;
    const unsigned short* wq; const unsigned short* wk; const unsigned short* wv; const unsigned short* wo;
    unsigned short* out; char* ws;
};

DEV unsigned pk2(float a, float b) { v2bf r = {(__bf16)a, (__bf16)b}; return __builtin_bit_cast(unsigned, r); }
DEV v4u pk8(v4f a, v4f b) { return (v4u){pk2(a.x, a.y), pk2(a.z, a.w), pk2(b.x, b.y), pk2(b.z, b.w)}; }
DEV v4f mfma32(v4u a, v4u b, v4f c) { return __builtin_amdgcn_mfma_f32_16x16x32_bf16(__builtin_bit_cast(v8bf, a), __builtin_bit_cast(v8bf, b), c, 0, 0, 0); }
DEV v4f mfma16(v2u a, v2u b, v4f c) { return __builtin_amdgcn_mfma_f32_16x16x16bf16_1k(__builtin_bit_cast(v4s, a), __builtin_bit_cast(v4s, b), c, 0, 0, 0); }
DEV v4f smf(v4u a, v4u b0, v4u b1, v4f c, int idx) {
    v8u b = {b0.x, b0.y, b0.z, b0.w, b1.x, b1.y, b1.z, b1.w};
    return __builtin_amdgcn_smfmac_f32_16x16x64_bf16(__builtin_bit_cast(v8bf, a), __builtin_bit_cast(v16bf, b), c, idx, 0, 0);
}
DEV float fmx(float a, float b) { return __builtin_elementwise_maximum(a, b); }
DEV float max_g(float s) {
    auto r = __builtin_amdgcn_permlane16_swap(__float_as_uint(s), __float_as_uint(s), false, false);
    s = fmx(__uint_as_float(r[0]), __uint_as_float(r[1]));
    auto q = __builtin_amdgcn_permlane32_swap(__float_as_uint(s), __float_as_uint(s), false, false);
    return fmx(__uint_as_float(q[0]), __uint_as_float(q[1]));
}
DEV float sum_g(float s) {
    auto r = __builtin_amdgcn_permlane16_swap(__float_as_uint(s), __float_as_uint(s), false, false);
    s = __uint_as_float(r[0]) + __uint_as_float(r[1]);
    auto q = __builtin_amdgcn_permlane32_swap(__float_as_uint(s), __float_as_uint(s), false, false);
    return __uint_as_float(q[0]) + __uint_as_float(q[1]);
}
DEV float ror8(float s) { return __int_as_float(__builtin_amdgcn_mov_dpp(__float_as_int(s), 0x128, 0xf, 0xf, true)); }
template <int CTRL> DEV unsigned dppu(unsigned x) { return (unsigned)__builtin_amdgcn_mov_dpp((int)x, CTRL, 0xf, 0xf, true); }
DEV v4u quad4(unsigned x) { return (v4u){x, dppu<0x55>(x), dppu<0xAA>(x), dppu<0xFF>(x)}; }
DEV unsigned umax4(v4u v) { return max(max(v.x, v.y), max(v.z, v.w)); }
DEV rsrc_t mkrsrc(const void* p, unsigned bytes) { return __builtin_amdgcn_make_buffer_rsrc((void*)p, 0, bytes, 0x00020000); }
DEV v4u ld16(rsrc_t r, int vo, int so, int aux) { return aux ? __builtin_amdgcn_raw_buffer_load_b128(r, vo, so, SC1) : __builtin_amdgcn_raw_buffer_load_b128(r, vo, so, 0); }
DEV v2u ld8s(rsrc_t r, int vo, int so) { return __builtin_amdgcn_raw_buffer_load_b64(r, vo, so, SC1); }
DEV unsigned ld4s(rsrc_t r, int vo, int so) { return __builtin_amdgcn_raw_buffer_load_b32(r, vo, so, SC1); }
DEV void st16s(rsrc_t r, int vo, int so, v4u v) {
    __builtin_amdgcn_raw_buffer_store_b128(v, r, vo, so, SC1); asm volatile("s_nop 1" :: "v"(v) : "memory");
}
DEV void st8s(rsrc_t r, int vo, int so, v2u v) { __builtin_amdgcn_raw_buffer_store_b64(v, r, vo, so, SC1); }
DEV void st4s(rsrc_t r, int vo, int so, unsigned v) { __builtin_amdgcn_raw_buffer_store_b32(v, r, vo, so, SC1); }
DEV void dma16(rsrc_t r, int vo, int so, int ldsaddr) {
    unsigned keep;
    asm volatile("s_mov_b32 %0, m0\n\ts_mov_b32 m0, %4\n\ts_nop 4\n\tbuffer_load_dwordx4 %1, %2, %3 offen lds\n\ts_mov_b32 m0, %0"
                 : "=&s"(keep) : "v"(vo), "s"(r), "s"(__builtin_amdgcn_readfirstlane(so)), "s"(__builtin_amdgcn_readfirstlane(ldsaddr)) : "memory");
}
DEV void dma16s(rsrc_t r, int vo, int so, int ldsaddr) {
    unsigned keep;
    asm volatile("s_mov_b32 %0, m0\n\ts_mov_b32 m0, %4\n\ts_nop 4\n\tbuffer_load_dwordx4 %1, %2, %3 offen sc1 lds\n\ts_mov_b32 m0, %0"
                 : "=&s"(keep) : "v"(vo), "s"(r), "s"(__builtin_amdgcn_readfirstlane(so)), "s"(__builtin_amdgcn_readfirstlane(ldsaddr)) : "memory");
}
#define BAR() asm volatile("s_waitcnt lgkmcnt(0)\n\ts_barrier" ::: "memory")
#define BAR0() asm volatile("s_barrier" ::: "memory")
#define VMWAIT(n) asm volatile("s_waitcnt vmcnt(" #n ")" ::: "memory")
#define FENCE() asm volatile("" ::: "memory")
#define SCHED() __builtin_amdgcn_sched_barrier(0)
DEV v4u lds16(const char* l, int a) { return *(const v4u*)(l + a); }
DEV v4u lds16a(int a) { return *(const LAS v4u*)(uintptr_t)(unsigned)a; }
DEV v2u lds8(const char* l, int a) { return *(const v2u*)(l + a); }
DEV v2u ldstr(const char* l, int a) { v4s r = __builtin_amdgcn_ds_read_tr16_b64_v4i16((LAS v4s*)(l + a)); return __builtin_bit_cast(v2u, r); }
template <int N> DEV void slp() { if constexpr (N > 0) __builtin_amdgcn_s_sleep(N); }
DEV int flagoff(int i) { return OFF_CTL + i * 128; }
template <int SL> DEV bool wait_flags(rsrc_t rs, int vo, unsigned need, unsigned& seen) {
    for (int sp = 0;; ++sp) {
        FENCE();
        seen = ld4s(rs, vo, 0);
        if (__builtin_amdgcn_ballot_w64(seen < need) == 0) return true;
        slp<SL>();
        if ((sp & 255) == 255) {
            if (sp > SPIN_CAP) st4s(rs, flagoff(FL_ABORT), 0, 1u);
            if (sp > SPIN_CAP || __builtin_amdgcn_readfirstlane(ld4s(rs, flagoff(FL_ABORT), 0))) return false;
        }
    }
}
DEV int img(int p) { return ((p & 7) >> 1) * 4 + (p >> 3) * 2 + (p & 1); }
DEV int pperm(int rho, int tau) { return (rho >> 2) * 8 + tau * 4 + (rho & 3); }

#define STAMP(cond, slot, s)
#define LSTAMP(cond, slot, s)
#define ACC_DECL
#define ACC_T(k)
#define ACC_OUT(slot)
#define CSTAMP(cond, slot, s)

template <int which> DEV void pro_tile(const Params& p, char* lds, rsrc_t rs, int b, int h, int b0, int c0, int lane, int wave) {
    const int rho = lane & 15, g = lane >> 4;
    const rsrc_t ra = mkrsrc(which ? p.wv : p.wk, DM * 4096 * 2), rb = mkrsrc(which ? p.wo : p.wq, DM * 4096 * 2);
    int sa[4], sb[2];
#pragma unroll
    for (int k = 0; k < 4; ++k) { const int r = (k & 1) * 8 + (lane >> 3); sa[k] = ((b0 + 32 * wave + pperm(r, k >> 1)) * 4096 + h * 256 + ((lane & 7) ^ (r >> 1)) * 8) * 2; }
#pragma unroll
    for (int k = 0; k < 2; ++k) { const int r = k * 8 + (lane >> 3), ch = (2 * wave + k) * 64 + lane, e = ch / 14;
        sb[k] = which ? ((h * 256 + e) * DM + c0 + (ch - e * 14) * 8) * 2 : ((c0 + wave * 16 + r) * 4096 + h * 256 + ((lane & 7) ^ (r >> 1)) * 8) * 2; }
    const int bstep = which ? 64 * DM * 2 : 128, la = 57344 + wave * 4096;
    v4u a3[4];
#pragma unroll
    for (int c = 0; c < 4; ++c) {
#pragma unroll
        for (int k = 0; k < 2; ++k) dma16(rb, sb[k], c * bstep, c * 14336 + (2 * wave + k) * 1024);
#pragma unroll
        for (int k = 0; k < 4; ++k) { if (c < 3) dma16(ra, sa[k], c * 128, la + c * 28672 + k * 1024); else a3[k] = ld16(ra, sa[k], 384, 0); }
    }
    const int rd = (rho >> 3) * 1024 + (rho & 7) * 128, rx0 = (g ^ (rho >> 1)) * 16, rx1 = ((4 + g) ^ (rho >> 1)) * 16;
    const int tra = (8 * g + (rho >> 2)) * 224 + (rho & 3) * 8;
    v4f acc[7][2];
#pragma unroll
    for (int ct = 0; ct < 7; ++ct) { acc[ct][0] = (v4f){0, 0, 0, 0}; acc[ct][1] = acc[ct][0]; }
    const float sc = which ? 1.f : 0.09016844005556021f;
    const int obase = (which ? OFF_BT : OFF_AP) + ((h * 28 + (c0 >> 4)) * 14 + (b0 >> 5) + wave) * 1024 + lane * 16;
    v4u af[2][4], f[2][7][2];
#pragma unroll
    for (int c = 0; c <= 4; ++c) {
        if (c < 4) {
            if (c == 0) VMWAIT(18); else if (c == 1) VMWAIT(12); else if (c == 2) VMWAIT(6); else VMWAIT(0);
            if (c == 3) {
#pragma unroll
                for (int k = 0; k < 4; ++k) *(v4u*)(lds + la + k * 1024 + lane * 16) = a3[k];
            }
            BAR();
            if (c == 3) { STAMP(b == 0 && wave == 0, 4, 9); STAMP(wave == 0 && b < 64, 30, b); }
        }
        const char* A = lds + la + (c % 3) * 28672; const char* B = lds + c * 14336;
        if (c < 4) { af[c & 1][0] = lds16(A, rd + rx0); af[c & 1][1] = lds16(A, rd + rx1); af[c & 1][2] = lds16(A, 2048 + rd + rx0); af[c & 1][3] = lds16(A, 2048 + rd + rx1); }
#pragma unroll
        for (int ct = 0; ct < 7; ++ct) {
            if (c < 4) {
                if (which == 0) { f[c & 1][ct][0] = lds16(B, ct * 2048 + rd + rx0); f[c & 1][ct][1] = lds16(B, ct * 2048 + rd + rx1); }
                else {
#pragma unroll
                    for (int kk = 0; kk < 2; ++kk) { const v2u x = ldstr(B, tra + kk * 7168 + ct * 32), y = ldstr(B, tra + kk * 7168 + 896 + ct * 32); f[c & 1][ct][kk] = (v4u){x.x, x.y, y.x, y.y}; }
                }
            }
            SCHED();
            if (c > 0) { const int q = (c - 1) & 1;
                acc[ct][0] = mfma32(af[q][1], f[q][ct][1], mfma32(af[q][0], f[q][ct][0], acc[ct][0])); acc[ct][1] = mfma32(af[q][3], f[q][ct][1], mfma32(af[q][2], f[q][ct][0], acc[ct][1])); }
            if (c == 4) st16s(rs, obase, ct * 14336, pk8(acc[ct][0] * sc, acc[ct][1] * sc));
            SCHED();
        }
    }
    STAMP(b == 0 && wave == 0, 4, 10);
    STAMP(wave == 0 && b >= 192, 31, b & 63);
}
extern "C" __global__ void __launch_bounds__(512, 2) k_pro(Params p) {
    extern __shared__ __attribute__((aligned(16))) char lds[];
    const int b = blockIdx.x, tid = threadIdx.x, lane = tid & 63, wave = __builtin_amdgcn_readfirstlane(tid >> 6);
    const int rho = lane & 15, g = lane >> 4;
    const int gemm = (b & 7) * 4 + (b >> 6), sub = (b >> 3) & 7, which = gemm & 1, h = gemm >> 1;
    const int b0 = 224 * (sub & 1), c0 = 112 * (sub >> 1);
    const rsrc_t rs = mkrsrc(p.ws, WS_BYTES);
    LSTAMP(b == 0 && wave == 0, 4, 8); STAMP(wave == 0 && b < 64, 29, b);
    if (wave == 7) {
        const rsrc_t rx = mkrsrc(p.xb, 2048 * DM * 2);
        v4u xf[7], m0[2];
#pragma unroll
        for (int k = 0; k < 7; ++k) { const int pc = b * 7 + k, rt = pc / 14, ks = pc - rt * 14; xf[k] = ld16(rx, ((rt * 16 + (rho ^ ((rt & 1) * 4))) * DM + ks * 32 + g * 8) * 2, 0, 0); }
        const v4u sent = {SENT, SENT, SENT, SENT};
#pragma unroll
        for (int k = 0; k < 2; ++k) {
            const int o = (b * 2 + k) * 1024 - OFF_MEM0, c = ((o & 8191) >> 4) + lane;
            m0[k] = sent;
            if (o >= 0 && o < 32768 && c < 448) { const unsigned* m = (const unsigned*)(p.mem0 + (((c >> 2) & 3) * 2 + (c & 1)) * DM + (c >> 4) * 16 + (c & 2)); m0[k] = (v4u){m[0], m[2], m[4], m[6]}; }
        }
        SCHED();
        BAR0(); BAR0(); BAR0(); BAR0();
        const unsigned rot = ((unsigned)__builtin_amdgcn_s_getreg(20 | (3 << 11)) - b) & 7u;
        if (lane < 32) st16s(rs, OFF_CTL + b * 512 + lane * 16, 0, (v4u){b * 4 == FL_ROT && lane == 0 ? rot : 0u, 0u, 0u, 0u});
#pragma unroll
        for (int k = 0; k < 2; ++k) st16s(rs, OFF_HO + (b * 2 + k) * 1024 + lane * 16, 0, m0[k]);
#pragma unroll
        for (int k = 62; k < 64; ++k)
            st16s(rs, (b < 112 ? OFF_T + k * NH * 7168 + b * 1024 : b < 144 ? OFF_PX + k * NH * 2048 + (b - 112) * 1024 : b < 152 ? OFF_PM + k * NH * 512 + (b - 144) * 1024 : OOB - 1024) + lane * 16, 0, sent);
#pragma unroll
        for (int k = 0; k < 4; ++k) st16s(rs, OFF_G0 + (b * 4 + k) * 1024 + lane * 16, 0, sent);
#pragma unroll
        for (int k = 0; k < 7; ++k) st16s(rs, OFF_XF + (b * 7 + k) * 1024 + lane * 16, 0, xf[k]);
        return;
    }
    if (which) pro_tile<1>(p, lds, rs, b, h, b0, c0, lane, wave); else pro_tile<0>(p, lds, rs, b, h, b0, c0, lane, wave);
}

struct Ctx { rsrc_t rs; char* lds; int lane, wave, rho, g; };

DEV int slot_base(int s, int cal) { return OFF_HO + (s & 3) * HO_SLOT + cal * 8192; }
DEV int mem_base(int s, int cal) { return s ? slot_base(s, cal) + HO_MEM : OFF_MEM0 + cal * 8192; }
#define STAGE_HEAD "s_add_i32 %[bud], %[bud], -1\n\ts_cmp_lt_i32 %[bud], 0\n\ts_cselect_b32 vcc_hi, -1, -2\n\tv_mov_b32 v233, vcc_hi\n\t"
#define STAGE_TAIL "v_cmp_gt_u32 vcc, v232, v233\n\ts_cbranch_vccnz 1b\n\ts_waitcnt lgkmcnt(0)\n\ts_barrier"
#define STAGE_CLOB "memory", "vcc", "scc", "v224", "v225", "v226", "v227", "v228", "v229", "v230", "v231", "v232", "v233"
DEV void stage1(rsrc_t rs, int vo, int so, int& budget) {
    asm volatile("1:\n\tbuffer_load_dwordx4 v[224:227], %[vo], %[rs], %[so] offen sc1\n\t" STAGE_HEAD
                 "s_waitcnt vmcnt(0)\n\tds_write_b128 %[vo], v[224:227]\n\tv_max3_u32 v232, v224, v225, v226\n\tv_max_u32 v232, v232, v227\n\t" STAGE_TAIL
                 : [bud] "+s"(budget) : [vo] "v"(vo), [rs] "s"(rs), [so] "s"(so) : STAGE_CLOB);
}
DEV void stage2(rsrc_t rs, int vo, int vo2, int so, int& budget) {
    asm volatile("1:\n\tbuffer_load_dwordx4 v[224:227], %[vo], %[rs], %[so] offen sc1\n\tbuffer_load_dwordx4 v[228:231], %[vo2], %[rs], %[so] offen sc1\n\t" STAGE_HEAD
                 "s_waitcnt vmcnt(1)\n\tds_write_b128 %[vo], v[224:227]\n\tv_max3_u32 v232, v224, v225, v226\n\t"
                 "s_waitcnt vmcnt(0)\n\tds_write_b128 %[vo2], v[228:231]\n\tv_max3_u32 v232, v232, v227, v228\n\tv_max3_u32 v232, v232, v229, v230\n\tv_max_u32 v232, v232, v231\n\t" STAGE_TAIL
                 : [bud] "+s"(budget) : [vo] "v"(vo), [vo2] "v"(vo2), [rs] "s"(rs), [so] "s"(so) : STAGE_CLOB);
}
DEV void stage2o(rsrc_t rs, int vo, int vo2, int so, int& budget, v4u& a, v4u& b) {
    asm volatile("1:\n\tbuffer_load_dwordx4 v[224:227], %[vo], %[rs], %[so] offen sc1\n\tbuffer_load_dwordx4 v[228:231], %[vo2], %[rs], %[so] offen sc1\n\t" STAGE_HEAD
                 "s_waitcnt vmcnt(1)\n\tds_write_b128 %[vo], v[224:227]\n\tv_max3_u32 v232, v224, v225, v226\n\t"
                 "s_waitcnt vmcnt(0)\n\tds_write_b128 %[vo2], v[228:231]\n\tv_max3_u32 v232, v232, v227, v228\n\tv_max3_u32 v232, v232, v229, v230\n\tv_max_u32 v232, v232, v231\n\t" STAGE_TAIL
                 : [bud] "+s"(budget), "=&{v[224:227]}"(a), "=&{v[228:231]}"(b) : [vo] "v"(vo), [vo2] "v"(vo2), [rs] "s"(rs), [so] "s"(so) : "memory", "vcc", "scc", "v232", "v233");
}
DEV void give_up(rsrc_t rs) { st4s(rs, flagoff(FL_ABORT), 0, 1u); }
#define PIN1(a) asm volatile("" : "+v"(a))
#define PIN2(a, b) asm volatile("" : "+v"(a), "+v"(b))
constexpr int CAL_STEP = 10;
DEV void role_cal(const Params& p, const Ctx& c, int pair) {
    if (c.wave) return;
    unsigned best[4] = {0xffffu, 0xffffu, 0xffffu, 0xffffu}, half = 0;
    if (pair) { if (!wait_flags<2>(c.rs, flagoff(FL_CAL), 1u, half)) return; half = __builtin_amdgcn_readfirstlane(half) & 1; }
#pragma unroll 1
    for (int r = 0; r < 12; ++r) {
#pragma unroll
        for (int k = 0; k < 4; ++k) {
            const int off = OFF_PROBE + (pair ? half + (k & 2) : k >> 1) * 8192 + (k & 1) * 4096 + (pair * 16 + r) * 128;
            unsigned long long t0, t1; unsigned v;
            asm volatile("s_memtime %0\n\ts_waitcnt lgkmcnt(0)\n\tbuffer_load_dword %2, %3, %4, 0 offen sc1\n\ts_waitcnt vmcnt(0)\n\ts_memtime %1\n\ts_waitcnt lgkmcnt(0)"
                         : "=&s"(t0), "=&s"(t1), "=&v"(v) : "v"(off), "s"(c.rs) : "memory");
            best[k] = min(best[k], (unsigned)(t1 - t0));
        }
        if (r == 2 && !pair) { if (c.lane == 0) st4s(c.rs, flagoff(FL_CAL), 0, min(best[2], best[3]) < min(best[0], best[1]) ? 3u : 2u); VMWAIT(0); }
    }
    if (c.lane == 0) st4s(c.rs, flagoff(FL_CAL + 1 + pair), 0, best[2] + best[3] < best[0] + best[1] ? 3u : 2u);
}
DEV int get_cal(const Ctx& c, int late, int& budget) {
    unsigned seen;
    if (!wait_flags<2>(c.rs, flagoff(FL_CAL + late + (late & c.lane)), 1u, seen)) budget = -1;
    return (__builtin_amdgcn_readlane(seen, 0) & 1) | (late ? (__builtin_amdgcn_readlane(seen, 1) & 1) << 1 : 0);
}
DEV unsigned now() { return (unsigned)__builtin_amdgcn_s_memtime(); }
DEV void wait_until(unsigned t) {
    int rem = (int)(t - now());
    while (rem > 260) { __builtin_amdgcn_s_sleep(1); rem = (int)(t - now()); }
    rem -= 60;
    asm volatile("1:\n\ts_add_i32 %0, %0, -40\n\ts_cmp_gt_i32 %0, 0\n\ts_cbranch_scc1 1b" : "+s"(rem) :: "scc");
}
#define T_PM 2000
#define T_OS 2075
#define T_OM 300
#define T_OU 650
#define T_G0 400
#define T_PG 200
#define ABL(b) true
#define W_LAG_FROM 8
#define S_AT 6

#define MEM_TIMES_W(lbase)                                                                                           \
    v4u a_[7];                                                                                                       \
    _Pragma("unroll") for (int f = 0; f < 7; ++f) a_[f] = lds16a((lbase) + f * 1024);                                \
    EXTRA_READS;                                                                                                     \
    SCHED();                                                                                                         \
    _Pragma("unroll") for (int f = 0; f < 7; ++f) { c0 = smf(a_[f], w[0][2 * f], w[0][2 * f + 1], c0, sidx); c1 = smf(a_[f], w[1][2 * f], w[1][2 * f + 1], c1, sidx); AFTER_STEP(f); } \
    const v2u tp = {pk2(c0.x + c0.z, c0.y + c0.w), pk2(c1.x + c1.z, c1.y + c1.w)};

#define MEM_TIMES_W_OWN()                                                                                            \
    v4u a_[5];                                                                                                       \
    _Pragma("unroll") for (int k = 0; k < 5; ++k) a_[k] = lds16a(fr[k]);                                             \
    EXTRA_READS;                                                                                                     \
    SCHED();                                                                                                         \
    c0 = smf(a0, w[0][0], w[0][1], c0, sidx); c1 = smf(a0, w[1][0], w[1][1], c1, sidx);                              \
    c0 = smf(a1, w[0][2], w[0][3], c0, sidx); c1 = smf(a1, w[1][2], w[1][3], c1, sidx);                              \
    SCHED();                                                                                                         \
    _Pragma("unroll") for (int k = 0; k < 5; ++k) { c0 = smf(a_[k], w[0][2 * k + 4], w[0][2 * k + 5], c0, sidx); c1 = smf(a_[k], w[1][2 * k + 4], w[1][2 * k + 5], c1, sidx); } \
    const v2u tp = {pk2(c0.x + c0.z, c0.y + c0.w), pk2(c1.x + c1.z, c1.y + c1.w)};

#define VCLOB "v224", "v225", "v226", "v227", "v228", "v229", "v230", "v231", VCLOB_HI
#define VCLOB_HI "v232", "v233", "v234", "v235", "v236", "v237", "v238", "v239", \
              "v240", "v241", "v242", "v243", "v244", "v245", "v246", "v247", "v248", "v249", "v250", "v251", "v252", "v253", "v254", "v255"
#define PUBLISH_ASM(ST)                                                                                                                     \
    "s_cmp_lg_u32 %[pub], 7\n\ts_waitcnt lgkmcnt(0)\n\ts_barrier\n\ts_cbranch_scc1 9f\n\t"                                                  \
    "ds_read_b64 v[224:225], %[la]\n\tds_read_b64 v[226:227], %[la] offset:" #ST "*1\n\tds_read_b64 v[228:229], %[la] offset:" #ST "*2\n\t" \
    "ds_read_b64 v[230:231], %[la] offset:" #ST "*3\n\tds_read_b64 v[232:233], %[la] offset:" #ST "*4\n\t"                                  \
    "ds_read_b64 v[234:235], %[la] offset:" #ST "*5\n\tds_read_b64 v[236:237], %[la] offset:" #ST "*6\n\t"                                  \
    "ds_read_b64 v[238:239], %[la] offset:" #ST "*7\n\t"                                                                                    \
    "s_waitcnt lgkmcnt(6)\n\tv_pk_add_f32 v[224:225], v[224:225], v[226:227]\n\t"                                                           \
    "s_waitcnt lgkmcnt(4)\n\tv_pk_add_f32 v[228:229], v[228:229], v[230:231]\n\t"                                                           \
    "s_waitcnt lgkmcnt(2)\n\tv_pk_add_f32 v[232:233], v[232:233], v[234:235]\n\tv_pk_add_f32 v[224:225], v[224:225], v[228:229]\n\t"        \
    "s_waitcnt lgkmcnt(0)\n\tv_pk_add_f32 v[236:237], v[236:237], v[238:239]\n\tv_pk_add_f32 v[224:225], v[224:225], v[232:233]\n\t"        \
    "s_nop 0\n\tv_pk_add_f32 v[224:225], v[224:225], v[236:237]\n\t"                                                                        \
    "s_nop 0\n\tv_cvt_pk_bf16_f32 v224, v224, v225\n\ts_nop 1\n\t"                                                                          \
    "v_mov_b32_dpp v225, v224 quad_perm:[1,1,1,1] row_mask:0xf bank_mask:0xf\n\t"                                                           \
    "v_mov_b32_dpp v226, v224 quad_perm:[2,2,2,2] row_mask:0xf bank_mask:0xf\n\t"                                                           \
    "v_mov_b32_dpp v227, v224 quad_perm:[3,3,3,3] row_mask:0xf bank_mask:0xf\n\t"                                                           \
    "buffer_store_dwordx4 v[224:227], %[vo], %[rs], %[so] offen sc1\n\t"                                                                    \
    "9:\n\ts_barrier\n\ts_nop 0"

DEV void role_p(const Params& p, const Ctx& c, int idx) {
    const int h = idx >> 2, q = idx & 3, lane = c.lane, wave = c.wave, rho = c.rho, g = c.g;
    const rsrc_t rs = c.rs; char* lds = c.lds;
    const int om = q * 4 + wave;
    const bool isT = wave < 4 && om < 14, isX = wave < 4 && om >= 14;
    constexpr int L_MEM = 0, L_PART = 8192, L_DUMP = 10240, L_CAL = 11264, L_X = 16384;
    const int sidx = (rho & 2) ? 0xEEEE : 0x4444;
    v2u sel; { const int r0 = rho - 2 * g; sel.x = (r0 == 0 ? 0x3f80u : 0u) | (r0 == 1 ? 0x3f800000u : 0u); sel.y = (r0 == 8 ? 0x3f80u : 0u) | (r0 == 9 ? 0x3f800000u : 0u); }
    const int f0 = isX ? 4 : 2 * (wave & 3);
    auto blk = [&](int k) { return f0 + k >= 7 ? f0 + k - 7 : f0 + k; };
    const int po = f0 * 1024 + lane * 16, po2 = blk(1) * 1024 + lane * 16;
    int fr[5];
#pragma unroll
    for (int k = 0; k < 5; ++k) fr[k] = L_MEM + blk(k + 2) * 1024 + lane * 16;
    int budget = SPIN_CAP;
#pragma unroll
    for (int k = 0; k < 7; ++k) dma16(rs, OFF_XF + lane * 16, (wave * 7 + k) * 1024, L_X + (wave * 7 + k) * 1024);
    if (isT) {
        v4u w[2][14];
#pragma unroll
        for (int t = 0; t < 2; ++t)
#pragma unroll
            for (int ks = 0; ks < 14; ++ks) w[t][ks] = ld16(rs, OFF_AP + (h * 28 + 2 * om + t) * 14336 + g * 256 + img(rho) * 16, (2 * blk(ks >> 1) + (ks & 1)) * 1024, 0);
        VMWAIT(28);
        BAR0();
        STAMP(wave == 0, 23, idx);
#pragma unroll 1
        for (int sg = 0; sg < 2; ++sg) {
            v4f a[2][2] = {{{0, 0, 0, 0}, {0, 0, 0, 0}}, {{0, 0, 0, 0}, {0, 0, 0, 0}}};
#pragma unroll
            for (int ks = 0; ks < 14; ++ks)
#pragma unroll
                for (int kt = 0; kt < 2; ++kt) { const v4u x = lds16(lds, L_X + sg * 28672 + kt * 14336 + (2 * blk(ks >> 1) + (ks & 1)) * 1024 + g * 256 + (rho ^ (kt * 4)) * 16);
                    a[kt][0] = mfma32(w[0][ks], x, a[kt][0]); a[kt][1] = mfma32(w[1][ks], x, a[kt][1]); }
#pragma unroll
            for (int kt = 0; kt < 2; ++kt) {
                auto x = __builtin_amdgcn_permlane32_swap(pk2(a[kt][0].x, a[kt][0].y), pk2(a[kt][1].x, a[kt][1].y), false, false);
                auto y = __builtin_amdgcn_permlane32_swap(pk2(a[kt][0].z, a[kt][0].w), pk2(a[kt][1].z, a[kt][1].w), false, false);
                st16s(rs, OFF_G0 + (h * 14 + om) * 2048 + kt * 1024 + lane * 16, sg * (NH * 28672), (v4u){x[0], x[1], y[0], y[1]});
            }
            BAR0();
            STAMP(wave == 0, 24 + sg, idx);
        }
        int cal = __builtin_amdgcn_readfirstlane(*(const int*)(lds + L_CAL));
        STAMP(wave == 0, 26, idx);
        const int qa = L_MEM + (2 * om + (rho >> 3)) * 256 + (((rho & 7) >> 1) * 4 + (g >> 1) * 2 + (rho & 1)) * 16 + (g & 1) * 8;
        const int pw = (g >> 1) == (rho >> 3) ? L_PART + (2 * wave + (rho >> 3)) * 256 + (rho & 7) * 32 + (g & 1) * 16 : L_DUMP + lane * 16;
        const int tw = lane < 32 ? OFF_T + (h * 14 + om) * 512 + ((rho >> 3) * 2 + g) * 128 + (rho & 7) * 16 : OOB;
#pragma unroll 1
        for (int s = 0; s < NSEG && budget >= 0; ++s) {
            if (s == CAL_STEP) cal = __builtin_amdgcn_readfirstlane(*(const int*)(lds + L_CAL));
            v4f c0 = {0, 0, 0, 0}, c1 = {0, 0, 0, 0}; PIN2(c0, c1);
            BAR0();
            v4u a0, a1; v2u mq;
            stage2o(rs, po, po2, mem_base(s, cal), budget, a0, a1);
#define EXTRA_READS mq = lds8(lds, qa)
            MEM_TIMES_W_OWN()
#undef EXTRA_READS
            const v4f d2 = mfma16(tp, sel, (v4f){0, 0, 0, 0});
            const v2u d = {pk2(d2.x, d2.y), pk2(d2.z, d2.w)};
            *(v4f*)(lds + pw) = mfma16(d, mq, (v4f){0, 0, 0, 0});
            BAR();
            BAR0();
            if (s < NSEG - 1) wait_until(now() + T_PG);
            if (ABL(8)) { auto x = __builtin_amdgcn_permlane32_swap(d.x, d.y, false, false);
              auto y = __builtin_amdgcn_permlane32_swap(x[0], x[0], false, false); auto z = __builtin_amdgcn_permlane32_swap(x[1], x[1], false, false);
              st16s(rs, tw, s * (NH * 7168), (v4u){y[0], z[0], y[1], z[1]}); }
        }
    } else if (isX) {
        v4u w[2][14];
        int cal = get_cal(c, 0, budget);
        BAR0(); BAR0();
        if (om == 14) wait_until(now() + T_G0);
        else BAR0();
        const int gl0 = OFF_G + h * 28672 + (rho >> 3) * 1024 + g * 256 + (pperm(rho, 0) & 15) * 16;
        const int gfl = flagoff(FL_W + h * 7 + (lane & 3));
        const int ew = rho < 8 ? HO_E + h * 512 + g * 128 + rho * 16 : OOB, mw = g == 0 ? L_PART + 1024 + (rho & 7) * 32 + (rho >> 3) * 16 : L_DUMP + lane * 16;
        auto load_g = [&](int s, int t) {
            FENCE();
#pragma unroll
            for (int ks = 0; ks < 14; ++ks) w[t][ks] = ld16(rs, gl0 + t * 64, s * (NH * 28672) + (s < 2 ? OFF_G0 - OFF_G : 0) + (ks < 6 ? ks + 8 : ks - 6) * 2048, 1);
            FENCE();
        };
        bool lack = false;
#pragma unroll 1
        for (int s = om - 14; s < NSEG; s += 2) {
            const int sl = min(s, NSEG - 2);
            if (s == 1) BAR0();
            if (s >= 1) wait_until(now() + T_PG);
            unsigned seen;
            if (lack && !wait_flags<1>(rs, gfl, sl, seen)) budget = -1;
            load_g(sl, 0);
            if (s >= 2) { BAR0(); BAR0(); BAR0(); BAR0(); wait_until(now() + T_PG); }
            load_g(sl, 1);
            seen = ld4s(rs, gfl, 0);
            if (s == 1) { BAR0(); BAR0(); BAR0(); }
            if (s < 2) {
#pragma unroll 1
                for (;;) {
                    unsigned m = 0;
#pragma unroll
                    for (int ks = 0; ks < 14; ++ks) m = max(m, max(umax4(w[0][ks]), umax4(w[1][ks])));
                    if (__builtin_amdgcn_ballot_w64(m == SENT) == 0 || --budget < 0) break;
                    load_g(s, 0); load_g(s, 1);
                }
                if (s == 0) BAR0();
            }
            if ((s | 1) == CAL_STEP + 1) cal = __builtin_amdgcn_readfirstlane(*(const int*)(lds + L_CAL));
            const int sb = slot_base(s, cal);
            v4f c0 = {0, 0, 0, 0}, c1 = {0, 0, 0, 0}; PIN2(c0, c1);
            lack = __builtin_amdgcn_ballot_w64(seen < (unsigned)min(s + 2, NSEG - 2)) != 0;
            SCHED();
            STAMP(idx == 3 && s == 0, 4, 12); STAMP(s == 0, 27, h); STAMP(s == 1, 27, 16 + h);
            BAR0();
            v4u a0, a1;
            stage2o(rs, po, po2, mem_base(s, cal), budget, a0, a1);
            if (s < NSEG - 1) {
#define EXTRA_READS
                MEM_TIMES_W_OWN()
#undef EXTRA_READS
                const v4f d2 = mfma16(tp, sel, (v4f){0, 0, 0, 0});
                float m = fmx(fmx(d2.x, d2.y), fmx(d2.z, d2.w));
                m = max_g(fmx(m, ror8(m)));
                *(v4f*)(lds + mw) = (v4f){-m, -m, -m, -m};
                SCHED();
                BAR();
                const float e0 = __builtin_amdgcn_exp2f(d2.x - m), e1 = __builtin_amdgcn_exp2f(d2.y - m), e2 = __builtin_amdgcn_exp2f(d2.z - m), e3 = __builtin_amdgcn_exp2f(d2.w - m);
                const unsigned u0 = pk2(e0, e1), u1 = pk2(e2, e3);
                st16s(rs, ew, sb, (v4u){u0, u1, dppu<0x128>(u0), dppu<0x128>(u1)});
            } else BAR0();
            BAR0();
        }
        if (om == 14) { BAR0(); BAR0(); BAR0(); BAR0(); }
    } else {
        if (wave == 4) { *(v4u*)(lds + L_PART + lane * 16) = (v4u){0u, 0u, 0u, 0u}; *(v4u*)(lds + L_PART + 1024 + lane * 16) = (v4u){0u, 0u, 0u, 0u}; }
        BAR();
        int cal = get_cal(c, 0, budget);
        if (wave == 6) *(int*)(lds + L_CAL) = cal;
        BAR(); BAR0();
        unsigned tref = now() - 65536;
        STAMP(idx == 0 && wave == 7, 4, 1);
        const bool stg = q == 3 && wave == 4;
        const int rw = lane < 8 ? HO_PART + idx * 128 + lane * 16 : q == 3 && lane >= 32 ? HO_E + h * 512 + (lane - 32) * 16 : OOB;
        int calp = cal, calq = cal;
        const int pr = L_PART + (lane & 31) * 8, pubo = lane < 32 && (lane & 3) == 0 ? HO_PART + idx * 128 + lane * 4 : OOB;
#pragma unroll 1
        for (int s = 0; s < NSEG && budget >= 0; ++s) {
            if (s == CAL_STEP - 1 && wave == 6) *(int*)(lds + L_CAL) = get_cal(c, 1, budget);
            if (s == CAL_STEP) cal = __builtin_amdgcn_readfirstlane(*(const int*)(lds + L_CAL));
            const int sb = slot_base(s, cal);
            if (wave == 5 && s >= 2) {
                wait_until(tref + T_PG);
                st16s(rs, rw, slot_base(s - 2, calp), (v4u){SENT, SENT, SENT, SENT});
                VMWAIT(0);
            }
            calp = calq; calq = cal;
            if (wave == 7) wait_until(tref + T_PM);
            BAR0();
            if (stg) {
                stage1(rs, 6144 + lane * 16, mem_base(s, cal), budget);
            } else BAR0();
            STAMP(idx == 0 && wave == 7, 0, s); STAMP(idx == 3 && wave == 6, 12, s);
            asm volatile(PUBLISH_ASM(256) :: [pub] "s"(wave), [la] "v"(pr), [vo] "v"(pubo), [rs] "s"(rs), [so] "s"(sb) : "memory", "scc", VCLOB);
            tref = now();
            if (wave == 7) {
                STAMP(idx == 0, 1, s); STAMP(s == 0, 18, idx); STAMP(s == 1, 17, idx);
                LSTAMP(idx == 0 && (s == 1 || s == 32 || s == 62), 14, s);
                if (lane == 0) st4s(rs, flagoff(FL_T + idx), 0, s);
            }
        }
    }
    if (budget < 0) give_up(rs);
    VMWAIT(0);
    BAR();
    if (wave == 7 && lane == 0) st4s(rs, flagoff(FL_T + idx), 0, NSEG);
}

DEV void role_o(const Params& p, const Ctx& c, int t) {
    const int lane = c.lane, wave = c.wave, rho = c.rho, g = c.g;
    const rsrc_t rs = c.rs; char* lds = c.lds;
    constexpr int L_MEM = 0, L_OUT = 8192, L_DUMP = 12288, L_V = 13312, L_CAL = 21504, L_X = 24576;
#pragma unroll
    for (int k = 0; k < 4; ++k) dma16(rs, wave * 4 + k < 28 ? OFF_XF + lane * 16 : OOB, (wave * 4 + k) * 1024, L_X + (wave * 4 + k) * 1024);
    v4u w[2][14];
#pragma unroll
    for (int k = 0; k < 2; ++k)
#pragma unroll
        for (int ks = 0; ks < 14; ++ks) w[k][ks] = ld16(rs, OFF_BT + ((2 * wave + k) * 28 + t) * 14336 + lane * 16, ks * 1024, 0);
    VMWAIT(28);
    BAR0();
    {
        v4u u[2];
#pragma unroll
        for (int k = 0; k < 2; ++k) {
            v4f a0 = {0, 0, 0, 0}, a1 = a0;
#pragma unroll
            for (int ks = 0; ks < 14; ++ks) { const int xo = L_X + (rho >> 3) * 14336 + ks * 1024 + g * 256;
                a0 = mfma32(lds16(lds, xo + ((pperm(rho, 0) & 15) ^ ((rho >> 3) * 4)) * 16), w[k][ks], a0); a1 = mfma32(lds16(lds, xo + ((pperm(rho, 1) & 15) ^ ((rho >> 3) * 4)) * 16), w[k][ks], a1); }
            u[k] = pk8(a0, a1);
        }
        asm volatile("v_mov_b32 v216, %0\n\tv_mov_b32 v217, %1\n\tv_mov_b32 v218, %2\n\tv_mov_b32 v219, %3\n\tv_mov_b32 v220, %4\n\tv_mov_b32 v221, %5\n\tv_mov_b32 v222, %6\n\tv_mov_b32 v223, %7"
                     :: "v"(u[0].x), "v"(u[0].y), "v"(u[0].z), "v"(u[0].w), "v"(u[1].x), "v"(u[1].y), "v"(u[1].z), "v"(u[1].w) : "v216", "v217", "v218", "v219", "v220", "v221", "v222", "v223");
    }
    int budget = SPIN_CAP;
    int cal = get_cal(c, 0, budget), caln = cal;
    const int sidx = (rho & 2) ? 0xEEEE : 0x4444;
    const int fa = L_MEM + lane * 16;
    const int hb = rho >> 3, hd = 2 * wave + hb, i = rho & 7;
    const int pa = HO_PART + hd * 512 + g * 128 + i * 16, ea = HO_E + hd * 512 + g * 128 + i * 16;
    const int ul = OFF_U + (t * 16 + 2 * wave) * 1024 + lane * 16;
    const int ufl = flagoff(FL_W + rho * 7 + ((14 + (t >> 1)) >> 2));
    const int mo = (lane & 3) == 0 ? HO_MEM + t * 256 + ((lane >> 4) * 4 + (lane & 4) / 2 + ((lane >> 3) & 1)) * 16 : OOB;
    const int p2 = (lane & 4) ? 2 * (lane & 3) + 1 : 2 * (lane & 3);
    const int ra = L_OUT + (lane >> 3) * 64 + p2 * 8;
    const int ow = hb == 0 ? L_OUT + wave * 512 + i * 64 + g * 16 : L_DUMP + lane * 16;
    const int po = wave * 1024 + lane * 16, po2 = min(wave + 4, 6) * 1024 + lane * 16;
    const v4u ones = {0x3f803f80u, 0x3f803f80u, 0x3f803f80u, 0x3f803f80u};
    const v2u ones2 = {0x3f803f80u, 0x3f803f80u};
    const unsigned mlo = hb ? 0u : 0xffffffffu, mhi = ~mlo;
    const unsigned long long hbm = 0xff00ff00ff00ff00ull;
    v4u psel; { const int kq = (rho >> 2) * 2 + (rho & 1); const unsigned one = (kq & 1) ? 0x3f800000u : 0x3f80u;
        psel = (v4u){(kq >> 1) == 0 ? one : 0u, (kq >> 1) == 1 ? one : 0u, (kq >> 1) == 2 ? one : 0u, (kq >> 1) == 3 ? one : 0u}; }
    asm volatile("v_mov_b32 v215, 0" ::: "v215");
    auto check_u = [&](int s) {
        unsigned long long lack; unsigned seen;
        asm volatile("v_cmp_gt_u32 %0, %1, v215" : "=s"(lack) : "s"(s) : "memory");
        if (lack != 0 && !wait_flags<1>(rs, ufl, s, seen)) budget = -1;
    };
    auto load_u = [&](int s) {
        asm volatile("s_nop 4\n\tbuffer_load_dwordx4 v[216:219], %0, %1, %2 offen sc1\n\tbuffer_load_dwordx4 v[220:223], %0, %1, %2 offen offset:1024 sc1"
                     :: "v"(ul), "s"(rs), "s"(s * (NH * 28672)) : "memory", "v216", "v217", "v218", "v219", "v220", "v221", "v222", "v223");
    };
    auto store_v = [&](int s) { st16s(rs, OFF_V + t * 4096 + (wave & 3) * 1024 + lane * 16, s * (NH * 7168), lds16(lds, L_V + (s & 1) * 4096 + (wave & 3) * 1024 + lane * 16)); };
    ACC_DECL;
    unsigned tref = now() - 65536;
    auto mem_times_b = [&](int sb, int s, bool last) -> v2u {
        v4f c0 = {0, 0, 0, 0}, c1 = {0, 0, 0, 0};
        if (wave < 4) stage2(rs, po, po2, s ? sb + HO_MEM : sb + OFF_MEM0, budget);
        else BAR0();
        if (last && wave == 7 && lane == 0) st4s(rs, flagoff(FL_V + t), 0, s);
        ACC_T(2);
#define AFTER_STEP(f) if (f == S_AT && !last) { SCHED(); ACC_T(4); if (wave == 0) wait_until(tref + T_OS); ACC_T(5); asm volatile("s_barrier\n\tbuffer_load_dwordx4 v[224:227], %0, %2, %3 offen sc1\n\tbuffer_load_dwordx4 v[228:231], %1, %2, %3 offen sc1" \
                          :: "v"(pa), "v"(ea), "s"(rs), "s"(sb) : "memory", VCLOB); SCHED(); }
#define EXTRA_READS if (wave < 4 && !last && s > 0 && ABL(1)) load_u(s)
        MEM_TIMES_W(fa)
#undef EXTRA_READS
        *(v2u*)(lds + L_V + (s & 1) * 4096 + wave * 512 + lane * 8) = tp;
        return tp;
    };
    STAMP(t == 0 && wave == 0, 4, 13); STAMP(wave == 0, 28, t); STAMP(wave == 7, 28, 32 + t);
    STAMP(t == 0 && wave == 0, 4, 14);
#pragma unroll 1
    for (int s = 0; s < NSEG - 1 && budget >= 0; ++s) {
        const int sb = slot_base(s, s < CAL_STEP ? cal : caln), sn = slot_base(s + 1, s + 1 < CAL_STEP ? cal : caln);
        ACC_T(0);
        const v2u vp = mem_times_b(sb, s, false);
        ACC_T(3);
        asm volatile(
            "s_branch 3f\n\t"
            "1:\n\t"
            "buffer_load_dwordx4 v[224:227], %[pa], %[rs], %[sb] offen sc1\n\t"
            "buffer_load_dwordx4 v[228:231], %[ea], %[rs], %[sb] offen sc1\n\t"
            "3:\n\t"
            "s_add_i32 %[bud], %[bud], -1\n\ts_cmp_lt_i32 %[bud], 0\n\ts_cselect_b32 vcc_hi, -1, -2\n\tv_mov_b32 v253, vcc_hi\n\t"
            "s_waitcnt vmcnt(1)\n\t"
            "v_mfma_f32_16x16x32_bf16 v[232:235], %[psel], v[224:227], 0\n\t"
            "v_max3_u32 v250, v224, v225, v226\n\t"
            "s_waitcnt vmcnt(0)\n\t"
            "v_mfma_f32_16x16x32_bf16 v[244:247], %[ones], v[228:231], 0\n\t"
            "v_max3_u32 v251, v228, v229, v230\n\t"
            "v_mfma_f32_16x16x32_bf16 v[236:239], v[216:219], v[228:231], 0\n\t"
            "v_max3_u32 v250, v250, v227, v231\n\t"
            "v_mfma_f32_16x16x32_bf16 v[240:243], v[220:223], v[228:231], 0\n\t"
            "v_max_u32 v250, v250, v251\n\t"
            "v_cmp_gt_u32 vcc, v250, v253\n\t"
            "s_cbranch_vccnz 1b\n\t"
            "v_exp_f32 v250, v232\n\tv_exp_f32 v251, v233\n\t"
            "v_max_f32 v252, v232, v233\n\t"
            "v_cvt_pk_bf16_f32 v250, v250, v251\n\t"
            "v_cmp_lt_f32 vcc, 0x42800000, v252\n\t"
            "v_and_b32 v248, v250, %[mlo]\n\tv_and_b32 v249, v250, %[mhi]\n\t"
            "v_cndmask_b32 v236, v236, v240, %[hbm]\n\tv_cndmask_b32 v237, v237, v241, %[hbm]\n\t"
            "v_mfma_f32_16x16x16_bf16 v[244:247], %[ones2], v[248:249], v[244:247]\n\t"
            "v_cndmask_b32 v238, v238, v242, %[hbm]\n\tv_cndmask_b32 v239, v239, v243, %[hbm]\n\t"
            "s_nop 1\n\t"
            "v_mfma_f32_16x16x16_bf16 v[236:239], %[vp], v[248:249], v[236:239]\n\t"
            "s_cbranch_vccz 2f\n\t"
            "s_nop 7\n\t"
            "v_max_f32 v252, 0, v252\n\tv_mov_b32 v253, v252\n\ts_nop 1\n\tv_permlane16_swap_b32 v252, v253\n\ts_nop 1\n\t"
            "v_max_f32 v252, v252, v253\n\tv_mov_b32 v253, v252\n\ts_nop 1\n\tv_permlane32_swap_b32 v252, v253\n\ts_nop 1\n\t"
            "v_max_f32 v252, v252, v253\n\t"
            "v_mfma_f32_16x16x32_bf16 v[244:247], %[ones], v[228:231], 0\n\t"
            "v_mfma_f32_16x16x32_bf16 v[236:239], v[216:219], v[228:231], 0\n\t"
            "v_sub_f32 v250, v232, v252\n\tv_sub_f32 v251, v233, v252\n\t"
            "v_exp_f32 v250, v250\n\tv_exp_f32 v251, v251\n\tv_exp_f32 v253, -v252\n\ts_nop 0\n\t"
            "v_cvt_pk_bf16_f32 v250, v250, v251\n\t"
            "v_and_b32 v248, v250, %[mlo]\n\tv_and_b32 v249, v250, %[mhi]\n\ts_nop 7\n\t"
            "v_cndmask_b32 v236, v236, v240, %[hbm]\n\tv_cndmask_b32 v237, v237, v241, %[hbm]\n\tv_cndmask_b32 v238, v238, v242, %[hbm]\n\tv_cndmask_b32 v239, v239, v243, %[hbm]\n\t"
            "v_mul_f32 v236, v236, v253\n\tv_mul_f32 v237, v237, v253\n\tv_mul_f32 v238, v238, v253\n\tv_mul_f32 v239, v239, v253\n\t"
            "v_mul_f32 v244, v244, v253\n\tv_mul_f32 v245, v245, v253\n\tv_mul_f32 v246, v246, v253\n\tv_mul_f32 v247, v247, v253\n\ts_nop 1\n\t"
            "v_mfma_f32_16x16x16_bf16 v[244:247], %[ones2], v[248:249], v[244:247]\n\t"
            "v_mfma_f32_16x16x16_bf16 v[236:239], %[vp], v[248:249], v[236:239]\n\t"
            "s_nop 7\n\t"
            "2:\n\t"
            "s_nop 1\n\t"
            "v_rcp_f32 v252, v244\n\t"
            "s_nop 3\n\t"
            "v_mul_f32 v236, v236, v252\n\tv_mul_f32 v237, v237, v252\n\tv_mul_f32 v238, v238, v252\n\tv_mul_f32 v239, v239, v252\n\t"
            "v_add_f32_dpp v240, v236, v236 row_ror:8 row_mask:0xf bank_mask:0xf\n\tv_add_f32_dpp v241, v237, v237 row_ror:8 row_mask:0xf bank_mask:0xf\n\t"
            "v_add_f32_dpp v242, v238, v238 row_ror:8 row_mask:0xf bank_mask:0xf\n\tv_add_f32_dpp v243, v239, v239 row_ror:8 row_mask:0xf bank_mask:0xf\n\t"
            "ds_write_b128 %[ow], v[240:243]\n\t"
            PUBLISH_ASM(512)
            : [bud] "+s"(budget)
            : [pa] "v"(pa), [ea] "v"(ea), [rs] "s"(rs), [sb] "s"(sb), [psel] "v"(psel), [ones] "v"(ones), [ones2] "v"(ones2), [vp] "v"(vp),
              [mlo] "v"(mlo), [mhi] "v"(mhi), [hbm] "s"(hbm), [ow] "v"(ow),
              [pub] "s"(wave), [la] "v"(ra), [vo] "v"(mo), [so] "s"(sn)
            : "memory", "vcc", "scc", VCLOB);
        tref = now();
        STAMP(t == 0 && wave == 7, 3, s); STAMP(wave == 7 && s < 2, 19, t + 32 * s);
        if (wave == 7 && lane == 0) st4s(rs, flagoff(FL_V + t), 0, s);
        if (s + 2 == CAL_STEP) caln = __builtin_amdgcn_readfirstlane(*(const int*)(lds + L_CAL));
        if (wave == 5 && s + 3 == CAL_STEP) *(int*)(lds + L_CAL) = get_cal(c, 1, budget);
        if (s < 2 && wave >= 4) check_u(s + 1);
        if (s > 0 && wave == 4) check_u(min(s + 2, NSEG - 2));
        ACC_T(6);
        if (wave < 4) wait_until(tref + T_OM);
        else {
            wait_until(tref + T_OU);
            if (s + 1 < NSEG - 1 && ABL(1)) load_u(s + 1);
            if (wave == 4) asm volatile("buffer_load_dword v215, %0, %1, 0 offen sc1" :: "v"(ufl), "s"(rs) : "memory", "v215");
            if (ABL(2)) store_v(s);
            if (wave == 6 && s >= 2) st16s(rs, lane < 16 ? HO_MEM + t * 256 + lane * 16 : OOB, slot_base(s - 1, s - 1 < CAL_STEP ? cal : caln), (v4u){SENT, SENT, SENT, SENT});
        }
        ACC_T(7);
    }
    VMWAIT(0);
    ACC_OUT(t * 8 + wave);
    mem_times_b(slot_base(NSEG - 1, caln), NSEG - 1, true);
    if (budget < 0) give_up(rs);
    BAR();
    if (wave >= 4) store_v(NSEG - 1);
    VMWAIT(0);
    BAR0();
    if (wave == 7 && lane == 0) st4s(rs, flagoff(FL_V + t), 0, NSEG);
}

DEV void role_w(const Params& p, const Ctx& c, int wi) {
    const int lane = c.lane, wave = c.wave, rho = c.rho, g = c.g;
    const rsrc_t rs = c.rs; char* lds = c.lds;
    if (wave >= 4) {
#pragma unroll 1
        for (int n = -3; n < NSEG; ++n) {
            const int vo = n + 3 < NSEG ? OFF_XF + lane * 16 : OOB, so = ((n + 4) & 63) * 28672 + (wave - 4) * 7168, la = ((n + 3) & 3) * 28672 + (wave - 4) * 7168;
#pragma unroll 1
            for (int k = 0; k < 7168; k += 1024) dma16(rs, vo, so + k, la + k);
            if (n < -1) continue;
            if (n == 24 && wave == 4 && wi >= 48) {
                unsigned seen; wait_flags<8>(rs, flagoff(FL_TAB), 28, seen);
                __builtin_amdgcn_fence(__ATOMIC_ACQUIRE, "agent");
                VMWAIT(0);
            }
            VMWAIT(14);
            BAR0();
        }
        VMWAIT(0);
        BAR0();
        return;
    }
    const int h = wi / 7, unit = (wi - h * 7) * 4 + wave;
    const bool isG = unit < 14;
    v4u w[2][14];
#pragma unroll
    for (int t = 0; t < 2; ++t) {
        const int row = isG ? unit * 32 + pperm(rho, t) : (unit - 14) * 32 + t * 16 + rho;
#pragma unroll
        for (int ks = 0; ks < 14; ++ks) w[t][ks] = ld16(rs, (isG ? OFF_AP : OFF_BT) + (h * 28 + (row >> 4)) * 14336 + g * 256 + (row & 15) * 16, ks * 1024, 0);
    }
    int xa[2];
#pragma unroll
    for (int k = 0; k < 2; ++k) { const int key = isG ? k * 16 + rho : pperm(rho, k), kt = key >> 4; xa[k] = kt * 14336 + (g * 16 + ((key & 15) ^ (kt * 4))) * 16; }
    const int ob = (isG ? OFF_G + (h * 14 + unit) * 2048 : OFF_U + ((unit - 14) * 32 + h) * 1024) + lane * 16;
    const int ostep = isG ? 1024 : 16384;
    BAR0();
    STAMP(wi == 20 && wave == 1, 4, 2);
#pragma unroll 1
    for (int n = 0; n < NSEG; ++n) {
        const char* xb = lds + (n & 3) * 28672;
        v4f a00 = {0, 0, 0, 0}, a01 = a00, a10 = a00, a11 = a00;
        v4u x[2][14];
#pragma unroll
        for (int ks = 0; ks < 4; ++ks) { x[0][ks] = lds16(xb, xa[0] + ks * 1024); x[1][ks] = lds16(xb, xa[1] + ks * 1024); }
        if (isG) {
#pragma unroll
            for (int ks = 0; ks < 14; ++ks) {
                if (ks + 4 < 14) { x[0][ks + 4] = lds16(xb, xa[0] + (ks + 4) * 1024); x[1][ks + 4] = lds16(xb, xa[1] + (ks + 4) * 1024); }
                SCHED();
                a00 = mfma32(w[0][ks], x[0][ks], a00); a01 = mfma32(w[1][ks], x[0][ks], a01); a10 = mfma32(w[0][ks], x[1][ks], a10); a11 = mfma32(w[1][ks], x[1][ks], a11);
                SCHED();
            }
        } else {
#pragma unroll
            for (int ks = 0; ks < 14; ++ks) {
                if (ks + 4 < 14) { x[0][ks + 4] = lds16(xb, xa[0] + (ks + 4) * 1024); x[1][ks + 4] = lds16(xb, xa[1] + (ks + 4) * 1024); }
                SCHED();
                a00 = mfma32(x[0][ks], w[0][ks], a00); a01 = mfma32(x[1][ks], w[0][ks], a01); a10 = mfma32(x[0][ks], w[1][ks], a10); a11 = mfma32(x[1][ks], w[1][ks], a11);
                SCHED();
            }
        }
        const int so = ((n + 1) & 63) * (NH * 28672);
        st16s(rs, ob, so, pk8(a00, a01)); st16s(rs, ob + ostep, so, pk8(a10, a11));
        if (n == 0) VMWAIT(0); else if (n < W_LAG_FROM) VMWAIT(2); else VMWAIT(4);
        BAR0();
        if (wave == 0 && lane == 0) st4s(rs, flagoff(FL_W + wi), 0, n == 0 ? 1 : n < W_LAG_FROM ? n : n - 1);
        STAMP(wi == 20 && wave == 1 && (n & 1), 5, n >> 1); STAMP(wave == 1 && wi >= 48 && n == 0, 16, wi - 48);
    }
    VMWAIT(0);
    BAR0();
    if (wave == 0 && lane == 0) st4s(rs, flagoff(FL_W + wi), 0, NSEG);
}

DEV int f_first(int set, int wave) { return set ? 33 + 2 * wave : wave; }
DEV int f_next(int s, int wave) { return s >= 32 ? s + 16 : s >= 24 ? 32 + 2 * wave : s + 8; }
DEV void role_f1(const Params& p, const Ctx& c, int h, int set) {
    const int lane = c.lane, wave = c.wave, rho = c.rho, g = c.g;
    const rsrc_t rs = c.rs;
    const int gfl = flagoff(FL_W + h * 7 + (lane & 3)), tfl = flagoff(FL_T + h * 4 + (lane & 3));
    const int gl0 = OFF_G + h * 28672 + (rho >> 3) * 1024 + g * 256 + (pperm(rho, 0) & 15) * 16;
    const int tl = OFF_T + h * 7168 + g * 128 + ((rho >> 2) * 2 + (rho & 1)) * 16;
    const float ninf = -__builtin_inff();
    bool wdone = false;
#pragma unroll 1
    for (int s = f_first(set, wave); s < NSEG; s = f_next(s, wave)) {
        unsigned seen;
        if (!wdone) { if (!wait_flags<8>(rs, gfl, s ? s : NSEG, seen)) return; wdone = __builtin_amdgcn_ballot_w64(seen < NSEG) == 0; }
        v4u x[2][14];
        v4f a[2][2] = {{{0, 0, 0, 0}, {0, 0, 0, 0}}, {{0, 0, 0, 0}, {0, 0, 0, 0}}};
#pragma unroll
        for (int hf = 0; hf < 2; ++hf) {
            v4u y[2][7];
#pragma unroll
            for (int k = 0; k < 7; ++k) {
#pragma unroll
                for (int qt = 0; qt < 2; ++qt) x[qt][hf * 7 + k] = ld16(rs, OFF_XF + qt * 14336 + g * 256 + (rho ^ (qt * 4)) * 16, s * 28672 + (hf * 7 + k) * 1024, 0);
                y[0][k] = ld16(rs, gl0, s * (NH * 28672) + (hf * 7 + k) * 2048, 1); y[1][k] = ld16(rs, gl0 + 64, s * (NH * 28672) + (hf * 7 + k) * 2048, 1);
            }
            SCHED();
#pragma unroll
            for (int k = 0; k < 7; ++k)
#pragma unroll
                for (int qt = 0; qt < 2; ++qt) { a[qt][0] = mfma32(y[0][k], x[qt][hf * 7 + k], a[qt][0]); a[qt][1] = mfma32(y[1][k], x[qt][hf * 7 + k], a[qt][1]); }
        }
        float v[2][8], m[2];
#pragma unroll
        for (int qt = 0; qt < 2; ++qt) {
            const float r[8] = {a[qt][0].x, a[qt][0].y, a[qt][0].z, a[qt][0].w, a[qt][1].x, a[qt][1].y, a[qt][1].z, a[qt][1].w};
            m[qt] = ninf;
#pragma unroll
            for (int k = 0; k < 8; ++k) { v[qt][k] = (8 * g + k <= qt * 16 + rho) ? r[k] : ninf; m[qt] = fmx(m[qt], v[qt][k]); }
        }
        asm volatile("" : "+v"(m[0]), "+v"(m[1]));

        const bool last = s >= NSEG - 2;
        if (!wait_flags<2>(rs, tfl, last ? s : s + 1, seen)) return;

        v4f am[2];
#pragma unroll 1
        for (int sp = 0; sp < SPIN_CAP; ++sp) {
            FENCE();
            v4u y[14]; unsigned mx = 0;
#pragma unroll
            for (int ks = 0; ks < 14; ++ks) y[ks] = ld16(rs, tl, s * (NH * 7168) + ks * 512, 1);
            SCHED();
            am[0] = am[1] = (v4f){0, 0, 0, 0};
#pragma unroll
            for (int ks = 0; ks < 14; ++ks) { am[0] = mfma32(y[ks], x[0][ks], am[0]); am[1] = mfma32(y[ks], x[1][ks], am[1]); mx = max(mx, umax4(y[ks])); PIN1(mx); PIN2(am[0], am[1]); }
            if (!last || __builtin_amdgcn_ballot_w64(mx == SENT) == 0) break;
        }
        unsigned pm[2];
#pragma unroll
        for (int qt = 0; qt < 2; ++qt) {
            const float mm = max_g(fmx(m[qt], fmx(am[qt].x, am[qt].y)));
            const float e0 = __builtin_amdgcn_exp2f(am[qt].x - mm), e1 = __builtin_amdgcn_exp2f(am[qt].y - mm);
            float z = e0 + e1;
#pragma unroll
            for (int k = 0; k < 8; ++k) { v[qt][k] = __builtin_amdgcn_exp2f(v[qt][k] - mm); z += v[qt][k]; }
            const float rz = __builtin_amdgcn_rcpf(sum_g(z));
            st16s(rs, OFF_PX + (h * 2 + qt) * 1024 + lane * 16, s * (NH * 2048),
                  (v4u){pk2(v[qt][0] * rz, v[qt][1] * rz), pk2(v[qt][2] * rz, v[qt][3] * rz), pk2(v[qt][4] * rz, v[qt][5] * rz), pk2(v[qt][6] * rz, v[qt][7] * rz)});
            pm[qt] = pk2(e0 * rz, e1 * rz);
        }
        st16s(rs, (lane & 1) == 0 ? OFF_PM + h * 512 + lane * 8 : OOB, s * (NH * 512), (v4u){pm[0], pm[1], dppu<0xF5>(pm[0]), dppu<0xF5>(pm[1])});
        if (!last) VMWAIT(0);
        if (lane == 0) st4s(rs, flagoff((set ? FL_P2 : FL_P) + h * 8 + wave), 0, s + 1);

    }
}

DEV void make_tables(const Params& p, const Ctx& c, int t) {
    const int lane = c.lane, wave = c.wave, rho = c.rho, g = c.g;
    const rsrc_t rs = c.rs;
    for (int ch = t * 512 + wave * 64 + lane; ch < 2 * 28672; ch += 28 * 512) {
        float v[8];
        if (ch < 28672) { const int fr = ch >> 6, tl = fr / 14, d0 = (fr - tl * 14) * 32 + g * 8, ri = tl >> 4, k = (tl & 15) * 16 + rho;
#pragma unroll
            for (int j = 0; j < 8; ++j) { const float ph = (float)((k * (d0 + j)) % 448) * (1.f / 448.f); v[j] = k > 224 ? 0.f : ri ? -__builtin_amdgcn_sinf(ph) : __builtin_amdgcn_cosf(ph); }
        } else { const int fr = (ch - 28672) >> 6, d = (fr >> 4) * 16 + rho, ri = (fr >> 3) & 1, k0 = (fr & 7) * 32 + g * 8;
#pragma unroll
            for (int j = 0; j < 8; ++j) { const int k = k0 + j; const float ph = (float)((k * d) % 448) * (1.f / 448.f); const bool edge = k == 0 || k == 224;
                const float wgt = (edge ? 1.f : 2.f) / (448.f * 2048.f); v[j] = k > 224 ? 0.f : ri ? (edge ? 0.f : -wgt * __builtin_amdgcn_sinf(ph)) : wgt * __builtin_amdgcn_cosf(ph); }
        }
        st16s(rs, OFF_FD + ch * 16, 0, (v4u){pk2(v[0], v[1]), pk2(v[2], v[3]), pk2(v[4], v[5]), pk2(v[6], v[7])});
    }
    VMWAIT(0);
    BAR();
    if (wave == 0 && lane == 0) __hip_atomic_fetch_add((unsigned*)(p.ws + flagoff(FL_TAB)), 1u, __ATOMIC_RELAXED, __HIP_MEMORY_SCOPE_AGENT);
}
DEV void role_f2(const Params& p, const Ctx& c, int t, int set) {
    const int lane = c.lane, wave = c.wave, rho = c.rho, g = c.g;
    const rsrc_t rs = c.rs;
    const int ufl = flagoff(FL_W + rho * 7 + ((14 + (t >> 1)) >> 2)), pfl = flagoff(lane < 16 ? (set ? FL_P2 : FL_P) + lane * 8 + wave : FL_V + t);
    const int ul = OFF_U + t * 16384 + lane * 16;
    bool wdone = false;
#pragma unroll 1
    for (int s = f_first(set, wave); s < NSEG; s = f_next(s, wave)) {
        unsigned seen;
        if (!wdone) { if (!wait_flags<8>(rs, ufl, s ? s : NSEG, seen)) return; wdone = __builtin_amdgcn_ballot_w64(seen < NSEG) == 0; }

        v4u u[16];
#pragma unroll
        for (int h = 0; h < 16; ++h) u[h] = ld16(rs, ul, s * (NH * 28672) + h * 1024, 1);
        if (!wait_flags<2>(rs, pfl, s + 1, seen)) return;

        v4f a[2], m[2];
        const bool last = s >= NSEG - 2;
#pragma unroll 1
        for (int sp = 0; sp < SPIN_CAP; ++sp) {
            FENCE();
            a[0] = a[1] = m[0] = m[1] = (v4f){0, 0, 0, 0};
            v4u px[16]; v2u pm[16], vv[8]; unsigned mx = 0;
#pragma unroll
            for (int h = 0; h < 16; ++h) { pm[h] = ld8s(rs, OFF_PM + lane * 8, s * (NH * 512) + h * 512); if (h & 1) vv[h >> 1] = ld8s(rs, OFF_V + t * 4096 + lane * 8, s * (NH * 7168) + (h >> 1) * 512); }
#pragma unroll
            for (int h = 0; h < 16; ++h) px[h] = ld16(rs, OFF_PX + lane * 16, s * (NH * 2048) + h * 2048, 1);
            SCHED();
#pragma unroll
            for (int k = 0; k < 8; ++k) { m[0] = mfma16(vv[k], (v2u){pm[2 * k].x, pm[2 * k + 1].x}, m[0]); m[1] = mfma16(vv[k], (v2u){pm[2 * k].y, pm[2 * k + 1].y}, m[1]);
                mx = max(max(mx, max(pm[2 * k].x, pm[2 * k].y)), max(pm[2 * k + 1].x, pm[2 * k + 1].y)); PIN1(mx); PIN2(m[0], m[1]); SCHED(); }
#pragma unroll
            for (int h = 0; h < 16; ++h) { a[0] = mfma32(u[h], px[h], a[0]); mx = max(mx, umax4(px[h])); PIN2(mx, a[0]); SCHED(); px[h] = ld16(rs, OFF_PX + 1024 + lane * 16, s * (NH * 2048) + h * 2048, 1); SCHED(); }
#pragma unroll
            for (int h = 0; h < 16; ++h) { a[1] = mfma32(u[h], px[h], a[1]); mx = max(mx, umax4(px[h])); PIN2(mx, a[1]); }
            if (!last || __builtin_amdgcn_ballot_w64(mx == SENT) == 0) break;
        }
#pragma unroll
        for (int qt = 0; qt < 2; ++qt) { const v4f o = a[qt] + m[qt];
            *(v2u*)(p.out + (size_t)(s * 32 + qt * 16 + rho) * 896 + DM + t * 16 + g * 4) = (v2u){pk2(o.x, o.y), pk2(o.z, o.w)}; }

    }
}

DEV bool fft_sync(const Params& p, const Ctx& c, int fl) {
    VMWAIT(0);
    BAR();
    int* okw = (int*)(c.lds + LDS_MAIN - 16);
    if (c.wave == 0) {
        if (c.lane == 0) __hip_atomic_fetch_add((unsigned*)(p.ws + flagoff(fl)), 1u, __ATOMIC_RELAXED, __HIP_MEMORY_SCOPE_AGENT);
        unsigned seen; const bool ok = wait_flags<2>(c.rs, flagoff(fl), 64, seen);
        if (c.lane == 0) *okw = ok;
    }
    BAR();
    return *okw != 0;
}
DEV v4u neg8(v4u v) { return v ^ 0x80008000u; }
DEV void twid(int a, const int (&n)[8], int mask, float inv, v4u& C, v4u& S) {
    float cs[8], sn[8];
#pragma unroll
    for (int j = 0; j < 8; ++j) { const float ph = (float)((a * n[j]) & mask) * inv; cs[j] = __builtin_amdgcn_cosf(ph); sn[j] = __builtin_amdgcn_sinf(ph); }
    C = (v4u){pk2(cs[0], cs[1]), pk2(cs[2], cs[3]), pk2(cs[4], cs[5]), pk2(cs[6], cs[7])};
    S = (v4u){pk2(sn[0], sn[1]), pk2(sn[2], sn[3]), pk2(sn[4], sn[5]), pk2(sn[6], sn[7])};
}
DEV void role_fft(const Params& p, const Ctx& c, int job) {
    const int lane = c.lane, wave = c.wave, rho = c.rho, g = c.g;
    const rsrc_t rs = c.rs; char* lds = c.lds;
    const v4f z4 = {0, 0, 0, 0};

    {
        const rsrc_t rx = mkrsrc(p.xa, 2048 * DM * 2);
        for (int pc = wave; pc < 28; pc += 8) { const int nt = pc >= 14, ks = pc - nt * 14; dma16(rx, ((64 * (nt * 16 + rho) + job) * DM + ks * 32 + g * 8) * 2, 0, pc * 1024); }

        v4u C[2], S[2];
        { int n[8];
#pragma unroll
          for (int j = 0; j < 8; ++j) n[j] = 64 * (16 * (j >> 2) + 4 * g + (j & 3)) + job;
          twid(rho, n, 2047, 1.f / 2048.f, C[0], S[0]); twid(16 + rho, n, 2047, 1.f / 2048.f, C[1], S[1]); }
        VMWAIT(0);
        BAR();
        v4f zr[2][2], zi[2][2];
#pragma unroll
        for (int t = 0; t < 2; ++t) {
            const int fo = OFF_FD + (wave * 2 + (rho >> 3)) * 14336 + g * 256 + (pperm(rho, t) & 15) * 16;
            v4f r0 = z4, r1 = z4, i0 = z4, i1 = z4;
#pragma unroll
            for (int hf = 0; hf < 2; ++hf) {
                v4u fr[7], fi[7];
#pragma unroll
                for (int k = 0; k < 7; ++k) { fr[k] = ld16(rs, fo, (hf * 7 + k) * 1024, 0); fi[k] = ld16(rs, fo, 256 * DM * 2 + (hf * 7 + k) * 1024, 0); }
                SCHED();
#pragma unroll
                for (int k = 0; k < 7; ++k) { const v4u x0 = lds16(lds, (hf * 7 + k) * 1024 + lane * 16), x1 = lds16(lds, (14 + hf * 7 + k) * 1024 + lane * 16);
                    r0 = mfma32(x0, fr[k], r0); r1 = mfma32(x1, fr[k], r1); i0 = mfma32(x0, fi[k], i0); i1 = mfma32(x1, fi[k], i1); }
            }
            const v4u yr = pk8(r0, r1), yi = pk8(i0, i1), nr = neg8(yr);
#pragma unroll
            for (int ct = 0; ct < 2; ++ct) { zr[ct][t] = mfma32(yi, S[ct], mfma32(yr, C[ct], z4)); zi[ct][t] = mfma32(nr, S[ct], mfma32(yi, C[ct], z4)); }
        }
        char* O = lds + 32768;
#pragma unroll
        for (int ct = 0; ct < 2; ++ct) { const int o = (ct * 16 + rho) * 1040 + wave * 64 + g * 16;
            *(v4u*)(O + o) = pk8(zr[ct][0], zr[ct][1]); *(v4u*)(O + o + 512) = pk8(zi[ct][0], zi[ct][1]); }
        BAR();
#pragma unroll
        for (int k = 0; k < 4; ++k) st16s(rs, OFF_Z1 + job * 1024 + lane * 16, (wave * 4 + k) * 65536, lds16(O, (wave * 4 + k) * 1040 + lane * 16));
    }
    STAMP(job == 0 && wave == 1, 4, 3);
    if (!fft_sync(p, c, FL_FA)) return;
    STAMP(job == 0 && wave == 1, 4, 4);
    if (wave < 4) {
        const int k1 = job >> 1, P = (job & 1) * 4 + wave;
        char* L = lds + wave * 8192;
#pragma unroll
        for (int pc = 0; pc < 8; ++pc) dma16s(rs, OFF_Z1 + (k1 * 64 + (pc & 3) * 16 + (lane >> 2)) * 1024 + (pc >> 2) * 512 + P * 64 + (lane & 3) * 16, 0, wave * 8192 + pc * 1024);
        v4u C[4][2], S[4][2];
#pragma unroll
        for (int ks = 0; ks < 2; ++ks) { int n[8];
#pragma unroll
            for (int j = 0; j < 8; ++j) n[j] = 32 * ks + 16 * (j >> 2) + 4 * g + (j & 3);
#pragma unroll
            for (int kt = 0; kt < 4; ++kt) twid(16 * kt + rho, n, 63, 1.f / 64.f, C[kt][ks], S[kt][ks]); }
        VMWAIT(0);
        v2u hr[4], hi[4];
#pragma unroll
        for (int t = 0; t < 2; ++t) {
            const int k = P * 32 + pperm(rho, t);
            v4u xr[2], xi[2];
#pragma unroll
            for (int ks = 0; ks < 2; ++ks) { const int a = (32 * ks + 4 * g + (rho >> 2)) * 64 + (rho & 3) * 16 + t * 8;
                const v2u a0 = ldstr(L, a), a1 = ldstr(L, a + 1024), b0 = ldstr(L, a + 4096), b1 = ldstr(L, a + 5120);
                xr[ks] = (v4u){a0.x, a0.y, a1.x, a1.y}; xi[ks] = (v4u){b0.x, b0.y, b1.x, b1.y}; }
            const v4u n0 = neg8(xr[0]), n1 = neg8(xr[1]);
            unsigned short fr[16], fi[16];
#pragma unroll
            for (int j = 0; j < 16; ++j) { const int o = (k1 + 32 * (16 * (j >> 2) + 4 * g + (j & 3))) * 225 + min(k, 224); fr[j] = p.wr[o]; fi[j] = p.wi[o]; }
            SCHED();
            v4f vr[4], vi[4];
#pragma unroll
            for (int kt = 0; kt < 4; ++kt) {
                const v4f ar = mfma32(S[kt][1], xi[1], mfma32(S[kt][0], xi[0], mfma32(C[kt][1], xr[1], mfma32(C[kt][0], xr[0], z4))));
                const v4f ai = mfma32(S[kt][1], n1, mfma32(S[kt][0], n0, mfma32(C[kt][1], xi[1], mfma32(C[kt][0], xi[0], z4))));
                float wr[4], wi[4];
#pragma unroll
                for (int r = 0; r < 4; ++r) { wr[r] = __uint_as_float((unsigned)fr[kt * 4 + r] << 16); wi[r] = __uint_as_float((unsigned)fi[kt * 4 + r] << 16); }
                vr[kt] = (v4f){ar.x * wr[0] - ai.x * wi[0], ar.y * wr[1] - ai.y * wi[1], ar.z * wr[2] - ai.z * wi[2], ar.w * wr[3] - ai.w * wi[3]};
                vi[kt] = (v4f){ar.x * wi[0] + ai.x * wr[0], ar.y * wi[1] + ai.y * wr[1], ar.z * wi[2] + ai.z * wr[2], ar.w * wi[3] + ai.w * wr[3]};
            }
            const v4u pr0 = pk8(vr[0], vr[1]), pr1 = pk8(vr[2], vr[3]), pi0 = pk8(vi[0], vi[1]), pi1 = pk8(vi[2], vi[3]), ni0 = neg8(pi0), ni1 = neg8(pi1);
#pragma unroll
            for (int nt = 0; nt < 4; ++nt) {
                const v4f rr = mfma32(ni1, S[nt][1], mfma32(ni0, S[nt][0], mfma32(pr1, C[nt][1], mfma32(pr0, C[nt][0], z4))));
                const v4f ri = mfma32(pr1, S[nt][1], mfma32(pr0, S[nt][0], mfma32(pi1, C[nt][1], mfma32(pi0, C[nt][0], z4))));
                const v2u qr = {pk2(rr.x, rr.y), pk2(rr.z, rr.w)}, qi = {pk2(ri.x, ri.y), pk2(ri.z, ri.w)};
                if (t == 0) { hr[nt] = qr; hi[nt] = qi; }
                else { char* o = lds + 32768 + (nt * 16 + rho) * 528 + wave * 64 + g * 16;
                    *(v4u*)o = (v4u){hr[nt].x, hr[nt].y, qr.x, qr.y}; *(v4u*)(o + 256) = (v4u){hi[nt].x, hi[nt].y, qi.x, qi.y}; }
            }
        }
    }
    BAR();
#pragma unroll
    for (int k = 0; k < 4; ++k) { const int n2 = wave * 8 + k * 2 + (lane >> 5);
        st16s(rs, OFF_Z2 + (n2 * 32 + (job >> 1)) * 1024 + (g & 1) * 512 + (job & 1) * 256 + rho * 16, 0, lds16(lds, 32768 + n2 * 528 + (lane & 31) * 16)); }
    STAMP(job == 0 && wave == 1, 4, 5);
    if (!fft_sync(p, c, FL_FB)) return;
    STAMP(job == 0 && wave == 1, 4, 6);
    {
        char* L = lds + wave * 4096;
#pragma unroll
        for (int pc = 0; pc < 4; ++pc) dma16s(rs, OFF_Z2 + (job * 32 + (pc & 1) * 16 + (lane >> 2)) * 1024 + (pc >> 1) * 512 + wave * 64 + (lane & 3) * 16, 0, wave * 4096 + pc * 1024);
        v4u C[2], S[2];
        { int n[8];
#pragma unroll
          for (int j = 0; j < 8; ++j) n[j] = 8 * g + j;
          twid(64 * rho + job, n, 2047, 1.f / 2048.f, C[0], S[0]); twid(64 * (16 + rho) + job, n, 2047, 1.f / 2048.f, C[1], S[1]); }
        VMWAIT(0);
        v4f rr[2][2], ri[2][2];
#pragma unroll
        for (int t = 0; t < 2; ++t) {
            const int a = (8 * g + (rho >> 2)) * 64 + (rho & 3) * 16 + t * 8;
            const v2u a0 = ldstr(L, a), a1 = ldstr(L, a + 256), b0 = ldstr(L, a + 2048), b1 = ldstr(L, a + 2304);
            const v4u xr = {a0.x, a0.y, a1.x, a1.y}, xi = {b0.x, b0.y, b1.x, b1.y}, ni = neg8(xi);
#pragma unroll
            for (int nt = 0; nt < 2; ++nt) { rr[nt][t] = mfma32(ni, S[nt], mfma32(xr, C[nt], z4)); ri[nt][t] = mfma32(xr, S[nt], mfma32(xi, C[nt], z4)); }
        }
        char* R = lds + 32768;
#pragma unroll
        for (int nt = 0; nt < 2; ++nt) { *(v4u*)(R + (nt * 16 + wave) * 1024 + lane * 16) = pk8(rr[nt][0], rr[nt][1]); *(v4u*)(R + (nt * 16 + 8 + wave) * 1024 + lane * 16) = pk8(ri[nt][0], ri[nt][1]); }
        BAR();
#pragma unroll 1
        for (int Q = wave; Q < 14; Q += 8) {
            v4f y[2][2] = {{z4, z4}, {z4, z4}};
#pragma unroll
            for (int hf = 0; hf < 2; ++hf) {
                v4u f0[8], f1[8];
#pragma unroll
                for (int k = 0; k < 8; ++k) { const int fo = OFF_FI + (Q * 2 + (rho >> 3)) * 16384 + g * 256 + (pperm(rho, 0) & 15) * 16; f0[k] = ld16(rs, fo, (hf * 8 + k) * 1024, 0); f1[k] = ld16(rs, fo + 64, (hf * 8 + k) * 1024, 0); }
                SCHED();
#pragma unroll
                for (int k = 0; k < 8; ++k) { const v4u b0 = lds16(R, (hf * 8 + k) * 1024 + lane * 16), b1 = lds16(R, (16 + hf * 8 + k) * 1024 + lane * 16);
                    y[0][0] = mfma32(f0[k], b0, y[0][0]); y[0][1] = mfma32(f1[k], b0, y[0][1]); y[1][0] = mfma32(f0[k], b1, y[1][0]); y[1][1] = mfma32(f1[k], b1, y[1][1]); }
            }
#pragma unroll
            for (int nt = 0; nt < 2; ++nt) *(v4u*)(p.out + (size_t)(64 * (nt * 16 + rho) + job) * 896 + Q * 32 + g * 8) = pk8(y[nt][0], y[nt][1]);
        }
    }
}

extern "C" __global__ void __launch_bounds__(512, 2) k_main(Params p) {
    extern __shared__ __attribute__((aligned(16))) char lds[];
    Ctx c; c.rs = mkrsrc(p.ws, WS_BYTES); c.lds = lds; c.lane = threadIdx.x & 63; c.wave = __builtin_amdgcn_readfirstlane(threadIdx.x >> 6); c.rho = c.lane & 15; c.g = c.lane >> 4;
    { const int lane = c.lane; (void)lane; STAMP(blockIdx.x == 0 && c.wave == 7, 4, 0); }
    const int rot = *(const int*)(p.ws + flagoff(FL_ROT));
    const int x = (blockIdx.x + rot) & 7, y = blockIdx.x >> 3;
    if (x < 2) role_p(p, c, x * 32 + y);
    else if (x == 2) { if (y < 28) role_o(p, c, y); else if (y == 28) role_cal(p, c, 1); }
    else if (x == 3 && y == 28) role_cal(p, c, 0);
    else {
        const int wi = (x - 4) * 32 + y - 16;
        if (x == 3) { if (y < 28) make_tables(p, c, y); } else if (wi >= 0) role_w(p, c, wi);
        if (wi >= 48) role_fft(p, c, wi - 48);
        else if (x == 3 ? y < 28 : wi >= 16 && wi < 44) role_f2(p, c, x == 3 ? y : wi - 16, x != 3);
        else if (x == 4) role_f1(p, c, y & 15, wi >= 0);
    }
    { const int lane = c.lane; (void)lane; LSTAMP(c.wave == 7, 8 + (blockIdx.x >> 6), blockIdx.x & 63); }
}

extern "C" void kernel_launch(void* const* d_in, const int* in_sizes, int n_in, void* d_out, int out_size, void* d_ws, size_t ws_size, hipStream_t stream) {
    Params p;
    p.xa = (const unsigned short*)d_in[0]; p.wr = (const unsigned short*)d_in[1]; p.wi = (const unsigned short*)d_in[2];
    p.xb = (const unsigned short*)d_in[3]; p.mem0 = (const unsigned short*)d_in[4];
    p.wq = (const unsigned short*)d_in[5]; p.wk = (const unsigned short*)d_in[6]; p.wv = (const unsigned short*)d_in[7]; p.wo = (const unsigned short*)d_in[8];
    p.out = (unsigned short*)d_out;
    p.ws = (char*)d_ws + (((size_t)2 << 20) - ((uintptr_t)d_ws & (((size_t)2 << 20) - 1))) % ((size_t)2 << 20);
    (void)hipFuncSetAttribute((const void*)k_main, hipFuncAttributeMaxDynamicSharedMemorySize, LDS_MAIN);
    (void)hipFuncSetAttribute((const void*)k_pro, hipFuncAttributeMaxDynamicSharedMemorySize, LDS_PRO);
    hipLaunchKernelGGL(k_pro, dim3(256), dim3(512), LDS_PRO, stream, p);
    hipLaunchKernelGGL(k_main, dim3(256), dim3(512), LDS_MAIN, stream, p);
}
```

```cpp
#define LITE
#include <hip/hip_runtime.h>
#include <hip/hip_bf16.h>
#include <stdint.h>

typedef unsigned v4u __attribute__((ext_vector_type(4)));
typedef unsigned v2u __attribute__((ext_vector_type(2)));
typedef float v4f __attribute__((ext_vector_type(4)));
typedef float v2f __attribute__((ext_vector_type(2)));
typedef short v4s __attribute__((ext_vector_type(4)));
typedef __bf16 v2bf __attribute__((ext_vector_type(2)));
typedef __bf16 v8bf __attribute__((ext_vector_type(8)));
typedef __bf16 v16bf __attribute__((ext_vector_type(16)));
typedef unsigned v8u __attribute__((ext_vector_type(8)));
typedef __amdgpu_buffer_rsrc_t rsrc_t;
#define LAS __attribute__((address_space(3)))
#define DEV __device__ __forceinline__

constexpr int NSEG = 64, NH = 16, DM = 448;
constexpr unsigned SENT = 0xFFFFFFFFu;
constexpr int OOB = 0x7fffffff;
constexpr int SC1 = 16;
constexpr int OFF_HO = 0;
constexpr int OFF_CTL = 4 << 20;
constexpr int OFF_AP = OFF_CTL + 131072;
constexpr int OFF_BT = OFF_AP + NH * DM * DM * 2;
constexpr int OFF_G = OFF_BT + NH * DM * DM * 2;
constexpr int OFF_U = OFF_G + NSEG * NH * 28672;
constexpr int OFF_T = OFF_U + NSEG * NH * 28672;
constexpr int OFF_V = OFF_T + NSEG * NH * 7168;
constexpr int OFF_PX = OFF_V + NSEG * NH * 7168;
constexpr int OFF_PM = OFF_PX + NSEG * NH * 2048;
constexpr int OFF_FD = OFF_PM + NSEG * NH * 512;
constexpr int OFF_FI = OFF_FD + 512 * DM * 2;
constexpr int OFF_Z1 = OFF_FI + 512 * DM * 2;
constexpr int OFF_Z2 = OFF_Z1 + (2 << 20);
constexpr int OFF_XF = OFF_Z2 + (2 << 20);
constexpr int OFF_G0 = OFF_XF + (2 << 20);
constexpr int OFF_PM2 = OFF_G0 + 2 * NH * 28672;
constexpr int OFF_DBG = OFF_G0 + (1 << 20);
constexpr int WS_BYTES = OFF_DBG + (1 << 20);
constexpr int HO_MEM = 0, HO_PART = 32768, HO_E = 65536, HO_SLOT = 98304;
constexpr int OFF_MEM0 = 4 * HO_SLOT, OFF_PROBE = OFF_MEM0 + 32768;
constexpr int FL_W = 0, FL_T = 112, FL_V = 176, FL_P = 208, FL_ROT = 336, FL_TAB = 337, FL_FA = 338, FL_FB = 339, FL_ABORT = 340, FL_CAL = 341, FL_P2 = 344;
constexpr int LDS_MAIN = 114688 + 2048;
constexpr int LDS_PRO = 143360;
constexpr int SPIN_CAP = 1 << 17;

struct Params {
    const unsigned short* xa; const unsigned short* wr; const unsigned short* wi;
    const unsigned short* xb; const unsigned short* mem0;
    const unsigned short* wq; const unsigned short* wk; const unsigned short* wv; const unsigned short* wo;
    unsigned short* out; char* ws;
};

DEV unsigned pk2(float a, float b) { v2bf r = {(__bf16)a, (__bf16)b}; return __builtin_bit_cast(unsigned, r); }
DEV v4u pk8(v4f a, v4f b) { return (v4u){pk2(a.x, a.y), pk2(a.z, a.w), pk2(b.x, b.y), pk2(b.z, b.w)}; }
DEV v4f mfma32(v4u a, v4u b, v4f c) { return __builtin_amdgcn_mfma_f32_16x16x32_bf16(__builtin_bit_cast(v8bf, a), __builtin_bit_cast(v8bf, b), c, 0, 0, 0); }
DEV v4f mfma16(v2u a, v2u b, v4f c) { return __builtin_amdgcn_mfma_f32_16x16x16bf16_1k(__builtin_bit_cast(v4s, a), __builtin_bit_cast(v4s, b), c, 0, 0, 0); }
DEV v4f smf(v4u a, v4u b0, v4u b1, v4f c, int idx) {
    v8u b = {b0.x, b0.y, b0.z, b0.w, b1.x, b1.y, b1.z, b1.w};
    return __builtin_amdgcn_smfmac_f32_16x16x64_bf16(__builtin_bit_cast(v8bf, a), __builtin_bit_cast(v16bf, b), c, idx, 0, 0);
}
DEV float fmx(float a, float b) { return __builtin_elementwise_maximum(a, b); }
DEV float max_g(float s) {
    auto r = __builtin_amdgcn_permlane16_swap(__float_as_uint(s), __float_as_uint(s), false, false);
    s = fmx(__uint_as_float(r[0]), __uint_as_float(r[1]));
    auto q = __builtin_amdgcn_permlane32_swap(__float_as_uint(s), __float_as_uint(s), false, false);
    return fmx(__uint_as_float(q[0]), __uint_as_float(q[1]));
}
DEV float sum_g(float s) {
    auto r = __builtin_amdgcn_permlane16_swap(__float_as_uint(s), __float_as_uint(s), false, false);
    s = __uint_as_float(r[0]) + __uint_as_float(r[1]);
    auto q = __builtin_amdgcn_permlane32_swap(__float_as_uint(s), __float_as_uint(s), false, false);
    return __uint_as_float(q[0]) + __uint_as_float(q[1]);
}
DEV float ror8(float s) { return __int_as_float(__builtin_amdgcn_mov_dpp(__float_as_int(s), 0x128, 0xf, 0xf, true)); }
template <int CTRL> DEV unsigned dppu(unsigned x) { return (unsigned)__builtin_amdgcn_mov_dpp((int)x, CTRL, 0xf, 0xf, true); }
DEV v4u quad4(unsigned x) { return (v4u){x, dppu<0x55>(x), dppu<0xAA>(x), dppu<0xFF>(x)}; }
DEV unsigned umax4(v4u v) { return max(max(v.x, v.y), max(v.z, v.w)); }
DEV rsrc_t mkrsrc(const void* p, unsigned bytes) { return __builtin_amdgcn_make_buffer_rsrc((void*)p, 0, bytes, 0x00020000); }
DEV v4u ld16(rsrc_t r, int vo, int so, int aux) { return aux ? __builtin_amdgcn_raw_buffer_load_b128(r, vo, so, SC1) : __builtin_amdgcn_raw_buffer_load_b128(r, vo, so, 0); }
DEV v2u ld8s(rsrc_t r, int vo, int so) { return __builtin_amdgcn_raw_buffer_load_b64(r, vo, so, SC1); }
DEV unsigned ld4s(rsrc_t r, int vo, int so) { return __builtin_amdgcn_raw_buffer_load_b32(r, vo, so, SC1); }
DEV void st16s(rsrc_t r, int vo, int so, v4u v) {
    __builtin_amdgcn_raw_buffer_store_b128(v, r, vo, so, SC1); asm volatile("s_nop 1" :: "v"(v) : "memory");
}
DEV void st8s(rsrc_t r, int vo, int so, v2u v) { __builtin_amdgcn_raw_buffer_store_b64(v, r, vo, so, SC1); }
DEV void st4s(rsrc_t r, int vo, int so, unsigned v) { __builtin_amdgcn_raw_buffer_store_b32(v, r, vo, so, SC1); }
DEV void dma16(rsrc_t r, int vo, int so, int ldsaddr) {
    unsigned keep;
    asm volatile("s_mov_b32 %0, m0\n\ts_mov_b32 m0, %4\n\ts_nop 4\n\tbuffer_load_dwordx4 %1, %2, %3 offen lds\n\ts_mov_b32 m0, %0"
                 : "=&s"(keep) : "v"(vo), "s"(r), "s"(__builtin_amdgcn_readfirstlane(so)), "s"(__builtin_amdgcn_readfirstlane(ldsaddr)) : "memory");
}
DEV void dma16s(rsrc_t r, int vo, int so, int ldsaddr) {
    unsigned keep;
    asm volatile("s_mov_b32 %0, m0\n\ts_mov_b32 m0, %4\n\ts_nop 4\n\tbuffer_load_dwordx4 %1, %2, %3 offen sc1 lds\n\ts_mov_b32 m0, %0"
                 : "=&s"(keep) : "v"(vo), "s"(r), "s"(__builtin_amdgcn_readfirstlane(so)), "s"(__builtin_amdgcn_readfirstlane(ldsaddr)) : "memory");
}
#define BAR() asm volatile("s_waitcnt lgkmcnt(0)\n\ts_barrier" ::: "memory")
#define BAR0() asm volatile("s_barrier" ::: "memory")
#define VMWAIT(n) asm volatile("s_waitcnt vmcnt(" #n ")" ::: "memory")
#define FENCE() asm volatile("" ::: "memory")
#define SCHED() __builtin_amdgcn_sched_barrier(0)
DEV v4u lds16(const char* l, int a) { return *(const v4u*)(l + a); }
DEV v4u lds16a(int a) { return *(const LAS v4u*)(uintptr_t)(unsigned)a; }
DEV v2u lds8(const char* l, int a) { return *(const v2u*)(l + a); }
DEV v2u ldstr(const char* l, int a) { v4s r = __builtin_amdgcn_ds_read_tr16_b64_v4i16((LAS v4s*)(l + a)); return __builtin_bit_cast(v2u, r); }
template <int N> DEV void slp() { if constexpr (N > 0) __builtin_amdgcn_s_sleep(N); }
DEV int flagoff(int i) { return OFF_CTL + i * 128; }
template <int SL> DEV bool wait_flags(rsrc_t rs, int vo, unsigned need, unsigned& seen) {
    for (int sp = 0;; ++sp) {
        FENCE();
        seen = ld4s(rs, vo, 0);
        if (__builtin_amdgcn_ballot_w64(seen < need) == 0) return true;
        slp<SL>();
        if ((sp & 255) == 255) {
            if (sp > SPIN_CAP) st4s(rs, flagoff(FL_ABORT), 0, 1u);
            if (sp > SPIN_CAP || __builtin_amdgcn_readfirstlane(ld4s(rs, flagoff(FL_ABORT), 0))) return false;
        }
    }
}
DEV int img(int p) { return ((p & 7) >> 1) * 4 + (p >> 3) * 2 + (p & 1); }
DEV int pperm(int rho, int tau) { return (rho >> 2) * 8 + tau * 4 + (rho & 3); }

#define STAMP(cond, slot, s)
#define LSTAMP(cond, slot, s) do { if ((cond) && lane == 0) { unsigned long long t_ = __builtin_amdgcn_s_memrealtime(); __hip_atomic_store((unsigned long long*)(p.ws + OFF_DBG + ((slot) * 64 + (s)) * 8), t_, __ATOMIC_RELAXED, __HIP_MEMORY_SCOPE_AGENT); } } while (0)
#define ACC_DECL
#define ACC_T(k)
#define ACC_OUT(slot)
#define CSTAMP(cond, slot, s)

template <int which> DEV void pro_tile(const Params& p, char* lds, rsrc_t rs, int b, int h, int b0, int c0, int lane, int wave) {
    const int rho = lane & 15, g = lane >> 4;
    const rsrc_t ra = mkrsrc(which ? p.wv : p.wk, DM * 4096 * 2), rb = mkrsrc(which ? p.wo : p.wq, DM * 4096 * 2);
    int sa[4], sb[2];
#pragma unroll
    for (int k = 0; k < 4; ++k) { const int r = (k & 1) * 8 + (lane >> 3); sa[k] = ((b0 + 32 * wave + pperm(r, k >> 1)) * 4096 + h * 256 + ((lane & 7) ^ (r >> 1)) * 8) * 2; }
#pragma unroll
    for (int k = 0; k < 2; ++k) { const int r = k * 8 + (lane >> 3), ch = (2 * wave + k) * 64 + lane, e = ch / 14;
        sb[k] = which ? ((h * 256 + e) * DM + c0 + (ch - e * 14) * 8) * 2 : ((c0 + wave * 16 + r) * 4096 + h * 256 + ((lane & 7) ^ (r >> 1)) * 8) * 2; }
    const int bstep = which ? 64 * DM * 2 : 128, la = 57344 + wave * 4096;
    v4u a3[4];
#pragma unroll
    for (int c = 0; c < 4; ++c) {
#pragma unroll
        for (int k = 0; k < 2; ++k) dma16(rb, sb[k], c * bstep, c * 14336 + (2 * wave + k) * 1024);
#pragma unroll
        for (int k = 0; k < 4; ++k) { if (c < 3) dma16(ra, sa[k], c * 128, la + c * 28672 + k * 1024); else a3[k] = ld16(ra, sa[k], 384, 0); }
    }
    const int rd = (rho >> 3) * 1024 + (rho & 7) * 128, rx0 = (g ^ (rho >> 1)) * 16, rx1 = ((4 + g) ^ (rho >> 1)) * 16;
    const int tra = (8 * g + (rho >> 2)) * 224 + (rho & 3) * 8;
    v4f acc[7][2];
#pragma unroll
    for (int ct = 0; ct < 7; ++ct) { acc[ct][0] = (v4f){0, 0, 0, 0}; acc[ct][1] = acc[ct][0]; }
    const float sc = which ? 1.f : 0.09016844005556021f;
    const int obase = (which ? OFF_BT : OFF_AP) + ((h * 28 + (c0 >> 4)) * 14 + (b0 >> 5) + wave) * 1024 + lane * 16;
    v4u af[2][4], f[2][7][2];
#pragma unroll
    for (int c = 0; c <= 4; ++c) {
        if (c < 4) {
            if (c == 0) VMWAIT(18); else if (c == 1) VMWAIT(12); else if (c == 2) VMWAIT(6); else VMWAIT(0);
            if (c == 3) {
#pragma unroll
                for (int k = 0; k < 4; ++k) *(v4u*)(lds + la + k * 1024 + lane * 16) = a3[k];
            }
            BAR();
            if (c == 3) { STAMP(b == 0 && wave == 0, 4, 9); STAMP(wave == 0 && b < 64, 30, b); }
        }
        const char* A = lds + la + (c % 3) * 28672; const char* B = lds + c * 14336;
        if (c < 4) { af[c & 1][0] = lds16(A, rd + rx0); af[c & 1][1] = lds16(A, rd + rx1); af[c & 1][2] = lds16(A, 2048 + rd + rx0); af[c & 1][3] = lds16(A, 2048 + rd + rx1); }
#pragma unroll
        for (int ct = 0; ct < 7; ++ct) {
            if (c < 4) {
                if (which == 0) { f[c & 1][ct][0] = lds16(B, ct * 2048 + rd + rx0); f[c & 1][ct][1] = lds16(B, ct * 2048 + rd + rx1); }
                else {
#pragma unroll
                    for (int kk = 0; kk < 2; ++kk) { const v2u x = ldstr(B, tra + kk * 7168 + ct * 32), y = ldstr(B, tra + kk * 7168 + 896 + ct * 32); f[c & 1][ct][kk] = (v4u){x.x, x.y, y.x, y.y}; }
                }
            }
            SCHED();
            if (c > 0) { const int q = (c - 1) & 1;
                acc[ct][0] = mfma32(af[q][1], f[q][ct][1], mfma32(af[q][0], f[q][ct][0], acc[ct][0])); acc[ct][1] = mfma32(af[q][3], f[q][ct][1], mfma32(af[q][2], f[q][ct][0], acc[ct][1])); }
            if (c == 4) st16s(rs, obase, ct * 14336, pk8(acc[ct][0] * sc, acc[ct][1] * sc));
            SCHED();
        }
    }
    STAMP(b == 0 && wave == 0, 4, 10);
    STAMP(wave == 0 && b >= 192, 31, b & 63);
}
extern "C" __global__ void __launch_bounds__(512, 2) k_pro(Params p) {
    extern __shared__ __attribute__((aligned(16))) char lds[];
    const int b = blockIdx.x, tid = threadIdx.x, lane = tid & 63, wave = __builtin_amdgcn_readfirstlane(tid >> 6);
    const int rho = lane & 15, g = lane >> 4;
    const int gemm = (b & 7) * 4 + (b >> 6), sub = (b >> 3) & 7, which = gemm & 1, h = gemm >> 1;
    const int b0 = 224 * (sub & 1), c0 = 112 * (sub >> 1);
    const rsrc_t rs = mkrsrc(p.ws, WS_BYTES);
    LSTAMP(b == 0 && wave == 0, 4, 8); STAMP(wave == 0 && b < 64, 29, b);
    if (wave == 7) {
        const rsrc_t rx = mkrsrc(p.xb, 2048 * DM * 2);
        v4u xf[7], m0[2];
#pragma unroll
        for (int k = 0; k < 7; ++k) { const int pc = b * 7 + k, rt = pc / 14, ks = pc - rt * 14; xf[k] = ld16(rx, ((rt * 16 + (rho ^ ((rt & 1) * 4))) * DM + ks * 32 + g * 8) * 2, 0, 0); }
        const v4u sent = {SENT, SENT, SENT, SENT};
#pragma unroll
        for (int k = 0; k < 2; ++k) {
            const int o = (b * 2 + k) * 1024 - OFF_MEM0, c = ((o & 8191) >> 4) + lane;
            m0[k] = sent;
            if (o >= 0 && o < 32768 && c < 448) { const unsigned* m = (const unsigned*)(p.mem0 + (((c >> 2) & 3) * 2 + (c & 1)) * DM + (c >> 4) * 16 + (c & 2)); m0[k] = (v4u){m[0], m[2], m[4], m[6]}; }
        }
        SCHED();
        BAR0(); BAR0(); BAR0(); BAR0();
        const unsigned rot = ((unsigned)__builtin_amdgcn_s_getreg(20 | (3 << 11)) - b) & 7u;
        if (lane < 32) st16s(rs, OFF_CTL + b * 512 + lane * 16, 0, (v4u){b * 4 == FL_ROT && lane == 0 ? rot : 0u, 0u, 0u, 0u});
#pragma unroll
        for (int k = 0; k < 2; ++k) st16s(rs, OFF_HO + (b * 2 + k) * 1024 + lane * 16, 0, m0[k]);
#pragma unroll
        for (int k = 62; k < 64; ++k)
            st16s(rs, (b < 112 ? OFF_T + k * NH * 7168 + b * 1024 : b < 144 ? OFF_PX + k * NH * 2048 + (b - 112) * 1024 : b < 152 ? OFF_PM + k * NH * 512 + (b - 144) * 1024 : b < 160 ? OFF_PM2 + (k - 62) * NH * 512 + (b - 152) * 1024 : OOB - 1024) + lane * 16, 0, sent);
#pragma unroll
        for (int k = 0; k < 4; ++k) st16s(rs, b < 224 ? OFF_G0 + (b * 4 + k) * 1024 + lane * 16 : OOB, 0, sent);
#pragma unroll
        for (int k = 0; k < 7; ++k) st16s(rs, OFF_XF + (b * 7 + k) * 1024 + lane * 16, 0, xf[k]);
        return;
    }
    if (which) pro_tile<1>(p, lds, rs, b, h, b0, c0, lane, wave); else pro_tile<0>(p, lds, rs, b, h, b0, c0, lane, wave);
}

struct Ctx { rsrc_t rs; char* lds; int lane, wave, rho, g; };

DEV int slot_base(int s, int cal) { return OFF_HO + (s & 3) * HO_SLOT + cal * 8192; }
DEV int mem_base(int s, int cal) { return s ? slot_base(s, cal) + HO_MEM : OFF_MEM0 + cal * 8192; }
#define STAGE_HEAD "s_add_i32 %[bud], %[bud], -1\n\ts_cmp_lt_i32 %[bud], 0\n\ts_cselect_b32 vcc_hi, -1, -2\n\tv_mov_b32 v233, vcc_hi\n\t"
#define STAGE_TAIL "v_cmp_gt_u32 vcc, v232, v233\n\ts_cbranch_vccnz 1b\n\ts_waitcnt lgkmcnt(0)\n\ts_barrier"
#define STAGE_CLOB "memory", "vcc", "scc", "v224", "v225", "v226", "v227", "v228", "v229", "v230", "v231", "v232", "v233"
DEV void stage1(rsrc_t rs, int vo, int so, int& budget) {
    asm volatile("1:\n\tbuffer_load_dwordx4 v[224:227], %[vo], %[rs], %[so] offen sc1\n\t" STAGE_HEAD
                 "s_waitcnt vmcnt(0)\n\tds_write_b128 %[vo], v[224:227]\n\tv_max3_u32 v232, v224, v225, v226\n\tv_max_u32 v232, v232, v227\n\t" STAGE_TAIL
                 : [bud] "+s"(budget) : [vo] "v"(vo), [rs] "s"(rs), [so] "s"(so) : STAGE_CLOB);
}
DEV void stage2(rsrc_t rs, int vo, int vo2, int so, int& budget) {
    asm volatile("1:\n\tbuffer_load_dwordx4 v[224:227], %[vo], %[rs], %[so] offen sc1\n\tbuffer_load_dwordx4 v[228:231], %[vo2], %[rs], %[so] offen sc1\n\t" STAGE_HEAD
                 "s_waitcnt vmcnt(1)\n\tds_write_b128 %[vo], v[224:227]\n\tv_max3_u32 v232, v224, v225, v226\n\t"
                 "s_waitcnt vmcnt(0)\n\tds_write_b128 %[vo2], v[228:231]\n\tv_max3_u32 v232, v232, v227, v228\n\tv_max3_u32 v232, v232, v229, v230\n\tv_max_u32 v232, v232, v231\n\t" STAGE_TAIL
                 : [bud] "+s"(budget) : [vo] "v"(vo), [vo2] "v"(vo2), [rs] "s"(rs), [so] "s"(so) : STAGE_CLOB);
}
DEV void stage2o(rsrc_t rs, int vo, int vo2, int so, int& budget, v4u& a, v4u& b) {
    asm volatile("1:\n\tbuffer_load_dwordx4 v[224:227], %[vo], %[rs], %[so] offen sc1\n\tbuffer_load_dwordx4 v[228:231], %[vo2], %[rs], %[so] offen sc1\n\t" STAGE_HEAD
                 "s_waitcnt vmcnt(1)\n\tds_write_b128 %[vo], v[224:227]\n\tv_max3_u32 v232, v224, v225, v226\n\t"
                 "s_waitcnt vmcnt(0)\n\tds_write_b128 %[vo2], v[228:231]\n\tv_max3_u32 v232, v232, v227, v228\n\tv_max3_u32 v232, v232, v229, v230\n\tv_max_u32 v232, v232, v231\n\t" STAGE_TAIL
                 : [bud] "+s"(budget), "=&{v[224:227]}"(a), "=&{v[228:231]}"(b) : [vo] "v"(vo), [vo2] "v"(vo2), [rs] "s"(rs), [so] "s"(so) : "memory", "vcc", "scc", "v232", "v233");
}
DEV void give_up(rsrc_t rs) { st4s(rs, flagoff(FL_ABORT), 0, 1u); }
#define PIN1(a) asm volatile("" : "+v"(a))
#define PIN2(a, b) asm volatile("" : "+v"(a), "+v"(b))
constexpr int CAL_STEP = 10;
DEV void role_cal(const Params& p, const Ctx& c, int pair) {
    if (c.wave) return;
    unsigned best[4] = {0xffffu, 0xffffu, 0xffffu, 0xffffu}, half = 0;
    if (pair) { if (!wait_flags<2>(c.rs, flagoff(FL_CAL), 1u, half)) return; half = __builtin_amdgcn_readfirstlane(half) & 1; }
#pragma unroll 1
    for (int r = 0; r < 12; ++r) {
#pragma unroll
        for (int k = 0; k < 4; ++k) {
            const int off = OFF_PROBE + (pair ? half + (k & 2) : k >> 1) * 8192 + (k & 1) * 4096 + (pair * 16 + r) * 128;
            unsigned long long t0, t1; unsigned v;
            asm volatile("s_memtime %0\n\ts_waitcnt lgkmcnt(0)\n\tbuffer_load_dword %2, %3, %4, 0 offen sc1\n\ts_waitcnt vmcnt(0)\n\ts_memtime %1\n\ts_waitcnt lgkmcnt(0)"
                         : "=&s"(t0), "=&s"(t1), "=&v"(v) : "v"(off), "s"(c.rs) : "memory");
            best[k] = min(best[k], (unsigned)(t1 - t0));
        }
        if (r == 2 && !pair) { if (c.lane == 0) st4s(c.rs, flagoff(FL_CAL), 0, min(best[2], best[3]) < min(best[0], best[1]) ? 3u : 2u); VMWAIT(0); }
    }
    if (c.lane == 0) st4s(c.rs, flagoff(FL_CAL + 1 + pair), 0, best[2] + best[3] < best[0] + best[1] ? 3u : 2u);
    if (c.lane == 0) for (int k = 0; k < 4; ++k) *(unsigned long long*)(p.ws + OFF_DBG + (13 * 64 + pair * 4 + k) * 8) = best[k];
    { const int lane = c.lane; LSTAMP(true, 4, 11 + 10 * pair); }
}
DEV int get_cal(const Ctx& c, int late, int& budget) {
    unsigned seen;
    if (!wait_flags<2>(c.rs, flagoff(FL_CAL + late + (late & c.lane)), 1u, seen)) budget = -1;
    return (__builtin_amdgcn_readlane(seen, 0) & 1) | (late ? (__builtin_amdgcn_readlane(seen, 1) & 1) << 1 : 0);
}
DEV unsigned now() { return (unsigned)__builtin_amdgcn_s_memtime(); }
DEV void wait_until(unsigned t) {
    int rem = (int)(t - now());
    while (rem > 260) { __builtin_amdgcn_s_sleep(1); rem = (int)(t - now()); }
    rem -= 60;
    asm volatile("1:\n\ts_add_i32 %0, %0, -40\n\ts_cmp_gt_i32 %0, 0\n\ts_cbranch_scc1 1b" : "+s"(rem) :: "scc");
}
#define T_PM 1920
#define T_OS 2065
#define T_OM 300
#define T_OU 650
#define T_G0 400
#define T_F1 2400
#define T_F2 500
#define T_PG 200
#define ABL(b) true
#define W_LAG_FROM 8
#define FSL 8
#define S_AT 6

#define MEM_TIMES_W(lbase)                                                                                           \
    v4u a_[7];                                                                                                       \
    _Pragma("unroll") for (int f = 0; f < 7; ++f) a_[f] = lds16a((lbase) + f * 1024);                                \
    EXTRA_READS;                                                                                                     \
    SCHED();                                                                                                         \
    _Pragma("unroll") for (int f = 0; f < 7; ++f) { c0 = smf(a_[f], w[0][2 * f], w[0][2 * f + 1], c0, sidx); c1 = smf(a_[f], w[1][2 * f], w[1][2 * f + 1], c1, sidx); AFTER_STEP(f); } \
    const v2u tp = {pk2(c0.x + c0.z, c0.y + c0.w), pk2(c1.x + c1.z, c1.y + c1.w)};

#define MEM_TIMES_W_OWN()                                                                                            \
    v4u a_[5];                                                                                                       \
    _Pragma("unroll") for (int k = 0; k < 5; ++k) a_[k] = lds16a(fr[k]);                                             \
    EXTRA_READS;                                                                                                     \
    SCHED();                                                                                                         \
    c0 = smf(a0, w[0][0], w[0][1], c0, sidx); c1 = smf(a0, w[1][0], w[1][1], c1, sidx);                              \
    c0 = smf(a1, w[0][2], w[0][3], c0, sidx); c1 = smf(a1, w[1][2], w[1][3], c1, sidx);                              \
    SCHED();                                                                                                         \
    _Pragma("unroll") for (int k = 0; k < 5; ++k) { c0 = smf(a_[k], w[0][2 * k + 4], w[0][2 * k + 5], c0, sidx); c1 = smf(a_[k], w[1][2 * k + 4], w[1][2 * k + 5], c1, sidx); } \
    const v2u tp = {pk2(c0.x + c0.z, c0.y + c0.w), pk2(c1.x + c1.z, c1.y + c1.w)};

#define VCLOB "v224", "v225", "v226", "v227", "v228", "v229", "v230", "v231", VCLOB_HI
#define VCLOB_HI "v232", "v233", "v234", "v235", "v236", "v237", "v238", "v239", \
              "v240", "v241", "v242", "v243", "v244", "v245", "v246", "v247", "v248", "v249", "v250", "v251", "v252", "v253", "v254", "v255"
#define PUBLISH_ASM(ST)                                                                                                                     \
    "s_cmp_lg_u32 %[pub], 7\n\ts_waitcnt lgkmcnt(0)\n\ts_barrier\n\ts_cbranch_scc1 9f\n\t"                                                  \
    "ds_read_b64 v[224:225], %[la]\n\tds_read_b64 v[226:227], %[la] offset:" #ST "*1\n\tds_read_b64 v[228:229], %[la] offset:" #ST "*2\n\t" \
    "ds_read_b64 v[230:231], %[la] offset:" #ST "*3\n\tds_read_b64 v[232:233], %[la] offset:" #ST "*4\n\t"                                  \
    "ds_read_b64 v[234:235], %[la] offset:" #ST "*5\n\tds_read_b64 v[236:237], %[la] offset:" #ST "*6\n\t"                                  \
    "ds_read_b64 v[238:239], %[la] offset:" #ST "*7\n\t"                                                                                    \
    "s_waitcnt lgkmcnt(6)\n\tv_pk_add_f32 v[224:225], v[224:225], v[226:227]\n\t"                                                           \
    "s_waitcnt lgkmcnt(4)\n\tv_pk_add_f32 v[228:229], v[228:229], v[230:231]\n\t"                                                           \
    "s_waitcnt lgkmcnt(2)\n\tv_pk_add_f32 v[232:233], v[232:233], v[234:235]\n\tv_pk_add_f32 v[224:225], v[224:225], v[228:229]\n\t"        \
    "s_waitcnt lgkmcnt(0)\n\tv_pk_add_f32 v[236:237], v[236:237], v[238:239]\n\tv_pk_add_f32 v[224:225], v[224:225], v[232:233]\n\t"        \
    "s_nop 0\n\tv_pk_add_f32 v[224:225], v[224:225], v[236:237]\n\t"                                                                        \
    "s_nop 0\n\tv_cvt_pk_bf16_f32 v224, v224, v225\n\ts_nop 1\n\t"                                                                          \
    "v_mov_b32_dpp v225, v224 quad_perm:[1,1,1,1] row_mask:0xf bank_mask:0xf\n\t"                                                           \
    "v_mov_b32_dpp v226, v224 quad_perm:[2,2,2,2] row_mask:0xf bank_mask:0xf\n\t"                                                           \
    "v_mov_b32_dpp v227, v224 quad_perm:[3,3,3,3] row_mask:0xf bank_mask:0xf\n\t"                                                           \
    "buffer_store_dwordx4 v[224:227], %[vo], %[rs], %[so] offen sc1\n\t"                                                                    \
    "9:\n\ts_barrier\n\ts_nop 0"

DEV void role_p(const Params& p, const Ctx& c, int idx) {
    const int h = idx >> 2, q = idx & 3, lane = c.lane, wave = c.wave, rho = c.rho, g = c.g;
    const rsrc_t rs = c.rs; char* lds = c.lds;
    const int om = q * 4 + wave;
    const bool isT = wave < 4 && om < 14, isX = wave < 4 && om >= 14;
    constexpr int L_MEM = 0, L_PART = 8192, L_DUMP = 10240, L_CAL = 11264, L_X = 16384;
    const int sidx = (rho & 2) ? 0xEEEE : 0x4444;
    v2u sel; { const int r0 = rho - 2 * g; sel.x = (r0 == 0 ? 0x3f80u : 0u) | (r0 == 1 ? 0x3f800000u : 0u); sel.y = (r0 == 8 ? 0x3f80u : 0u) | (r0 == 9 ? 0x3f800000u : 0u); }
    const int f0 = isX ? 4 : 2 * (wave & 3);
    auto blk = [&](int k) { return f0 + k >= 7 ? f0 + k - 7 : f0 + k; };
    const int po = f0 * 1024 + lane * 16, po2 = blk(1) * 1024 + lane * 16;
    int fr[5];
#pragma unroll
    for (int k = 0; k < 5; ++k) fr[k] = L_MEM + blk(k + 2) * 1024 + lane * 16;
    int budget = SPIN_CAP;
#pragma unroll
    for (int k = 0; k < 7; ++k) dma16(rs, OFF_XF + lane * 16, (wave * 7 + k) * 1024, L_X + (wave * 7 + k) * 1024);
    if (isT) {
        v4u w[2][14];
#pragma unroll
        for (int t = 0; t < 2; ++t)
#pragma unroll
            for (int ks = 0; ks < 14; ++ks) w[t][ks] = ld16(rs, OFF_AP + (h * 28 + 2 * om + t) * 14336 + g * 256 + img(rho) * 16, (2 * blk(ks >> 1) + (ks & 1)) * 1024, 0);
        VMWAIT(28);
        BAR0();
        STAMP(wave == 0, 23, idx);
#pragma unroll 1
        for (int sg = 0; sg < 2; ++sg) {
            v4f a[2][2] = {{{0, 0, 0, 0}, {0, 0, 0, 0}}, {{0, 0, 0, 0}, {0, 0, 0, 0}}};
#pragma unroll
            for (int ks = 0; ks < 14; ++ks)
#pragma unroll
                for (int kt = 0; kt < 2; ++kt) { const v4u x = lds16(lds, L_X + sg * 28672 + kt * 14336 + (2 * blk(ks >> 1) + (ks & 1)) * 1024 + g * 256 + (rho ^ (kt * 4)) * 16);
                    a[kt][0] = mfma32(w[0][ks], x, a[kt][0]); a[kt][1] = mfma32(w[1][ks], x, a[kt][1]); }
#pragma unroll
            for (int kt = 0; kt < 2; ++kt) {
                auto x = __builtin_amdgcn_permlane32_swap(pk2(a[kt][0].x, a[kt][0].y), pk2(a[kt][1].x, a[kt][1].y), false, false);
                auto y = __builtin_amdgcn_permlane32_swap(pk2(a[kt][0].z, a[kt][0].w), pk2(a[kt][1].z, a[kt][1].w), false, false);
                st16s(rs, OFF_G0 + (h * 14 + om) * 2048 + kt * 1024 + lane * 16, sg * (NH * 28672), (v4u){x[0], x[1], y[0], y[1]});
            }
            BAR0();
            STAMP(wave == 0, 24 + sg, idx);
        }
        int cal = __builtin_amdgcn_readfirstlane(*(const int*)(lds + L_CAL));
        STAMP(wave == 0, 26, idx);
        const int qa = L_MEM + (2 * om + (rho >> 3)) * 256 + (((rho & 7) >> 1) * 4 + (g >> 1) * 2 + (rho & 1)) * 16 + (g & 1) * 8;
        const int pw = (g >> 1) == (rho >> 3) ? L_PART + (2 * wave + (rho >> 3)) * 256 + (rho & 7) * 32 + (g & 1) * 16 : L_DUMP + lane * 16;
        const int tw = lane < 32 ? OFF_T + (h * 14 + om) * 512 + ((rho >> 3) * 2 + g) * 128 + (rho & 7) * 16 : OOB;
#pragma unroll 1
        for (int s = 0; s < NSEG && budget >= 0; ++s) {
            if (s == CAL_STEP) cal = __builtin_amdgcn_readfirstlane(*(const int*)(lds + L_CAL));
            v4f c0 = {0, 0, 0, 0}, c1 = {0, 0, 0, 0}; PIN2(c0, c1);
            BAR0();
            v4u a0, a1; v2u mq;
            stage2o(rs, po, po2, mem_base(s, cal), budget, a0, a1);
#define EXTRA_READS mq = lds8(lds, qa)
            MEM_TIMES_W_OWN()
#undef EXTRA_READS
            const v4f d2 = mfma16(tp, sel, (v4f){0, 0, 0, 0});
            const v2u d = {pk2(d2.x, d2.y), pk2(d2.z, d2.w)};
            *(v4f*)(lds + pw) = mfma16(d, mq, (v4f){0, 0, 0, 0});
            BAR();
            BAR0();
            if (s < NSEG - 1) wait_until(now() + T_PG);
            if (ABL(8)) { auto x = __builtin_amdgcn_permlane32_swap(d.x, d.y, false, false);
              auto y = __builtin_amdgcn_permlane32_swap(x[0], x[0], false, false); auto z = __builtin_amdgcn_permlane32_swap(x[1], x[1], false, false);
              st16s(rs, tw, s * (NH * 7168), (v4u){y[0], z[0], y[1], z[1]}); }
        }
    } else if (isX) {
        v4u w[2][14];
        int cal = get_cal(c, 0, budget);
        BAR0(); BAR0();
        if (om == 14) wait_until(now() + T_G0);
        else BAR0();
        const int gl0 = OFF_G + h * 28672 + (rho >> 3) * 1024 + g * 256 + (pperm(rho, 0) & 15) * 16;
        const int gfl = flagoff(FL_W + h * 7 + (lane & 3));
        const int ew = rho < 8 ? HO_E + h * 512 + g * 128 + rho * 16 : OOB, mw = g == 0 ? L_PART + 1024 + (rho & 7) * 32 + (rho >> 3) * 16 : L_DUMP + lane * 16;
        auto load_g = [&](int s, int t) {
            FENCE();
#pragma unroll
            for (int ks = 0; ks < 14; ++ks) w[t][ks] = ld16(rs, gl0 + t * 64, s * (NH * 28672) + (s < 2 ? OFF_G0 - OFF_G : 0) + (ks < 6 ? ks + 8 : ks - 6) * 2048, 1);
            FENCE();
        };
        bool lack = false;
#pragma unroll 1
        for (int s = om - 14; s < NSEG; s += 2) {
            const int sl = min(s, NSEG - 2);
            if (s == 1) BAR0();
            if (s >= 1) wait_until(now() + T_PG);
            unsigned seen;
            if (lack && !wait_flags<1>(rs, gfl, sl, seen)) budget = -1;
            load_g(sl, 0);
            if (s >= 2) { BAR0(); BAR0(); BAR0(); BAR0(); wait_until(now() + T_PG); }
            load_g(sl, 1);
            seen = ld4s(rs, gfl, 0);
            if (s == 1) { BAR0(); BAR0(); BAR0(); }
            if (s < 2) {
#pragma unroll 1
                for (;;) {
                    unsigned m = 0;
#pragma unroll
                    for (int ks = 0; ks < 14; ++ks) m = max(m, max(umax4(w[0][ks]), umax4(w[1][ks])));
                    if (__builtin_amdgcn_ballot_w64(m == SENT) == 0 || --budget < 0) break;
                    load_g(s, 0); load_g(s, 1);
                }
                if (s == 0) BAR0();
            }
            if ((s | 1) == CAL_STEP + 1) cal = __builtin_amdgcn_readfirstlane(*(const int*)(lds + L_CAL));
            const int sb = slot_base(s, cal);
            v4f c0 = {0, 0, 0, 0}, c1 = {0, 0, 0, 0}; PIN2(c0, c1);
            lack = __builtin_amdgcn_ballot_w64(seen < (unsigned)min(s + 2, NSEG - 2)) != 0;
            SCHED();
            STAMP(idx == 3 && s == 0, 4, 12); STAMP(s == 0, 27, h); STAMP(s == 1, 27, 16 + h);
            BAR0();
            v4u a0, a1;
            stage2o(rs, po, po2, mem_base(s, cal), budget, a0, a1);
            if (s < NSEG - 1) {
#define EXTRA_READS
                MEM_TIMES_W_OWN()
#undef EXTRA_READS
                const v4f d2 = mfma16(tp, sel, (v4f){0, 0, 0, 0});
                float m = fmx(fmx(d2.x, d2.y), fmx(d2.z, d2.w));
                m = max_g(fmx(m, ror8(m)));
                *(v4f*)(lds + mw) = (v4f){-m, -m, -m, -m};
                SCHED();
                BAR();
                PIN1(m);
                const float e0 = __builtin_amdgcn_exp2f(d2.x - m), e1 = __builtin_amdgcn_exp2f(d2.y - m), e2 = __builtin_amdgcn_exp2f(d2.z - m), e3 = __builtin_amdgcn_exp2f(d2.w - m);
                const unsigned u0 = pk2(e0, e1), u1 = pk2(e2, e3);
                st16s(rs, ew, sb, (v4u){u0, u1, dppu<0x128>(u0), dppu<0x128>(u1)});
            } else BAR0();
            BAR0();
        }
        if (om == 14) { BAR0(); BAR0(); BAR0(); BAR0(); }
    } else {
        if (wave == 4) { *(v4u*)(lds + L_PART + lane * 16) = (v4u){0u, 0u, 0u, 0u}; *(v4u*)(lds + L_PART + 1024 + lane * 16) = (v4u){0u, 0u, 0u, 0u}; }
        BAR();
        int cal = get_cal(c, 0, budget);
        if (wave == 6) *(int*)(lds + L_CAL) = cal;
        BAR(); BAR0();
        unsigned tref = now() - 65536;
        STAMP(idx == 0 && wave == 7, 4, 1);
        const bool stg = q == 3 && wave == 4;
        const int rw = lane < 8 ? HO_PART + idx * 128 + lane * 16 : q == 3 && lane >= 32 ? HO_E + h * 512 + (lane - 32) * 16 : OOB;
        int calp = cal, calq = cal;
        const int pr = L_PART + (lane & 31) * 8, pubo = lane < 32 && (lane & 3) == 0 ? HO_PART + idx * 128 + lane * 4 : OOB;
#pragma unroll 1
        for (int s = 0; s < NSEG && budget >= 0; ++s) {
            if (s == CAL_STEP - 1 && wave == 6) *(int*)(lds + L_CAL) = get_cal(c, 1, budget);
            if (s == CAL_STEP) cal = __builtin_amdgcn_readfirstlane(*(const int*)(lds + L_CAL));
            const int sb = slot_base(s, cal);
            if (wave == 5 && s >= 2) {
                wait_until(tref + T_PG);
                st16s(rs, rw, slot_base(s - 2, calp), (v4u){SENT, SENT, SENT, SENT});
                VMWAIT(0);
            }
            calp = calq; calq = cal;
            if (wave == 7) wait_until(tref + T_PM);
            BAR0();
            if (stg) {
                stage1(rs, 6144 + lane * 16, mem_base(s, cal), budget);
            } else BAR0();
            STAMP(idx == 0 && wave == 7, 0, s); STAMP(idx == 3 && wave == 6, 12, s);
            asm volatile(PUBLISH_ASM(256) :: [pub] "s"(wave), [la] "v"(pr), [vo] "v"(pubo), [rs] "s"(rs), [so] "s"(sb) : "memory", "scc", VCLOB);
            tref = now();
            if (wave == 7) {
                STAMP(idx == 0, 1, s);
                LSTAMP(idx == 0 && (s == 1 || s == 32 || s == 62), 14, s);
                if (lane == 0) st4s(rs, flagoff(FL_T + idx), 0, s);
            }
        }
    }
    if (budget < 0) give_up(rs);
    VMWAIT(0);
    BAR();
    if (wave == 7 && lane == 0) st4s(rs, flagoff(FL_T + idx), 0, NSEG);
}

DEV void role_o(const Params& p, const Ctx& c, int t) {
    const int lane = c.lane, wave = c.wave, rho = c.rho, g = c.g;
    const rsrc_t rs = c.rs; char* lds = c.lds;
    constexpr int L_MEM = 0, L_OUT = 8192, L_DUMP = 12288, L_V = 13312, L_CAL = 21504, L_X = 24576;
#pragma unroll
    for (int k = 0; k < 4; ++k) dma16(rs, wave * 4 + k < 28 ? OFF_XF + lane * 16 : OOB, (wave * 4 + k) * 1024, L_X + (wave * 4 + k) * 1024);
    v4u w[2][14];
#pragma unroll
    for (int k = 0; k < 2; ++k)
#pragma unroll
        for (int ks = 0; ks < 14; ++ks) w[k][ks] = ld16(rs, OFF_BT + ((2 * wave + k) * 28 + t) * 14336 + lane * 16, ks * 1024, 0);
    VMWAIT(28);
    BAR0();
    {
        v4u u[2];
#pragma unroll
        for (int k = 0; k < 2; ++k) {
            v4f a0 = {0, 0, 0, 0}, a1 = a0;
#pragma unroll
            for (int ks = 0; ks < 14; ++ks) { const int xo = L_X + (rho >> 3) * 14336 + ks * 1024 + g * 256;
                a0 = mfma32(lds16(lds, xo + ((pperm(rho, 0) & 15) ^ ((rho >> 3) * 4)) * 16), w[k][ks], a0); a1 = mfma32(lds16(lds, xo + ((pperm(rho, 1) & 15) ^ ((rho >> 3) * 4)) * 16), w[k][ks], a1); }
            u[k] = pk8(a0, a1);
        }
        asm volatile("v_mov_b32 v216, %0\n\tv_mov_b32 v217, %1\n\tv_mov_b32 v218, %2\n\tv_mov_b32 v219, %3\n\tv_mov_b32 v220, %4\n\tv_mov_b32 v221, %5\n\tv_mov_b32 v222, %6\n\tv_mov_b32 v223, %7"
                     :: "v"(u[0].x), "v"(u[0].y), "v"(u[0].z), "v"(u[0].w), "v"(u[1].x), "v"(u[1].y), "v"(u[1].z), "v"(u[1].w) : "v216", "v217", "v218", "v219", "v220", "v221", "v222", "v223");
    }
    int budget = SPIN_CAP;
    int cal = get_cal(c, 0, budget), caln = cal;
    const int sidx = (rho & 2) ? 0xEEEE : 0x4444;
    const int fa = L_MEM + lane * 16;
    const int hb = rho >> 3, hd = 2 * wave + hb, i = rho & 7;
    const int pa = HO_PART + hd * 512 + g * 128 + i * 16, ea = HO_E + hd * 512 + g * 128 + i * 16;
    const int ul = OFF_U + (t * 16 + 2 * wave) * 1024 + lane * 16;
    const int ufl = flagoff(FL_W + rho * 7 + ((14 + (t >> 1)) >> 2));
    const int mo = (lane & 3) == 0 ? HO_MEM + t * 256 + ((lane >> 4) * 4 + (lane & 4) / 2 + ((lane >> 3) & 1)) * 16 : OOB;
    const int p2 = (lane & 4) ? 2 * (lane & 3) + 1 : 2 * (lane & 3);
    const int ra = L_OUT + (lane >> 3) * 64 + p2 * 8;
    const int ow = hb == 0 ? L_OUT + wave * 512 + i * 64 + g * 16 : L_DUMP + lane * 16;
    const int po = wave * 1024 + lane * 16, po2 = min(wave + 4, 6) * 1024 + lane * 16;
    const v4u ones = {0x3f803f80u, 0x3f803f80u, 0x3f803f80u, 0x3f803f80u};
    const v2u ones2 = {0x3f803f80u, 0x3f803f80u};
    const unsigned mlo = hb ? 0u : 0xffffffffu, mhi = ~mlo;
    const unsigned long long hbm = 0xff00ff00ff00ff00ull;
    v4u psel; { const int kq = (rho >> 2) * 2 + (rho & 1); const unsigned one = (kq & 1) ? 0x3f800000u : 0x3f80u;
        psel = (v4u){(kq >> 1) == 0 ? one : 0u, (kq >> 1) == 1 ? one : 0u, (kq >> 1) == 2 ? one : 0u, (kq >> 1) == 3 ? one : 0u}; }
    asm volatile("v_mov_b32 v215, 0" ::: "v215");
    auto check_u = [&](int s) {
        unsigned long long lack; unsigned seen;
        asm volatile("v_cmp_gt_u32 %0, %1, v215" : "=s"(lack) : "s"(s) : "memory");
        if (lack != 0 && !wait_flags<1>(rs, ufl, s, seen)) budget = -1;
    };
    auto load_u = [&](int s) {
        asm volatile("s_nop 4\n\tbuffer_load_dwordx4 v[216:219], %0, %1, %2 offen sc1\n\tbuffer_load_dwordx4 v[220:223], %0, %1, %2 offen offset:1024 sc1"
                     :: "v"(ul), "s"(rs), "s"(s * (NH * 28672)) : "memory", "v216", "v217", "v218", "v219", "v220", "v221", "v222", "v223");
    };
    auto store_v = [&](int s) { st16s(rs, OFF_V + t * 4096 + (wave & 3) * 1024 + lane * 16, s * (NH * 7168), lds16(lds, L_V + (s & 1) * 4096 + (wave & 3) * 1024 + lane * 16)); };
    ACC_DECL;
    unsigned tref = now() - 65536;
    auto mem_times_b = [&](int sb, int s, bool last) -> v2u {
        v4f c0 = {0, 0, 0, 0}, c1 = {0, 0, 0, 0};
        if (wave < 4) stage2(rs, po, po2, s ? sb + HO_MEM : sb + OFF_MEM0, budget);
        else BAR0();
        if (last && wave == 7 && lane == 0) st4s(rs, flagoff(FL_V + t), 0, s);
        ACC_T(2);
#define AFTER_STEP(f) if (f == S_AT && !last) { SCHED(); ACC_T(4); if (wave == 0) wait_until(tref + T_OS); ACC_T(5); asm volatile("s_barrier\n\tbuffer_load_dwordx4 v[224:227], %0, %2, %3 offen sc1\n\tbuffer_load_dwordx4 v[228:231], %1, %2, %3 offen sc1" \
                          :: "v"(pa), "v"(ea), "s"(rs), "s"(sb) : "memory", VCLOB); if (wave >= 4 && ABL(32)) __builtin_amdgcn_s_setprio(1); SCHED(); }
#define EXTRA_READS if (wave < 4 && !last && s > 0 && ABL(1)) load_u(s)
        MEM_TIMES_W(fa)
#undef EXTRA_READS
        *(v2u*)(lds + L_V + (s & 1) * 4096 + wave * 512 + lane * 8) = tp;
        return tp;
    };
    STAMP(t == 0 && wave == 0, 4, 13); STAMP(wave == 0, 28, t); STAMP(wave == 7, 28, 32 + t);
    STAMP(t == 0 && wave == 0, 4, 14);
#pragma unroll 1
    for (int s = 0; s < NSEG - 1 && budget >= 0; ++s) {
        const int sb = slot_base(s, s < CAL_STEP ? cal : caln), sn = slot_base(s + 1, s + 1 < CAL_STEP ? cal : caln);
        ACC_T(0);
        const v2u vp = mem_times_b(sb, s, false);
        ACC_T(3);
        asm volatile(
            "s_branch 3f\n\t"
            "1:\n\t"
            "buffer_load_dwordx4 v[224:227], %[pa], %[rs], %[sb] offen sc1\n\t"
            "buffer_load_dwordx4 v[228:231], %[ea], %[rs], %[sb] offen sc1\n\t"
            "3:\n\t"
            "s_add_i32 %[bud], %[bud], -1\n\ts_cmp_lt_i32 %[bud], 0\n\ts_cselect_b32 vcc_hi, -1, -2\n\tv_mov_b32 v253, vcc_hi\n\t"
            "s_waitcnt vmcnt(1)\n\t"
            "v_mfma_f32_16x16x32_bf16 v[232:235], %[psel], v[224:227], 0\n\t"
            "v_max3_u32 v250, v224, v225, v226\n\t"
            "s_waitcnt vmcnt(0)\n\t"
            "v_mfma_f32_16x16x32_bf16 v[244:247], %[ones], v[228:231], 0\n\t"
            "v_max3_u32 v251, v228, v229, v230\n\t"
            "v_mfma_f32_16x16x32_bf16 v[236:239], v[216:219], v[228:231], 0\n\t"
            "v_max3_u32 v250, v250, v227, v231\n\t"
            "v_mfma_f32_16x16x32_bf16 v[240:243], v[220:223], v[228:231], 0\n\t"
            "v_max_u32 v250, v250, v251\n\t"
            "v_cmp_gt_u32 vcc, v250, v253\n\t"
            "s_cbranch_vccnz 1b\n\t"
            "v_exp_f32 v250, v232\n\tv_exp_f32 v251, v233\n\t"
            "v_max_f32 v252, v232, v233\n\t"
            "v_cvt_pk_bf16_f32 v250, v250, v251\n\t"
            "v_cmp_lt_f32 vcc, 0x42800000, v252\n\t"
            "v_and_b32 v248, v250, %[mlo]\n\tv_and_b32 v249, v250, %[mhi]\n\t"
            "v_cndmask_b32 v236, v236, v240, %[hbm]\n\tv_cndmask_b32 v237, v237, v241, %[hbm]\n\t"
            "v_mfma_f32_16x16x16_bf16 v[244:247], %[ones2], v[248:249], v[244:247]\n\t"
            "v_cndmask_b32 v238, v238, v242, %[hbm]\n\tv_cndmask_b32 v239, v239, v243, %[hbm]\n\t"
            "s_nop 1\n\t"
            "v_mfma_f32_16x16x16_bf16 v[236:239], %[vp], v[248:249], v[236:239]\n\t"
            "s_cbranch_vccz 2f\n\t"
            "s_nop 7\n\t"
            "v_max_f32 v252, 0, v252\n\tv_mov_b32 v253, v252\n\ts_nop 1\n\tv_permlane16_swap_b32 v252, v253\n\ts_nop 1\n\t"
            "v_max_f32 v252, v252, v253\n\tv_mov_b32 v253, v252\n\ts_nop 1\n\tv_permlane32_swap_b32 v252, v253\n\ts_nop 1\n\t"
            "v_max_f32 v252, v252, v253\n\t"
            "v_mfma_f32_16x16x32_bf16 v[244:247], %[ones], v[228:231], 0\n\t"
            "v_mfma_f32_16x16x32_bf16 v[236:239], v[216:219], v[228:231], 0\n\t"
            "v_sub_f32 v250, v232, v252\n\tv_sub_f32 v251, v233, v252\n\t"
            "v_exp_f32 v250, v250\n\tv_exp_f32 v251, v251\n\tv_exp_f32 v253, -v252\n\ts_nop 0\n\t"
            "v_cvt_pk_bf16_f32 v250, v250, v251\n\t"
            "v_and_b32 v248, v250, %[mlo]\n\tv_and_b32 v249, v250, %[mhi]\n\ts_nop 7\n\t"
            "v_cndmask_b32 v236, v236, v240, %[hbm]\n\tv_cndmask_b32 v237, v237, v241, %[hbm]\n\tv_cndmask_b32 v238, v238, v242, %[hbm]\n\tv_cndmask_b32 v239, v239, v243, %[hbm]\n\t"
            "v_mul_f32 v236, v236, v253\n\tv_mul_f32 v237, v237, v253\n\tv_mul_f32 v238, v238, v253\n\tv_mul_f32 v239, v239, v253\n\t"
            "v_mul_f32 v244, v244, v253\n\tv_mul_f32 v245, v245, v253\n\tv_mul_f32 v246, v246, v253\n\tv_mul_f32 v247, v247, v253\n\ts_nop 1\n\t"
            "v_mfma_f32_16x16x16_bf16 v[244:247], %[ones2], v[248:249], v[244:247]\n\t"
            "v_mfma_f32_16x16x16_bf16 v[236:239], %[vp], v[248:249], v[236:239]\n\t"
            "s_nop 7\n\t"
            "2:\n\t"
            "s_nop 1\n\t"
            "v_rcp_f32 v252, v244\n\t"
            "s_nop 3\n\t"
            "v_mul_f32 v236, v236, v252\n\tv_mul_f32 v237, v237, v252\n\tv_mul_f32 v238, v238, v252\n\tv_mul_f32 v239, v239, v252\n\t"
            "v_add_f32_dpp v240, v236, v236 row_ror:8 row_mask:0xf bank_mask:0xf\n\tv_add_f32_dpp v241, v237, v237 row_ror:8 row_mask:0xf bank_mask:0xf\n\t"
            "v_add_f32_dpp v242, v238, v238 row_ror:8 row_mask:0xf bank_mask:0xf\n\tv_add_f32_dpp v243, v239, v239 row_ror:8 row_mask:0xf bank_mask:0xf\n\t"
            "ds_write_b128 %[ow], v[240:243]\n\t"
            PUBLISH_ASM(512)
            : [bud] "+s"(budget)
            : [pa] "v"(pa), [ea] "v"(ea), [rs] "s"(rs), [sb] "s"(sb), [psel] "v"(psel), [ones] "v"(ones), [ones2] "v"(ones2), [vp] "v"(vp),
              [mlo] "v"(mlo), [mhi] "v"(mhi), [hbm] "s"(hbm), [ow] "v"(ow),
              [pub] "s"(wave), [la] "v"(ra), [vo] "v"(mo), [so] "s"(sn)
            : "memory", "vcc", "scc", VCLOB);
        __builtin_amdgcn_s_setprio(0);
        tref = now();
        STAMP(t == 0 && wave == 7, 3, s);
        if (wave == 7 && lane == 0) st4s(rs, flagoff(FL_V + t), 0, s);
        if (s + 2 == CAL_STEP) caln = __builtin_amdgcn_readfirstlane(*(const int*)(lds + L_CAL));
        if (wave == 5 && s + 3 == CAL_STEP) *(int*)(lds + L_CAL) = get_cal(c, 1, budget);
        if (s < 2 && wave >= 4) check_u(s + 1);
        if (s > 0 && wave == 4) check_u(min(s + 2, NSEG - 2));
        ACC_T(6);
        if (wave < 4) wait_until(tref + T_OM);
        else {
            wait_until(tref + T_OU);
            if (s + 1 < NSEG - 1 && ABL(1)) load_u(s + 1);
            if (wave == 4) asm volatile("buffer_load_dword v215, %0, %1, 0 offen sc1" :: "v"(ufl), "s"(rs) : "memory", "v215");
            if (ABL(2)) store_v(s);
            if (wave == 6 && s >= 2) st16s(rs, lane < 16 ? HO_MEM + t * 256 + lane * 16 : OOB, slot_base(s - 1, s - 1 < CAL_STEP ? cal : caln), (v4u){SENT, SENT, SENT, SENT});
        }
        ACC_T(7);
    }
    VMWAIT(0);
    ACC_OUT(t * 8 + wave);
    mem_times_b(slot_base(NSEG - 1, caln), NSEG - 1, true);
    if (budget < 0) give_up(rs);
    BAR();
    if (wave >= 4) store_v(NSEG - 1);
    VMWAIT(0);
    BAR0();
    STAMP(wave == 7, 20, t);
    if (wave == 7 && lane == 0) st4s(rs, flagoff(FL_V + t), 0, NSEG);
}

DEV void role_w(const Params& p, const Ctx& c, int wi) {
    const int lane = c.lane, wave = c.wave, rho = c.rho, g = c.g;
    const rsrc_t rs = c.rs; char* lds = c.lds;
    if (wave >= 4) {
#pragma unroll 1
        for (int n = -3; n < NSEG; ++n) {
            const int vo = n + 3 < NSEG ? OFF_XF + lane * 16 : OOB, so = ((n + 4) & 63) * 28672 + (wave - 4) * 7168, la = ((n + 3) & 3) * 28672 + (wave - 4) * 7168;
#pragma unroll 1
            for (int k = 0; k < 7168; k += 1024) dma16(rs, vo, so + k, la + k);
            if (n < -1) continue;
            if (n == 24 && wave == 4 && wi >= 48) {
                unsigned seen; wait_flags<8>(rs, flagoff(FL_TAB), 28, seen);
                __builtin_amdgcn_fence(__ATOMIC_ACQUIRE, "agent");
                VMWAIT(0);
            }
            VMWAIT(14);
            BAR0();
        }
        VMWAIT(0);
        BAR0();
        return;
    }
    const int h = wi / 7, unit = (wi - h * 7) * 4 + wave;
    const bool isG = unit < 14;
    v4u w[2][14];
#pragma unroll
    for (int t = 0; t < 2; ++t) {
        const int row = isG ? unit * 32 + pperm(rho, t) : (unit - 14) * 32 + t * 16 + rho;
#pragma unroll
        for (int ks = 0; ks < 14; ++ks) w[t][ks] = ld16(rs, (isG ? OFF_AP : OFF_BT) + (h * 28 + (row >> 4)) * 14336 + g * 256 + (row & 15) * 16, ks * 1024, 0);
    }
    int xa[2];
#pragma unroll
    for (int k = 0; k < 2; ++k) { const int key = isG ? k * 16 + rho : pperm(rho, k), kt = key >> 4; xa[k] = kt * 14336 + (g * 16 + ((key & 15) ^ (kt * 4))) * 16; }
    const int ob = (isG ? OFF_G + (h * 14 + unit) * 2048 : OFF_U + ((unit - 14) * 32 + h) * 1024) + lane * 16;
    const int ostep = isG ? 1024 : 16384;
    BAR0();
    STAMP(wi == 20 && wave == 1, 4, 2);
#pragma unroll 1
    for (int n = 0; n < NSEG; ++n) {
        const char* xb = lds + (n & 3) * 28672;
        v4f a00 = {0, 0, 0, 0}, a01 = a00, a10 = a00, a11 = a00;
        v4u x[2][14];
#pragma unroll
        for (int ks = 0; ks < 4; ++ks) { x[0][ks] = lds16(xb, xa[0] + ks * 1024); x[1][ks] = lds16(xb, xa[1] + ks * 1024); }
        if (isG) {
#pragma unroll
            for (int ks = 0; ks < 14; ++ks) {
                if (ks + 4 < 14) { x[0][ks + 4] = lds16(xb, xa[0] + (ks + 4) * 1024); x[1][ks + 4] = lds16(xb, xa[1] + (ks + 4) * 1024); }
                SCHED();
                a00 = mfma32(w[0][ks], x[0][ks], a00); a01 = mfma32(w[1][ks], x[0][ks], a01); a10 = mfma32(w[0][ks], x[1][ks], a10); a11 = mfma32(w[1][ks], x[1][ks], a11);
                SCHED();
            }
        } else {
#pragma unroll
            for (int ks = 0; ks < 14; ++ks) {
                if (ks + 4 < 14) { x[0][ks + 4] = lds16(xb, xa[0] + (ks + 4) * 1024); x[1][ks + 4] = lds16(xb, xa[1] + (ks + 4) * 1024); }
                SCHED();
                a00 = mfma32(x[0][ks], w[0][ks], a00); a01 = mfma32(x[1][ks], w[0][ks], a01); a10 = mfma32(x[0][ks], w[1][ks], a10); a11 = mfma32(x[1][ks], w[1][ks], a11);
                SCHED();
            }
        }
        const int so = ((n + 1) & 63) * (NH * 28672);
        st16s(rs, ob, so, pk8(a00, a01)); st16s(rs, ob + ostep, so, pk8(a10, a11));
        if (n == 0) VMWAIT(0); else if (n < W_LAG_FROM) VMWAIT(2); else VMWAIT(4);
        BAR0();
        if (wave == 0 && lane == 0) st4s(rs, flagoff(FL_W + wi), 0, n == 0 ? 1 : n < W_LAG_FROM ? n : n - 1);
        STAMP(wi == 20 && wave == 1 && (n & 1), 5, n >> 1);
    }
    VMWAIT(0);
    BAR0();
    if (wave == 0 && lane == 0) st4s(rs, flagoff(FL_W + wi), 0, NSEG);
}

DEV int f_first(int set, int wave) { return set ? 33 + 2 * wave : wave; }
DEV int f_next(int s, int wave, int set) { return s == 48 + set ? 63 - set : s >= 32 ? s + 16 : s >= 24 ? 32 + 2 * wave : s + 8; }
DEV int pm_base(int s, int set) { return s >= NSEG - 2 && set ? OFF_PM2 - OFF_PM + (s - 62) * (NH * 512) : s * (NH * 512); }
DEV void role_f1(const Params& p, const Ctx& c, int h, int set) {
    const int lane = c.lane, wave = c.wave, rho = c.rho, g = c.g;
    const rsrc_t rs = c.rs;
    const int gfl = flagoff(FL_W + h * 7 + (lane & 3)), tfl = flagoff(FL_T + h * 4 + (lane & 3));
    const int gl0 = OFF_G + h * 28672 + (rho >> 3) * 1024 + g * 256 + (pperm(rho, 0) & 15) * 16;
    const int tl = OFF_T + h * 7168 + g * 128 + ((rho >> 2) * 2 + (rho & 1)) * 16;
    const float ninf = -__builtin_inff();
    bool wdone = false;
#pragma unroll 1
    for (int s = f_first(set, wave); s < NSEG; s = f_next(s, wave, set)) {
        unsigned seen;
        if (!wdone) { if (!wait_flags<8>(rs, gfl, s ? s : NSEG, seen)) return; wdone = __builtin_amdgcn_ballot_w64(seen < NSEG) == 0; }
        v4u x[2][14];
        v4f a[2][2] = {{{0, 0, 0, 0}, {0, 0, 0, 0}}, {{0, 0, 0, 0}, {0, 0, 0, 0}}};
#pragma unroll
        for (int hf = 0; hf < 2; ++hf) {
            v4u y[2][7];
#pragma unroll
            for (int k = 0; k < 7; ++k) {
#pragma unroll
                for (int qt = 0; qt < 2; ++qt) x[qt][hf * 7 + k] = ld16(rs, OFF_XF + qt * 14336 + g * 256 + (rho ^ (qt * 4)) * 16, s * 28672 + (hf * 7 + k) * 1024, 0);
                y[0][k] = ld16(rs, gl0, s * (NH * 28672) + (hf * 7 + k) * 2048, 1); y[1][k] = ld16(rs, gl0 + 64, s * (NH * 28672) + (hf * 7 + k) * 2048, 1);
            }
            SCHED();
#pragma unroll
            for (int k = 0; k < 7; ++k)
#pragma unroll
                for (int qt = 0; qt < 2; ++qt) { a[qt][0] = mfma32(y[0][k], x[qt][hf * 7 + k], a[qt][0]); a[qt][1] = mfma32(y[1][k], x[qt][hf * 7 + k], a[qt][1]); }
        }
        float v[2][8], m[2];
#pragma unroll
        for (int qt = 0; qt < 2; ++qt) {
            const float r[8] = {a[qt][0].x, a[qt][0].y, a[qt][0].z, a[qt][0].w, a[qt][1].x, a[qt][1].y, a[qt][1].z, a[qt][1].w};
            m[qt] = ninf;
#pragma unroll
            for (int k = 0; k < 8; ++k) { v[qt][k] = (8 * g + k <= qt * 16 + rho) ? r[k] : ninf; m[qt] = fmx(m[qt], v[qt][k]); }
        }
        asm volatile("" : "+v"(m[0]), "+v"(m[1]));

        const bool last = s >= NSEG - 2;
        if (!wait_flags<FSL>(rs, tfl, last ? s - 2 : s + 1, seen) || (last && !wait_flags<1>(rs, tfl, s - 1, seen))) return;
        if (last) wait_until(now() + T_F1);

        STAMP(s == 63, 16, set * 16 + h);
        v4f am[2];
#pragma unroll 1
        for (int sp = 0; sp < SPIN_CAP; ++sp) {
            FENCE();
            v4u y[14]; unsigned mx = 0;
#pragma unroll
            for (int ks = 0; ks < 14; ++ks) y[ks] = ld16(rs, tl, s * (NH * 7168) + ks * 512, 1);
            SCHED();
            am[0] = am[1] = (v4f){0, 0, 0, 0};
#pragma unroll
            for (int ks = 0; ks < 14; ++ks) { am[0] = mfma32(y[ks], x[0][ks], am[0]); am[1] = mfma32(y[ks], x[1][ks], am[1]); mx = max(mx, umax4(y[ks])); PIN1(mx); PIN2(am[0], am[1]); }
            if (!last || __builtin_amdgcn_ballot_w64(mx == SENT) == 0) break;
        }
        STAMP(s == 63, 16, 32 + set * 16 + h);
        unsigned pm[2] = {0u, 0u};
#pragma unroll
        for (int qt = 0; qt < 2; ++qt) {
            if (last && qt != set) continue;
            const float mm = max_g(fmx(m[qt], fmx(am[qt].x, am[qt].y)));
            const float e0 = __builtin_amdgcn_exp2f(am[qt].x - mm), e1 = __builtin_amdgcn_exp2f(am[qt].y - mm);
            float z = e0 + e1;
#pragma unroll
            for (int k = 0; k < 8; ++k) { v[qt][k] = __builtin_amdgcn_exp2f(v[qt][k] - mm); z += v[qt][k]; }
            const float rz = __builtin_amdgcn_rcpf(sum_g(z));
            st16s(rs, OFF_PX + (h * 2 + qt) * 1024 + lane * 16, s * (NH * 2048),
                  (v4u){pk2(v[qt][0] * rz, v[qt][1] * rz), pk2(v[qt][2] * rz, v[qt][3] * rz), pk2(v[qt][4] * rz, v[qt][5] * rz), pk2(v[qt][6] * rz, v[qt][7] * rz)});
            pm[qt] = pk2(e0 * rz, e1 * rz);
        }
        if (last) pm[0] = pm[1] = pm[0] | pm[1];
        st16s(rs, (lane & 1) == 0 ? OFF_PM + h * 512 + lane * 8 : OOB, pm_base(s, set), (v4u){pm[0], pm[1], dppu<0xF5>(pm[0]), dppu<0xF5>(pm[1])});
        STAMP(s == 63, 17, set * 16 + h);
        if (last) continue;
        VMWAIT(0);
        if (lane == 0) st4s(rs, flagoff((set ? FL_P2 : FL_P) + h * 8 + wave), 0, s + 1);

    }
}

DEV void make_tables(const Params& p, const Ctx& c, int t) {
    const int lane = c.lane, wave = c.wave, rho = c.rho, g = c.g;
    const rsrc_t rs = c.rs;
    for (int ch = t * 512 + wave * 64 + lane; ch < 2 * 28672; ch += 28 * 512) {
        float v[8];
        if (ch < 28672) { const int fr = ch >> 6, tl = fr / 14, d0 = (fr - tl * 14) * 32 + g * 8, ri = tl >> 4, k = (tl & 15) * 16 + rho;
#pragma unroll
            for (int j = 0; j < 8; ++j) { const float ph = (float)((k * (d0 + j)) % 448) * (1.f / 448.f); v[j] = k > 224 ? 0.f : ri ? -__builtin_amdgcn_sinf(ph) : __builtin_amdgcn_cosf(ph); }
        } else { const int fr = (ch - 28672) >> 6, d = (fr >> 4) * 16 + rho, ri = (fr >> 3) & 1, k0 = (fr & 7) * 32 + g * 8;
#pragma unroll
            for (int j = 0; j < 8; ++j) { const int k = k0 + j; const float ph = (float)((k * d) % 448) * (1.f / 448.f); const bool edge = k == 0 || k == 224;
                const float wgt = (edge ? 1.f : 2.f) / (448.f * 2048.f); v[j] = k > 224 ? 0.f : ri ? (edge ? 0.f : -wgt * __builtin_amdgcn_sinf(ph)) : wgt * __builtin_amdgcn_cosf(ph); }
        }
        st16s(rs, OFF_FD + ch * 16, 0, (v4u){pk2(v[0], v[1]), pk2(v[2], v[3]), pk2(v[4], v[5]), pk2(v[6], v[7])});
    }
    VMWAIT(0);
    BAR();
    if (wave == 0 && lane == 0) __hip_atomic_fetch_add((unsigned*)(p.ws + flagoff(FL_TAB)), 1u, __ATOMIC_RELAXED, __HIP_MEMORY_SCOPE_AGENT);
}
DEV void role_f2(const Params& p, const Ctx& c, int t, int set) {
    const int lane = c.lane, wave = c.wave, rho = c.rho, g = c.g;
    const rsrc_t rs = c.rs;
    const int ufl = flagoff(FL_W + rho * 7 + ((14 + (t >> 1)) >> 2)), pfl = flagoff(lane < 16 ? (set ? FL_P2 : FL_P) + lane * 8 + wave : FL_V + t);
    const int ul = OFF_U + t * 16384 + lane * 16;
    bool wdone = false;
#pragma unroll 1
    for (int s = f_first(set, wave); s < NSEG; s = f_next(s, wave, set)) {
        unsigned seen;
        if (!wdone) { if (!wait_flags<8>(rs, ufl, s ? s : NSEG, seen)) return; wdone = __builtin_amdgcn_ballot_w64(seen < NSEG) == 0; }

        v4u u[16];
#pragma unroll
        for (int h = 0; h < 16; ++h) u[h] = ld16(rs, ul, s * (NH * 28672) + h * 1024, 1);
        const bool last = s >= NSEG - 2;
        if (!wait_flags<FSL>(rs, last ? flagoff(FL_V + t) : pfl, last ? s - 1 : s + 1, seen) || (last && !wait_flags<1>(rs, flagoff(FL_V + t), s + 1, seen))) return;
        if (s == NSEG - 1) wait_until(now() + T_F2);
        STAMP(s == 63, 18, set * 32 + t);
        v4f a[2], m[2];
        const int q0 = last ? set : 0;
#pragma unroll 1
        for (int sp = 0; sp < SPIN_CAP; ++sp) {
            FENCE();
            a[0] = a[1] = m[0] = m[1] = (v4f){0, 0, 0, 0};
            v4u px[16]; v2u pm[16], vv[8]; unsigned mx = 0;
#pragma unroll
            for (int h = 0; h < 16; ++h) { pm[h] = ld8s(rs, OFF_PM + lane * 8, pm_base(s, set) + h * 512); if (h & 1) vv[h >> 1] = ld8s(rs, OFF_V + t * 4096 + lane * 8, s * (NH * 7168) + (h >> 1) * 512); }
#pragma unroll
            for (int h = 0; h < 16; ++h) px[h] = ld16(rs, OFF_PX + lane * 16, s * (NH * 2048) + q0 * 1024 + h * 2048, 1);
            SCHED();
#pragma unroll
            for (int k = 0; k < 8; ++k) { m[0] = mfma16(vv[k], (v2u){pm[2 * k].x, pm[2 * k + 1].x}, m[0]); m[1] = mfma16(vv[k], (v2u){pm[2 * k].y, pm[2 * k + 1].y}, m[1]);
                mx = max(max(mx, max(pm[2 * k].x, pm[2 * k].y)), max(pm[2 * k + 1].x, pm[2 * k + 1].y)); PIN1(mx); PIN2(m[0], m[1]); SCHED(); }
#pragma unroll
            for (int h = 0; h < 16; ++h) { a[0] = mfma32(u[h], px[h], a[0]); mx = max(mx, umax4(px[h])); PIN2(mx, a[0]); SCHED(); }
            if (last) { if (__builtin_amdgcn_ballot_w64(mx == SENT) == 0) break; continue; }
#pragma unroll
            for (int h = 0; h < 16; ++h) px[h] = ld16(rs, OFF_PX + 1024 + lane * 16, s * (NH * 2048) + h * 2048, 1);
            SCHED();
#pragma unroll
            for (int h = 0; h < 16; ++h) { a[1] = mfma32(u[h], px[h], a[1]); PIN1(a[1]); }
            break;
        }
        STAMP(s == 63, 19, set * 32 + t);
#pragma unroll
        for (int qt = 0; qt < 2; ++qt) { const v4f o = a[qt] + m[qt];
            if (qt == 0 || !last) *(v2u*)(p.out + (size_t)(s * 32 + (qt + q0) * 16 + rho) * 896 + DM + t * 16 + g * 4) = (v2u){pk2(o.x, o.y), pk2(o.z, o.w)}; }

    }
}

DEV bool fft_sync(const Params& p, const Ctx& c, int fl) {
    VMWAIT(0);
    BAR();
    int* okw = (int*)(c.lds + LDS_MAIN - 16);
    if (c.wave == 0) {
        if (c.lane == 0) __hip_atomic_fetch_add((unsigned*)(p.ws + flagoff(fl)), 1u, __ATOMIC_RELAXED, __HIP_MEMORY_SCOPE_AGENT);
        unsigned seen; const bool ok = wait_flags<2>(c.rs, flagoff(fl), 64, seen);
        if (c.lane == 0) *okw = ok;
    }
    BAR();
    return *okw != 0;
}
DEV v4u neg8(v4u v) { return v ^ 0x80008000u; }
DEV void twid(int a, const int (&n)[8], int mask, float inv, v4u& C, v4u& S) {
    float cs[8], sn[8];
#pragma unroll
    for (int j = 0; j < 8; ++j) { const float ph = (float)((a * n[j]) & mask) * inv; cs[j] = __builtin_amdgcn_cosf(ph); sn[j] = __builtin_amdgcn_sinf(ph); }
    C = (v4u){pk2(cs[0], cs[1]), pk2(cs[2], cs[3]), pk2(cs[4], cs[5]), pk2(cs[6], cs[7])};
    S = (v4u){pk2(sn[0], sn[1]), pk2(sn[2], sn[3]), pk2(sn[4], sn[5]), pk2(sn[6], sn[7])};
}
DEV void role_fft(const Params& p, const Ctx& c, int job) {
    const int lane = c.lane, wave = c.wave, rho = c.rho, g = c.g;
    const rsrc_t rs = c.rs; char* lds = c.lds;
    const v4f z4 = {0, 0, 0, 0};

    {
        const rsrc_t rx = mkrsrc(p.xa, 2048 * DM * 2);
        for (int pc = wave; pc < 28; pc += 8) { const int nt = pc >= 14, ks = pc - nt * 14; dma16(rx, ((64 * (nt * 16 + rho) + job) * DM + ks * 32 + g * 8) * 2, 0, pc * 1024); }

        v4u C[2], S[2];
        { int n[8];
#pragma unroll
          for (int j = 0; j < 8; ++j) n[j] = 64 * (16 * (j >> 2) + 4 * g + (j & 3)) + job;
          twid(rho, n, 2047, 1.f / 2048.f, C[0], S[0]); twid(16 + rho, n, 2047, 1.f / 2048.f, C[1], S[1]); }
        VMWAIT(0);
        BAR();
        v4f zr[2][2], zi[2][2];
#pragma unroll
        for (int t = 0; t < 2; ++t) {
            const int fo = OFF_FD + (wave * 2 + (rho >> 3)) * 14336 + g * 256 + (pperm(rho, t) & 15) * 16;
            v4f r0 = z4, r1 = z4, i0 = z4, i1 = z4;
#pragma unroll
            for (int hf = 0; hf < 2; ++hf) {
                v4u fr[7], fi[7];
#pragma unroll
                for (int k = 0; k < 7; ++k) { fr[k] = ld16(rs, fo, (hf * 7 + k) * 1024, 0); fi[k] = ld16(rs, fo, 256 * DM * 2 + (hf * 7 + k) * 1024, 0); }
                SCHED();
#pragma unroll
                for (int k = 0; k < 7; ++k) { const v4u x0 = lds16(lds, (hf * 7 + k) * 1024 + lane * 16), x1 = lds16(lds, (14 + hf * 7 + k) * 1024 + lane * 16);
                    r0 = mfma32(x0, fr[k], r0); r1 = mfma32(x1, fr[k], r1); i0 = mfma32(x0, fi[k], i0); i1 = mfma32(x1, fi[k], i1); }
            }
            const v4u yr = pk8(r0, r1), yi = pk8(i0, i1), nr = neg8(yr);
#pragma unroll
            for (int ct = 0; ct < 2; ++ct) { zr[ct][t] = mfma32(yi, S[ct], mfma32(yr, C[ct], z4)); zi[ct][t] = mfma32(nr, S[ct], mfma32(yi, C[ct], z4)); }
        }
        char* O = lds + 32768;
#pragma unroll
        for (int ct = 0; ct < 2; ++ct) { const int o = (ct * 16 + rho) * 1040 + wave * 64 + g * 16;
            *(v4u*)(O + o) = pk8(zr[ct][0], zr[ct][1]); *(v4u*)(O + o + 512) = pk8(zi[ct][0], zi[ct][1]); }
        BAR();
#pragma unroll
        for (int k = 0; k < 4; ++k) st16s(rs, OFF_Z1 + job * 1024 + lane * 16, (wave * 4 + k) * 65536, lds16(O, (wave * 4 + k) * 1040 + lane * 16));
    }
    STAMP(job == 0 && wave == 1, 4, 3);
    if (!fft_sync(p, c, FL_FA)) return;
    STAMP(job == 0 && wave == 1, 4, 4);
    if (wave < 4) {
        const int k1 = job >> 1, P = (job & 1) * 4 + wave;
        char* L = lds + wave * 8192;
#pragma unroll
        for (int pc = 0; pc < 8; ++pc) dma16s(rs, OFF_Z1 + (k1 * 64 + (pc & 3) * 16 + (lane >> 2)) * 1024 + (pc >> 2) * 512 + P * 64 + (lane & 3) * 16, 0, wave * 8192 + pc * 1024);
        v4u C[4][2], S[4][2];
#pragma unroll
        for (int ks = 0; ks < 2; ++ks) { int n[8];
#pragma unroll
            for (int j = 0; j < 8; ++j) n[j] = 32 * ks + 16 * (j >> 2) + 4 * g + (j & 3);
#pragma unroll
            for (int kt = 0; kt < 4; ++kt) twid(16 * kt + rho, n, 63, 1.f / 64.f, C[kt][ks], S[kt][ks]); }
        VMWAIT(0);
        v2u hr[4], hi[4];
#pragma unroll
        for (int t = 0; t < 2; ++t) {
            const int k = P * 32 + pperm(rho, t);
            v4u xr[2], xi[2];
#pragma unroll
            for (int ks = 0; ks < 2; ++ks) { const int a = (32 * ks + 4 * g + (rho >> 2)) * 64 + (rho & 3) * 16 + t * 8;
                const v2u a0 = ldstr(L, a), a1 = ldstr(L, a + 1024), b0 = ldstr(L, a + 4096), b1 = ldstr(L, a + 5120);
                xr[ks] = (v4u){a0.x, a0.y, a1.x, a1.y}; xi[ks] = (v4u){b0.x, b0.y, b1.x, b1.y}; }
            const v4u n0 = neg8(xr[0]), n1 = neg8(xr[1]);
            unsigned short fr[16], fi[16];
#pragma unroll
            for (int j = 0; j < 16; ++j) { const int o = (k1 + 32 * (16 * (j >> 2) + 4 * g + (j & 3))) * 225 + min(k, 224); fr[j] = p.wr[o]; fi[j] = p.wi[o]; }
            SCHED();
            v4f vr[4], vi[4];
#pragma unroll
            for (int kt = 0; kt < 4; ++kt) {
                const v4f ar = mfma32(S[kt][1], xi[1], mfma32(S[kt][0], xi[0], mfma32(C[kt][1], xr[1], mfma32(C[kt][0], xr[0], z4))));
                const v4f ai = mfma32(S[kt][1], n1, mfma32(S[kt][0], n0, mfma32(C[kt][1], xi[1], mfma32(C[kt][0], xi[0], z4))));
                float wr[4], wi[4];
#pragma unroll
                for (int r = 0; r < 4; ++r) { wr[r] = __uint_as_float((unsigned)fr[kt * 4 + r] << 16); wi[r] = __uint_as_float((unsigned)fi[kt * 4 + r] << 16); }
                vr[kt] = (v4f){ar.x * wr[0] - ai.x * wi[0], ar.y * wr[1] - ai.y * wi[1], ar.z * wr[2] - ai.z * wi[2], ar.w * wr[3] - ai.w * wi[3]};
                vi[kt] = (v4f){ar.x * wi[0] + ai.x * wr[0], ar.y * wi[1] + ai.y * wr[1], ar.z * wi[2] + ai.z * wr[2], ar.w * wi[3] + ai.w * wr[3]};
            }
            const v4u pr0 = pk8(vr[0], vr[1]), pr1 = pk8(vr[2], vr[3]), pi0 = pk8(vi[0], vi[1]), pi1 = pk8(vi[2], vi[3]), ni0 = neg8(pi0), ni1 = neg8(pi1);
#pragma unroll
            for (int nt = 0; nt < 4; ++nt) {
                const v4f rr = mfma32(ni1, S[nt][1], mfma32(ni0, S[nt][0], mfma32(pr1, C[nt][1], mfma32(pr0, C[nt][0], z4))));
                const v4f ri = mfma32(pr1, S[nt][1], mfma32(pr0, S[nt][0], mfma32(pi1, C[nt][1], mfma32(pi0, C[nt][0], z4))));
                const v2u qr = {pk2(rr.x, rr.y), pk2(rr.z, rr.w)}, qi = {pk2(ri.x, ri.y), pk2(ri.z, ri.w)};
                if (t == 0) { hr[nt] = qr; hi[nt] = qi; }
                else { char* o = lds + 32768 + (nt * 16 + rho) * 528 + wave * 64 + g * 16;
                    *(v4u*)o = (v4u){hr[nt].x, hr[nt].y, qr.x, qr.y}; *(v4u*)(o + 256) = (v4u){hi[nt].x, hi[nt].y, qi.x, qi.y}; }
            }
        }
    }
    BAR();
#pragma unroll
    for (int k = 0; k < 4; ++k) { const int n2 = wave * 8 + k * 2 + (lane >> 5);
        st16s(rs, OFF_Z2 + (n2 * 32 + (job >> 1)) * 1024 + (g & 1) * 512 + (job & 1) * 256 + rho * 16, 0, lds16(lds, 32768 + n2 * 528 + (lane & 31) * 16)); }
    STAMP(job == 0 && wave == 1, 4, 5);
    if (!fft_sync(p, c, FL_FB)) return;
    STAMP(job == 0 && wave == 1, 4, 6);
    {
        char* L = lds + wave * 4096;
#pragma unroll
        for (int pc = 0; pc < 4; ++pc) dma16s(rs, OFF_Z2 + (job * 32 + (pc & 1) * 16 + (lane >> 2)) * 1024 + (pc >> 1) * 512 + wave * 64 + (lane & 3) * 16, 0, wave * 4096 + pc * 1024);
        v4u C[2], S[2];
        { int n[8];
#pragma unroll
          for (int j = 0; j < 8; ++j) n[j] = 8 * g + j;
          twid(64 * rho + job, n, 2047, 1.f / 2048.f, C[0], S[0]); twid(64 * (16 + rho) + job, n, 2047, 1.f / 2048.f, C[1], S[1]); }
        VMWAIT(0);
        v4f rr[2][2], ri[2][2];
#pragma unroll
        for (int t = 0; t < 2; ++t) {
            const int a = (8 * g + (rho >> 2)) * 64 + (rho & 3) * 16 + t * 8;
            const v2u a0 = ldstr(L, a), a1 = ldstr(L, a + 256), b0 = ldstr(L, a + 2048), b1 = ldstr(L, a + 2304);
            const v4u xr = {a0.x, a0.y, a1.x, a1.y}, xi = {b0.x, b0.y, b1.x, b1.y}, ni = neg8(xi);
#pragma unroll
            for (int nt = 0; nt < 2; ++nt) { rr[nt][t] = mfma32(ni, S[nt], mfma32(xr, C[nt], z4)); ri[nt][t] = mfma32(xr, S[nt], mfma32(xi, C[nt], z4)); }
        }
        char* R = lds + 32768;
#pragma unroll
        for (int nt = 0; nt < 2; ++nt) { *(v4u*)(R + (nt * 16 + wave) * 1024 + lane * 16) = pk8(rr[nt][0], rr[nt][1]); *(v4u*)(R + (nt * 16 + 8 + wave) * 1024 + lane * 16) = pk8(ri[nt][0], ri[nt][1]); }
        BAR();
#pragma unroll 1
        for (int Q = wave; Q < 14; Q += 8) {
            v4f y[2][2] = {{z4, z4}, {z4, z4}};
#pragma unroll
            for (int hf = 0; hf < 2; ++hf) {
                v4u f0[8], f1[8];
#pragma unroll
                for (int k = 0; k < 8; ++k) { const int fo = OFF_FI + (Q * 2 + (rho >> 3)) * 16384 + g * 256 + (pperm(rho, 0) & 15) * 16; f0[k] = ld16(rs, fo, (hf * 8 + k) * 1024, 0); f1[k] = ld16(rs, fo + 64, (hf * 8 + k) * 1024, 0); }
                SCHED();
#pragma unroll
                for (int k = 0; k < 8; ++k) { const v4u b0 = lds16(R, (hf * 8 + k) * 1024 + lane * 16), b1 = lds16(R, (16 + hf * 8 + k) * 1024 + lane * 16);
                    y[0][0] = mfma32(f0[k], b0, y[0][0]); y[0][1] = mfma32(f1[k], b0, y[0][1]); y[1][0] = mfma32(f0[k], b1, y[1][0]); y[1][1] = mfma32(f1[k], b1, y[1][1]); }
            }
#pragma unroll
            for (int nt = 0; nt < 2; ++nt) *(v4u*)(p.out + (size_t)(64 * (nt * 16 + rho) + job) * 896 + Q * 32 + g * 8) = pk8(y[nt][0], y[nt][1]);
        }
    }
}

extern "C" __global__ void __launch_bounds__(512, 2) k_main(Params p) {
    extern __shared__ __attribute__((aligned(16))) char lds[];
    Ctx c; c.rs = mkrsrc(p.ws, WS_BYTES); c.lds = lds; c.lane = threadIdx.x & 63; c.wave = __builtin_amdgcn_readfirstlane(threadIdx.x >> 6); c.rho = c.lane & 15; c.g = c.lane >> 4;
    { const int lane = c.lane; (void)lane; STAMP(blockIdx.x == 0 && c.wave == 7, 4, 0); }
    const int rot = *(const int*)(p.ws + flagoff(FL_ROT));
    const int x = (blockIdx.x + rot) & 7, y = blockIdx.x >> 3;
    if (x < 2) role_p(p, c, x * 32 + y);
    else if (x == 2) { if (y < 28) role_o(p, c, y); else if (y == 28) role_cal(p, c, 1); }
    else if (x == 3 && y == 28) role_cal(p, c, 0);
    else {
        const int wi = (x - 4) * 32 + y - 16;
        if (x == 3) { if (y < 28) make_tables(p, c, y); } else if (wi >= 0) role_w(p, c, wi);
        if (wi >= 48) role_fft(p, c, wi - 48);
        else if (x == 3 ? y < 28 : wi >= 16 && wi < 44) role_f2(p, c, x == 3 ? y : wi - 16, x != 3);
        else if (x == 4) role_f1(p, c, y & 15, wi >= 0);
    }
    { const int lane = c.lane; (void)lane; LSTAMP(c.wave == 7, 8 + (blockIdx.x >> 6), blockIdx.x & 63); LSTAMP(c.wave == 0 && x == 3, 12, y); }
}

extern "C" __global__ void k_end(Params p) {
    const long long t = (long long)__builtin_amdgcn_s_memrealtime();
    long long m = 0; for (int k = 0; k < 4; ++k) { const long long v = *(const long long*)(p.ws + OFF_DBG + ((8 + k) * 64 + threadIdx.x) * 8); m = v > m ? v : m; }
    for (int o = 32; o; o >>= 1) { const long long v = __shfl_xor(m, o); m = v > m ? v : m; }
    { const long long v = threadIdx.x < 28 ? *(const long long*)(p.ws + OFF_DBG + (12 * 64 + threadIdx.x) * 8) : 0; long long u = v; for (int o = 32; o; o >>= 1) { const long long z = __shfl_xor(u, o); u = z > u ? z : u; } m = u > m ? u : m; }
    auto S = [&](int sl, int k) { return *(const long long*)(p.ws + OFF_DBG + (sl * 64 + k) * 8); };
    long long I = 1000 + 0 * m; I = I < 0 ? 0 : I > 20000 ? 20000 : I;
    const long long until = t + 4 * I;
    while ((long long)__builtin_amdgcn_s_memrealtime() < until) __builtin_amdgcn_s_sleep(1);
}
extern "C" void kernel_launch(void* const* d_in, const int* in_sizes, int n_in, void* d_out, int out_size, void* d_ws, size_t ws_size, hipStream_t stream) {
    Params p;
    p.xa = (const unsigned short*)d_in[0]; p.wr = (const unsigned short*)d_in[1]; p.wi = (const unsigned short*)d_in[2];
    p.xb = (const unsigned short*)d_in[3]; p.mem0 = (const unsigned short*)d_in[4];
    p.wq = (const unsigned short*)d_in[5]; p.wk = (const unsigned short*)d_in[6]; p.wv = (const unsigned short*)d_in[7]; p.wo = (const unsigned short*)d_in[8];
    p.out = (unsigned short*)d_out;
    p.ws = (char*)d_ws + (((size_t)2 << 20) - ((uintptr_t)d_ws & (((size_t)2 << 20) - 1))) % ((size_t)2 << 20);
    (void)hipFuncSetAttribute((const void*)k_main, hipFuncAttributeMaxDynamicSharedMemorySize, LDS_MAIN);
    (void)hipFuncSetAttribute((const void*)k_pro, hipFuncAttributeMaxDynamicSharedMemorySize, LDS_PRO);
    hipLaunchKernelGGL(k_pro, dim3(256), dim3(512), LDS_PRO, stream, p);
    hipLaunchKernelGGL(k_main, dim3(256), dim3(512), LDS_MAIN, stream, p);
    hipLaunchKernelGGL(k_end, dim3(1), dim3(64), 0, stream, p);
}
```

```cpp
#include <hip/hip_runtime.h>
#include <hip/hip_bf16.h>
#include <stdint.h>

typedef unsigned v4u __attribute__((ext_vector_type(4)));
typedef unsigned v2u __attribute__((ext_vector_type(2)));
typedef float v4f __attribute__((ext_vector_type(4)));
typedef float v2f __attribute__((ext_vector_type(2)));
typedef short v4s __attribute__((ext_vector_type(4)));
typedef __bf16 v2bf __attribute__((ext_vector_type(2)));
typedef __bf16 v8bf __attribute__((ext_vector_type(8)));
typedef __bf16 v16bf __attribute__((ext_vector_type(16)));
typedef unsigned v8u __attribute__((ext_vector_type(8)));
typedef __amdgpu_buffer_rsrc_t rsrc_t;
#define LAS __attribute__((address_space(3)))
#define DEV __device__ __forceinline__

constexpr int NSEG = 64, NH = 16, DM = 448;
constexpr unsigned SENT = 0xFFFFFFFFu;
constexpr int OOB = 0x7fffffff;
constexpr int SC1 = 16;
constexpr int OFF_HO = 0;
constexpr int OFF_CTL = 4 << 20;
constexpr int OFF_AP = OFF_CTL + 131072;
constexpr int OFF_BT = OFF_AP + NH * DM * DM * 2;
constexpr int OFF_G = OFF_BT + NH * DM * DM * 2;
constexpr int OFF_U = OFF_G + NSEG * NH * 28672;
constexpr int OFF_T = OFF_U + NSEG * NH * 28672;
constexpr int OFF_V = OFF_T + NSEG * NH * 7168;
constexpr int OFF_PX = OFF_V + NSEG * NH * 7168;
constexpr int OFF_PM = OFF_PX + NSEG * NH * 2048;
constexpr int OFF_FD = OFF_PM + NSEG * NH * 512;
constexpr int OFF_FI = OFF_FD + 512 * DM * 2;
constexpr int OFF_Z1 = OFF_FI + 512 * DM * 2;
constexpr int OFF_Z2 = OFF_Z1 + (2 << 20);
constexpr int OFF_XF = OFF_Z2 + (2 << 20);
constexpr int OFF_G0 = OFF_XF + (2 << 20);
constexpr int OFF_PM2 = OFF_G0 + 2 * NH * 28672;
constexpr int OFF_DBG = OFF_G0 + (1 << 20);
constexpr int WS_BYTES = OFF_DBG + (1 << 20);
constexpr int HO_MEM = 0, HO_PART = 32768, HO_E = 65536, HO_SLOT = 98304;
constexpr int OFF_MEM0 = 4 * HO_SLOT, OFF_PROBE = OFF_MEM0 + 32768;
constexpr int FL_W = 0, FL_T = 112, FL_V = 176, FL_P = 208, FL_ROT = 336, FL_TAB = 337, FL_FA = 338, FL_FB = 339, FL_ABORT = 340, FL_CAL = 341, FL_P2 = 344;
constexpr int LDS_MAIN = 114688 + 2048;
constexpr int LDS_PRO = 143360;
constexpr int SPIN_CAP = 1 << 19;

struct Params {
    const unsigned short* xa; const unsigned short* wr; const unsigned short* wi;
    const unsigned short* xb; const unsigned short* mem0;
    const unsigned short* wq; const unsigned short* wk; const unsigned short* wv; const unsigned short* wo;
    unsigned short* out; char* ws;
};

DEV unsigned pk2(float a, float b) { v2bf r = {(__bf16)a, (__bf16)b}; return __builtin_bit_cast(unsigned, r); }
DEV v4u pk8(v4f a, v4f b) { return (v4u){pk2(a.x, a.y), pk2(a.z, a.w), pk2(b.x, b.y), pk2(b.z, b.w)}; }
DEV v4f mfma32(v4u a, v4u b, v4f c) { return __builtin_amdgcn_mfma_f32_16x16x32_bf16(__builtin_bit_cast(v8bf, a), __builtin_bit_cast(v8bf, b), c, 0, 0, 0); }
DEV v4f mfma16(v2u a, v2u b, v4f c) { return __builtin_amdgcn_mfma_f32_16x16x16bf16_1k(__builtin_bit_cast(v4s, a), __builtin_bit_cast(v4s, b), c, 0, 0, 0); }
DEV v4f smf(v4u a, v4u b0, v4u b1, v4f c, int idx) {
    v8u b = {b0.x, b0.y, b0.z, b0.w, b1.x, b1.y, b1.z, b1.w};
    return __builtin_amdgcn_smfmac_f32_16x16x64_bf16(__builtin_bit_cast(v8bf, a), __builtin_bit_cast(v16bf, b), c, idx, 0, 0);
}
DEV float fmx(float a, float b) { return __builtin_elementwise_maximum(a, b); }
DEV float max_g(float s) {
    auto r = __builtin_amdgcn_permlane16_swap(__float_as_uint(s), __float_as_uint(s), false, false);
    s = fmx(__uint_as_float(r[0]), __uint_as_float(r[1]));
    auto q = __builtin_amdgcn_permlane32_swap(__float_as_uint(s), __float_as_uint(s), false, false);
    return fmx(__uint_as_float(q[0]), __uint_as_float(q[1]));
}
DEV float sum_g(float s) {
    auto r = __builtin_amdgcn_permlane16_swap(__float_as_uint(s), __float_as_uint(s), false, false);
    s = __uint_as_float(r[0]) + __uint_as_float(r[1]);
    auto q = __builtin_amdgcn_permlane32_swap(__float_as_uint(s), __float_as_uint(s), false, false);
    return __uint_as_float(q[0]) + __uint_as_float(q[1]);
}
DEV float ror8(float s) { return __int_as_float(__builtin_amdgcn_mov_dpp(__float_as_int(s), 0x128, 0xf, 0xf, true)); }
template <int CTRL> DEV unsigned dppu(unsigned x) { return (unsigned)__builtin_amdgcn_mov_dpp((int)x, CTRL, 0xf, 0xf, true); }
DEV v4u quad4(unsigned x) { return (v4u){x, dppu<0x55>(x), dppu<0xAA>(x), dppu<0xFF>(x)}; }
DEV unsigned umax4(v4u v) { return max(max(v.x, v.y), max(v.z, v.w)); }
DEV rsrc_t mkrsrc(const void* p, unsigned bytes) { return __builtin_amdgcn_make_buffer_rsrc((void*)p, 0, bytes, 0x00020000); }
DEV v4u ld16(rsrc_t r, int vo, int so, int aux) { return aux ? __builtin_amdgcn_raw_buffer_load_b128(r, vo, so, SC1) : __builtin_amdgcn_raw_buffer_load_b128(r, vo, so, 0); }
DEV v2u ld8s(rsrc_t r, int vo, int so) { return __builtin_amdgcn_raw_buffer_load_b64(r, vo, so, SC1); }
DEV unsigned ld4s(rsrc_t r, int vo, int so) { return __builtin_amdgcn_raw_buffer_load_b32(r, vo, so, SC1); }
DEV void st16s(rsrc_t r, int vo, int so, v4u v) {
    __builtin_amdgcn_raw_buffer_store_b128(v, r, vo, so, SC1); asm volatile("s_nop 1" :: "v"(v) : "memory");
}
DEV void st8s(rsrc_t r, int vo, int so, v2u v) { __builtin_amdgcn_raw_buffer_store_b64(v, r, vo, so, SC1); }
DEV void st4s(rsrc_t r, int vo, int so, unsigned v) { __builtin_amdgcn_raw_buffer_store_b32(v, r, vo, so, SC1); }
DEV void dma16(rsrc_t r, int vo, int so, int ldsaddr) {
    unsigned keep;
    asm volatile("s_mov_b32 %0, m0\n\ts_mov_b32 m0, %4\n\ts_nop 4\n\tbuffer_load_dwordx4 %1, %2, %3 offen lds\n\ts_mov_b32 m0, %0"
                 : "=&s"(keep) : "v"(vo), "s"(r), "s"(__builtin_amdgcn_readfirstlane(so)), "s"(__builtin_amdgcn_readfirstlane(ldsaddr)) : "memory");
}
DEV void dma16s(rsrc_t r, int vo, int so, int ldsaddr) {
    unsigned keep;
    asm volatile("s_mov_b32 %0, m0\n\ts_mov_b32 m0, %4\n\ts_nop 4\n\tbuffer_load_dwordx4 %1, %2, %3 offen sc1 lds\n\ts_mov_b32 m0, %0"
                 : "=&s"(keep) : "v"(vo), "s"(r), "s"(__builtin_amdgcn_readfirstlane(so)), "s"(__builtin_amdgcn_readfirstlane(ldsaddr)) : "memory");
}
#define BAR() asm volatile("s_waitcnt lgkmcnt(0)\n\ts_barrier" ::: "memory")
#define BAR0() asm volatile("s_barrier" ::: "memory")
#define VMWAIT(n) asm volatile("s_waitcnt vmcnt(" #n ")" ::: "memory")
#define FENCE() asm volatile("" ::: "memory")
#define SCHED() __builtin_amdgcn_sched_barrier(0)
DEV v4u lds16(const char* l, int a) { return *(const v4u*)(l + a); }
DEV v4u lds16a(int a) { return *(const LAS v4u*)(uintptr_t)(unsigned)a; }
DEV v2u lds8(const char* l, int a) { return *(const v2u*)(l + a); }
DEV v2u ldstr(const char* l, int a) { v4s r = __builtin_amdgcn_ds_read_tr16_b64_v4i16((LAS v4s*)(l + a)); return __builtin_bit_cast(v2u, r); }
template <int N> DEV void slp() { if constexpr (N > 0) __builtin_amdgcn_s_sleep(N); }
DEV int flagoff(int i) { return OFF_CTL + i * 128; }
template <int SL> DEV bool wait_flags(rsrc_t rs, int vo, unsigned need, unsigned& seen) {
    for (int sp = 0;; ++sp) {
        FENCE();
        seen = ld4s(rs, vo, 0);
        if (__builtin_amdgcn_ballot_w64(seen < need) == 0) return true;
        slp<SL>();
        if ((sp & 255) == 255) {
            if (sp > SPIN_CAP) st4s(rs, flagoff(FL_ABORT), 0, 1u);
            if (sp > SPIN_CAP || __builtin_amdgcn_readfirstlane(ld4s(rs, flagoff(FL_ABORT), 0))) return false;
        }
    }
}
DEV int img(int p) { return ((p & 7) >> 1) * 4 + (p >> 3) * 2 + (p & 1); }
DEV int pperm(int rho, int tau) { return (rho >> 2) * 8 + tau * 4 + (rho & 3); }

template <int which> DEV void pro_tile(const Params& p, char* lds, rsrc_t rs, int b, int h, int b0, int c0, int lane, int wave) {
    const int rho = lane & 15, g = lane >> 4;
    const rsrc_t ra = mkrsrc(which ? p.wv : p.wk, DM * 4096 * 2), rb = mkrsrc(which ? p.wo : p.wq, DM * 4096 * 2);
    int sa[4], sb[2];
#pragma unroll
    for (int k = 0; k < 4; ++k) { const int r = (k & 1) * 8 + (lane >> 3); sa[k] = ((b0 + 32 * wave + pperm(r, k >> 1)) * 4096 + h * 256 + ((lane & 7) ^ (r >> 1)) * 8) * 2; }
#pragma unroll
    for (int k = 0; k < 2; ++k) { const int r = k * 8 + (lane >> 3), ch = (2 * wave + k) * 64 + lane, e = ch / 14;
        sb[k] = which ? ((h * 256 + e) * DM + c0 + (ch - e * 14) * 8) * 2 : ((c0 + wave * 16 + r) * 4096 + h * 256 + ((lane & 7) ^ (r >> 1)) * 8) * 2; }
    const int bstep = which ? 64 * DM * 2 : 128, la = 57344 + wave * 4096;
    v4u a3[4];
#pragma unroll
    for (int c = 0; c < 4; ++c) {
#pragma unroll
        for (int k = 0; k < 2; ++k) dma16(rb, sb[k], c * bstep, c * 14336 + (2 * wave + k) * 1024);
#pragma unroll
        for (int k = 0; k < 4; ++k) { if (c < 3) dma16(ra, sa[k], c * 128, la + c * 28672 + k * 1024); else a3[k] = ld16(ra, sa[k], 384, 0); }
    }
    const int rd = (rho >> 3) * 1024 + (rho & 7) * 128, rx0 = (g ^ (rho >> 1)) * 16, rx1 = ((4 + g) ^ (rho >> 1)) * 16;
    const int tra = (8 * g + (rho >> 2)) * 224 + (rho & 3) * 8;
    v4f acc[7][2];
#pragma unroll
    for (int ct = 0; ct < 7; ++ct) { acc[ct][0] = (v4f){0, 0, 0, 0}; acc[ct][1] = acc[ct][0]; }
    const float sc = which ? 1.f : 0.09016844005556021f;
    const int obase = (which ? OFF_BT : OFF_AP) + ((h * 28 + (c0 >> 4)) * 14 + (b0 >> 5) + wave) * 1024 + lane * 16;
    v4u af[2][4], f[2][7][2];
#pragma unroll
    for (int c = 0; c <= 4; ++c) {
        if (c < 4) {
            if (c == 0) VMWAIT(18); else if (c == 1) VMWAIT(12); else if (c == 2) VMWAIT(6); else VMWAIT(0);
            if (c == 3) {
#pragma unroll
                for (int k = 0; k < 4; ++k) *(v4u*)(lds + la + k * 1024 + lane * 16) = a3[k];
            }
            BAR();
        }
        const char* A = lds + la + (c % 3) * 28672; const char* B = lds + c * 14336;
        if (c < 4) { af[c & 1][0] = lds16(A, rd + rx0); af[c & 1][1] = lds16(A, rd + rx1); af[c & 1][2] = lds16(A, 2048 + rd + rx0); af[c & 1][3] = lds16(A, 2048 + rd + rx1); }
#pragma unroll
        for (int ct = 0; ct < 7; ++ct) {
            if (c < 4) {
                if (which == 0) { f[c & 1][ct][0] = lds16(B, ct * 2048 + rd + rx0); f[c & 1][ct][1] = lds16(B, ct * 2048 + rd + rx1); }
                else {
#pragma unroll
                    for (int kk = 0; kk < 2; ++kk) { const v2u x = ldstr(B, tra + kk * 7168 + ct * 32), y = ldstr(B, tra + kk * 7168 + 896 + ct * 32); f[c & 1][ct][kk] = (v4u){x.x, x.y, y.x, y.y}; }
                }
            }
            SCHED();
            if (c > 0) { const int q = (c - 1) & 1;
                acc[ct][0] = mfma32(af[q][1], f[q][ct][1], mfma32(af[q][0], f[q][ct][0], acc[ct][0])); acc[ct][1] = mfma32(af[q][3], f[q][ct][1], mfma32(af[q][2], f[q][ct][0], acc[ct][1])); }
            if (c == 4) st16s(rs, obase, ct * 14336, pk8(acc[ct][0] * sc, acc[ct][1] * sc));
            SCHED();
        }
    }
}
extern "C" __global__ void __launch_bounds__(512, 2) k_pro(Params p) {
    extern __shared__ __attribute__((aligned(16))) char lds[];
    const int b = blockIdx.x, tid = threadIdx.x, lane = tid & 63, wave = __builtin_amdgcn_readfirstlane(tid >> 6);
    const int rho = lane & 15, g = lane >> 4;
    const int gemm = (b & 7) * 4 + (b >> 6), sub = (b >> 3) & 7, which = gemm & 1, h = gemm >> 1;
    const int b0 = 224 * (sub & 1), c0 = 112 * (sub >> 1);
    const rsrc_t rs = mkrsrc(p.ws, WS_BYTES);
    if (wave == 7) {
        const rsrc_t rx = mkrsrc(p.xb, 2048 * DM * 2);
        v4u xf[7], m0[2];
#pragma unroll
        for (int k = 0; k < 7; ++k) { const int pc = b * 7 + k, rt = pc / 14, ks = pc - rt * 14; xf[k] = ld16(rx, ((rt * 16 + (rho ^ ((rt & 1) * 4))) * DM + ks * 32 + g * 8) * 2, 0, 0); }
        const v4u sent = {SENT, SENT, SENT, SENT};
#pragma unroll
        for (int k = 0; k < 2; ++k) {
            const int o = (b * 2 + k) * 1024 - OFF_MEM0, c = ((o & 8191) >> 4) + lane;
            m0[k] = sent;
            if (o >= 0 && o < 32768 && c < 448) { const unsigned* m = (const unsigned*)(p.mem0 + (((c >> 2) & 3) * 2 + (c & 1)) * DM + (c >> 4) * 16 + (c & 2)); m0[k] = (v4u){m[0], m[2], m[4], m[6]}; }
        }
        SCHED();
        BAR0(); BAR0(); BAR0(); BAR0();
        const unsigned rot = ((unsigned)__builtin_amdgcn_s_getreg(20 | (3 << 11)) - b) & 7u;
        if (lane < 32) st16s(rs, OFF_CTL + b * 512 + lane * 16, 0, (v4u){b * 4 == FL_ROT && lane == 0 ? rot : 0u, 0u, 0u, 0u});
#pragma unroll
        for (int k = 0; k < 2; ++k) st16s(rs, OFF_HO + (b * 2 + k) * 1024 + lane * 16, 0, m0[k]);
#pragma unroll
        for (int k = 62; k < 64; ++k)
            st16s(rs, (b < 112 ? OFF_T + k * NH * 7168 + b * 1024 : b < 144 ? OFF_PX + k * NH * 2048 + (b - 112) * 1024 : b < 152 ? OFF_PM + k * NH * 512 + (b - 144) * 1024 : b < 160 ? OFF_PM2 + (k - 62) * NH * 512 + (b - 152) * 1024 : OOB - 1024) + lane * 16, 0, sent);
#pragma unroll
        for (int k = 0; k < 4; ++k) st16s(rs, b < 224 ? OFF_G0 + (b * 4 + k) * 1024 + lane * 16 : OOB, 0, sent);
#pragma unroll
        for (int k = 0; k < 7; ++k) st16s(rs, OFF_XF + (b * 7 + k) * 1024 + lane * 16, 0, xf[k]);
        return;
    }
    if (which) pro_tile<1>(p, lds, rs, b, h, b0, c0, lane, wave); else pro_tile<0>(p, lds, rs, b, h, b0, c0, lane, wave);
}

struct Ctx { rsrc_t rs; char* lds; int lane, wave, rho, g; };

DEV int slot_base(int s, int cal) { return OFF_HO + (s & 3) * HO_SLOT + cal * 8192; }
DEV int mem_base(int s, int cal) { return s ? slot_base(s, cal) + HO_MEM : OFF_MEM0 + cal * 8192; }
#define STAGE_HEAD "s_add_i32 %[bud], %[bud], -1\n\ts_cmp_lt_i32 %[bud], 0\n\ts_cselect_b32 vcc_hi, -1, -2\n\tv_mov_b32 v233, vcc_hi\n\t"
#define STAGE_TAIL "v_cmp_gt_u32 vcc, v232, v233\n\ts_cbranch_vccnz 1b\n\ts_waitcnt lgkmcnt(0)\n\ts_barrier"
#define STAGE_CLOB "memory", "vcc", "scc", "v224", "v225", "v226", "v227", "v228", "v229", "v230", "v231", "v232", "v233"
DEV void stage1(rsrc_t rs, int vo, int so, int& budget) {
    asm volatile("1:\n\tbuffer_load_dwordx4 v[224:227], %[vo], %[rs], %[so] offen sc1\n\t" STAGE_HEAD
                 "s_waitcnt vmcnt(0)\n\tds_write_b128 %[vo], v[224:227]\n\tv_max3_u32 v232, v224, v225, v226\n\tv_max_u32 v232, v232, v227\n\t" STAGE_TAIL
                 : [bud] "+s"(budget) : [vo] "v"(vo), [rs] "s"(rs), [so] "s"(so) : STAGE_CLOB);
}
DEV void stage2(rsrc_t rs, int vo, int vo2, int so, int& budget) {
    asm volatile("1:\n\tbuffer_load_dwordx4 v[224:227], %[vo], %[rs], %[so] offen sc1\n\tbuffer_load_dwordx4 v[228:231], %[vo2], %[rs], %[so] offen sc1\n\t" STAGE_HEAD
                 "s_waitcnt vmcnt(1)\n\tds_write_b128 %[vo], v[224:227]\n\tv_max3_u32 v232, v224, v225, v226\n\t"
                 "s_waitcnt vmcnt(0)\n\tds_write_b128 %[vo2], v[228:231]\n\tv_max3_u32 v232, v232, v227, v228\n\tv_max3_u32 v232, v232, v229, v230\n\tv_max_u32 v232, v232, v231\n\t" STAGE_TAIL
                 : [bud] "+s"(budget) : [vo] "v"(vo), [vo2] "v"(vo2), [rs] "s"(rs), [so] "s"(so) : STAGE_CLOB);
}
DEV void stage2o(rsrc_t rs, int vo, int vo2, int so, int& budget, v4u& a, v4u& b) {
    asm volatile("1:\n\tbuffer_load_dwordx4 v[224:227], %[vo], %[rs], %[so] offen sc1\n\tbuffer_load_dwordx4 v[228:231], %[vo2], %[rs], %[so] offen sc1\n\t" STAGE_HEAD
                 "s_waitcnt vmcnt(1)\n\tds_write_b128 %[vo], v[224:227]\n\tv_max3_u32 v232, v224, v225, v226\n\t"
                 "s_waitcnt vmcnt(0)\n\tds_write_b128 %[vo2], v[228:231]\n\tv_max3_u32 v232, v232, v227, v228\n\tv_max3_u32 v232, v232, v229, v230\n\tv_max_u32 v232, v232, v231\n\t" STAGE_TAIL
                 : [bud] "+s"(budget), "=&{v[224:227]}"(a), "=&{v[228:231]}"(b) : [vo] "v"(vo), [vo2] "v"(vo2), [rs] "s"(rs), [so] "s"(so) : "memory", "vcc", "scc", "v232", "v233");
}
DEV void give_up(rsrc_t rs) { st4s(rs, flagoff(FL_ABORT), 0, 1u); }
#define PIN1(a) asm volatile("" : "+v"(a))
#define PIN2(a, b) asm volatile("" : "+v"(a), "+v"(b))
constexpr int CAL_STEP = 10;
DEV void role_cal(const Params& p, const Ctx& c, int pair) {
    if (c.wave) return;
    unsigned best[4] = {0xffffu, 0xffffu, 0xffffu, 0xffffu}, half = 0;
    if (pair) { if (!wait_flags<2>(c.rs, flagoff(FL_CAL), 1u, half)) return; half = __builtin_amdgcn_readfirstlane(half) & 1; }
#pragma unroll 1
    for (int r = 0; r < 12; ++r) {
#pragma unroll
        for (int k = 0; k < 4; ++k) {
            const int off = OFF_PROBE + (pair ? half + (k & 2) : k >> 1) * 8192 + (k & 1) * 4096 + (pair * 16 + r) * 128;
            unsigned long long t0, t1; unsigned v;
            asm volatile("s_memtime %0\n\ts_waitcnt lgkmcnt(0)\n\tbuffer_load_dword %2, %3, %4, 0 offen sc1\n\ts_waitcnt vmcnt(0)\n\ts_memtime %1\n\ts_waitcnt lgkmcnt(0)"
                         : "=&s"(t0), "=&s"(t1), "=&v"(v) : "v"(off), "s"(c.rs) : "memory");
            best[k] = min(best[k], (unsigned)(t1 - t0));
        }
        if (r == 2 && !pair) { if (c.lane == 0) st4s(c.rs, flagoff(FL_CAL), 0, min(best[2], best[3]) < min(best[0], best[1]) ? 3u : 2u); VMWAIT(0); }
    }
    if (c.lane == 0) st4s(c.rs, flagoff(FL_CAL + 1 + pair), 0, best[2] + best[3] < best[0] + best[1] ? 3u : 2u);
}
DEV int get_cal(const Ctx& c, int late, int& budget) {
    unsigned seen;
    if (!wait_flags<2>(c.rs, flagoff(FL_CAL + late + (late & c.lane)), 1u, seen)) budget = -1;
    return (__builtin_amdgcn_readlane(seen, 0) & 1) | (late ? (__builtin_amdgcn_readlane(seen, 1) & 1) << 1 : 0);
}
DEV unsigned now() { return (unsigned)__builtin_amdgcn_s_memtime(); }
DEV void wait_until(unsigned t) {
    int rem = (int)(t - now());
    while (rem > 260) { __builtin_amdgcn_s_sleep(1); rem = (int)(t - now()); }
    rem -= 60;
    asm volatile("1:\n\ts_add_i32 %0, %0, -40\n\ts_cmp_gt_i32 %0, 0\n\ts_cbranch_scc1 1b" : "+s"(rem) :: "scc");
}
#define T_PM 1920
#define T_OS 2065
#define T_OM 300
#define T_OU 650
#define T_G0 400
#define T_F1 2400
#define T_F2 500
#define T_PG 200
#define W_LAG_FROM 8
#define FSL 8
#define S_AT 6

#define MEM_TIMES_W(lbase)                                                                                           \
    v4u a_[7];                                                                                                       \
    _Pragma("unroll") for (int f = 0; f < 7; ++f) a_[f] = lds16a((lbase) + f * 1024);                                \
    EXTRA_READS;                                                                                                     \
    SCHED();                                                                                                         \
    _Pragma("unroll") for (int f = 0; f < 7; ++f) { c0 = smf(a_[f], w[0][2 * f], w[0][2 * f + 1], c0, sidx); c1 = smf(a_[f], w[1][2 * f], w[1][2 * f + 1], c1, sidx); AFTER_STEP(f); } \
    const v2u tp = {pk2(c0.x + c0.z, c0.y + c0.w), pk2(c1.x + c1.z, c1.y + c1.w)};

#define MEM_TIMES_W_OWN()                                                                                            \
    v4u a_[5];                                                                                                       \
    _Pragma("unroll") for (int k = 0; k < 5; ++k) a_[k] = lds16a(fr[k]);                                             \
    EXTRA_READS;                                                                                                     \
    SCHED();                                                                                                         \
    c0 = smf(a0, w[0][0], w[0][1], c0, sidx); c1 = smf(a0, w[1][0], w[1][1], c1, sidx);                              \
    c0 = smf(a1, w[0][2], w[0][3], c0, sidx); c1 = smf(a1, w[1][2], w[1][3], c1, sidx);                              \
    SCHED();                                                                                                         \
    _Pragma("unroll") for (int k = 0; k < 5; ++k) { c0 = smf(a_[k], w[0][2 * k + 4], w[0][2 * k + 5], c0, sidx); c1 = smf(a_[k], w[1][2 * k + 4], w[1][2 * k + 5], c1, sidx); } \
    const v2u tp = {pk2(c0.x + c0.z, c0.y + c0.w), pk2(c1.x + c1.z, c1.y + c1.w)};

#define VCLOB "v224", "v225", "v226", "v227", "v228", "v229", "v230", "v231", VCLOB_HI
#define VCLOB_HI "v232", "v233", "v234", "v235", "v236", "v237", "v238", "v239", \
              "v240", "v241", "v242", "v243", "v244", "v245", "v246", "v247", "v248", "v249", "v250", "v251", "v252", "v253", "v254", "v255"
#define PUBLISH_ASM(ST)                                                                                                                     \
    "s_cmp_lg_u32 %[pub], 7\n\ts_waitcnt lgkmcnt(0)\n\ts_barrier\n\ts_cbranch_scc1 9f\n\t"                                                  \
    "ds_read_b64 v[224:225], %[la]\n\tds_read_b64 v[226:227], %[la] offset:" #ST "*1\n\tds_read_b64 v[228:229], %[la] offset:" #ST "*2\n\t" \
    "ds_read_b64 v[230:231], %[la] offset:" #ST "*3\n\tds_read_b64 v[232:233], %[la] offset:" #ST "*4\n\t"                                  \
    "ds_read_b64 v[234:235], %[la] offset:" #ST "*5\n\tds_read_b64 v[236:237], %[la] offset:" #ST "*6\n\t"                                  \
    "ds_read_b64 v[238:239], %[la] offset:" #ST "*7\n\t"                                                                                    \
    "s_waitcnt lgkmcnt(6)\n\tv_pk_add_f32 v[224:225], v[224:225], v[226:227]\n\t"                                                           \
    "s_waitcnt lgkmcnt(4)\n\tv_pk_add_f32 v[228:229], v[228:229], v[230:231]\n\t"                                                           \
    "s_waitcnt lgkmcnt(2)\n\tv_pk_add_f32 v[232:233], v[232:233], v[234:235]\n\tv_pk_add_f32 v[224:225], v[224:225], v[228:229]\n\t"        \
    "s_waitcnt lgkmcnt(0)\n\tv_pk_add_f32 v[236:237], v[236:237], v[238:239]\n\tv_pk_add_f32 v[224:225], v[224:225], v[232:233]\n\t"        \
    "s_nop 0\n\tv_pk_add_f32 v[224:225], v[224:225], v[236:237]\n\t"                                                                        \
    "s_nop 0\n\tv_cvt_pk_bf16_f32 v224, v224, v225\n\ts_nop 1\n\t"                                                                          \
    "v_mov_b32_dpp v225, v224 quad_perm:[1,1,1,1] row_mask:0xf bank_mask:0xf\n\t"                                                           \
    "v_mov_b32_dpp v226, v224 quad_perm:[2,2,2,2] row_mask:0xf bank_mask:0xf\n\t"                                                           \
    "v_mov_b32_dpp v227, v224 quad_perm:[3,3,3,3] row_mask:0xf bank_mask:0xf\n\t"                                                           \
    "buffer_store_dwordx4 v[224:227], %[vo], %[rs], %[so] offen sc1\n\t"                                                                    \
    "9:\n\ts_barrier\n\ts_nop 0"

DEV void role_p(const Params& p, const Ctx& c, int idx) {
    const int h = idx >> 2, q = idx & 3, lane = c.lane, wave = c.wave, rho = c.rho, g = c.g;
    const rsrc_t rs = c.rs; char* lds = c.lds;
    const int om = q * 4 + wave;
    const bool isT = wave < 4 && om < 14, isX = wave < 4 && om >= 14;
    constexpr int L_MEM = 0, L_PART = 8192, L_DUMP = 10240, L_CAL = 11264, L_X = 16384;
    const int sidx = (rho & 2) ? 0xEEEE : 0x4444;
    v2u sel; { const int r0 = rho - 2 * g; sel.x = (r0 == 0 ? 0x3f80u : 0u) | (r0 == 1 ? 0x3f800000u : 0u); sel.y = (r0 == 8 ? 0x3f80u : 0u) | (r0 == 9 ? 0x3f800000u : 0u); }
    const int f0 = isX ? 4 : 2 * (wave & 3);
    auto blk = [&](int k) { return f0 + k >= 7 ? f0 + k - 7 : f0 + k; };
    const int po = f0 * 1024 + lane * 16, po2 = blk(1) * 1024 + lane * 16;
    int fr[5];
#pragma unroll
    for (int k = 0; k < 5; ++k) fr[k] = L_MEM + blk(k + 2) * 1024 + lane * 16;
    int budget = SPIN_CAP;
#pragma unroll
    for (int k = 0; k < 7; ++k) dma16(rs, OFF_XF + lane * 16, (wave * 7 + k) * 1024, L_X + (wave * 7 + k) * 1024);
    if (isT) {
        v4u w[2][14];
#pragma unroll
        for (int t = 0; t < 2; ++t)
#pragma unroll
            for (int ks = 0; ks < 14; ++ks) w[t][ks] = ld16(rs, OFF_AP + (h * 28 + 2 * om + t) * 14336 + g * 256 + img(rho) * 16, (2 * blk(ks >> 1) + (ks & 1)) * 1024, 0);
        VMWAIT(28);
        BAR0();
#pragma unroll 1
        for (int sg = 0; sg < 2; ++sg) {
            v4f a[2][2] = {{{0, 0, 0, 0}, {0, 0, 0, 0}}, {{0, 0, 0, 0}, {0, 0, 0, 0}}};
#pragma unroll
            for (int ks = 0; ks < 14; ++ks)
#pragma unroll
                for (int kt = 0; kt < 2; ++kt) { const v4u x = lds16(lds, L_X + sg * 28672 + kt * 14336 + (2 * blk(ks >> 1) + (ks & 1)) * 1024 + g * 256 + (rho ^ (kt * 4)) * 16);
                    a[kt][0] = mfma32(w[0][ks], x, a[kt][0]); a[kt][1] = mfma32(w[1][ks], x, a[kt][1]); }
#pragma unroll
            for (int kt = 0; kt < 2; ++kt) {
                auto x = __builtin_amdgcn_permlane32_swap(pk2(a[kt][0].x, a[kt][0].y), pk2(a[kt][1].x, a[kt][1].y), false, false);
                auto y = __builtin_amdgcn_permlane32_swap(pk2(a[kt][0].z, a[kt][0].w), pk2(a[kt][1].z, a[kt][1].w), false, false);
                st16s(rs, OFF_G0 + (h * 14 + om) * 2048 + kt * 1024 + lane * 16, sg * (NH * 28672), (v4u){x[0], x[1], y[0], y[1]});
            }
            BAR0();
        }
        int cal = __builtin_amdgcn_readfirstlane(*(const int*)(lds + L_CAL));
        const int qa = L_MEM + (2 * om + (rho >> 3)) * 256 + (((rho & 7) >> 1) * 4 + (g >> 1) * 2 + (rho & 1)) * 16 + (g & 1) * 8;
        const int pw = (g >> 1) == (rho >> 3) ? L_PART + (2 * wave + (rho >> 3)) * 256 + (rho & 7) * 32 + (g & 1) * 16 : L_DUMP + lane * 16;
        const int tw = lane < 32 ? OFF_T + (h * 14 + om) * 512 + ((rho >> 3) * 2 + g) * 128 + (rho & 7) * 16 : OOB;
#pragma unroll 1
        for (int s = 0; s < NSEG && budget >= 0; ++s) {
            if (s == CAL_STEP) cal = __builtin_amdgcn_readfirstlane(*(const int*)(lds + L_CAL));
            v4f c0 = {0, 0, 0, 0}, c1 = {0, 0, 0, 0}; PIN2(c0, c1);
            BAR0();
            v4u a0, a1; v2u mq;
            stage2o(rs, po, po2, mem_base(s, cal), budget, a0, a1);
#define EXTRA_READS mq = lds8(lds, qa)
            MEM_TIMES_W_OWN()
#undef EXTRA_READS
            const v4f d2 = mfma16(tp, sel, (v4f){0, 0, 0, 0});
            const v2u d = {pk2(d2.x, d2.y), pk2(d2.z, d2.w)};
            *(v4f*)(lds + pw) = mfma16(d, mq, (v4f){0, 0, 0, 0});
            BAR();
            BAR0();
            if (s < NSEG - 1) wait_until(now() + T_PG);
            { auto x = __builtin_amdgcn_permlane32_swap(d.x, d.y, false, false);
              auto y = __builtin_amdgcn_permlane32_swap(x[0], x[0], false, false); auto z = __builtin_amdgcn_permlane32_swap(x[1], x[1], false, false);
              st16s(rs, tw, s * (NH * 7168), (v4u){y[0], z[0], y[1], z[1]}); }
        }
    } else if (isX) {
        v4u w[2][14];
        int cal = get_cal(c, 0, budget);
        BAR0(); BAR0();
        if (om == 14) wait_until(now() + T_G0);
        else BAR0();
        const int gl0 = OFF_G + h * 28672 + (rho >> 3) * 1024 + g * 256 + (pperm(rho, 0) & 15) * 16;
        const int gfl = flagoff(FL_W + h * 7 + (lane & 3));
        const int ew = rho < 8 ? HO_E + h * 512 + g * 128 + rho * 16 : OOB, mw = g == 0 ? L_PART + 1024 + (rho & 7) * 32 + (rho >> 3) * 16 : L_DUMP + lane * 16;
        auto load_g = [&](int s, int t) {
            FENCE();
#pragma unroll
            for (int ks = 0; ks < 14; ++ks) w[t][ks] = ld16(rs, gl0 + t * 64, s * (NH * 28672) + (s < 2 ? OFF_G0 - OFF_G : 0) + (ks < 6 ? ks + 8 : ks - 6) * 2048, 1);
            FENCE();
        };
        bool lack = false;
#pragma unroll 1
        for (int s = om - 14; s < NSEG; s += 2) {
            const int sl = min(s, NSEG - 2);
            if (s == 1) BAR0();
            if (s >= 1) wait_until(now() + T_PG);
            unsigned seen;
            if (lack && !wait_flags<1>(rs, gfl, sl, seen)) budget = -1;
            load_g(sl, 0);
            if (s >= 2) { BAR0(); BAR0(); BAR0(); BAR0(); wait_until(now() + T_PG); }
            load_g(sl, 1);
            seen = ld4s(rs, gfl, 0);
            if (s == 1) { BAR0(); BAR0(); BAR0(); }
            if (s < 2) {
#pragma unroll 1
                for (;;) {
                    unsigned m = 0;
#pragma unroll
                    for (int ks = 0; ks < 14; ++ks) m = max(m, max(umax4(w[0][ks]), umax4(w[1][ks])));
                    if (__builtin_amdgcn_ballot_w64(m == SENT) == 0 || --budget < 0) break;
                    load_g(s, 0); load_g(s, 1);
                }
                if (s == 0) BAR0();
            }
            if ((s | 1) == CAL_STEP + 1) cal = __builtin_amdgcn_readfirstlane(*(const int*)(lds + L_CAL));
            const int sb = slot_base(s, cal);
            v4f c0 = {0, 0, 0, 0}, c1 = {0, 0, 0, 0}; PIN2(c0, c1);
            lack = __builtin_amdgcn_ballot_w64(seen < (unsigned)min(s + 2, NSEG - 2)) != 0;
            SCHED();
            BAR0();
            v4u a0, a1;
            stage2o(rs, po, po2, mem_base(s, cal), budget, a0, a1);
            if (s < NSEG - 1) {
#define EXTRA_READS
                MEM_TIMES_W_OWN()
#undef EXTRA_READS
                const v4f d2 = mfma16(tp, sel, (v4f){0, 0, 0, 0});
                float m = fmx(fmx(d2.x, d2.y), fmx(d2.z, d2.w));
                m = max_g(fmx(m, ror8(m)));
                *(v4f*)(lds + mw) = (v4f){-m, -m, -m, -m};
                SCHED();
                BAR();
                PIN1(m);
                const float e0 = __builtin_amdgcn_exp2f(d2.x - m), e1 = __builtin_amdgcn_exp2f(d2.y - m), e2 = __builtin_amdgcn_exp2f(d2.z - m), e3 = __builtin_amdgcn_exp2f(d2.w - m);
                const unsigned u0 = pk2(e0, e1), u1 = pk2(e2, e3);
                st16s(rs, ew, sb, (v4u){u0, u1, dppu<0x128>(u0), dppu<0x128>(u1)});
            } else BAR0();
            BAR0();
        }
        if (om == 14) { BAR0(); BAR0(); BAR0(); BAR0(); }
    } else {
        if (wave == 4) { *(v4u*)(lds + L_PART + lane * 16) = (v4u){0u, 0u, 0u, 0u}; *(v4u*)(lds + L_PART + 1024 + lane * 16) = (v4u){0u, 0u, 0u, 0u}; }
        BAR();
        int cal = get_cal(c, 0, budget);
        if (wave == 6) *(int*)(lds + L_CAL) = cal;
        BAR(); BAR0();
        unsigned tref = now() - 65536;
        const bool stg = q == 3 && wave == 4;
        const int rw = lane < 8 ? HO_PART + idx * 128 + lane * 16 : q == 3 && lane >= 32 ? HO_E + h * 512 + (lane - 32) * 16 : OOB;
        int calp = cal, calq = cal;
        const int pr = L_PART + (lane & 31) * 8, pubo = lane < 32 && (lane & 3) == 0 ? HO_PART + idx * 128 + lane * 4 : OOB;
#pragma unroll 1
        for (int s = 0; s < NSEG && budget >= 0; ++s) {
            if (s == CAL_STEP - 1 && wave == 6) *(int*)(lds + L_CAL) = get_cal(c, 1, budget);
            if (s == CAL_STEP) cal = __builtin_amdgcn_readfirstlane(*(const int*)(lds + L_CAL));
            const int sb = slot_base(s, cal);
            if (wave == 5 && s >= 2) {
                wait_until(tref + T_PG);
                st16s(rs, rw, slot_base(s - 2, calp), (v4u){SENT, SENT, SENT, SENT});
                VMWAIT(0);
            }
            calp = calq; calq = cal;
            if (wave == 7) wait_until(tref + T_PM);
            BAR0();
            if (stg) {
                stage1(rs, 6144 + lane * 16, mem_base(s, cal), budget);
            } else BAR0();
            asm volatile(PUBLISH_ASM(256) :: [pub] "s"(wave), [la] "v"(pr), [vo] "v"(pubo), [rs] "s"(rs), [so] "s"(sb) : "memory", "scc", VCLOB);
            tref = now();
            if (wave == 7) {
                if (lane == 0) st4s(rs, flagoff(FL_T + idx), 0, s);
            }
        }
    }
    if (budget < 0) give_up(rs);
    VMWAIT(0);
    BAR();
    if (wave == 7 && lane == 0) st4s(rs, flagoff(FL_T + idx), 0, NSEG);
}

DEV void role_o(const Params& p, const Ctx& c, int t) {
    const int lane = c.lane, wave = c.wave, rho = c.rho, g = c.g;
    const rsrc_t rs = c.rs; char* lds = c.lds;
    constexpr int L_MEM = 0, L_OUT = 8192, L_DUMP = 12288, L_V = 13312, L_CAL = 21504, L_X = 24576;
#pragma unroll
    for (int k = 0; k < 4; ++k) dma16(rs, wave * 4 + k < 28 ? OFF_XF + lane * 16 : OOB, (wave * 4 + k) * 1024, L_X + (wave * 4 + k) * 1024);
    v4u w[2][14];
#pragma unroll
    for (int k = 0; k < 2; ++k)
#pragma unroll
        for (int ks = 0; ks < 14; ++ks) w[k][ks] = ld16(rs, OFF_BT + ((2 * wave + k) * 28 + t) * 14336 + lane * 16, ks * 1024, 0);
    VMWAIT(28);
    BAR0();
    {
        v4u u[2];
#pragma unroll
        for (int k = 0; k < 2; ++k) {
            v4f a0 = {0, 0, 0, 0}, a1 = a0;
#pragma unroll
            for (int ks = 0; ks < 14; ++ks) { const int xo = L_X + (rho >> 3) * 14336 + ks * 1024 + g * 256;
                a0 = mfma32(lds16(lds, xo + ((pperm(rho, 0) & 15) ^ ((rho >> 3) * 4)) * 16), w[k][ks], a0); a1 = mfma32(lds16(lds, xo + ((pperm(rho, 1) & 15) ^ ((rho >> 3) * 4)) * 16), w[k][ks], a1); }
            u[k] = pk8(a0, a1);
        }
        asm volatile("v_mov_b32 v216, %0\n\tv_mov_b32 v217, %1\n\tv_mov_b32 v218, %2\n\tv_mov_b32 v219, %3\n\tv_mov_b32 v220, %4\n\tv_mov_b32 v221, %5\n\tv_mov_b32 v222, %6\n\tv_mov_b32 v223, %7"
                     :: "v"(u[0].x), "v"(u[0].y), "v"(u[0].z), "v"(u[0].w), "v"(u[1].x), "v"(u[1].y), "v"(u[1].z), "v"(u[1].w) : "v216", "v217", "v218", "v219", "v220", "v221", "v222", "v223");
    }
    int budget = SPIN_CAP;
    int cal = get_cal(c, 0, budget), caln = cal;
    const int sidx = (rho & 2) ? 0xEEEE : 0x4444;
    const int fa = L_MEM + lane * 16;
    const int hb = rho >> 3, hd = 2 * wave + hb, i = rho & 7;
    const int pa = HO_PART + hd * 512 + g * 128 + i * 16, ea = HO_E + hd * 512 + g * 128 + i * 16;
    const int ul = OFF_U + (t * 16 + 2 * wave) * 1024 + lane * 16;
    const int ufl = flagoff(FL_W + rho * 7 + ((14 + (t >> 1)) >> 2));
    const int mo = (lane & 3) == 0 ? HO_MEM + t * 256 + ((lane >> 4) * 4 + (lane & 4) / 2 + ((lane >> 3) & 1)) * 16 : OOB;
    const int p2 = (lane & 4) ? 2 * (lane & 3) + 1 : 2 * (lane & 3);
    const int ra = L_OUT + (lane >> 3) * 64 + p2 * 8;
    const int ow = hb == 0 ? L_OUT + wave * 512 + i * 64 + g * 16 : L_DUMP + lane * 16;
    const int po = wave * 1024 + lane * 16, po2 = min(wave + 4, 6) * 1024 + lane * 16;
    const v4u ones = {0x3f803f80u, 0x3f803f80u, 0x3f803f80u, 0x3f803f80u};
    const v2u ones2 = {0x3f803f80u, 0x3f803f80u};
    const unsigned mlo = hb ? 0u : 0xffffffffu, mhi = ~mlo;
    const unsigned long long hbm = 0xff00ff00ff00ff00ull;
    v4u psel; { const int kq = (rho >> 2) * 2 + (rho & 1); const unsigned one = (kq & 1) ? 0x3f800000u : 0x3f80u;
        psel = (v4u){(kq >> 1) == 0 ? one : 0u, (kq >> 1) == 1 ? one : 0u, (kq >> 1) == 2 ? one : 0u, (kq >> 1) == 3 ? one : 0u}; }
    asm volatile("v_mov_b32 v215, 0" ::: "v215");
    auto check_u = [&](int s) {
        unsigned long long lack; unsigned seen;
        asm volatile("v_cmp_gt_u32 %0, %1, v215" : "=s"(lack) : "s"(s) : "memory");
        if (lack != 0 && !wait_flags<1>(rs, ufl, s, seen)) budget = -1;
    };
    auto load_u = [&](int s) {
        asm volatile("s_nop 4\n\tbuffer_load_dwordx4 v[216:219], %0, %1, %2 offen sc1\n\tbuffer_load_dwordx4 v[220:223], %0, %1, %2 offen offset:1024 sc1"
                     :: "v"(ul), "s"(rs), "s"(s * (NH * 28672)) : "memory", "v216", "v217", "v218", "v219", "v220", "v221", "v222", "v223");
    };
    auto store_v = [&](int s) { st16s(rs, OFF_V + t * 4096 + (wave & 3) * 1024 + lane * 16, s * (NH * 7168), lds16(lds, L_V + (s & 1) * 4096 + (wave & 3) * 1024 + lane * 16)); };
    unsigned tref = now() - 65536;
    auto mem_times_b = [&](int sb, int s, bool last) -> v2u {
        v4f c0 = {0, 0, 0, 0}, c1 = {0, 0, 0, 0};
        if (wave < 4) stage2(rs, po, po2, s ? sb + HO_MEM : sb + OFF_MEM0, budget);
        else BAR0();
        if (last && wave == 7 && lane == 0) st4s(rs, flagoff(FL_V + t), 0, s);
#define AFTER_STEP(f) if (f == S_AT && !last) { SCHED(); if (wave == 0) wait_until(tref + T_OS); asm volatile("s_barrier\n\tbuffer_load_dwordx4 v[224:227], %0, %2, %3 offen sc1\n\tbuffer_load_dwordx4 v[228:231], %1, %2, %3 offen sc1" \
                          :: "v"(pa), "v"(ea), "s"(rs), "s"(sb) : "memory", VCLOB); if (wave >= 4) __builtin_amdgcn_s_setprio(1); SCHED(); }
#define EXTRA_READS if (wave < 4 && !last && s > 0) load_u(s)
        MEM_TIMES_W(fa)
#undef EXTRA_READS
        *(v2u*)(lds + L_V + (s & 1) * 4096 + wave * 512 + lane * 8) = tp;
        return tp;
    };
#pragma unroll 1
    for (int s = 0; s < NSEG - 1 && budget >= 0; ++s) {
        const int sb = slot_base(s, s < CAL_STEP ? cal : caln), sn = slot_base(s + 1, s + 1 < CAL_STEP ? cal : caln);
        const v2u vp = mem_times_b(sb, s, false);
        asm volatile(
            "s_branch 3f\n\t"
            "1:\n\t"
            "buffer_load_dwordx4 v[224:227], %[pa], %[rs], %[sb] offen sc1\n\t"
            "buffer_load_dwordx4 v[228:231], %[ea], %[rs], %[sb] offen sc1\n\t"
            "3:\n\t"
            "s_add_i32 %[bud], %[bud], -1\n\ts_cmp_lt_i32 %[bud], 0\n\ts_cselect_b32 vcc_hi, -1, -2\n\tv_mov_b32 v253, vcc_hi\n\t"
            "s_waitcnt vmcnt(1)\n\t"
            "v_mfma_f32_16x16x32_bf16 v[232:235], %[psel], v[224:227], 0\n\t"
            "v_max3_u32 v250, v224, v225, v226\n\t"
            "s_waitcnt vmcnt(0)\n\t"
            "v_mfma_f32_16x16x32_bf16 v[244:247], %[ones], v[228:231], 0\n\t"
            "v_max3_u32 v251, v228, v229, v230\n\t"
            "v_mfma_f32_16x16x32_bf16 v[236:239], v[216:219], v[228:231], 0\n\t"
            "v_max3_u32 v250, v250, v227, v231\n\t"
            "v_mfma_f32_16x16x32_bf16 v[240:243], v[220:223], v[228:231], 0\n\t"
            "v_max_u32 v250, v250, v251\n\t"
            "v_cmp_gt_u32 vcc, v250, v253\n\t"
            "s_cbranch_vccnz 1b\n\t"
            "v_exp_f32 v250, v232\n\tv_exp_f32 v251, v233\n\t"
            "v_max_f32 v252, v232, v233\n\t"
            "v_cvt_pk_bf16_f32 v250, v250, v251\n\t"
            "v_cmp_lt_f32 vcc, 0x42800000, v252\n\t"
            "v_and_b32 v248, v250, %[mlo]\n\tv_and_b32 v249, v250, %[mhi]\n\t"
            "v_cndmask_b32 v236, v236, v240, %[hbm]\n\tv_cndmask_b32 v237, v237, v241, %[hbm]\n\t"
            "v_mfma_f32_16x16x16_bf16 v[244:247], %[ones2], v[248:249], v[244:247]\n\t"
            "v_cndmask_b32 v238, v238, v242, %[hbm]\n\tv_cndmask_b32 v239, v239, v243, %[hbm]\n\t"
            "s_nop 1\n\t"
            "v_mfma_f32_16x16x16_bf16 v[236:239], %[vp], v[248:249], v[236:239]\n\t"
            "s_cbranch_vccz 2f\n\t"
            "s_nop 7\n\t"
            "v_max_f32 v252, 0, v252\n\tv_mov_b32 v253, v252\n\ts_nop 1\n\tv_permlane16_swap_b32 v252, v253\n\ts_nop 1\n\t"
            "v_max_f32 v252, v252, v253\n\tv_mov_b32 v253, v252\n\ts_nop 1\n\tv_permlane32_swap_b32 v252, v253\n\ts_nop 1\n\t"
            "v_max_f32 v252, v252, v253\n\t"
            "v_mfma_f32_16x16x32_bf16 v[244:247], %[ones], v[228:231], 0\n\t"
            "v_mfma_f32_16x16x32_bf16 v[236:239], v[216:219], v[228:231], 0\n\t"
            "v_sub_f32 v250, v232, v252\n\tv_sub_f32 v251, v233, v252\n\t"
            "v_exp_f32 v250, v250\n\tv_exp_f32 v251, v251\n\tv_exp_f32 v253, -v252\n\ts_nop 0\n\t"
            "v_cvt_pk_bf16_f32 v250, v250, v251\n\t"
            "v_and_b32 v248, v250, %[mlo]\n\tv_and_b32 v249, v250, %[mhi]\n\ts_nop 7\n\t"
            "v_cndmask_b32 v236, v236, v240, %[hbm]\n\tv_cndmask_b32 v237, v237, v241, %[hbm]\n\tv_cndmask_b32 v238, v238, v242, %[hbm]\n\tv_cndmask_b32 v239, v239, v243, %[hbm]\n\t"
            "v_mul_f32 v236, v236, v253\n\tv_mul_f32 v237, v237, v253\n\tv_mul_f32 v238, v238, v253\n\tv_mul_f32 v239, v239, v253\n\t"
            "v_mul_f32 v244, v244, v253\n\tv_mul_f32 v245, v245, v253\n\tv_mul_f32 v246, v246, v253\n\tv_mul_f32 v247, v247, v253\n\ts_nop 1\n\t"
            "v_mfma_f32_16x16x16_bf16 v[244:247], %[ones2], v[248:249], v[244:247]\n\t"
            "v_mfma_f32_16x16x16_bf16 v[236:239], %[vp], v[248:249], v[236:239]\n\t"
            "s_nop 7\n\t"
            "2:\n\t"
            "s_nop 1\n\t"
            "v_rcp_f32 v252, v244\n\t"
            "s_nop 3\n\t"
            "v_mul_f32 v236, v236, v252\n\tv_mul_f32 v237, v237, v252\n\tv_mul_f32 v238, v238, v252\n\tv_mul_f32 v239, v239, v252\n\t"
            "v_add_f32_dpp v240, v236, v236 row_ror:8 row_mask:0xf bank_mask:0xf\n\tv_add_f32_dpp v241, v237, v237 row_ror:8 row_mask:0xf bank_mask:0xf\n\t"
            "v_add_f32_dpp v242, v238, v238 row_ror:8 row_mask:0xf bank_mask:0xf\n\tv_add_f32_dpp v243, v239, v239 row_ror:8 row_mask:0xf bank_mask:0xf\n\t"
            "ds_write_b128 %[ow], v[240:243]\n\t"
            PUBLISH_ASM(512)
            : [bud] "+s"(budget)
            : [pa] "v"(pa), [ea] "v"(ea), [rs] "s"(rs), [sb] "s"(sb), [psel] "v"(psel), [ones] "v"(ones), [ones2] "v"(ones2), [vp] "v"(vp),
              [mlo] "v"(mlo), [mhi] "v"(mhi), [hbm] "s"(hbm), [ow] "v"(ow),
              [pub] "s"(wave), [la] "v"(ra), [vo] "v"(mo), [so] "s"(sn)
            : "memory", "vcc", "scc", VCLOB);
        __builtin_amdgcn_s_setprio(0);
        tref = now();
        if (wave == 7 && lane == 0) st4s(rs, flagoff(FL_V + t), 0, s);
        if (s + 2 == CAL_STEP) caln = __builtin_amdgcn_readfirstlane(*(const int*)(lds + L_CAL));
        if (wave == 5 && s + 3 == CAL_STEP) *(int*)(lds + L_CAL) = get_cal(c, 1, budget);
        if (s < 2 && wave >= 4) check_u(s + 1);
        if (s > 0 && wave == 4) check_u(min(s + 2, NSEG - 2));
        if (wave < 4) wait_until(tref + T_OM);
        else {
            wait_until(tref + T_OU);
            if (s + 1 < NSEG - 1) load_u(s + 1);
            if (wave == 4) asm volatile("buffer_load_dword v215, %0, %1, 0 offen sc1" :: "v"(ufl), "s"(rs) : "memory", "v215");
            store_v(s);
            if (wave == 6 && s >= 2) st16s(rs, lane < 16 ? HO_MEM + t * 256 + lane * 16 : OOB, slot_base(s - 1, s - 1 < CAL_STEP ? cal : caln), (v4u){SENT, SENT, SENT, SENT});
        }
    }
    VMWAIT(0);
    mem_times_b(slot_base(NSEG - 1, caln), NSEG - 1, true);
    if (budget < 0) give_up(rs);
    BAR();
    if (wave >= 4) store_v(NSEG - 1);
    VMWAIT(0);
    BAR0();
    if (wave == 7 && lane == 0) st4s(rs, flagoff(FL_V + t), 0, NSEG);
}

DEV void role_w(const Params& p, const Ctx& c, int wi) {
    const int lane = c.lane, wave = c.wave, rho = c.rho, g = c.g;
    const rsrc_t rs = c.rs; char* lds = c.lds;
    if (wave >= 4) {
#pragma unroll 1
        for (int n = -3; n < NSEG; ++n) {
            const int vo = n + 3 < NSEG ? OFF_XF + lane * 16 : OOB, so = ((n + 4) & 63) * 28672 + (wave - 4) * 7168, la = ((n + 3) & 3) * 28672 + (wave - 4) * 7168;
#pragma unroll 1
            for (int k = 0; k < 7168; k += 1024) dma16(rs, vo, so + k, la + k);
            if (n < -1) continue;
            if (n == 24 && wave == 4 && wi >= 48) {
                unsigned seen; wait_flags<8>(rs, flagoff(FL_TAB), 28, seen);
                __builtin_amdgcn_fence(__ATOMIC_ACQUIRE, "agent");
                VMWAIT(0);
            }
            VMWAIT(14);
            BAR0();
        }
        VMWAIT(0);
        BAR0();
        return;
    }
    const int h = wi / 7, unit = (wi - h * 7) * 4 + wave;
    const bool isG = unit < 14;
    v4u w[2][14];
#pragma unroll
    for (int t = 0; t < 2; ++t) {
        const int row = isG ? unit * 32 + pperm(rho, t) : (unit - 14) * 32 + t * 16 + rho;
#pragma unroll
        for (int ks = 0; ks < 14; ++ks) w[t][ks] = ld16(rs, (isG ? OFF_AP : OFF_BT) + (h * 28 + (row >> 4)) * 14336 + g * 256 + (row & 15) * 16, ks * 1024, 0);
    }
    int xa[2];
#pragma unroll
    for (int k = 0; k < 2; ++k) { const int key = isG ? k * 16 + rho : pperm(rho, k), kt = key >> 4; xa[k] = kt * 14336 + (g * 16 + ((key & 15) ^ (kt * 4))) * 16; }
    const int ob = (isG ? OFF_G + (h * 14 + unit) * 2048 : OFF_U + ((unit - 14) * 32 + h) * 1024) + lane * 16;
    const int ostep = isG ? 1024 : 16384;
    BAR0();
#pragma unroll 1
    for (int n = 0; n < NSEG; ++n) {
        const char* xb = lds + (n & 3) * 28672;
        v4f a00 = {0, 0, 0, 0}, a01 = a00, a10 = a00, a11 = a00;
        v4u x[2][14];
#pragma unroll
        for (int ks = 0; ks < 4; ++ks) { x[0][ks] = lds16(xb, xa[0] + ks * 1024); x[1][ks] = lds16(xb, xa[1] + ks * 1024); }
        if (isG) {
#pragma unroll
            for (int ks = 0; ks < 14; ++ks) {
                if (ks + 4 < 14) { x[0][ks + 4] = lds16(xb, xa[0] + (ks + 4) * 1024); x[1][ks + 4] = lds16(xb, xa[1] + (ks + 4) * 1024); }
                SCHED();
                a00 = mfma32(w[0][ks], x[0][ks], a00); a01 = mfma32(w[1][ks], x[0][ks], a01); a10 = mfma32(w[0][ks], x[1][ks], a10); a11 = mfma32(w[1][ks], x[1][ks], a11);
                SCHED();
            }
        } else {
#pragma unroll
            for (int ks = 0; ks < 14; ++ks) {
                if (ks + 4 < 14) { x[0][ks + 4] = lds16(xb, xa[0] + (ks + 4) * 1024); x[1][ks + 4] = lds16(xb, xa[1] + (ks + 4) * 1024); }
                SCHED();
                a00 = mfma32(x[0][ks], w[0][ks], a00); a01 = mfma32(x[1][ks], w[0][ks], a01); a10 = mfma32(x[0][ks], w[1][ks], a10); a11 = mfma32(x[1][ks], w[1][ks], a11);
                SCHED();
            }
        }
        const int so = ((n + 1) & 63) * (NH * 28672);
        st16s(rs, ob, so, pk8(a00, a01)); st16s(rs, ob + ostep, so, pk8(a10, a11));
        if (n == 0) VMWAIT(0); else if (n < W_LAG_FROM) VMWAIT(2); else VMWAIT(4);
        BAR0();
        if (wave == 0 && lane == 0) st4s(rs, flagoff(FL_W + wi), 0, n == 0 ? 1 : n < W_LAG_FROM ? n : n - 1);
    }
    VMWAIT(0);
    BAR0();
    if (wave == 0 && lane == 0) st4s(rs, flagoff(FL_W + wi), 0, NSEG);
}

DEV int f_first(int set, int wave) { return set ? 33 + 2 * wave : wave; }
DEV int f_next(int s, int wave, int set) { return s == 48 + set ? 63 - set : s >= 32 ? s + 16 : s >= 24 ? 32 + 2 * wave : s + 8; }
DEV int pm_base(int s, int set) { return s >= NSEG - 2 && set ? OFF_PM2 - OFF_PM + (s - 62) * (NH * 512) : s * (NH * 512); }
DEV void role_f1(const Params& p, const Ctx& c, int h, int set) {
    const int lane = c.lane, wave = c.wave, rho = c.rho, g = c.g;
    const rsrc_t rs = c.rs;
    const int gfl = flagoff(FL_W + h * 7 + (lane & 3)), tfl = flagoff(FL_T + h * 4 + (lane & 3));
    const int gl0 = OFF_G + h * 28672 + (rho >> 3) * 1024 + g * 256 + (pperm(rho, 0) & 15) * 16;
    const int tl = OFF_T + h * 7168 + g * 128 + ((rho >> 2) * 2 + (rho & 1)) * 16;
    const float ninf = -__builtin_inff();
    bool wdone = false;
#pragma unroll 1
    for (int s = f_first(set, wave); s < NSEG; s = f_next(s, wave, set)) {
        unsigned seen;
        if (!wdone) { if (!wait_flags<8>(rs, gfl, s ? s : NSEG, seen)) return; wdone = __builtin_amdgcn_ballot_w64(seen < NSEG) == 0; }
        v4u x[2][14];
        v4f a[2][2] = {{{0, 0, 0, 0}, {0, 0, 0, 0}}, {{0, 0, 0, 0}, {0, 0, 0, 0}}};
#pragma unroll
        for (int hf = 0; hf < 2; ++hf) {
            v4u y[2][7];
#pragma unroll
            for (int k = 0; k < 7; ++k) {
#pragma unroll
                for (int qt = 0; qt < 2; ++qt) x[qt][hf * 7 + k] = ld16(rs, OFF_XF + qt * 14336 + g * 256 + (rho ^ (qt * 4)) * 16, s * 28672 + (hf * 7 + k) * 1024, 0);
                y[0][k] = ld16(rs, gl0, s * (NH * 28672) + (hf * 7 + k) * 2048, 1); y[1][k] = ld16(rs, gl0 + 64, s * (NH * 28672) + (hf * 7 + k) * 2048, 1);
            }
            SCHED();
#pragma unroll
            for (int k = 0; k < 7; ++k)
#pragma unroll
                for (int qt = 0; qt < 2; ++qt) { a[qt][0] = mfma32(y[0][k], x[qt][hf * 7 + k], a[qt][0]); a[qt][1] = mfma32(y[1][k], x[qt][hf * 7 + k], a[qt][1]); }
        }
        float v[2][8], m[2];
#pragma unroll
        for (int qt = 0; qt < 2; ++qt) {
            const float r[8] = {a[qt][0].x, a[qt][0].y, a[qt][0].z, a[qt][0].w, a[qt][1].x, a[qt][1].y, a[qt][1].z, a[qt][1].w};
            m[qt] = ninf;
#pragma unroll
            for (int k = 0; k < 8; ++k) { v[qt][k] = (8 * g + k <= qt * 16 + rho) ? r[k] : ninf; m[qt] = fmx(m[qt], v[qt][k]); }
        }
        asm volatile("" : "+v"(m[0]), "+v"(m[1]));
        const bool last = s >= NSEG - 2;
        if (!wait_flags<FSL>(rs, tfl, last ? s - 2 : s + 1, seen) || (last && !wait_flags<1>(rs, tfl, s - 1, seen))) return;
        if (last) wait_until(now() + T_F1);
        v4f am[2];
#pragma unroll 1
        for (int sp = 0; sp < SPIN_CAP; ++sp) {
            FENCE();
            v4u y[14]; unsigned mx = 0;
#pragma unroll
            for (int ks = 0; ks < 14; ++ks) y[ks] = ld16(rs, tl, s * (NH * 7168) + ks * 512, 1);
            SCHED();
            am[0] = am[1] = (v4f){0, 0, 0, 0};
#pragma unroll
            for (int ks = 0; ks < 14; ++ks) { am[0] = mfma32(y[ks], x[0][ks], am[0]); am[1] = mfma32(y[ks], x[1][ks], am[1]); mx = max(mx, umax4(y[ks])); PIN1(mx); PIN2(am[0], am[1]); }
            if (!last || __builtin_amdgcn_ballot_w64(mx == SENT) == 0) break;
        }
        unsigned pm[2] = {0u, 0u};
#pragma unroll
        for (int qt = 0; qt < 2; ++qt) {
            if (last && qt != set) continue;
            const float mm = max_g(fmx(m[qt], fmx(am[qt].x, am[qt].y)));
            const float e0 = __builtin_amdgcn_exp2f(am[qt].x - mm), e1 = __builtin_amdgcn_exp2f(am[qt].y - mm);
            float z = e0 + e1;
#pragma unroll
            for (int k = 0; k < 8; ++k) { v[qt][k] = __builtin_amdgcn_exp2f(v[qt][k] - mm); z += v[qt][k]; }
            const float rz = __builtin_amdgcn_rcpf(sum_g(z));
            st16s(rs, OFF_PX + (h * 2 + qt) * 1024 + lane * 16, s * (NH * 2048),
                  (v4u){pk2(v[qt][0] * rz, v[qt][1] * rz), pk2(v[qt][2] * rz, v[qt][3] * rz), pk2(v[qt][4] * rz, v[qt][5] * rz), pk2(v[qt][6] * rz, v[qt][7] * rz)});
            pm[qt] = pk2(e0 * rz, e1 * rz);
        }
        if (last) pm[0] = pm[1] = pm[0] | pm[1];
        st16s(rs, (lane & 1) == 0 ? OFF_PM + h * 512 + lane * 8 : OOB, pm_base(s, set), (v4u){pm[0], pm[1], dppu<0xF5>(pm[0]), dppu<0xF5>(pm[1])});
        if (last) continue;
        VMWAIT(0);
        if (lane == 0) st4s(rs, flagoff((set ? FL_P2 : FL_P) + h * 8 + wave), 0, s + 1);
    }
}

DEV void make_tables(const Params& p, const Ctx& c, int t) {
    const int lane = c.lane, wave = c.wave, rho = c.rho, g = c.g;
    const rsrc_t rs = c.rs;
    for (int ch = t * 512 + wave * 64 + lane; ch < 2 * 28672; ch += 28 * 512) {
        float v[8];
        if (ch < 28672) { const int fr = ch >> 6, tl = fr / 14, d0 = (fr - tl * 14) * 32 + g * 8, ri = tl >> 4, k = (tl & 15) * 16 + rho;
#pragma unroll
            for (int j = 0; j < 8; ++j) { const float ph = (float)((k * (d0 + j)) % 448) * (1.f / 448.f); v[j] = k > 224 ? 0.f : ri ? -__builtin_amdgcn_sinf(ph) : __builtin_amdgcn_cosf(ph); }
        } else { const int fr = (ch - 28672) >> 6, d = (fr >> 4) * 16 + rho, ri = (fr >> 3) & 1, k0 = (fr & 7) * 32 + g * 8;
#pragma unroll
            for (int j = 0; j < 8; ++j) { const int k = k0 + j; const float ph = (float)((k * d) % 448) * (1.f / 448.f); const bool edge = k == 0 || k == 224;
                const float wgt = (edge ? 1.f : 2.f) / (448.f * 2048.f); v[j] = k > 224 ? 0.f : ri ? (edge ? 0.f : -wgt * __builtin_amdgcn_sinf(ph)) : wgt * __builtin_amdgcn_cosf(ph); }
        }
        st16s(rs, OFF_FD + ch * 16, 0, (v4u){pk2(v[0], v[1]), pk2(v[2], v[3]), pk2(v[4], v[5]), pk2(v[6], v[7])});
    }
    VMWAIT(0);
    BAR();
    if (wave == 0 && lane == 0) __hip_atomic_fetch_add((unsigned*)(p.ws + flagoff(FL_TAB)), 1u, __ATOMIC_RELAXED, __HIP_MEMORY_SCOPE_AGENT);
}
DEV void role_f2(const Params& p, const Ctx& c, int t, int set) {
    const int lane = c.lane, wave = c.wave, rho = c.rho, g = c.g;
    const rsrc_t rs = c.rs;
    const int ufl = flagoff(FL_W + rho * 7 + ((14 + (t >> 1)) >> 2)), pfl = flagoff(lane < 16 ? (set ? FL_P2 : FL_P) + lane * 8 + wave : FL_V + t);
    const int ul = OFF_U + t * 16384 + lane * 16;
    bool wdone = false;
#pragma unroll 1
    for (int s = f_first(set, wave); s < NSEG; s = f_next(s, wave, set)) {
        unsigned seen;
        if (!wdone) { if (!wait_flags<8>(rs, ufl, s ? s : NSEG, seen)) return; wdone = __builtin_amdgcn_ballot_w64(seen < NSEG) == 0; }
        v4u u[16];
#pragma unroll
        for (int h = 0; h < 16; ++h) u[h] = ld16(rs, ul, s * (NH * 28672) + h * 1024, 1);
        const bool last = s >= NSEG - 2;
        if (!wait_flags<FSL>(rs, last ? flagoff(FL_V + t) : pfl, last ? s - 1 : s + 1, seen) || (last && !wait_flags<1>(rs, flagoff(FL_V + t), s + 1, seen))) return;
        if (s == NSEG - 1) wait_until(now() + T_F2);
        v4f a[2], m[2];
        const int q0 = last ? set : 0;
#pragma unroll 1
        for (int sp = 0; sp < SPIN_CAP; ++sp) {
            FENCE();
            a[0] = a[1] = m[0] = m[1] = (v4f){0, 0, 0, 0};
            v4u px[16]; v2u pm[16], vv[8]; unsigned mx = 0;
#pragma unroll
            for (int h = 0; h < 16; ++h) { pm[h] = ld8s(rs, OFF_PM + lane * 8, pm_base(s, set) + h * 512); if (h & 1) vv[h >> 1] = ld8s(rs, OFF_V + t * 4096 + lane * 8, s * (NH * 7168) + (h >> 1) * 512); }
#pragma unroll
            for (int h = 0; h < 16; ++h) px[h] = ld16(rs, OFF_PX + lane * 16, s * (NH * 2048) + q0 * 1024 + h * 2048, 1);
            SCHED();
#pragma unroll
            for (int k = 0; k < 8; ++k) { m[0] = mfma16(vv[k], (v2u){pm[2 * k].x, pm[2 * k + 1].x}, m[0]); m[1] = mfma16(vv[k], (v2u){pm[2 * k].y, pm[2 * k + 1].y}, m[1]);
                mx = max(max(mx, max(pm[2 * k].x, pm[2 * k].y)), max(pm[2 * k + 1].x, pm[2 * k + 1].y)); PIN1(mx); PIN2(m[0], m[1]); SCHED(); }
#pragma unroll
            for (int h = 0; h < 16; ++h) { a[0] = mfma32(u[h], px[h], a[0]); mx = max(mx, umax4(px[h])); PIN2(mx, a[0]); SCHED(); }
            if (last) { if (__builtin_amdgcn_ballot_w64(mx == SENT) == 0) break; continue; }
#pragma unroll
            for (int h = 0; h < 16; ++h) px[h] = ld16(rs, OFF_PX + 1024 + lane * 16, s * (NH * 2048) + h * 2048, 1);
            SCHED();
#pragma unroll
            for (int h = 0; h < 16; ++h) { a[1] = mfma32(u[h], px[h], a[1]); PIN1(a[1]); }
            break;
        }
#pragma unroll
        for (int qt = 0; qt < 2; ++qt) { const v4f o = a[qt] + m[qt];
            if (qt == 0 || !last) *(v2u*)(p.out + (size_t)(s * 32 + (qt + q0) * 16 + rho) * 896 + DM + t * 16 + g * 4) = (v2u){pk2(o.x, o.y), pk2(o.z, o.w)}; }
    }
}

DEV bool fft_sync(const Params& p, const Ctx& c, int fl) {
    VMWAIT(0);
    BAR();
    int* okw = (int*)(c.lds + LDS_MAIN - 16);
    if (c.wave == 0) {
        if (c.lane == 0) __hip_atomic_fetch_add((unsigned*)(p.ws + flagoff(fl)), 1u, __ATOMIC_RELAXED, __HIP_MEMORY_SCOPE_AGENT);
        unsigned seen; const bool ok = wait_flags<2>(c.rs, flagoff(fl), 64, seen);
        if (c.lane == 0) *okw = ok;
    }
    BAR();
    return *okw != 0;
}
DEV v4u neg8(v4u v) { return v ^ 0x80008000u; }
DEV void twid(int a, const int (&n)[8], int mask, float inv, v4u& C, v4u& S) {
    float cs[8], sn[8];
#pragma unroll
    for (int j = 0; j < 8; ++j) { const float ph = (float)((a * n[j]) & mask) * inv; cs[j] = __builtin_amdgcn_cosf(ph); sn[j] = __builtin_amdgcn_sinf(ph); }
    C = (v4u){pk2(cs[0], cs[1]), pk2(cs[2], cs[3]), pk2(cs[4], cs[5]), pk2(cs[6], cs[7])};
    S = (v4u){pk2(sn[0], sn[1]), pk2(sn[2], sn[3]), pk2(sn[4], sn[5]), pk2(sn[6], sn[7])};
}
DEV void role_fft(const Params& p, const Ctx& c, int job) {
    const int lane = c.lane, wave = c.wave, rho = c.rho, g = c.g;
    const rsrc_t rs = c.rs; char* lds = c.lds;
    const v4f z4 = {0, 0, 0, 0};
    {
        const rsrc_t rx = mkrsrc(p.xa, 2048 * DM * 2);
        for (int pc = wave; pc < 28; pc += 8) { const int nt = pc >= 14, ks = pc - nt * 14; dma16(rx, ((64 * (nt * 16 + rho) + job) * DM + ks * 32 + g * 8) * 2, 0, pc * 1024); }
        v4u C[2], S[2];
        { int n[8];
#pragma unroll
          for (int j = 0; j < 8; ++j) n[j] = 64 * (16 * (j >> 2) + 4 * g + (j & 3)) + job;
          twid(rho, n, 2047, 1.f / 2048.f, C[0], S[0]); twid(16 + rho, n, 2047, 1.f / 2048.f, C[1], S[1]); }
        VMWAIT(0);
        BAR();
        v4f zr[2][2], zi[2][2];
#pragma unroll
        for (int t = 0; t < 2; ++t) {
            const int fo = OFF_FD + (wave * 2 + (rho >> 3)) * 14336 + g * 256 + (pperm(rho, t) & 15) * 16;
            v4f r0 = z4, r1 = z4, i0 = z4, i1 = z4;
#pragma unroll
            for (int hf = 0; hf < 2; ++hf) {
                v4u fr[7], fi[7];
#pragma unroll
                for (int k = 0; k < 7; ++k) { fr[k] = ld16(rs, fo, (hf * 7 + k) * 1024, 0); fi[k] = ld16(rs, fo, 256 * DM * 2 + (hf * 7 + k) * 1024, 0); }
                SCHED();
#pragma unroll
                for (int k = 0; k < 7; ++k) { const v4u x0 = lds16(lds, (hf * 7 + k) * 1024 + lane * 16), x1 = lds16(lds, (14 + hf * 7 + k) * 1024 + lane * 16);
                    r0 = mfma32(x0, fr[k], r0); r1 = mfma32(x1, fr[k], r1); i0 = mfma32(x0, fi[k], i0); i1 = mfma32(x1, fi[k], i1); }
            }
            const v4u yr = pk8(r0, r1), yi = pk8(i0, i1), nr = neg8(yr);
#pragma unroll
            for (int ct = 0; ct < 2; ++ct) { zr[ct][t] = mfma32(yi, S[ct], mfma32(yr, C[ct], z4)); zi[ct][t] = mfma32(nr, S[ct], mfma32(yi, C[ct], z4)); }
        }
        char* O = lds + 32768;
#pragma unroll
        for (int ct = 0; ct < 2; ++ct) { const int o = (ct * 16 + rho) * 1040 + wave * 64 + g * 16;
            *(v4u*)(O + o) = pk8(zr[ct][0], zr[ct][1]); *(v4u*)(O + o + 512) = pk8(zi[ct][0], zi[ct][1]); }
        BAR();
#pragma unroll
        for (int k = 0; k < 4; ++k) st16s(rs, OFF_Z1 + job * 1024 + lane * 16, (wave * 4 + k) * 65536, lds16(O, (wave * 4 + k) * 1040 + lane * 16));
    }
    if (!fft_sync(p, c, FL_FA)) return;
    if (wave < 4) {
        const int k1 = job >> 1, P = (job & 1) * 4 + wave;
        char* L = lds + wave * 8192;
#pragma unroll
        for (int pc = 0; pc < 8; ++pc) dma16s(rs, OFF_Z1 + (k1 * 64 + (pc & 3) * 16 + (lane >> 2)) * 1024 + (pc >> 2) * 512 + P * 64 + (lane & 3) * 16, 0, wave * 8192 + pc * 1024);
        v4u C[4][2], S[4][2];
#pragma unroll
        for (int ks = 0; ks < 2; ++ks) { int n[8];
#pragma unroll
            for (int j = 0; j < 8; ++j) n[j] = 32 * ks + 16 * (j >> 2) + 4 * g + (j & 3);
#pragma unroll
            for (int kt = 0; kt < 4; ++kt) twid(16 * kt + rho, n, 63, 1.f / 64.f, C[kt][ks], S[kt][ks]); }
        VMWAIT(0);
        v2u hr[4], hi[4];
#pragma unroll
        for (int t = 0; t < 2; ++t) {
            const int k = P * 32 + pperm(rho, t);
            v4u xr[2], xi[2];
#pragma unroll
            for (int ks = 0; ks < 2; ++ks) { const int a = (32 * ks + 4 * g + (rho >> 2)) * 64 + (rho & 3) * 16 + t * 8;
                const v2u a0 = ldstr(L, a), a1 = ldstr(L, a + 1024), b0 = ldstr(L, a + 4096), b1 = ldstr(L, a + 5120);
                xr[ks] = (v4u){a0.x, a0.y, a1.x, a1.y}; xi[ks] = (v4u){b0.x, b0.y, b1.x, b1.y}; }
            const v4u n0 = neg8(xr[0]), n1 = neg8(xr[1]);
            unsigned short fr[16], fi[16];
#pragma unroll
            for (int j = 0; j < 16; ++j) { const int o = (k1 + 32 * (16 * (j >> 2) + 4 * g + (j & 3))) * 225 + min(k, 224); fr[j] = p.wr[o]; fi[j] = p.wi[o]; }
            SCHED();
            v4f vr[4], vi[4];
#pragma unroll
            for (int kt = 0; kt < 4; ++kt) {
                const v4f ar = mfma32(S[kt][1], xi[1], mfma32(S[kt][0], xi[0], mfma32(C[kt][1], xr[1], mfma32(C[kt][0], xr[0], z4))));
                const v4f ai = mfma32(S[kt][1], n1, mfma32(S[kt][0], n0, mfma32(C[kt][1], xi[1], mfma32(C[kt][0], xi[0], z4))));
                float wr[4], wi[4];
#pragma unroll
                for (int r = 0; r < 4; ++r) { wr[r] = __uint_as_float((unsigned)fr[kt * 4 + r] << 16); wi[r] = __uint_as_float((unsigned)fi[kt * 4 + r] << 16); }
                vr[kt] = (v4f){ar.x * wr[0] - ai.x * wi[0], ar.y * wr[1] - ai.y * wi[1], ar.z * wr[2] - ai.z * wi[2], ar.w * wr[3] - ai.w * wi[3]};
                vi[kt] = (v4f){ar.x * wi[0] + ai.x * wr[0], ar.y * wi[1] + ai.y * wr[1], ar.z * wi[2] + ai.z * wr[2], ar.w * wi[3] + ai.w * wr[3]};
            }
            const v4u pr0 = pk8(vr[0], vr[1]), pr1 = pk8(vr[2], vr[3]), pi0 = pk8(vi[0], vi[1]), pi1 = pk8(vi[2], vi[3]), ni0 = neg8(pi0), ni1 = neg8(pi1);
#pragma unroll
            for (int nt = 0; nt < 4; ++nt) {
                const v4f rr = mfma32(ni1, S[nt][1], mfma32(ni0, S[nt][0], mfma32(pr1, C[nt][1], mfma32(pr0, C[nt][0], z4))));
                const v4f ri = mfma32(pr1, S[nt][1], mfma32(pr0, S[nt][0], mfma32(pi1, C[nt][1], mfma32(pi0, C[nt][0], z4))));
                const v2u qr = {pk2(rr.x, rr.y), pk2(rr.z, rr.w)}, qi = {pk2(ri.x, ri.y), pk2(ri.z, ri.w)};
                if (t == 0) { hr[nt] = qr; hi[nt] = qi; }
                else { char* o = lds + 32768 + (nt * 16 + rho) * 528 + wave * 64 + g * 16;
                    *(v4u*)o = (v4u){hr[nt].x, hr[nt].y, qr.x, qr.y}; *(v4u*)(o + 256) = (v4u){hi[nt].x, hi[nt].y, qi.x, qi.y}; }
            }
        }
    }
    BAR();
#pragma unroll
    for (int k = 0; k < 4; ++k) { const int n2 = wave * 8 + k * 2 + (lane >> 5);
        st16s(rs, OFF_Z2 + (n2 * 32 + (job >> 1)) * 1024 + (g & 1) * 512 + (job & 1) * 256 + rho * 16, 0, lds16(lds, 32768 + n2 * 528 + (lane & 31) * 16)); }
    if (!fft_sync(p, c, FL_FB)) return;
    {
        char* L = lds + wave * 4096;
#pragma unroll
        for (int pc = 0; pc < 4; ++pc) dma16s(rs, OFF_Z2 + (job * 32 + (pc & 1) * 16 + (lane >> 2)) * 1024 + (pc >> 1) * 512 + wave * 64 + (lane & 3) * 16, 0, wave * 4096 + pc * 1024);
        v4u C[2], S[2];
        { int n[8];
#pragma unroll
          for (int j = 0; j < 8; ++j) n[j] = 8 * g + j;
          twid(64 * rho + job, n, 2047, 1.f / 2048.f, C[0], S[0]); twid(64 * (16 + rho) + job, n, 2047, 1.f / 2048.f, C[1], S[1]); }
        VMWAIT(0);
        v4f rr[2][2], ri[2][2];
#pragma unroll
        for (int t = 0; t < 2; ++t) {
            const int a = (8 * g + (rho >> 2)) * 64 + (rho & 3) * 16 + t * 8;
            const v2u a0 = ldstr(L, a), a1 = ldstr(L, a + 256), b0 = ldstr(L, a + 2048), b1 = ldstr(L, a + 2304);
            const v4u xr = {a0.x, a0.y, a1.x, a1.y}, xi = {b0.x, b0.y, b1.x, b1.y}, ni = neg8(xi);
#pragma unroll
            for (int nt = 0; nt < 2; ++nt) { rr[nt][t] = mfma32(ni, S[nt], mfma32(xr, C[nt], z4)); ri[nt][t] = mfma32(xr, S[nt], mfma32(xi, C[nt], z4)); }
        }
        char* R = lds + 32768;
#pragma unroll
        for (int nt = 0; nt < 2; ++nt) { *(v4u*)(R + (nt * 16 + wave) * 1024 + lane * 16) = pk8(rr[nt][0], rr[nt][1]); *(v4u*)(R + (nt * 16 + 8 + wave) * 1024 + lane * 16) = pk8(ri[nt][0], ri[nt][1]); }
        BAR();
#pragma unroll 1
        for (int Q = wave; Q < 14; Q += 8) {
            v4f y[2][2] = {{z4, z4}, {z4, z4}};
#pragma unroll
            for (int hf = 0; hf < 2; ++hf) {
                v4u f0[8], f1[8];
#pragma unroll
                for (int k = 0; k < 8; ++k) { const int fo = OFF_FI + (Q * 2 + (rho >> 3)) * 16384 + g * 256 + (pperm(rho, 0) & 15) * 16; f0[k] = ld16(rs, fo, (hf * 8 + k) * 1024, 0); f1[k] = ld16(rs, fo + 64, (hf * 8 + k) * 1024, 0); }
                SCHED();
#pragma unroll
                for (int k = 0; k < 8; ++k) { const v4u b0 = lds16(R, (hf * 8 + k) * 1024 + lane * 16), b1 = lds16(R, (16 + hf * 8 + k) * 1024 + lane * 16);
                    y[0][0] = mfma32(f0[k], b0, y[0][0]); y[0][1] = mfma32(f1[k], b0, y[0][1]); y[1][0] = mfma32(f0[k], b1, y[1][0]); y[1][1] = mfma32(f1[k], b1, y[1][1]); }
            }
#pragma unroll
            for (int nt = 0; nt < 2; ++nt) *(v4u*)(p.out + (size_t)(64 * (nt * 16 + rho) + job) * 896 + Q * 32 + g * 8) = pk8(y[nt][0], y[nt][1]);
        }
    }
}

extern "C" __global__ void __launch_bounds__(512, 2) k_main(Params p) {
    extern __shared__ __attribute__((aligned(16))) char lds[];
    Ctx c; c.rs = mkrsrc(p.ws, WS_BYTES); c.lds = lds; c.lane = threadIdx.x & 63; c.wave = __builtin_amdgcn_readfirstlane(threadIdx.x >> 6); c.rho = c.lane & 15; c.g = c.lane >> 4;
    const int rot = *(const int*)(p.ws + flagoff(FL_ROT));
    const int x = (blockIdx.x + rot) & 7, y = blockIdx.x >> 3;
    if (x < 2) role_p(p, c, x * 32 + y);
    else if (x == 2) { if (y < 28) role_o(p, c, y); else if (y == 28) role_cal(p, c, 1); }
    else if (x == 3 && y == 28) role_cal(p, c, 0);
    else {
        const int wi = (x - 4) * 32 + y - 16;
        if (x == 3) { if (y < 28) make_tables(p, c, y); } else if (wi >= 0) role_w(p, c, wi);
        if (wi >= 48) role_fft(p, c, wi - 48);
        else if (x == 3 ? y < 28 : wi >= 16 && wi < 44) role_f2(p, c, x == 3 ? y : wi - 16, x != 3);
        else if (x == 4) role_f1(p, c, y & 15, wi >= 0);
    }
}

extern "C" void kernel_launch(void* const* d_in, const int* in_sizes, int n_in, void* d_out, int out_size, void* d_ws, size_t ws_size, hipStream_t stream) {
    Params p;
    p.xa = (const unsigned short*)d_in[0]; p.wr = (const unsigned short*)d_in[1]; p.wi = (const unsigned short*)d_in[2];
    p.xb = (const unsigned short*)d_in[3]; p.mem0 = (const unsigned short*)d_in[4];
    p.wq = (const unsigned short*)d_in[5]; p.wk = (const unsigned short*)d_in[6]; p.wv = (const unsigned short*)d_in[7]; p.wo = (const unsigned short*)d_in[8];
    p.out = (unsigned short*)d_out;
    p.ws = (char*)d_ws + (((size_t)2 << 20) - ((uintptr_t)d_ws & (((size_t)2 << 20) - 1))) % ((size_t)2 << 20);
    (void)hipFuncSetAttribute((const void*)k_main, hipFuncAttributeMaxDynamicSharedMemorySize, LDS_MAIN);
    (void)hipFuncSetAttribute((const void*)k_pro, hipFuncAttributeMaxDynamicSharedMemorySize, LDS_PRO);
    hipLaunchKernelGGL(k_pro, dim3(256), dim3(512), LDS_PRO, stream, p);
    hipLaunchKernelGGL(k_main, dim3(256), dim3(512), LDS_MAIN, stream, p);
}
```
